```python
import torch

def _softmax(x, axis=-1):
    return torch.softmax(x, dim=axis)
def _gelu(x):
    return torch.nn.functional.gelu(x, approximate="tanh")
def _silu(x):
    return torch.nn.functional.silu(x)
def _sigmoid(x):
    return torch.sigmoid(x)
def _layernorm(x, axis=-1, eps=1e-5):
    m = x.mean(dim=axis, keepdim=True)
    v = x.var(dim=axis, keepdim=True, unbiased=False)
    return (x - m) / torch.sqrt(v + eps)
def _scatter_add(tgt, idx, src, axis=0):
    return tgt.clone().index_add_(axis, idx, src)
def _segment_sum(src, seg, n):
    out = torch.zeros((n,) + src.shape[1:], dtype=src.dtype)
    return out.index_add_(0, seg, src)
def _pad(x, pw):
    flat = tuple(v for pair in reversed(pw) for v in pair)
    return torch.nn.functional.pad(x, flat)

kernel_name = 'parallel_sum__linear_attn_block__swiglu_ffn__prompt'

def setup_inputs():

    _w = (lambda a: (lambda t: t.to(torch.bfloat16) if t.is_floating_point() else t)(torch.as_tensor(a)))
    def _A_setup():
        B, S, D = 1, 2048, 640
        return {
            "x": _w((lambda *s: torch.randn(*s, dtype=torch.bfloat16) * 0.05)(B, S, D)),
            "wqkv": _w((lambda *s: torch.randn(*s, dtype=torch.bfloat16) * 0.2)(D, 3 * D)),
            "wo": _w((lambda *s: torch.randn(*s, dtype=torch.bfloat16) * 0.2)(D, D)),
        }
    _A_in = _A_setup()

    def _B_setup():
        B, S, D, F = 1, 2048, 640, 1728
        return {
            "x": _w((lambda *s: torch.randn(*s, dtype=torch.bfloat16) * 0.05)(B, S, D)),
            "w_gate": _w((lambda *s: torch.randn(*s, dtype=torch.bfloat16) * 0.05)(D, F)),
            "w_up": _w((lambda *s: torch.randn(*s, dtype=torch.bfloat16) * 0.05)(D, F)),
            "w_down": _w((lambda *s: torch.randn(*s, dtype=torch.bfloat16) * 0.05)(F, D)),
        }
    _B_in = _B_setup()

    return {
        "A_x": _A_in["x"],
        "A_wqkv": _A_in["wqkv"],
        "A_wo": _A_in["wo"],
        "B_x": _B_in["x"],
        "B_w_gate": _B_in["w_gate"],
        "B_w_up": _B_in["w_up"],
        "B_w_down": _B_in["w_down"],
    }

def reference(A_x, A_wqkv, A_wo, B_x, B_w_gate, B_w_up, B_w_down):
    def _A_ref(x, wqkv, wo):
        B, S, D = x.shape
        H = 32; Dh = D // H
        qkv = (lambda x, *d: x.permute(*d))((x @ wqkv).reshape(B, S, 3, H, Dh), 2, 0, 3, 1, 4)
        q, k, v = qkv[0], qkv[1], qkv[2]
        phi = lambda t: torch.where(t > 0, t + 1, torch.exp(t)) + 1e-6
        pq, pk = phi(q), phi(k)
        kv = torch.einsum("bhsd,bhse->bhde", pk, v)
        z = torch.einsum("bhsd->bhd", pk)
        num = torch.einsum("bhqd,bhde->bhqe", pq, kv)
        den = torch.einsum("bhqd,bhd->bhq", pq, z)[..., None] + 1e-6
        o = num / den
        return (lambda x, *d: x.permute(*d))(o, 0, 2, 1, 3).reshape(B, S, D) @ wo

    def _B_ref(x, w_gate, w_up, w_down):
        g = _silu(x @ w_gate)
        u = x @ w_up
        return (g * u) @ w_down

    x = A_x
    xn = _layernorm(x, axis=-1)
    return x + _A_ref(xn, A_wqkv, A_wo) + _B_ref(xn, B_w_gate, B_w_up, B_w_down)
```

```cpp
#include <hip/hip_runtime.h>
#include <hip/hip_bf16.h>
typedef unsigned short u16;
__device__ __forceinline__ float bf2f(u16 v){ return __uint_as_float(((unsigned)v)<<16); }
__device__ __forceinline__ u16 f2bf(float f){ unsigned u=__float_as_uint(f); u += 0x7fffu + ((u>>16)&1u); return (u16)(u>>16); }
constexpr int S=2048,D=640,F=1728,H=32,DH=20;
__global__ void k_ln(const u16* x, float* xn){
  int r=blockIdx.x; __shared__ float red[64];
  float s=0; for(int i=threadIdx.x;i<D;i+=64) s+=bf2f(x[r*D+i]);
  for(int o=32;o;o>>=1) s+=__shfl_xor(s,o);
  float m=s/D; float v=0; for(int i=threadIdx.x;i<D;i+=64){float d=bf2f(x[r*D+i])-m; v+=d*d;}
  for(int o=32;o;o>>=1) v+=__shfl_xor(v,o);
  float rs=rsqrtf(v/D+1e-5f);
  for(int i=threadIdx.x;i<D;i+=64) xn[r*D+i]=(bf2f(x[r*D+i])-m)*rs;
}
__global__ void k_mm(const float* A, const u16* W, float* C, int N, int K){
  int n=blockIdx.x*64+threadIdx.x; int r0=blockIdx.y*8; if(n>=N) return;
  float acc[8]={0,0,0,0,0,0,0,0};
  for(int k=0;k<K;++k){ float w=bf2f(W[k*N+n]);
#pragma unroll
    for(int i=0;i<8;++i) acc[i]+=A[(r0+i)*K+k]*w; }
#pragma unroll
  for(int i=0;i<8;++i) C[(r0+i)*N+n]=acc[i];
}
__device__ __forceinline__ float phi(float t){ return (t>0? t+1.f : __expf(t))+1e-6f; }
__global__ void k_kv(const float* qkv, float* kv){
  int h=blockIdx.x,t=threadIdx.x; if(t>=420) return; int d=t/21,e=t%21; float s=0;
  for(int r=0;r<S;++r){ float pk=phi(qkv[r*1920+640+h*20+d]); float v= e<20? qkv[r*1920+1280+h*20+e]:1.f; s+=pk*v; }
  kv[h*420+t]=s;
}
__global__ void k_o(const float* qkv,const float* kv,float* o){
  int r=blockIdx.x; int c=threadIdx.x; int h=c/20,e=c%20; float num=0,den=0;
  for(int d=0;d<20;++d){ float pq=phi(qkv[r*1920+h*20+d]); num+=pq*kv[h*420+d*21+e]; den+=pq*kv[h*420+d*21+20]; }
  o[r*640+c]=bf2f(f2bf(num/(den+1e-6f)));
}
__global__ void k_h(const float* g,const float* u,float* h){ int i=blockIdx.x*256+threadIdx.x; float a=g[i]; h[i]=bf2f(f2bf(a/(1.f+__expf(-a))*u[i])); }
__global__ void k_out(const u16* x,const float* a,const float* b,u16* out){ int i=blockIdx.x*256+threadIdx.x; out[i]=f2bf(bf2f(x[i])+a[i]+b[i]); }
extern "C" void kernel_launch(void* const* d_in, const int* in_sizes, int n_in, void* d_out, int out_size, void* d_ws, size_t ws_size, hipStream_t stream){
  const u16* x=(const u16*)d_in[0]; const u16* wqkv=(const u16*)d_in[1]; const u16* wo=(const u16*)d_in[2];
  const u16* wg=(const u16*)d_in[4]; const u16* wu=(const u16*)d_in[5]; const u16* wd=(const u16*)d_in[6];
  float* ws=(float*)d_ws; float* xn=ws; float* qkv=xn+S*D; float* g=qkv+S*1920; float* u=g+S*F; float* h=u+S*F; float* o=h+S*F; float* a=o+S*D; float* b=a+S*D; float* kv=b+S*D;
  k_ln<<<S,64,0,stream>>>(x,xn);
  k_mm<<<dim3(30,S/8),64,0,stream>>>(xn,wqkv,qkv,1920,D);
  k_mm<<<dim3(27,S/8),64,0,stream>>>(xn,wg,g,F,D);
  k_mm<<<dim3(27,S/8),64,0,stream>>>(xn,wu,u,F,D);
  k_kv<<<H,448,0,stream>>>(qkv,kv);
  k_o<<<S,640,0,stream>>>(qkv,kv,o);
  k_h<<<S*F/256,256,0,stream>>>(g,u,h);
  k_mm<<<dim3(10,S/8),64,0,stream>>>(o,wo,a,D,D);
  k_mm<<<dim3(10,S/8),64,0,stream>>>(h,wd,b,D,F);
  k_out<<<S*D/256,256,0,stream>>>(x,a,b,(u16*)d_out);
}
```

```cpp
#define PROBE 0
#include <hip/hip_runtime.h>
#include <hip/hip_bf16.h>
typedef unsigned short u16;
__device__ __forceinline__ float bf2f(u16 v){ return __uint_as_float(((unsigned)v)<<16); }
__device__ __forceinline__ u16 f2bf(float f){ unsigned u=__float_as_uint(f); u += 0x7fffu + ((u>>16)&1u); return (u16)(u>>16); }
constexpr int S=2048,D=640,F=1728,H=32,DH=20;
__global__ void k_ln(const u16* x, float* xn){
  int r=blockIdx.x; __shared__ float red[64];
  float s=0; for(int i=threadIdx.x;i<D;i+=64) s+=bf2f(x[r*D+i]);
  for(int o=32;o;o>>=1) s+=__shfl_xor(s,o);
  float m=s/D; float v=0; for(int i=threadIdx.x;i<D;i+=64){float d=bf2f(x[r*D+i])-m; v+=d*d;}
  for(int o=32;o;o>>=1) v+=__shfl_xor(v,o);
  float rs=rsqrtf(v/D+1e-5f);
  for(int i=threadIdx.x;i<D;i+=64) xn[r*D+i]=(bf2f(x[r*D+i])-m)*rs;
}
__global__ void k_mm(const float* A, const u16* W, float* C, int N, int K){
  int n=blockIdx.x*64+threadIdx.x; int r0=blockIdx.y*8; if(n>=N) return;
  float acc[8]={0,0,0,0,0,0,0,0};
  for(int k=0;k<K;++k){ float w=bf2f(W[k*N+n]);
#pragma unroll
    for(int i=0;i<8;++i) acc[i]+=A[(r0+i)*K+k]*w; }
#pragma unroll
  for(int i=0;i<8;++i) C[(r0+i)*N+n]=acc[i];
}
__device__ __forceinline__ float phi(float t){ return (t>0? t+1.f : __expf(t))+1e-6f; }
__global__ void k_kv(const float* qkv, float* kv){
  int h=blockIdx.x,t=threadIdx.x; if(t>=420) return; int d=t/21,e=t%21; float s=0;
  for(int r=0;r<S;++r){ float pk=phi(qkv[r*1920+640+h*20+d]); float v= e<20? qkv[r*1920+1280+h*20+e]:1.f; s+=pk*v; }
  kv[h*420+t]=s;
}
__global__ void k_o(const float* qkv,const float* kv,float* o){
  int r=blockIdx.x; int c=threadIdx.x; int h=c/20,e=c%20; float num=0,den=0;
  for(int d=0;d<20;++d){ float pq=phi(qkv[r*1920+h*20+d]); num+=pq*kv[h*420+d*21+e]; den+=pq*kv[h*420+d*21+20]; }
  o[r*640+c]=bf2f(f2bf(num/(den+1e-6f)));
}
__global__ void k_h(const float* g,const float* u,float* h){ int i=blockIdx.x*256+threadIdx.x; float a=g[i]; h[i]=bf2f(f2bf(a/(1.f+__expf(-a))*u[i])); }
__global__ void k_out(const u16* x,const float* a,const float* b,u16* out){ int i=blockIdx.x*256+threadIdx.x; out[i]=f2bf(bf2f(x[i])+a[i]+b[i]); }

typedef unsigned v4u __attribute__((ext_vector_type(4)));
__global__ __launch_bounds__(256) void kprobe(const v4u* a0,const v4u* a1,const v4u* a2,const v4u* a4,const v4u* a5,const v4u* a6, v4u* out){
  unsigned long long r0=__builtin_amdgcn_s_memrealtime();
  int t=blockIdx.x*256+threadIdx.x; v4u s={0,0,0,0};
  asm volatile("s_waitcnt vmcnt(0)":"+v"(s)::"memory");
  __syncthreads();
  unsigned long long r1=__builtin_amdgcn_s_memrealtime();
  unsigned long long d=(r1-r0)*200; if(d>400000) d=400000;
  while(__builtin_amdgcn_s_memrealtime()-r0<d) __builtin_amdgcn_s_sleep(8);
  out[t]=s;
}
extern "C" void kernel_launch(void* const* d_in, const int* in_sizes, int n_in, void* d_out, int out_size, void* d_ws, size_t ws_size, hipStream_t stream){
  const u16* x=(const u16*)d_in[0]; const u16* wqkv=(const u16*)d_in[1]; const u16* wo=(const u16*)d_in[2];
  const u16* wg=(const u16*)d_in[4]; const u16* wu=(const u16*)d_in[5]; const u16* wd=(const u16*)d_in[6];
  float* ws=(float*)d_ws; float* xn=ws; float* qkv=xn+S*D; float* g=qkv+S*1920; float* u=g+S*F; float* h=u+S*F; float* o=h+S*F; float* a=o+S*D; float* b=a+S*D; float* kv=b+S*D;
  kprobe<<<256,256,0,stream>>>((const v4u*)d_in[0],(const v4u*)d_in[1],(const v4u*)d_in[2],(const v4u*)d_in[4],(const v4u*)d_in[5],(const v4u*)d_in[6],(v4u*)d_out);
  k_ln<<<S,64,0,stream>>>(x,xn);
  k_mm<<<dim3(30,S/8),64,0,stream>>>(xn,wqkv,qkv,1920,D);
  k_mm<<<dim3(27,S/8),64,0,stream>>>(xn,wg,g,F,D);
  k_mm<<<dim3(27,S/8),64,0,stream>>>(xn,wu,u,F,D);
  k_kv<<<H,448,0,stream>>>(qkv,kv);
  k_o<<<S,640,0,stream>>>(qkv,kv,o);
  k_h<<<S*F/256,256,0,stream>>>(g,u,h);
  k_mm<<<dim3(10,S/8),64,0,stream>>>(o,wo,a,D,D);
  k_mm<<<dim3(10,S/8),64,0,stream>>>(h,wd,b,D,F);
  k_out<<<S*D/256,256,0,stream>>>(x,a,b,(u16*)d_out);
}
```

```cpp
#include <hip/hip_runtime.h>
#include <hip/hip_bf16.h>

struct BlockTab { unsigned v[512]; };

__global__ __launch_bounds__(256, 1) void fused_block_kernel(const void* x, const void* wqkv, const void* wo, const void* wg,
                                                              const void* wu, const void* wd, void* out, void* ws, BlockTab tab) {
    __shared__ __attribute__((aligned(16))) char lds[163840];
    unsigned tid = threadIdx.x + (unsigned)(size_t)(__attribute__((address_space(3))) char*)lds;
    asm volatile(
"s_mov_b64 s[0:1], %1\n"
"s_mov_b32 s2, %2\n"
"v_mov_b32 v0, %0\n"
"s_load_dwordx16 s[36:51], s[0:1], 0\n"
"s_lshl_b32 s60, s2, 3\n"
"s_load_dwordx2 s[34:35], s[0:1], s60 offset:64\n"
"s_getpc_b64 s[86:87]\n"
".Lfb_pc:\n"
"s_lshr_b32 s61, s2, 3\n"
"s_lshl_b32 s4, s61, 5\n"
"v_lshrrev_b32 v6, 3, v0\n"
"v_add_lshl_u32 v5, v6, s4, 7\n"
"v_min_u32 v5, .Lfb_end-.Lfb_pc-4, v5\n"
"global_load_dword v255, v5, s[86:87]\n"
"v_lshrrev_b32 v5, 6, v0\n"
"v_and_b32 v1, 63, v0\n"
"v_readfirstlane_b32 s3, v5\n"
"v_lshrrev_b32 v52, 4, v1\n"
"v_xor_b32 v52, v52, v1\n"
"v_and_b32 v52, 7, v52\n"
"v_lshrrev_b32 v10, 3, v1\n"
"v_mul_u32_u24 v10, 1280, v10\n"
"v_lshl_add_u32 v10, v52, 4, v10\n"
"s_mov_b32 s6, 2621440\n"
"s_mov_b32 s7, 0x20000\n"
"s_mov_b32 s11, 0x20000\n"
"s_cmp_lt_u32 s61, 11\n"
"s_cbranch_scc0 .Lfb_ffnd\n"
".Lfb_att:\n"
"s_mul_i32 s62, s3, 61440\n"
"s_mov_b32 s64, 0\n"
"s_mov_b32 s65, 20480\n"
"s_mov_b32 s66, 40960\n"
"s_mul_i32 s72, s3, 18432\n"
"s_add_u32 s72, s72, 81920\n"
"v_xor_b32 v11, 64, v10\n"
"v_add_u32 v11, 10240, v11\n"
"s_waitcnt lgkmcnt(0)\n"
"s_mov_b32 s4, s36\n"
"s_and_b32 s5, s37, 0xffff\n"
"s_and_b32 s63, s35, 0x3fffff\n"
"s_add_u32 s62, s62, s63\n"
"v_add_u32 v10, s62, v10\n"
"v_add_u32 v11, s62, v11\n"
"s_add_u32 m0, s72, 0\n"
"s_nop 0\n"
"buffer_load_dwordx4 v10, s[4:7], s64 offen lds\n"
"s_add_u32 m0, s72, 1024\n"
"s_nop 0\n"
"buffer_load_dwordx4 v11, s[4:7], s64 offen lds\n"
"s_add_u32 m0, s72, 2048\n"
"s_nop 0\n"
"buffer_load_dwordx4 v10, s[4:7], s65 offen lds\n"
"s_branch .Lfb_attm\n"
".Lfb_ffnd:\n"
"s_and_b32 s60, s2, 6\n"
"s_cmp_eq_u32 s60, 6\n"
"s_cselect_b32 s60, s2, 0\n"
"s_cmp_ge_u32 s60, 232\n"
"s_cbranch_scc1 .Lfb_idle\n"
"s_and_b32 s60, s3, 1\n"
"s_lshr_b32 s61, s3, 1\n"
"s_lshl_b32 s59, s3, 10\n"
"s_lshl_b32 s62, s60, 2\n"
"v_bfe_u32 v52, v1, 4, 1\n"
"v_lshl_or_b32 v52, v52, 1, s62\n"
"v_xor_b32 v52, v52, v1\n"
"v_and_b32 v52, 7, v52\n"
"v_mul_u32_u24 v6, 3456, v6\n"
"v_lshl_add_u32 v6, v52, 4, v6\n"
"s_lshl_b32 s62, s60, 6\n"
"v_xor_b32 v5, s62, v10\n"
"s_mul_i32 s62, s61, 22\n"
"s_add_u32 s62, s62, s60\n"
"s_mul_i32 s62, s62, 10240\n"
"s_mov_b32 s10, 2211840\n"
"s_mov_b32 s14, 2211840\n"
"s_mov_b32 s15, 0x20000\n"
"s_mov_b32 s64, 0\n"
"s_mov_b32 s65, 20480\n"
"s_waitcnt lgkmcnt(0)\n"
"s_mov_b32 s8, s42\n"
"s_and_b32 s9, s43, 0xffff\n"
"s_and_b32 s75, s34, 0x3fffff\n"
"s_add_u32 m0, s59, 32768\n"
"s_nop 0\n"
"buffer_load_dwordx4 v6, s[8:11], s75 offen lds\n"
"s_add_u32 s33, s75, 110592\n"
"s_add_u32 m0, s59, 36864\n"
"s_nop 0\n"
"buffer_load_dwordx4 v6, s[8:11], s33 offen lds\n"
"s_mov_b32 s12, s44\n"
"s_and_b32 s13, s45, 0xffff\n"
"s_add_u32 m0, s59, 40960\n"
"s_nop 0\n"
"buffer_load_dwordx4 v6, s[12:15], s75 offen lds\n"
"s_add_u32 m0, s59, 45056\n"
"s_nop 0\n"
"buffer_load_dwordx4 v6, s[12:15], s33 offen lds\n"
"s_mov_b32 s4, s36\n"
"s_and_b32 s5, s37, 0xffff\n"
"s_and_b32 s63, s35, 0x3fffff\n"
"s_add_u32 s62, s62, s63\n"
"v_add_u32 v5, s62, v5\n"
"s_add_u32 m0, s59, 73728\n"
"s_nop 0\n"
"buffer_load_dwordx4 v5, s[4:7], s64 offen lds\n"
"s_add_u32 m0, s59, 77824\n"
"s_nop 0\n"
"buffer_load_dwordx4 v5, s[4:7], s65 offen lds\n"
"s_add_u32 m0, s59, 81920\n"
"s_mov_b32 s66, 40960\n"
"buffer_load_dwordx4 v5, s[4:7], s66 offen lds\n"
"s_add_u32 m0, s59, 86016\n"
"s_mov_b32 s67, 61440\n"
"buffer_load_dwordx4 v5, s[4:7], s67 offen lds\n"
"s_add_u32 m0, s59, 90112\n"
"s_mov_b32 s68, 81920\n"
"buffer_load_dwordx4 v5, s[4:7], s68 offen lds\n"
"s_add_u32 m0, s59, 94208\n"
"s_mov_b32 s69, 102400\n"
"buffer_load_dwordx4 v5, s[4:7], s69 offen lds\n"
"s_branch .Lfb_ffnm\n"
".Lfb_attm:\n"
"s_add_u32 m0, s72, 3072\n"
"s_nop 0\n"
"buffer_load_dwordx4 v11, s[4:7], s65 offen lds\n"
"s_mov_b32 s8, s38\n"
"s_and_b32 s9, s39, 0xffff\n"
"s_mov_b32 s10, 2457600\n"
"s_and_b32 s60, s3, 1\n"
"s_mul_i32 s14, s60, 122880\n"
"s_lshr_b32 s61, s3, 1\n"
"s_mul_i32 s61, s61, 1280\n"
"s_and_b32 s68, s34, 0x3fffff\n"
"s_add_u32 m0, s72, 4096\n"
"s_nop 0\n"
"buffer_load_dwordx4 v10, s[4:7], s66 offen lds\n"
"s_add_u32 s68, s68, s61\n"
"s_add_u32 s68, s68, s14\n"
"s_mul_i32 s71, s3, 5120\n"
"v_add_u32 v132, 0, v1\n"
"v_mul_u32_u24 v133, 6554, v132\n"
"v_lshrrev_b32 v133, 16, v133\n"
"v_mul_u32_u24 v134, 10, v133\n"
"v_sub_u32 v132, v132, v134\n"
"s_add_u32 m0, s72, 5120\n"
"s_nop 0\n"
"buffer_load_dwordx4 v11, s[4:7], s66 offen lds\n"
"v_and_b32 v134, 0x13, v133\n"
"v_bfe_u32 v135, v133, 2, 1\n"
"v_lshl_add_u32 v134, v135, 3, v134\n"
"v_bfe_u32 v135, v133, 3, 1\n"
"v_lshl_add_u32 v134, v135, 2, v134\n"
"v_mul_u32_u24 v134, 3840, v134\n"
"v_lshl_add_u32 v5, v132, 4, v134\n"
"s_add_u32 m0, s71, 0\n"
"s_nop 0\n"
"buffer_load_dwordx4 v5, s[8:11], s68 offen offset:0 lds\n"
"v_add_u32 v132, 64, v1\n"
"v_mul_u32_u24 v133, 6554, v132\n"
"v_lshrrev_b32 v133, 16, v133\n"
"v_mul_u32_u24 v134, 10, v133\n"
"v_sub_u32 v132, v132, v134\n"
"v_and_b32 v134, 0x13, v133\n"
"v_bfe_u32 v135, v133, 2, 1\n"
"v_lshl_add_u32 v134, v135, 3, v134\n"
"v_bfe_u32 v135, v133, 3, 1\n"
"v_lshl_add_u32 v134, v135, 2, v134\n"
"v_mul_u32_u24 v134, 3840, v134\n"
"v_lshl_add_u32 v6, v132, 4, v134\n"
"v_subrev_u32 v6, 1024, v6\n"
"buffer_load_dwordx4 v6, s[8:11], s68 offen offset:1024 lds\n"
"v_add_u32 v132, 128, v1\n"
"v_mul_u32_u24 v133, 6554, v132\n"
"v_lshrrev_b32 v133, 16, v133\n"
"v_mul_u32_u24 v134, 10, v133\n"
"v_sub_u32 v132, v132, v134\n"
"v_and_b32 v134, 0x13, v133\n"
"v_bfe_u32 v135, v133, 2, 1\n"
"v_lshl_add_u32 v134, v135, 3, v134\n"
"v_bfe_u32 v135, v133, 3, 1\n"
"v_lshl_add_u32 v134, v135, 2, v134\n"
"v_mul_u32_u24 v134, 3840, v134\n"
"v_lshl_add_u32 v7, v132, 4, v134\n"
"v_subrev_u32 v7, 2048, v7\n"
"buffer_load_dwordx4 v7, s[8:11], s68 offen offset:2048 lds\n"
"v_add_u32 v132, 192, v1\n"
"v_mul_u32_u24 v133, 6554, v132\n"
"v_lshrrev_b32 v133, 16, v133\n"
"v_mul_u32_u24 v134, 10, v133\n"
"v_sub_u32 v132, v132, v134\n"
"v_and_b32 v134, 0x13, v133\n"
"v_bfe_u32 v135, v133, 2, 1\n"
"v_lshl_add_u32 v134, v135, 3, v134\n"
"v_bfe_u32 v135, v133, 3, 1\n"
"v_lshl_add_u32 v134, v135, 2, v134\n"
"v_mul_u32_u24 v134, 3840, v134\n"
"v_lshl_add_u32 v8, v132, 4, v134\n"
"v_subrev_u32 v8, 3072, v8\n"
"buffer_load_dwordx4 v8, s[8:11], s68 offen offset:3072 lds\n"
"v_add_u32 v132, 256, v1\n"
"v_mul_u32_u24 v133, 6554, v132\n"
"v_lshrrev_b32 v133, 16, v133\n"
"v_mul_u32_u24 v134, 10, v133\n"
"v_sub_u32 v132, v132, v134\n"
"v_and_b32 v134, 0x13, v133\n"
"v_bfe_u32 v135, v133, 2, 1\n"
"v_lshl_add_u32 v134, v135, 3, v134\n"
"v_bfe_u32 v135, v133, 3, 1\n"
"v_lshl_add_u32 v134, v135, 2, v134\n"
"v_mul_u32_u24 v134, 3840, v134\n"
"v_lshl_add_u32 v9, v132, 4, v134\n"
"s_add_u32 m0, s71, 4096\n"
"s_nop 0\n"
"buffer_load_dwordx4 v9, s[8:11], s68 offen offset:0 lds\n"
"s_bfe_u32 s53, s34, 0x50016\n"
"s_lshr_b32 s32, s34, 27\n"
"s_lshr_b32 s54, s35, 22\n"
"s_mul_i32 s60, s54, 192\n"
"s_mul_i32 s61, s3, 48\n"
"s_add_u32 s67, s60, s61\n"
"s_mul_i32 s60, s53, 160\n"
"s_add_u32 s14, s14, s60\n"
"s_cmp_eq_u32 s32, 9\n"
"s_cselect_b32 s85, -9, 1\n"
"v_lshl_add_u32 v10, s85, 7, v10\n"
"v_lshl_add_u32 v11, s85, 7, v11\n"
"s_add_u32 m0, s72, 6144\n"
"s_nop 0\n"
"buffer_load_dwordx4 v10, s[4:7], s64 offen lds\n"
"v_bfe_u32 v132, v1, 1, 3\n"
"v_lshrrev_b32 v12, 4, v1\n"
"v_xor_b32 v132, v132, v12\n"
"v_and_b32 v12, 15, v1\n"
"v_lshlrev_b32 v12, 7, v12\n"
"v_lshl_add_u32 v12, v132, 4, v12\n"
"v_add_u32 v12, s72, v12\n"
"v_xor_b32 v13, 64, v12\n"
"v_bfe_u32 v132, v1, 2, 3\n"
"v_lshrrev_b32 v133, 5, v1\n"
"s_add_u32 m0, s72, 7168\n"
"s_nop 0\n"
"buffer_load_dwordx4 v11, s[4:7], s64 offen lds\n"
"v_lshl_add_u32 v132, v133, 4, v132\n"
"v_mul_u32_u24 v132, 160, v132\n"
"v_and_b32 v133, 3, v1\n"
"v_lshl_add_u32 v14, v133, 3, v132\n"
"v_add_u32 v15, 40960, v14\n"
"s_load_dword s52, s[50:51], 0\n"
"v_lshlrev_b32 v2, 4, v1\n"
"v_mov_b32 v132, 1073741824\n"
"v_cmp_eq_u32 vcc, 0, v1\n"
"s_mov_b32 s16, s50\n"
"s_add_u32 m0, s72, 8192\n"
"s_nop 0\n"
"buffer_load_dwordx4 v10, s[4:7], s65 offen lds\n"
"s_and_b32 s17, s51, 0xffff\n"
"s_mov_b32 s18, 1456128\n"
"s_mov_b32 s19, 0x20000\n"
"v_cndmask_b32 v3, v132, v1, vcc\n"
"s_add_u32 s24, s50, 20971520\n"
"s_addc_u32 s25, s51, 0\n"
"s_and_b32 s25, s25, 0xffff\n"
"s_mov_b32 s26, 3031040\n"
"s_mov_b32 s27, 0x20000\n"
"s_add_u32 s20, s50, 4194304\n"
"s_addc_u32 s21, s51, 0\n"
"s_add_u32 m0, s72, 9216\n"
"s_nop 0\n"
"buffer_load_dwordx4 v11, s[4:7], s65 offen lds\n"
"s_and_b32 s21, s21, 0xffff\n"
"s_mov_b32 s22, 14942208\n"
"s_mov_b32 s23, 0x20000\n"
"s_add_u32 s80, s50, 262144\n"
"s_addc_u32 s81, s51, 0\n"
"s_and_b32 s81, s81, 0xffff\n"
"s_mov_b32 s82, 32768\n"
"s_mov_b32 s83, 0x20000\n"
"s_add_u32 s42, s2, 168\n"
"s_lshl_b32 s42, s42, 7\n"
"s_add_u32 m0, s72, 10240\n"
"s_nop 0\n"
"buffer_load_dwordx4 v10, s[4:7], s66 offen lds\n"
"s_add_u32 s42, s42, 212992\n"
"s_cmp_eq_u32 s3, 0\n"
"s_cselect_b32 s42, s42, 1073741824\n"
"s_lshl_b32 s60, s53, 2\n"
"s_add_u32 s60, s60, s3\n"
"s_lshl_b32 s73, s60, 11\n"
"s_add_u32 s73, s73, 147456\n"
"s_lshl_b32 s61, s54, 7\n"
"s_add_u32 s57, s73, s61\n"
"s_mul_i32 s74, s60, 22528\n"
"s_add_u32 m0, s72, 11264\n"
"s_nop 0\n"
"buffer_load_dwordx4 v11, s[4:7], s66 offen lds\n"
"s_add_u32 s74, s74, 327680\n"
"s_mul_i32 s61, s54, 896\n"
"s_add_u32 s75, s74, s61\n"
"s_mul_i32 s70, s53, 320\n"
"s_add_u32 s70, s70, 245760\n"
"s_mul_i32 s59, s53, 5\n"
"s_lshr_b32 s59, s59, 2\n"
"s_lshl_b32 s59, s59, 7\n"
"s_add_u32 s59, s59, 253952\n"
"s_mul_i32 s95, s3, 10240\n"
"s_mul_i32 s33, s85, 245760\n"
"s_add_u32 s68, s68, s33\n"
"s_add_u32 m0, s71, 20480\n"
"s_nop 0\n"
"buffer_load_dwordx4 v5, s[8:11], s68 offen offset:0 lds\n"
"s_add_u32 s95, s95, 81920\n"
"s_mul_i32 s60, s53, 44\n"
"s_lshl_b32 s61, s54, 2\n"
"s_add_u32 s60, s60, s61\n"
"s_add_u32 s60, s60, s3\n"
"s_lshl_b32 s76, s60, 7\n"
"s_add_u32 s76, s76, 98304\n"
"s_lshr_b32 s60, s67, 4\n"
"s_mul_i32 s60, s60, 114\n"
"s_lshr_b32 s61, s53, 1\n"
"buffer_load_dwordx4 v6, s[8:11], s68 offen offset:1024 lds\n"
"s_mul_i32 s61, s61, 5\n"
"s_add_u32 s60, s60, s61\n"
"s_add_u32 s60, s60, 54\n"
"s_lshl_b32 s60, s60, 10\n"
"s_and_b32 s61, s53, 1\n"
"s_lshl_b32 s62, s61, 11\n"
"s_add_u32 s77, s60, s62\n"
"s_lshl_b32 s62, s61, 9\n"
"s_add_u32 s94, s60, s62\n"
"s_add_u32 s94, s94, 4096\n"
"buffer_load_dwordx4 v7, s[8:11], s68 offen offset:2048 lds\n"
"s_mov_b32 s88, 0xffff\n"
"s_mov_b32 s89, 0\n"
"s_mov_b32 s90, -1\n"
"s_mov_b32 s91, 0\n"
"s_mov_b32 s92, -1\n"
"s_mov_b32 s93, 0xffff\n"
"v_mov_b32 v75, 0x358637bd\n"
"s_waitcnt lgkmcnt(0)\n"
"v_mov_b32 v73, s52\n"
"s_add_u32 s52, s52, 1\n"
"v_mov_b32 v74, s52\n"
"buffer_load_dwordx4 v8, s[8:11], s68 offen offset:3072 lds\n"
"s_add_u32 m0, s71, 24576\n"
"s_nop 0\n"
"buffer_load_dwordx4 v9, s[8:11], s68 offen offset:0 lds\n"
"s_waitcnt vmcnt(16)\n"
"ds_read_b128 a[0:3], v12 offset:0\n"
"ds_read_b128 a[4:7], v12 offset:2048\n"
"ds_read_b128 a[8:11], v12 offset:4096\n"
"ds_read_b128 a[12:15], v13 offset:0\n"
"ds_read_b128 a[16:19], v13 offset:2048\n"
"ds_read_b128 a[20:23], v13 offset:4096\n"
"s_waitcnt vmcnt(11) lgkmcnt(0)\n"
"s_barrier\n"
"ds_read_b64_tr_b16 v[16:17], v14 offset:0\n"
"ds_read_b64_tr_b16 v[18:19], v14 offset:1280\n"
"ds_read_b64_tr_b16 v[20:21], v14 offset:5120\n"
"ds_read_b64_tr_b16 v[22:23], v14 offset:6400\n"
"ds_read_b64_tr_b16 v[24:25], v14 offset:32\n"
"ds_read_b64_tr_b16 v[26:27], v14 offset:1312\n"
"ds_read_b64_tr_b16 v[28:29], v14 offset:5152\n"
"ds_read_b64_tr_b16 v[30:31], v14 offset:6432\n"
"s_waitcnt lgkmcnt(6)\n"
"v_mfma_f32_16x16x32_bf16 v[136:139], a[0:3], v[16:19], 0\n"
"ds_read_b64_tr_b16 v[32:33], v14 offset:64\n"
"s_waitcnt lgkmcnt(5)\n"
"v_mfma_f32_16x16x32_bf16 v[136:139], a[12:15], v[20:23], v[136:139]\n"
"ds_read_b64_tr_b16 v[34:35], v14 offset:1344\n"
"v_mfma_f32_16x16x32_bf16 v[140:143], a[4:7], v[16:19], 0\n"
"s_cmp_eq_u32 s32, 8\n"
"s_cselect_b32 s85, -9, 1\n"
"v_mfma_f32_16x16x32_bf16 v[140:143], a[16:19], v[20:23], v[140:143]\n"
"ds_read_b64_tr_b16 v[36:37], v14 offset:5184\n"
"v_lshl_add_u32 v10, s85, 7, v10\n"
"v_mfma_f32_16x16x32_bf16 v[144:147], a[8:11], v[16:19], 0\n"
"ds_read_b64_tr_b16 v[38:39], v14 offset:6464\n"
"v_lshl_add_u32 v11, s85, 7, v11\n"
"v_mfma_f32_16x16x32_bf16 v[144:147], a[20:23], v[20:23], v[144:147]\n"
"s_waitcnt lgkmcnt(4)\n"
"s_add_u32 m0, s72, 12288\n"
"v_mfma_f32_16x16x32_bf16 v[148:151], a[0:3], v[24:27], 0\n"
"ds_read_b64_tr_b16 v[40:41], v14 offset:96\n"
"buffer_load_dwordx4 v10, s[4:7], s64 offen lds\n"
"v_mfma_f32_16x16x32_bf16 v[148:151], a[12:15], v[28:31], v[148:151]\n"
"ds_read_b64_tr_b16 v[42:43], v14 offset:1376\n"
"s_lshr_b32 s60, s2, 3\n"
"v_mfma_f32_16x16x32_bf16 v[152:155], a[4:7], v[24:27], 0\n"
"buffer_store_dword v73, v3, s[16:19], s57 offen sc1\n"
"v_mfma_f32_16x16x32_bf16 v[152:155], a[16:19], v[28:31], v[152:155]\n"
"ds_read_b64_tr_b16 v[44:45], v14 offset:5216\n"
"s_add_u32 m0, s72, 13312\n"
"v_mfma_f32_16x16x32_bf16 v[156:159], a[8:11], v[24:27], 0\n"
"ds_read_b64_tr_b16 v[46:47], v14 offset:6496\n"
"ds_read_b64_tr_b16 v[16:17], v14 offset:128\n"
"v_mfma_f32_16x16x32_bf16 v[156:159], a[20:23], v[28:31], v[156:159]\n"
"ds_read_b64_tr_b16 v[18:19], v14 offset:1408\n"
"s_waitcnt lgkmcnt(6)\n"
"v_mfma_f32_16x16x32_bf16 v[160:163], a[0:3], v[32:35], 0\n"
"ds_read_b64_tr_b16 v[20:21], v14 offset:5248\n"
"buffer_load_dwordx4 v11, s[4:7], s64 offen lds\n"
"v_mfma_f32_16x16x32_bf16 v[160:163], a[12:15], v[36:39], v[160:163]\n"
"ds_read_b64_tr_b16 v[22:23], v14 offset:6528\n"
"s_waitcnt vmcnt(8)\n"
"v_mfma_f32_16x16x32_bf16 v[164:167], a[4:7], v[32:35], 0\n"
"ds_read_b128 a[24:27], v12 offset:6144\n"
"s_mov_b32 s61, 1\n"
"v_mfma_f32_16x16x32_bf16 v[164:167], a[16:19], v[36:39], v[164:167]\n"
"ds_read_b64_tr_b16 v[24:25], v14 offset:10240\n"
"ds_read_b128 a[28:31], v12 offset:8192\n"
"v_mfma_f32_16x16x32_bf16 v[168:171], a[8:11], v[32:35], 0\n"
"ds_read_b64_tr_b16 v[26:27], v14 offset:11520\n"
"ds_read_b128 a[32:35], v12 offset:10240\n"
"v_mfma_f32_16x16x32_bf16 v[168:171], a[20:23], v[36:39], v[168:171]\n"
"ds_read_b128 a[36:39], v13 offset:6144\n"
"s_waitcnt lgkmcnt(10)\n"
"v_mfma_f32_16x16x32_bf16 v[172:175], a[0:3], v[40:43], 0\n"
"ds_read_b64_tr_b16 v[28:29], v14 offset:15360\n"
"ds_read_b128 a[40:43], v13 offset:8192\n"
"v_mfma_f32_16x16x32_bf16 v[172:175], a[12:15], v[44:47], v[172:175]\n"
"ds_read_b64_tr_b16 v[30:31], v14 offset:16640\n"
"ds_read_b128 a[44:47], v13 offset:10240\n"
"v_mfma_f32_16x16x32_bf16 v[176:179], a[4:7], v[40:43], 0\n"
"buffer_store_dword v73, v3, s[16:19], s76 offen sc1\n"
"v_mfma_f32_16x16x32_bf16 v[176:179], a[16:19], v[44:47], v[176:179]\n"
"ds_read_b64_tr_b16 v[32:33], v14 offset:10272\n"
"s_add_u32 m0, s72, 14336\n"
"v_mfma_f32_16x16x32_bf16 v[180:183], a[8:11], v[40:43], 0\n"
"ds_read_b64_tr_b16 v[34:35], v14 offset:11552\n"
"buffer_load_dwordx4 v10, s[4:7], s65 offen lds\n"
"v_mfma_f32_16x16x32_bf16 v[180:183], a[20:23], v[44:47], v[180:183]\n"
"s_waitcnt lgkmcnt(12)\n"
"s_add_u32 m0, s72, 15360\n"
"v_mfma_f32_16x16x32_bf16 v[184:187], a[0:3], v[16:19], 0\n"
"ds_read_b64_tr_b16 v[36:37], v14 offset:15392\n"
"buffer_load_dwordx4 v11, s[4:7], s65 offen lds\n"
"v_mfma_f32_16x16x32_bf16 v[184:187], a[12:15], v[20:23], v[184:187]\n"
"ds_read_b64_tr_b16 v[38:39], v14 offset:16672\n"
"s_add_u32 m0, s72, 16384\n"
"v_mfma_f32_16x16x32_bf16 v[188:191], a[4:7], v[16:19], 0\n"
"buffer_load_dwordx4 v10, s[4:7], s66 offen lds\n"
"s_mov_b32 s63, 1886070304\n"
"v_mfma_f32_16x16x32_bf16 v[188:191], a[16:19], v[20:23], v[188:191]\n"
"ds_read_b64_tr_b16 v[40:41], v14 offset:10304\n"
"ds_read_b64_tr_b16 v[42:43], v14 offset:11584\n"
"v_mfma_f32_16x16x32_bf16 v[192:195], a[8:11], v[16:19], 0\n"
"s_add_u32 m0, s72, 17408\n"
"s_lshr_b32 s56, s60, 1\n"
"v_mfma_f32_16x16x32_bf16 v[192:195], a[20:23], v[20:23], v[192:195]\n"
"buffer_load_dwordx4 v11, s[4:7], s66 offen lds\n"
"s_waitcnt lgkmcnt(7)\n"
"v_mfma_f32_16x16x32_bf16 v[196:199], a[0:3], v[24:27], 0\n"
"ds_read_b64_tr_b16 v[44:45], v14 offset:15424\n"
"ds_read_b64_tr_b16 v[46:47], v14 offset:16704\n"
"v_mfma_f32_16x16x32_bf16 v[196:199], a[12:15], v[28:31], v[196:199]\n"
"ds_read_b64_tr_b16 v[16:17], v14 offset:10336\n"
"ds_read_b64_tr_b16 v[18:19], v14 offset:11616\n"
"v_mfma_f32_16x16x32_bf16 v[200:203], a[4:7], v[24:27], 0\n"
"ds_read_b64_tr_b16 v[20:21], v14 offset:15456\n"
"ds_read_b64_tr_b16 v[22:23], v14 offset:16736\n"
"v_mfma_f32_16x16x32_bf16 v[200:203], a[16:19], v[28:31], v[200:203]\n"
"s_mul_i32 s33, s85, 245760\n"
"s_add_u32 s68, s68, s33\n"
"v_mfma_f32_16x16x32_bf16 v[204:207], a[8:11], v[24:27], 0\n"
"s_add_u32 m0, s71, 40960\n"
"s_and_b32 s60, s60, 1\n"
"v_mfma_f32_16x16x32_bf16 v[204:207], a[20:23], v[28:31], v[204:207]\n"
"buffer_load_dwordx4 v5, s[8:11], s68 offen offset:0 lds\n"
"s_waitcnt lgkmcnt(8)\n"
"v_mfma_f32_16x16x32_bf16 v[208:211], a[0:3], v[32:35], 0\n"
"ds_read_b64_tr_b16 v[24:25], v14 offset:10368\n"
"ds_read_b64_tr_b16 v[26:27], v14 offset:11648\n"
"v_mfma_f32_16x16x32_bf16 v[208:211], a[12:15], v[36:39], v[208:211]\n"
"ds_read_b64_tr_b16 v[28:29], v14 offset:15488\n"
"ds_read_b64_tr_b16 v[30:31], v14 offset:16768\n"
"v_mfma_f32_16x16x32_bf16 v[212:215], a[4:7], v[32:35], 0\n"
"buffer_load_dwordx4 v6, s[8:11], s68 offen offset:1024 lds\n"
"s_lshl_b32 s60, s60, 1\n"
"v_mfma_f32_16x16x32_bf16 v[212:215], a[16:19], v[36:39], v[212:215]\n"
"s_add_u32 s60, s60, s61\n"
"s_and_b32 s62, s2, 7\n"
"v_mfma_f32_16x16x32_bf16 v[216:219], a[8:11], v[32:35], 0\n"
"s_lshl_b32 s62, s62, 2\n"
"s_add_u32 s55, s62, s60\n"
"v_mfma_f32_16x16x32_bf16 v[216:219], a[20:23], v[36:39], v[216:219]\n"
"s_waitcnt lgkmcnt(8)\n"
"s_lshl_b32 s62, s3, 3\n"
"v_mfma_f32_16x16x32_bf16 v[220:223], a[0:3], v[40:43], 0\n"
"s_lshr_b32 s63, s63, s62\n"
"s_and_b32 s78, s63, 63\n"
"v_mfma_f32_16x16x32_bf16 v[220:223], a[12:15], v[44:47], v[220:223]\n"
"s_bfe_u32 s58, s63, 0x20006\n"
"s_and_b32 s49, s49, 0xffff\n"
"v_mfma_f32_16x16x32_bf16 v[224:227], a[4:7], v[40:43], 0\n"
"buffer_load_dwordx4 v7, s[8:11], s68 offen offset:2048 lds\n"
"s_mov_b32 s50, 2621440\n"
"v_mfma_f32_16x16x32_bf16 v[224:227], a[16:19], v[44:47], v[224:227]\n"
"s_mov_b32 s51, 0x20000\n"
"s_mul_i32 s60, s55, 456\n"
"v_mfma_f32_16x16x32_bf16 v[228:231], a[8:11], v[40:43], 0\n"
"s_add_u32 s44, s60, s78\n"
"s_lshl_b32 s44, s44, 10\n"
"v_mfma_f32_16x16x32_bf16 v[228:231], a[20:23], v[44:47], v[228:231]\n"
"s_waitcnt vmcnt(11) lgkmcnt(0)\n"
"s_barrier\n"
"v_mfma_f32_16x16x32_bf16 v[232:235], a[0:3], v[16:19], 0\n"
"ds_read_b64_tr_b16 v[32:33], v14 offset:20480\n"
"buffer_load_dwordx4 v8, s[8:11], s68 offen offset:3072 lds\n"
"v_mfma_f32_16x16x32_bf16 v[232:235], a[12:15], v[20:23], v[232:235]\n"
"ds_read_b64_tr_b16 v[34:35], v14 offset:21760\n"
"s_mul_i32 s62, s3, 5\n"
"v_mfma_f32_16x16x32_bf16 v[236:239], a[4:7], v[16:19], 0\n"
"s_add_u32 s62, s62, 54\n"
"s_add_u32 s84, s60, s62\n"
"v_mfma_f32_16x16x32_bf16 v[236:239], a[16:19], v[20:23], v[236:239]\n"
"ds_read_b64_tr_b16 v[36:37], v14 offset:25600\n"
"s_add_u32 m0, s71, 45056\n"
"v_mfma_f32_16x16x32_bf16 v[240:243], a[8:11], v[16:19], 0\n"
"ds_read_b64_tr_b16 v[38:39], v14 offset:26880\n"
"s_lshl_b32 s84, s84, 10\n"
"v_mfma_f32_16x16x32_bf16 v[240:243], a[20:23], v[20:23], v[240:243]\n"
"buffer_load_dwordx4 v9, s[8:11], s68 offen offset:0 lds\n"
"v_mfma_f32_16x16x32_bf16 v[244:247], a[0:3], v[24:27], 0\n"
"ds_read_b64_tr_b16 v[40:41], v14 offset:20512\n"
"s_xor_b32 s60, s55, 1\n"
"v_mfma_f32_16x16x32_bf16 v[244:247], a[12:15], v[28:31], v[244:247]\n"
"ds_read_b64_tr_b16 v[42:43], v14 offset:21792\n"
"s_mul_i32 s60, s60, 456\n"
"v_mfma_f32_16x16x32_bf16 v[248:251], a[4:7], v[24:27], 0\n"
"s_cmp_eq_u32 s32, 7\n"
"s_cselect_b32 s85, -9, 1\n"
"v_mfma_f32_16x16x32_bf16 v[248:251], a[16:19], v[28:31], v[248:251]\n"
"ds_read_b64_tr_b16 v[44:45], v14 offset:25632\n"
"v_lshl_add_u32 v10, s85, 7, v10\n"
"v_mfma_f32_16x16x32_bf16 v[252:255], a[8:11], v[24:27], 0\n"
"ds_read_b64_tr_b16 v[46:47], v14 offset:26912\n"
"ds_read_b64_tr_b16 v[16:17], v14 offset:20544\n"
"v_mfma_f32_16x16x32_bf16 v[252:255], a[20:23], v[28:31], v[252:255]\n"
"ds_read_b64_tr_b16 v[18:19], v14 offset:21824\n"
"s_waitcnt lgkmcnt(6)\n"
"v_mfma_f32_16x16x32_bf16 v[136:139], a[24:27], v[32:35], v[136:139]\n"
"ds_read_b64_tr_b16 v[20:21], v14 offset:25664\n"
"v_lshl_add_u32 v11, s85, 7, v11\n"
"v_mfma_f32_16x16x32_bf16 v[136:139], a[36:39], v[36:39], v[136:139]\n"
"ds_read_b64_tr_b16 v[22:23], v14 offset:26944\n"
"s_add_u32 m0, s72, 0\n"
"v_mfma_f32_16x16x32_bf16 v[140:143], a[28:31], v[32:35], v[140:143]\n"
"buffer_load_dwordx4 v10, s[4:7], s64 offen lds\n"
"s_add_u32 m0, s72, 1024\n"
"v_mfma_f32_16x16x32_bf16 v[140:143], a[40:43], v[36:39], v[140:143]\n"
"ds_read_b64_tr_b16 v[24:25], v14 offset:20576\n"
"buffer_load_dwordx4 v11, s[4:7], s64 offen lds\n"
"v_mfma_f32_16x16x32_bf16 v[144:147], a[32:35], v[32:35], v[144:147]\n"
"ds_read_b64_tr_b16 v[26:27], v14 offset:21856\n"
"s_add_u32 m0, s72, 2048\n"
"v_mfma_f32_16x16x32_bf16 v[144:147], a[44:47], v[36:39], v[144:147]\n"
"s_waitcnt lgkmcnt(6)\n"
"s_add_u32 s45, s60, s78\n"
"v_mfma_f32_16x16x32_bf16 v[148:151], a[24:27], v[40:43], v[148:151]\n"
"ds_read_b64_tr_b16 v[28:29], v14 offset:25696\n"
"buffer_load_dwordx4 v10, s[4:7], s65 offen lds\n"
"v_mfma_f32_16x16x32_bf16 v[148:151], a[36:39], v[44:47], v[148:151]\n"
"ds_read_b64_tr_b16 v[30:31], v14 offset:26976\n"
"s_lshl_b32 s45, s45, 10\n"
"v_mfma_f32_16x16x32_bf16 v[152:155], a[28:31], v[40:43], v[152:155]\n"
"s_add_u32 m0, s72, 3072\n"
"s_mul_i32 s61, s56, 74\n"
"v_mfma_f32_16x16x32_bf16 v[152:155], a[40:43], v[44:47], v[152:155]\n"
"ds_read_b64_tr_b16 v[32:33], v14 offset:20608\n"
"s_add_u32 s62, s62, s61\n"
"v_mfma_f32_16x16x32_bf16 v[156:159], a[32:35], v[40:43], v[156:159]\n"
"ds_read_b64_tr_b16 v[34:35], v14 offset:21888\n"
"buffer_load_dwordx4 v11, s[4:7], s65 offen lds\n"
"v_mfma_f32_16x16x32_bf16 v[156:159], a[44:47], v[44:47], v[156:159]\n"
"s_waitcnt lgkmcnt(6)\n"
"s_mul_i32 s97, s62, 5120\n"
"v_mfma_f32_16x16x32_bf16 v[160:163], a[24:27], v[16:19], v[160:163]\n"
"ds_read_b64_tr_b16 v[36:37], v14 offset:25728\n"
"s_add_u32 m0, s72, 4096\n"
"v_mfma_f32_16x16x32_bf16 v[160:163], a[36:39], v[20:23], v[160:163]\n"
"ds_read_b64_tr_b16 v[38:39], v14 offset:27008\n"
"s_waitcnt vmcnt(9)\n"
"v_mfma_f32_16x16x32_bf16 v[164:167], a[28:31], v[16:19], v[164:167]\n"
"ds_read_b128 a[48:51], v12 offset:12288\n"
"buffer_load_dwordx4 v10, s[4:7], s66 offen lds\n"
"v_mfma_f32_16x16x32_bf16 v[164:167], a[40:43], v[20:23], v[164:167]\n"
"ds_read_b64_tr_b16 v[40:41], v14 offset:30720\n"
"ds_read_b128 a[52:55], v12 offset:14336\n"
"v_mfma_f32_16x16x32_bf16 v[168:171], a[32:35], v[16:19], v[168:171]\n"
"ds_read_b64_tr_b16 v[42:43], v14 offset:32000\n"
"ds_read_b128 a[56:59], v12 offset:16384\n"
"v_mfma_f32_16x16x32_bf16 v[168:171], a[44:47], v[20:23], v[168:171]\n"
"ds_read_b128 a[60:63], v13 offset:12288\n"
"s_waitcnt lgkmcnt(10)\n"
"v_mfma_f32_16x16x32_bf16 v[172:175], a[24:27], v[24:27], v[172:175]\n"
"ds_read_b64_tr_b16 v[44:45], v14 offset:35840\n"
"ds_read_b128 a[64:67], v13 offset:14336\n"
"v_mfma_f32_16x16x32_bf16 v[172:175], a[36:39], v[28:31], v[172:175]\n"
"ds_read_b64_tr_b16 v[46:47], v14 offset:37120\n"
"ds_read_b128 a[68:71], v13 offset:16384\n"
"v_mfma_f32_16x16x32_bf16 v[176:179], a[28:31], v[24:27], v[176:179]\n"
"s_add_u32 m0, s72, 5120\n"
"s_add_u32 s61, s61, s78\n"
"v_mfma_f32_16x16x32_bf16 v[176:179], a[40:43], v[28:31], v[176:179]\n"
"ds_read_b64_tr_b16 v[16:17], v14 offset:30752\n"
"buffer_load_dwordx4 v11, s[4:7], s66 offen lds\n"
"v_mfma_f32_16x16x32_bf16 v[180:183], a[32:35], v[24:27], v[180:183]\n"
"ds_read_b64_tr_b16 v[18:19], v14 offset:32032\n"
"s_mul_i32 s33, s85, 245760\n"
"v_mfma_f32_16x16x32_bf16 v[180:183], a[44:47], v[28:31], v[180:183]\n"
"s_waitcnt lgkmcnt(12)\n"
"s_add_u32 s68, s68, s33\n"
"v_mfma_f32_16x16x32_bf16 v[184:187], a[24:27], v[32:35], v[184:187]\n"
"ds_read_b64_tr_b16 v[20:21], v14 offset:35872\n"
"s_add_u32 m0, s71, 61440\n"
"v_mfma_f32_16x16x32_bf16 v[184:187], a[36:39], v[36:39], v[184:187]\n"
"ds_read_b64_tr_b16 v[22:23], v14 offset:37152\n"
"buffer_load_dwordx4 v5, s[8:11], s68 offen offset:0 lds\n"
"v_mfma_f32_16x16x32_bf16 v[188:191], a[28:31], v[32:35], v[188:191]\n"
"s_mul_i32 s43, s61, 5120\n"
"s_add_u32 s96, s43, 10240\n"
"v_mfma_f32_16x16x32_bf16 v[188:191], a[40:43], v[36:39], v[188:191]\n"
"ds_read_b64_tr_b16 v[24:25], v14 offset:30784\n"
"ds_read_b64_tr_b16 v[26:27], v14 offset:32064\n"
"v_mfma_f32_16x16x32_bf16 v[192:195], a[32:35], v[32:35], v[192:195]\n"
"buffer_load_dwordx4 v6, s[8:11], s68 offen offset:1024 lds\n"
"v_mfma_f32_16x16x32_bf16 v[192:195], a[44:47], v[36:39], v[192:195]\n"
"s_waitcnt lgkmcnt(7)\n"
"v_mfma_f32_16x16x32_bf16 v[196:199], a[24:27], v[40:43], v[196:199]\n"
"ds_read_b64_tr_b16 v[28:29], v14 offset:35904\n"
"ds_read_b64_tr_b16 v[30:31], v14 offset:37184\n"
"v_mfma_f32_16x16x32_bf16 v[196:199], a[36:39], v[44:47], v[196:199]\n"
"ds_read_b64_tr_b16 v[32:33], v14 offset:30816\n"
"ds_read_b64_tr_b16 v[34:35], v14 offset:32096\n"
"v_mfma_f32_16x16x32_bf16 v[200:203], a[28:31], v[40:43], v[200:203]\n"
"ds_read_b64_tr_b16 v[36:37], v14 offset:35936\n"
"ds_read_b64_tr_b16 v[38:39], v14 offset:37216\n"
"v_mfma_f32_16x16x32_bf16 v[200:203], a[40:43], v[44:47], v[200:203]\n"
"s_cmp_ge_u32 s58, 1\n"
"s_cselect_b32 s43, 1073741824, s43\n"
"v_mfma_f32_16x16x32_bf16 v[204:207], a[32:35], v[40:43], v[204:207]\n"
"buffer_load_dwordx4 v7, s[8:11], s68 offen offset:2048 lds\n"
"s_lshl_b32 s60, s55, 2\n"
"v_mfma_f32_16x16x32_bf16 v[204:207], a[44:47], v[44:47], v[204:207]\n"
"s_waitcnt lgkmcnt(8)\n"
"s_add_u32 s60, s60, s3\n"
"v_mfma_f32_16x16x32_bf16 v[208:211], a[24:27], v[16:19], v[208:211]\n"
"ds_read_b64_tr_b16 v[40:41], v14 offset:30848\n"
"ds_read_b64_tr_b16 v[42:43], v14 offset:32128\n"
"v_mfma_f32_16x16x32_bf16 v[208:211], a[36:39], v[20:23], v[208:211]\n"
"ds_read_b64_tr_b16 v[44:45], v14 offset:35968\n"
"ds_read_b64_tr_b16 v[46:47], v14 offset:37248\n"
"v_mfma_f32_16x16x32_bf16 v[212:215], a[28:31], v[16:19], v[212:215]\n"
"s_lshl_b32 s60, s60, 3\n"
"s_add_u32 s40, s60, s56\n"
"v_mfma_f32_16x16x32_bf16 v[212:215], a[40:43], v[20:23], v[212:215]\n"
"s_lshr_b32 s61, s55, 1\n"
"s_lshl_b32 s61, s61, 3\n"
"v_mfma_f32_16x16x32_bf16 v[216:219], a[32:35], v[16:19], v[216:219]\n"
"buffer_load_dwordx4 v8, s[8:11], s68 offen offset:3072 lds\n"
"s_mul_i32 s62, s61, 373\n"
"v_mfma_f32_16x16x32_bf16 v[216:219], a[44:47], v[20:23], v[216:219]\n"
"s_waitcnt lgkmcnt(8)\n"
"s_lshr_b32 s62, s62, 12\n"
"v_mfma_f32_16x16x32_bf16 v[220:223], a[24:27], v[24:27], v[220:223]\n"
"s_mul_i32 s62, s62, 54\n"
"s_add_u32 s63, s61, 7\n"
"v_mfma_f32_16x16x32_bf16 v[220:223], a[36:39], v[28:31], v[220:223]\n"
"s_mul_i32 s63, s63, 373\n"
"s_lshr_b32 s63, s63, 12\n"
"v_mfma_f32_16x16x32_bf16 v[224:227], a[28:31], v[24:27], v[224:227]\n"
"s_add_u32 m0, s71, 65536\n"
"s_mul_i32 s63, s63, 54\n"
"v_mfma_f32_16x16x32_bf16 v[224:227], a[40:43], v[28:31], v[224:227]\n"
"s_sub_u32 s46, s63, s62\n"
"s_add_u32 s41, s62, s78\n"
"v_mfma_f32_16x16x32_bf16 v[228:231], a[32:35], v[24:27], v[228:231]\n"
"buffer_load_dwordx4 v9, s[8:11], s68 offen offset:0 lds\n"
"s_add_u32 s41, s41, 64\n"
"v_mfma_f32_16x16x32_bf16 v[228:231], a[44:47], v[28:31], v[228:231]\n"
"s_waitcnt vmcnt(11) lgkmcnt(0)\n"
"s_barrier\n"
"v_mfma_f32_16x16x32_bf16 v[232:235], a[24:27], v[32:35], v[232:235]\n"
"ds_read_b64_tr_b16 v[16:17], v15 offset:0\n"
"s_lshl_b32 s61, s55, 2\n"
"v_mfma_f32_16x16x32_bf16 v[232:235], a[36:39], v[36:39], v[232:235]\n"
"ds_read_b64_tr_b16 v[18:19], v15 offset:1280\n"
"s_mul_i32 s62, s61, 1366\n"
"v_mfma_f32_16x16x32_bf16 v[236:239], a[28:31], v[32:35], v[236:239]\n"
"s_cmp_eq_u32 s32, 6\n"
"s_cselect_b32 s85, -9, 1\n"
"v_mfma_f32_16x16x32_bf16 v[236:239], a[40:43], v[36:39], v[236:239]\n"
"ds_read_b64_tr_b16 v[20:21], v15 offset:5120\n"
"v_lshl_add_u32 v10, s85, 7, v10\n"
"v_mfma_f32_16x16x32_bf16 v[240:243], a[32:35], v[32:35], v[240:243]\n"
"ds_read_b64_tr_b16 v[22:23], v15 offset:6400\n"
"v_lshl_add_u32 v11, s85, 7, v11\n"
"v_mfma_f32_16x16x32_bf16 v[240:243], a[44:47], v[36:39], v[240:243]\n"
"s_add_u32 m0, s72, 6144\n"
"v_mfma_f32_16x16x32_bf16 v[244:247], a[24:27], v[40:43], v[244:247]\n"
"ds_read_b64_tr_b16 v[24:25], v15 offset:32\n"
"buffer_load_dwordx4 v10, s[4:7], s64 offen lds\n"
"v_mfma_f32_16x16x32_bf16 v[244:247], a[36:39], v[44:47], v[244:247]\n"
"ds_read_b64_tr_b16 v[26:27], v15 offset:1312\n"
"s_add_u32 m0, s72, 7168\n"
"v_mfma_f32_16x16x32_bf16 v[248:251], a[28:31], v[40:43], v[248:251]\n"
"buffer_load_dwordx4 v11, s[4:7], s64 offen lds\n"
"s_lshr_b32 s62, s62, 12\n"
"v_mfma_f32_16x16x32_bf16 v[248:251], a[40:43], v[44:47], v[248:251]\n"
"ds_read_b64_tr_b16 v[28:29], v15 offset:5152\n"
"s_add_u32 m0, s72, 8192\n"
"v_mfma_f32_16x16x32_bf16 v[252:255], a[32:35], v[40:43], v[252:255]\n"
"ds_read_b64_tr_b16 v[30:31], v15 offset:6432\n"
"ds_read_b64_tr_b16 v[32:33], v15 offset:64\n"
"v_mfma_f32_16x16x32_bf16 v[252:255], a[44:47], v[44:47], v[252:255]\n"
"ds_read_b64_tr_b16 v[34:35], v15 offset:1344\n"
"s_waitcnt lgkmcnt(6)\n"
"v_mfma_f32_16x16x32_bf16 v[136:139], a[48:51], v[16:19], v[136:139]\n"
"ds_read_b64_tr_b16 v[36:37], v15 offset:5184\n"
"buffer_load_dwordx4 v10, s[4:7], s65 offen lds\n"
"v_mfma_f32_16x16x32_bf16 v[136:139], a[60:63], v[20:23], v[136:139]\n"
"ds_read_b64_tr_b16 v[38:39], v15 offset:6464\n"
"s_add_u32 m0, s72, 9216\n"
"v_mfma_f32_16x16x32_bf16 v[140:143], a[52:55], v[16:19], v[140:143]\n"
"s_mul_i32 s63, s3, 88\n"
"s_add_u32 s62, s62, s63\n"
"v_mfma_f32_16x16x32_bf16 v[140:143], a[64:67], v[20:23], v[140:143]\n"
"ds_read_b64_tr_b16 v[40:41], v15 offset:96\n"
"buffer_load_dwordx4 v11, s[4:7], s65 offen lds\n"
"v_mfma_f32_16x16x32_bf16 v[144:147], a[56:59], v[16:19], v[144:147]\n"
"ds_read_b64_tr_b16 v[42:43], v15 offset:1376\n"
"s_add_u32 s47, s62, 768\n"
"v_mfma_f32_16x16x32_bf16 v[144:147], a[68:71], v[20:23], v[144:147]\n"
"s_waitcnt lgkmcnt(6)\n"
"s_add_u32 m0, s72, 10240\n"
"v_mfma_f32_16x16x32_bf16 v[148:151], a[48:51], v[24:27], v[148:151]\n"
"ds_read_b64_tr_b16 v[44:45], v15 offset:5216\n"
"s_add_u32 s61, s61, s3\n"
"v_mfma_f32_16x16x32_bf16 v[148:151], a[60:63], v[28:31], v[148:151]\n"
"ds_read_b64_tr_b16 v[46:47], v15 offset:6496\n"
"buffer_load_dwordx4 v10, s[4:7], s66 offen lds\n"
"v_mfma_f32_16x16x32_bf16 v[152:155], a[52:55], v[24:27], v[152:155]\n"
"s_mul_i32 s60, s61, 20480\n"
"s_mul_i32 s61, s56, 160\n"
"v_mfma_f32_16x16x32_bf16 v[152:155], a[64:67], v[28:31], v[152:155]\n"
"ds_read_b64_tr_b16 v[16:17], v15 offset:128\n"
"s_add_u32 m0, s72, 11264\n"
"v_mfma_f32_16x16x32_bf16 v[156:159], a[56:59], v[24:27], v[156:159]\n"
"ds_read_b64_tr_b16 v[18:19], v15 offset:1408\n"
"s_add_u32 s79, s60, s61\n"
"v_mfma_f32_16x16x32_bf16 v[156:159], a[68:71], v[28:31], v[156:159]\n"
"buffer_load_dwordx4 v11, s[4:7], s66 offen lds\n"
"s_waitcnt lgkmcnt(6)\n"
"v_mfma_f32_16x16x32_bf16 v[160:163], a[48:51], v[32:35], v[160:163]\n"
"ds_read_b64_tr_b16 v[20:21], v15 offset:5248\n"
"s_mul_i32 s33, s85, 245760\n"
"v_mfma_f32_16x16x32_bf16 v[160:163], a[60:63], v[36:39], v[160:163]\n"
"ds_read_b64_tr_b16 v[22:23], v15 offset:6528\n"
"s_waitcnt vmcnt(11)\n"
"v_mfma_f32_16x16x32_bf16 v[164:167], a[52:55], v[32:35], v[164:167]\n"
"ds_read_b128 a[72:75], v12 offset:0\n"
"s_add_u32 s68, s68, s33\n"
"v_mfma_f32_16x16x32_bf16 v[164:167], a[64:67], v[36:39], v[164:167]\n"
"ds_read_b64_tr_b16 v[24:25], v15 offset:10240\n"
"ds_read_b128 a[76:79], v12 offset:2048\n"
"v_mfma_f32_16x16x32_bf16 v[168:171], a[56:59], v[32:35], v[168:171]\n"
"ds_read_b64_tr_b16 v[26:27], v15 offset:11520\n"
"ds_read_b128 a[80:83], v12 offset:4096\n"
"v_mfma_f32_16x16x32_bf16 v[168:171], a[68:71], v[36:39], v[168:171]\n"
"ds_read_b128 a[84:87], v13 offset:0\n"
"s_waitcnt lgkmcnt(10)\n"
"v_mfma_f32_16x16x32_bf16 v[172:175], a[48:51], v[40:43], v[172:175]\n"
"ds_read_b64_tr_b16 v[28:29], v15 offset:15360\n"
"ds_read_b128 a[88:91], v13 offset:2048\n"
"v_mfma_f32_16x16x32_bf16 v[172:175], a[60:63], v[44:47], v[172:175]\n"
"ds_read_b64_tr_b16 v[30:31], v15 offset:16640\n"
"ds_read_b128 a[92:95], v13 offset:4096\n"
"v_mfma_f32_16x16x32_bf16 v[176:179], a[52:55], v[40:43], v[176:179]\n"
"s_add_u32 m0, s71, 0\n"
"s_sub_u32 s55, s96, 5120\n"
"v_mfma_f32_16x16x32_bf16 v[176:179], a[64:67], v[44:47], v[176:179]\n"
"ds_read_b64_tr_b16 v[32:33], v15 offset:10272\n"
"buffer_load_dwordx4 v5, s[8:11], s68 offen offset:0 lds\n"
"v_mfma_f32_16x16x32_bf16 v[180:183], a[56:59], v[40:43], v[180:183]\n"
"ds_read_b64_tr_b16 v[34:35], v15 offset:11552\n"
"v_mfma_f32_16x16x32_bf16 v[180:183], a[68:71], v[44:47], v[180:183]\n"
"s_waitcnt lgkmcnt(12)\n"
"v_mfma_f32_16x16x32_bf16 v[184:187], a[48:51], v[16:19], v[184:187]\n"
"ds_read_b64_tr_b16 v[36:37], v15 offset:15392\n"
"buffer_load_dwordx4 v6, s[8:11], s68 offen offset:1024 lds\n"
"v_mfma_f32_16x16x32_bf16 v[184:187], a[60:63], v[20:23], v[184:187]\n"
"ds_read_b64_tr_b16 v[38:39], v15 offset:16672\n"
"v_mfma_f32_16x16x32_bf16 v[188:191], a[52:55], v[16:19], v[188:191]\n"
"s_cmp_ge_u32 s58, 2\n"
"s_cselect_b32 s55, 1073741824, s55\n"
"v_mfma_f32_16x16x32_bf16 v[188:191], a[64:67], v[20:23], v[188:191]\n"
"ds_read_b64_tr_b16 v[40:41], v15 offset:10304\n"
"ds_read_b64_tr_b16 v[42:43], v15 offset:11584\n"
"v_mfma_f32_16x16x32_bf16 v[192:195], a[56:59], v[16:19], v[192:195]\n"
"s_lshr_b32 s62, s67, 4\n"
"s_mul_i32 s61, s3, 5\n"
"v_mfma_f32_16x16x32_bf16 v[192:195], a[68:71], v[20:23], v[192:195]\n"
"s_waitcnt lgkmcnt(7)\n"
"s_add_u32 s61, s61, 48\n"
"v_mfma_f32_16x16x32_bf16 v[196:199], a[48:51], v[24:27], v[196:199]\n"
"ds_read_b64_tr_b16 v[44:45], v15 offset:15424\n"
"ds_read_b64_tr_b16 v[46:47], v15 offset:16704\n"
"v_mfma_f32_16x16x32_bf16 v[196:199], a[60:63], v[28:31], v[196:199]\n"
"ds_read_b64_tr_b16 v[16:17], v15 offset:10336\n"
"ds_read_b64_tr_b16 v[18:19], v15 offset:11616\n"
"v_mfma_f32_16x16x32_bf16 v[200:203], a[52:55], v[24:27], v[200:203]\n"
"ds_read_b64_tr_b16 v[20:21], v15 offset:15456\n"
"ds_read_b64_tr_b16 v[22:23], v15 offset:16736\n"
"v_mfma_f32_16x16x32_bf16 v[200:203], a[64:67], v[28:31], v[200:203]\n"
"buffer_load_dwordx4 v7, s[8:11], s68 offen offset:2048 lds\n"
"v_min_u32 v48, 10, v1\n"
"v_mfma_f32_16x16x32_bf16 v[204:207], a[56:59], v[24:27], v[204:207]\n"
"v_cmp_gt_u32 vcc, 6, v48\n"
"v_add_u32 v49, s61, v48\n"
"v_mfma_f32_16x16x32_bf16 v[204:207], a[68:71], v[28:31], v[204:207]\n"
"v_add_u32 v48, s78, v48\n"
"s_waitcnt lgkmcnt(8)\n"
"v_mfma_f32_16x16x32_bf16 v[208:211], a[48:51], v[32:35], v[208:211]\n"
"ds_read_b64_tr_b16 v[24:25], v15 offset:10368\n"
"ds_read_b64_tr_b16 v[26:27], v15 offset:11648\n"
"v_mfma_f32_16x16x32_bf16 v[208:211], a[60:63], v[36:39], v[208:211]\n"
"ds_read_b64_tr_b16 v[28:29], v15 offset:15488\n"
"ds_read_b64_tr_b16 v[30:31], v15 offset:16768\n"
"v_mfma_f32_16x16x32_bf16 v[212:215], a[52:55], v[32:35], v[212:215]\n"
"buffer_load_dwordx4 v8, s[8:11], s68 offen offset:3072 lds\n"
"s_lshr_b32 s60, s56, 2\n"
"v_mfma_f32_16x16x32_bf16 v[212:215], a[64:67], v[36:39], v[212:215]\n"
"v_cndmask_b32 v48, v49, v48, vcc\n"
"v_lshl_add_u32 v48, v48, 1, s60\n"
"v_mfma_f32_16x16x32_bf16 v[216:219], a[56:59], v[32:35], v[216:219]\n"
"v_and_b32 v49, 15, v1\n"
"s_add_u32 m0, s71, 4096\n"
"v_mfma_f32_16x16x32_bf16 v[216:219], a[68:71], v[36:39], v[216:219]\n"
"v_min_u32 v49, 2, v49\n"
"s_waitcnt lgkmcnt(8)\n"
"v_mfma_f32_16x16x32_bf16 v[220:223], a[48:51], v[40:43], v[220:223]\n"
"buffer_load_dwordx4 v9, s[8:11], s68 offen offset:0 lds\n"
"v_add_u32 v49, s62, v49\n"
"v_mfma_f32_16x16x32_bf16 v[220:223], a[60:63], v[44:47], v[220:223]\n"
"v_cmp_gt_u32 vcc, 32, v1\n"
"v_min_u32 v49, 127, v49\n"
"v_mfma_f32_16x16x32_bf16 v[224:227], a[52:55], v[40:43], v[224:227]\n"
"s_cmp_eq_u32 s32, 5\n"
"s_cselect_b32 s85, -9, 1\n"
"v_mfma_f32_16x16x32_bf16 v[224:227], a[64:67], v[44:47], v[224:227]\n"
"v_lshl_add_u32 v10, s85, 7, v10\n"
"v_lshl_add_u32 v11, s85, 7, v11\n"
"v_mfma_f32_16x16x32_bf16 v[228:231], a[56:59], v[40:43], v[228:231]\n"
"s_add_u32 m0, s72, 12288\n"
"s_nop 0\n"
"v_mfma_f32_16x16x32_bf16 v[228:231], a[68:71], v[44:47], v[228:231]\n"
"s_waitcnt vmcnt(11) lgkmcnt(0)\n"
"s_barrier\n"
"v_mfma_f32_16x16x32_bf16 v[232:235], a[48:51], v[16:19], v[232:235]\n"
"ds_read_b64_tr_b16 v[32:33], v15 offset:20480\n"
"buffer_load_dwordx4 v10, s[4:7], s64 offen lds\n"
"v_mfma_f32_16x16x32_bf16 v[232:235], a[60:63], v[20:23], v[232:235]\n"
"ds_read_b64_tr_b16 v[34:35], v15 offset:21760\n"
"v_cndmask_b32 v48, v49, v48, vcc\n"
"v_mfma_f32_16x16x32_bf16 v[236:239], a[52:55], v[16:19], v[236:239]\n"
"v_lshlrev_b32 v48, 7, v48\n"
"v_add_u32 v48, 212992, v48\n"
"v_mfma_f32_16x16x32_bf16 v[236:239], a[64:67], v[20:23], v[236:239]\n"
"ds_read_b64_tr_b16 v[36:37], v15 offset:25600\n"
"v_bfe_u32 v49, v1, 3, 1\n"
"v_mfma_f32_16x16x32_bf16 v[240:243], a[56:59], v[16:19], v[240:243]\n"
"ds_read_b64_tr_b16 v[38:39], v15 offset:26880\n"
"v_mul_u32_u24 v49, 10, v49\n"
"v_mfma_f32_16x16x32_bf16 v[240:243], a[68:71], v[20:23], v[240:243]\n"
"v_bfe_u32 v50, v1, 2, 1\n"
"v_mfma_f32_16x16x32_bf16 v[244:247], a[48:51], v[24:27], v[244:247]\n"
"ds_read_b64_tr_b16 v[40:41], v15 offset:20512\n"
"v_add3_u32 v49, v49, v50, 10\n"
"v_mfma_f32_16x16x32_bf16 v[244:247], a[60:63], v[28:31], v[244:247]\n"
"ds_read_b64_tr_b16 v[42:43], v15 offset:21792\n"
"v_lshl_add_u32 v49, v49, 7, s59\n"
"v_mfma_f32_16x16x32_bf16 v[248:251], a[52:55], v[24:27], v[248:251]\n"
"buffer_store_dword v74, v3, s[16:19], s42 offen sc1\n"
"v_mfma_f32_16x16x32_bf16 v[248:251], a[64:67], v[28:31], v[248:251]\n"
"ds_read_b64_tr_b16 v[44:45], v15 offset:25632\n"
"s_add_u32 m0, s72, 13312\n"
"v_mfma_f32_16x16x32_bf16 v[252:255], a[56:59], v[24:27], v[252:255]\n"
"ds_read_b64_tr_b16 v[46:47], v15 offset:26912\n"
"ds_read_b64_tr_b16 v[16:17], v15 offset:20544\n"
"v_mfma_f32_16x16x32_bf16 v[252:255], a[68:71], v[28:31], v[252:255]\n"
"ds_read_b64_tr_b16 v[18:19], v15 offset:21824\n"
"s_waitcnt lgkmcnt(6)\n"
"v_mfma_f32_16x16x32_bf16 v[136:139], a[72:75], v[32:35], v[136:139]\n"
"ds_read_b64_tr_b16 v[20:21], v15 offset:25664\n"
"buffer_load_dwordx4 v11, s[4:7], s64 offen lds\n"
"v_mfma_f32_16x16x32_bf16 v[136:139], a[84:87], v[36:39], v[136:139]\n"
"ds_read_b64_tr_b16 v[22:23], v15 offset:26944\n"
"s_add_u32 m0, s72, 14336\n"
"v_mfma_f32_16x16x32_bf16 v[140:143], a[76:79], v[32:35], v[140:143]\n"
"buffer_load_dwordx4 v10, s[4:7], s65 offen lds\n"
"s_add_u32 m0, s72, 15360\n"
"v_mfma_f32_16x16x32_bf16 v[140:143], a[88:91], v[36:39], v[140:143]\n"
"ds_read_b64_tr_b16 v[24:25], v15 offset:20576\n"
"v_and_b32 v133, 15, v1\n"
"v_mfma_f32_16x16x32_bf16 v[144:147], a[80:83], v[32:35], v[144:147]\n"
"ds_read_b64_tr_b16 v[26:27], v15 offset:21856\n"
"buffer_load_dwordx4 v11, s[4:7], s65 offen lds\n"
"v_mfma_f32_16x16x32_bf16 v[144:147], a[92:95], v[36:39], v[144:147]\n"
"v_cndmask_b32 v48, v49, v48, s[92:93]\n"
"s_waitcnt lgkmcnt(6)\n"
"v_mfma_f32_16x16x32_bf16 v[148:151], a[72:75], v[40:43], v[148:151]\n"
"ds_read_b64_tr_b16 v[28:29], v15 offset:25696\n"
"s_add_u32 m0, s72, 16384\n"
"v_mfma_f32_16x16x32_bf16 v[148:151], a[84:87], v[44:47], v[148:151]\n"
"ds_read_b64_tr_b16 v[30:31], v15 offset:26976\n"
"v_cmp_lt_u32 vcc, 19, v1\n"
"v_mfma_f32_16x16x32_bf16 v[152:155], a[76:79], v[40:43], v[152:155]\n"
"buffer_load_dwordx4 v10, s[4:7], s66 offen lds\n"
"s_add_u32 s63, s70, 2560\n"
"v_mfma_f32_16x16x32_bf16 v[152:155], a[88:91], v[44:47], v[152:155]\n"
"ds_read_b64_tr_b16 v[32:33], v15 offset:20608\n"
"v_add_u32 v60, s63, v2\n"
"v_mfma_f32_16x16x32_bf16 v[156:159], a[80:83], v[40:43], v[156:159]\n"
"ds_read_b64_tr_b16 v[34:35], v15 offset:21888\n"
"s_add_u32 m0, s72, 17408\n"
"v_mfma_f32_16x16x32_bf16 v[156:159], a[92:95], v[44:47], v[156:159]\n"
"v_add_u32 v61, 2240, v60\n"
"s_waitcnt lgkmcnt(6)\n"
"v_mfma_f32_16x16x32_bf16 v[160:163], a[72:75], v[16:19], v[160:163]\n"
"ds_read_b64_tr_b16 v[36:37], v15 offset:25728\n"
"buffer_load_dwordx4 v11, s[4:7], s66 offen lds\n"
"v_mfma_f32_16x16x32_bf16 v[160:163], a[84:87], v[20:23], v[160:163]\n"
"ds_read_b64_tr_b16 v[38:39], v15 offset:27008\n"
"s_waitcnt vmcnt(12)\n"
"v_mfma_f32_16x16x32_bf16 v[164:167], a[76:79], v[16:19], v[164:167]\n"
"ds_read_b128 a[96:99], v12 offset:6144\n"
"s_mul_i32 s33, s85, 245760\n"
"v_mfma_f32_16x16x32_bf16 v[164:167], a[88:91], v[20:23], v[164:167]\n"
"ds_read_b64_tr_b16 v[40:41], v15 offset:30720\n"
"ds_read_b128 a[100:103], v12 offset:8192\n"
"v_mfma_f32_16x16x32_bf16 v[168:171], a[80:83], v[16:19], v[168:171]\n"
"ds_read_b64_tr_b16 v[42:43], v15 offset:32000\n"
"ds_read_b128 a[104:107], v12 offset:10240\n"
"v_mfma_f32_16x16x32_bf16 v[168:171], a[92:95], v[20:23], v[168:171]\n"
"ds_read_b128 a[108:111], v13 offset:6144\n"
"s_waitcnt lgkmcnt(10)\n"
"v_mfma_f32_16x16x32_bf16 v[172:175], a[72:75], v[24:27], v[172:175]\n"
"ds_read_b64_tr_b16 v[44:45], v15 offset:35840\n"
"ds_read_b128 a[112:115], v13 offset:8192\n"
"v_mfma_f32_16x16x32_bf16 v[172:175], a[84:87], v[28:31], v[172:175]\n"
"ds_read_b64_tr_b16 v[46:47], v15 offset:37120\n"
"ds_read_b128 a[116:119], v13 offset:10240\n"
"v_mfma_f32_16x16x32_bf16 v[176:179], a[76:79], v[24:27], v[176:179]\n"
"s_add_u32 s68, s68, s33\n"
"s_add_u32 m0, s71, 20480\n"
"v_mfma_f32_16x16x32_bf16 v[176:179], a[88:91], v[28:31], v[176:179]\n"
"ds_read_b64_tr_b16 v[16:17], v15 offset:30752\n"
"buffer_load_dwordx4 v5, s[8:11], s68 offen offset:0 lds\n"
"v_mfma_f32_16x16x32_bf16 v[180:183], a[80:83], v[24:27], v[180:183]\n"
"ds_read_b64_tr_b16 v[18:19], v15 offset:32032\n"
"v_cndmask_b32 v60, v60, v61, vcc\n"
"v_mfma_f32_16x16x32_bf16 v[180:183], a[92:95], v[28:31], v[180:183]\n"
"s_waitcnt lgkmcnt(12)\n"
"s_lshl_b32 s35, s67, 3\n"
"v_mfma_f32_16x16x32_bf16 v[184:187], a[72:75], v[32:35], v[184:187]\n"
"ds_read_b64_tr_b16 v[20:21], v15 offset:35872\n"
"buffer_load_dwordx4 v6, s[8:11], s68 offen offset:1024 lds\n"
"v_mfma_f32_16x16x32_bf16 v[184:187], a[84:87], v[36:39], v[184:187]\n"
"ds_read_b64_tr_b16 v[22:23], v15 offset:37152\n"
"s_sub_u32 s35, s35, 640\n"
"v_mfma_f32_16x16x32_bf16 v[188:191], a[76:79], v[32:35], v[188:191]\n"
"v_add_u32 v61, s35, v2\n"
"v_lshrrev_b32 v62, 14, v61\n"
"v_mfma_f32_16x16x32_bf16 v[188:191], a[88:91], v[36:39], v[188:191]\n"
"ds_read_b64_tr_b16 v[24:25], v15 offset:30784\n"
"ds_read_b64_tr_b16 v[26:27], v15 offset:32064\n"
"v_mfma_f32_16x16x32_bf16 v[192:195], a[80:83], v[32:35], v[192:195]\n"
"v_cmp_lt_u32 vcc, 39, v1\n"
"v_lshl_add_u32 v61, v62, 30, v61\n"
"v_mfma_f32_16x16x32_bf16 v[192:195], a[92:95], v[36:39], v[192:195]\n"
"v_add_u32 v61, 294912, v61\n"
"s_waitcnt lgkmcnt(7)\n"
"v_mfma_f32_16x16x32_bf16 v[196:199], a[72:75], v[40:43], v[196:199]\n"
"ds_read_b64_tr_b16 v[28:29], v15 offset:35904\n"
"ds_read_b64_tr_b16 v[30:31], v15 offset:37184\n"
"v_mfma_f32_16x16x32_bf16 v[196:199], a[84:87], v[44:47], v[196:199]\n"
"ds_read_b64_tr_b16 v[32:33], v15 offset:30816\n"
"ds_read_b64_tr_b16 v[34:35], v15 offset:32096\n"
"v_mfma_f32_16x16x32_bf16 v[200:203], a[76:79], v[40:43], v[200:203]\n"
"ds_read_b64_tr_b16 v[36:37], v15 offset:35936\n"
"ds_read_b64_tr_b16 v[38:39], v15 offset:37216\n"
"v_mfma_f32_16x16x32_bf16 v[200:203], a[88:91], v[44:47], v[200:203]\n"
"buffer_load_dwordx4 v7, s[8:11], s68 offen offset:2048 lds\n"
"v_cndmask_b32 v60, v60, v61, vcc\n"
"v_mfma_f32_16x16x32_bf16 v[204:207], a[80:83], v[40:43], v[204:207]\n"
"s_lshl_b32 s12, s3, 10\n"
"s_add_u32 s12, s12, 155648\n"
"v_mfma_f32_16x16x32_bf16 v[204:207], a[92:95], v[44:47], v[204:207]\n"
"v_lshrrev_b32 v132, 4, v1\n"
"s_waitcnt lgkmcnt(8)\n"
"v_mfma_f32_16x16x32_bf16 v[208:211], a[72:75], v[16:19], v[208:211]\n"
"ds_read_b64_tr_b16 v[40:41], v15 offset:30848\n"
"ds_read_b64_tr_b16 v[42:43], v15 offset:32128\n"
"v_mfma_f32_16x16x32_bf16 v[208:211], a[84:87], v[20:23], v[208:211]\n"
"ds_read_b64_tr_b16 v[44:45], v15 offset:35968\n"
"ds_read_b64_tr_b16 v[46:47], v15 offset:37248\n"
"v_mfma_f32_16x16x32_bf16 v[212:215], a[76:79], v[16:19], v[212:215]\n"
"buffer_load_dwordx4 v8, s[8:11], s68 offen offset:3072 lds\n"
"v_lshl_add_u32 v132, v132, 5, s12\n"
"v_mfma_f32_16x16x32_bf16 v[212:215], a[88:91], v[20:23], v[212:215]\n"
"v_lshl_add_u32 v134, v133, 2, s12\n"
"v_mfma_f32_16x16x32_bf16 v[216:219], a[80:83], v[16:19], v[216:219]\n"
"s_add_u32 m0, s71, 24576\n"
"s_mul_i32 s60, s3, 8320\n"
"v_mfma_f32_16x16x32_bf16 v[216:219], a[92:95], v[20:23], v[216:219]\n"
"s_waitcnt lgkmcnt(8)\n"
"s_add_u32 s60, s60, 81920\n"
"v_mfma_f32_16x16x32_bf16 v[220:223], a[72:75], v[24:27], v[220:223]\n"
"buffer_load_dwordx4 v9, s[8:11], s68 offen offset:0 lds\n"
"v_and_b32 v135, 48, v1\n"
"v_mfma_f32_16x16x32_bf16 v[220:223], a[84:87], v[28:31], v[220:223]\n"
"v_mul_u32_u24 v68, 416, v133\n"
"v_mov_b32 v55, v68\n"
"v_mfma_f32_16x16x32_bf16 v[224:227], a[76:79], v[24:27], v[224:227]\n"
"s_cmp_eq_u32 s32, 4\n"
"s_cselect_b32 s85, -9, 1\n"
"v_mfma_f32_16x16x32_bf16 v[224:227], a[88:91], v[28:31], v[224:227]\n"
"v_lshl_add_u32 v10, s85, 7, v10\n"
"v_lshl_add_u32 v11, s85, 7, v11\n"
"v_mfma_f32_16x16x32_bf16 v[228:231], a[80:83], v[24:27], v[228:231]\n"
"v_add3_u32 v68, v68, v135, s60\n"
"s_add_u32 m0, s72, 0\n"
"v_mfma_f32_16x16x32_bf16 v[228:231], a[92:95], v[28:31], v[228:231]\n"
"s_waitcnt vmcnt(12) lgkmcnt(0)\n"
"s_barrier\n"
"v_mfma_f32_16x16x32_bf16 v[232:235], a[72:75], v[32:35], v[232:235]\n"
"ds_read_b64_tr_b16 v[16:17], v14 offset:0\n"
"buffer_load_dwordx4 v10, s[4:7], s64 offen lds\n"
"v_mfma_f32_16x16x32_bf16 v[232:235], a[84:87], v[36:39], v[232:235]\n"
"ds_read_b64_tr_b16 v[18:19], v14 offset:1280\n"
"v_and_b32 v53, 3, v1\n"
"v_mfma_f32_16x16x32_bf16 v[236:239], a[76:79], v[32:35], v[236:239]\n"
"v_mul_u32_u24 v69, 416, v53\n"
"s_add_u32 m0, s72, 1024\n"
"v_mfma_f32_16x16x32_bf16 v[236:239], a[88:91], v[36:39], v[236:239]\n"
"ds_read_b64_tr_b16 v[20:21], v14 offset:5120\n"
"v_add3_u32 v69, v69, v135, s60\n"
"v_mfma_f32_16x16x32_bf16 v[240:243], a[80:83], v[32:35], v[240:243]\n"
"ds_read_b64_tr_b16 v[22:23], v14 offset:6400\n"
"buffer_load_dwordx4 v11, s[4:7], s64 offen lds\n"
"v_mfma_f32_16x16x32_bf16 v[240:243], a[92:95], v[36:39], v[240:243]\n"
"v_cmp_gt_u32 vcc, 4, v133\n"
"v_mfma_f32_16x16x32_bf16 v[244:247], a[72:75], v[40:43], v[244:247]\n"
"ds_read_b64_tr_b16 v[24:25], v14 offset:32\n"
"s_add_u32 m0, s72, 2048\n"
"v_mfma_f32_16x16x32_bf16 v[244:247], a[84:87], v[44:47], v[244:247]\n"
"ds_read_b64_tr_b16 v[26:27], v14 offset:1312\n"
"v_add_u32 v70, 39936, v69\n"
"v_mfma_f32_16x16x32_bf16 v[248:251], a[76:79], v[40:43], v[248:251]\n"
"buffer_load_dwordx4 v10, s[4:7], s65 offen lds\n"
"v_add_u32 v53, 148480, v135\n"
"v_mfma_f32_16x16x32_bf16 v[248:251], a[88:91], v[44:47], v[248:251]\n"
"ds_read_b64_tr_b16 v[28:29], v14 offset:5152\n"
"s_add_u32 m0, s72, 3072\n"
"v_mfma_f32_16x16x32_bf16 v[252:255], a[80:83], v[40:43], v[252:255]\n"
"ds_read_b64_tr_b16 v[30:31], v14 offset:6432\n"
"ds_read_b64_tr_b16 v[32:33], v14 offset:64\n"
"v_mfma_f32_16x16x32_bf16 v[252:255], a[92:95], v[44:47], v[252:255]\n"
"ds_read_b64_tr_b16 v[34:35], v14 offset:1344\n"
"s_waitcnt lgkmcnt(6)\n"
"v_mfma_f32_16x16x32_bf16 v[136:139], a[96:99], v[16:19], v[136:139]\n"
"ds_read_b64_tr_b16 v[36:37], v14 offset:5184\n"
"buffer_load_dwordx4 v11, s[4:7], s65 offen lds\n"
"v_mfma_f32_16x16x32_bf16 v[136:139], a[108:111], v[20:23], v[136:139]\n"
"ds_read_b64_tr_b16 v[38:39], v14 offset:6464\n"
"s_add_u32 m0, s72, 4096\n"
"v_mfma_f32_16x16x32_bf16 v[140:143], a[100:103], v[16:19], v[140:143]\n"
"v_cndmask_b32 v70, v53, v70, vcc\n"
"v_mov_b32 v53, 1073741824\n"
"v_mfma_f32_16x16x32_bf16 v[140:143], a[112:115], v[20:23], v[140:143]\n"
"ds_read_b64_tr_b16 v[40:41], v14 offset:96\n"
"buffer_load_dwordx4 v10, s[4:7], s66 offen lds\n"
"v_mfma_f32_16x16x32_bf16 v[144:147], a[104:107], v[16:19], v[144:147]\n"
"ds_read_b64_tr_b16 v[42:43], v14 offset:1376\n"
"v_lshlrev_b32 v67, 3, v1\n"
"v_mfma_f32_16x16x32_bf16 v[144:147], a[116:119], v[20:23], v[144:147]\n"
"s_waitcnt lgkmcnt(6)\n"
"s_add_u32 m0, s72, 5120\n"
"v_mfma_f32_16x16x32_bf16 v[148:151], a[96:99], v[24:27], v[148:151]\n"
"ds_read_b64_tr_b16 v[44:45], v14 offset:5216\n"
"v_cmp_gt_u32 vcc, 5, v133\n"
"v_mfma_f32_16x16x32_bf16 v[148:151], a[108:111], v[28:31], v[148:151]\n"
"ds_read_b64_tr_b16 v[46:47], v14 offset:6496\n"
"buffer_load_dwordx4 v11, s[4:7], s66 offen lds\n"
"v_mfma_f32_16x16x32_bf16 v[152:155], a[100:103], v[24:27], v[152:155]\n"
"s_mul_i32 s33, s85, 245760\n"
"s_add_u32 s68, s68, s33\n"
"v_mfma_f32_16x16x32_bf16 v[152:155], a[112:115], v[28:31], v[152:155]\n"
"ds_read_b64_tr_b16 v[16:17], v14 offset:128\n"
"s_add_u32 m0, s71, 40960\n"
"v_mfma_f32_16x16x32_bf16 v[156:159], a[104:107], v[24:27], v[156:159]\n"
"ds_read_b64_tr_b16 v[18:19], v14 offset:1408\n"
"v_cndmask_b32 v71, v53, v67, s[88:89]\n"
"v_mfma_f32_16x16x32_bf16 v[156:159], a[116:119], v[28:31], v[156:159]\n"
"buffer_load_dwordx4 v5, s[8:11], s68 offen offset:0 lds\n"
"s_waitcnt lgkmcnt(6)\n"
"v_mfma_f32_16x16x32_bf16 v[160:163], a[96:99], v[32:35], v[160:163]\n"
"ds_read_b64_tr_b16 v[20:21], v14 offset:5248\n"
"v_mul_u32_u24 v72, 5, v135\n"
"v_mfma_f32_16x16x32_bf16 v[160:163], a[108:111], v[36:39], v[160:163]\n"
"ds_read_b64_tr_b16 v[22:23], v14 offset:6528\n"
"s_waitcnt vmcnt(12)\n"
"v_mfma_f32_16x16x32_bf16 v[164:167], a[100:103], v[32:35], v[164:167]\n"
"ds_read_b128 a[120:123], v12 offset:12288\n"
"v_lshl_add_u32 v72, v133, 4, v72\n"
"v_mfma_f32_16x16x32_bf16 v[164:167], a[112:115], v[36:39], v[164:167]\n"
"ds_read_b64_tr_b16 v[24:25], v14 offset:10240\n"
"ds_read_b128 a[124:127], v12 offset:14336\n"
"v_mfma_f32_16x16x32_bf16 v[168:171], a[104:107], v[32:35], v[168:171]\n"
"ds_read_b64_tr_b16 v[26:27], v14 offset:11520\n"
"ds_read_b128 a[128:131], v12 offset:16384\n"
"v_mfma_f32_16x16x32_bf16 v[168:171], a[116:119], v[36:39], v[168:171]\n"
"ds_read_b128 a[132:135], v13 offset:12288\n"
"s_waitcnt lgkmcnt(10)\n"
"v_mfma_f32_16x16x32_bf16 v[172:175], a[96:99], v[40:43], v[172:175]\n"
"ds_read_b64_tr_b16 v[28:29], v14 offset:15360\n"
"ds_read_b128 a[136:139], v13 offset:14336\n"
"v_mfma_f32_16x16x32_bf16 v[172:175], a[108:111], v[44:47], v[172:175]\n"
"ds_read_b64_tr_b16 v[30:31], v14 offset:16640\n"
"ds_read_b128 a[140:143], v13 offset:16384\n"
"v_mfma_f32_16x16x32_bf16 v[176:179], a[100:103], v[40:43], v[176:179]\n"
"buffer_load_dwordx4 v6, s[8:11], s68 offen offset:1024 lds\n"
"v_lshrrev_b32 v72, 1, v72\n"
"v_mfma_f32_16x16x32_bf16 v[176:179], a[112:115], v[44:47], v[176:179]\n"
"ds_read_b64_tr_b16 v[32:33], v14 offset:10272\n"
"v_cndmask_b32 v72, v53, v72, vcc\n"
"v_mfma_f32_16x16x32_bf16 v[180:183], a[104:107], v[40:43], v[180:183]\n"
"ds_read_b64_tr_b16 v[34:35], v14 offset:11552\n"
"v_cndmask_b32 v73, v53, v71, vcc\n"
"v_mfma_f32_16x16x32_bf16 v[180:183], a[116:119], v[44:47], v[180:183]\n"
"buffer_load_dwordx4 v7, s[8:11], s68 offen offset:2048 lds\n"
"s_waitcnt lgkmcnt(12)\n"
"v_mfma_f32_16x16x32_bf16 v[184:187], a[96:99], v[16:19], v[184:187]\n"
"ds_read_b64_tr_b16 v[36:37], v14 offset:15392\n"
"v_lshrrev_b32 v53, 1, v135\n"
"v_mfma_f32_16x16x32_bf16 v[184:187], a[108:111], v[20:23], v[184:187]\n"
"ds_read_b64_tr_b16 v[38:39], v14 offset:16672\n"
"s_mul_i32 s60, s3, 96\n"
"v_mfma_f32_16x16x32_bf16 v[188:191], a[100:103], v[16:19], v[188:191]\n"
"s_add_u32 s60, s60, 81920\n"
"v_mfma_f32_16x16x32_bf16 v[188:191], a[112:115], v[20:23], v[188:191]\n"
"ds_read_b64_tr_b16 v[40:41], v14 offset:10304\n"
"ds_read_b64_tr_b16 v[42:43], v14 offset:11584\n"
"v_mfma_f32_16x16x32_bf16 v[192:195], a[104:107], v[16:19], v[192:195]\n"
"v_add3_u32 v55, v55, v53, s60\n"
"v_add_u32 v58, v55, v53\n"
"v_mfma_f32_16x16x32_bf16 v[192:195], a[116:119], v[20:23], v[192:195]\n"
"buffer_load_dwordx4 v8, s[8:11], s68 offen offset:3072 lds\n"
"s_waitcnt lgkmcnt(7)\n"
"v_mfma_f32_16x16x32_bf16 v[196:199], a[96:99], v[24:27], v[196:199]\n"
"ds_read_b64_tr_b16 v[44:45], v14 offset:15424\n"
"ds_read_b64_tr_b16 v[46:47], v14 offset:16704\n"
"v_mfma_f32_16x16x32_bf16 v[196:199], a[108:111], v[28:31], v[196:199]\n"
"ds_read_b64_tr_b16 v[16:17], v14 offset:10336\n"
"ds_read_b64_tr_b16 v[18:19], v14 offset:11616\n"
"v_mfma_f32_16x16x32_bf16 v[200:203], a[100:103], v[24:27], v[200:203]\n"
"ds_read_b64_tr_b16 v[20:21], v14 offset:15456\n"
"ds_read_b64_tr_b16 v[22:23], v14 offset:16736\n"
"v_mfma_f32_16x16x32_bf16 v[200:203], a[112:115], v[28:31], v[200:203]\n"
"v_cmp_gt_u32 vcc, 8, v1\n"
"s_add_u32 m0, s71, 45056\n"
"v_mfma_f32_16x16x32_bf16 v[204:207], a[104:107], v[24:27], v[204:207]\n"
"v_and_b32 v54, 1, v1\n"
"v_min_u32 v53, 11, v1\n"
"v_mfma_f32_16x16x32_bf16 v[204:207], a[116:119], v[28:31], v[204:207]\n"
"buffer_load_dwordx4 v9, s[8:11], s68 offen offset:0 lds\n"
"s_waitcnt lgkmcnt(8)\n"
"v_mfma_f32_16x16x32_bf16 v[208:211], a[96:99], v[32:35], v[208:211]\n"
"ds_read_b64_tr_b16 v[24:25], v14 offset:10368\n"
"ds_read_b64_tr_b16 v[26:27], v14 offset:11648\n"
"v_mfma_f32_16x16x32_bf16 v[208:211], a[108:111], v[36:39], v[208:211]\n"
"ds_read_b64_tr_b16 v[28:29], v14 offset:15488\n"
"ds_read_b64_tr_b16 v[30:31], v14 offset:16768\n"
"v_mfma_f32_16x16x32_bf16 v[212:215], a[100:103], v[32:35], v[212:215]\n"
"s_cmp_eq_u32 s32, 3\n"
"s_cselect_b32 s85, -9, 1\n"
"v_mfma_f32_16x16x32_bf16 v[212:215], a[112:115], v[36:39], v[212:215]\n"
"v_lshl_add_u32 v10, s85, 7, v10\n"
"v_lshl_add_u32 v11, s85, 7, v11\n"
"v_mfma_f32_16x16x32_bf16 v[216:219], a[104:107], v[32:35], v[216:219]\n"
"v_cndmask_b32 v54, 2, v54, vcc\n"
"s_add_u32 m0, s72, 6144\n"
"v_mfma_f32_16x16x32_bf16 v[216:219], a[116:119], v[36:39], v[216:219]\n"
"buffer_load_dwordx4 v10, s[4:7], s64 offen lds\n"
"s_waitcnt lgkmcnt(8)\n"
"v_mfma_f32_16x16x32_bf16 v[220:223], a[96:99], v[40:43], v[220:223]\n"
"v_lshl_add_u32 v54, v54, 4, s67\n"
"v_cmp_gt_u32 vcc, 2048, v54\n"
"v_mfma_f32_16x16x32_bf16 v[220:223], a[108:111], v[44:47], v[220:223]\n"
"s_add_u32 m0, s72, 7168\n"
"s_add_u32 s60, s60, 66560\n"
"v_mfma_f32_16x16x32_bf16 v[224:227], a[100:103], v[40:43], v[224:227]\n"
"v_lshl_add_u32 v53, v53, 3, s60\n"
"v_mov_b32 v54, 0x3f803f80\n"
"v_mfma_f32_16x16x32_bf16 v[224:227], a[112:115], v[44:47], v[224:227]\n"
"buffer_load_dwordx4 v11, s[4:7], s64 offen lds\n"
"v_cndmask_b32 v56, 0, v54, vcc\n"
"v_mfma_f32_16x16x32_bf16 v[228:231], a[104:107], v[40:43], v[228:231]\n"
"s_lshr_b32 s34, s2, 3\n"
"s_sub_u32 s34, s34, 0\n"
"v_mfma_f32_16x16x32_bf16 v[228:231], a[116:119], v[44:47], v[228:231]\n"
"s_waitcnt vmcnt(13) lgkmcnt(0)\n"
"s_barrier\n"
"v_mfma_f32_16x16x32_bf16 v[232:235], a[96:99], v[16:19], v[232:235]\n"
"ds_read_b64_tr_b16 v[32:33], v14 offset:20480\n"
"s_lshl_b32 s34, s34, 5\n"
"v_mfma_f32_16x16x32_bf16 v[232:235], a[108:111], v[20:23], v[232:235]\n"
"ds_read_b64_tr_b16 v[34:35], v14 offset:21760\n"
"v_lshrrev_b32 v51, 3, v0\n"
"v_mfma_f32_16x16x32_bf16 v[236:239], a[100:103], v[16:19], v[236:239]\n"
"v_add_lshl_u32 v51, v51, s34, 7\n"
"v_add_u32 v51, .Lfb_atte-.Lfb_pc, v51\n"
"v_mfma_f32_16x16x32_bf16 v[236:239], a[112:115], v[20:23], v[236:239]\n"
"ds_read_b64_tr_b16 v[36:37], v14 offset:25600\n"
"v_min_u32 v51, .Lfb_end-.Lfb_pc-4, v51\n"
"v_mfma_f32_16x16x32_bf16 v[240:243], a[104:107], v[16:19], v[240:243]\n"
"ds_read_b64_tr_b16 v[38:39], v14 offset:26880\n"
"global_load_dword v52, v51, s[86:87]\n"
"v_mfma_f32_16x16x32_bf16 v[240:243], a[116:119], v[20:23], v[240:243]\n"
"s_add_u32 m0, s72, 8192\n"
"v_mfma_f32_16x16x32_bf16 v[244:247], a[96:99], v[24:27], v[244:247]\n"
"ds_read_b64_tr_b16 v[40:41], v14 offset:20512\n"
"buffer_load_dwordx4 v10, s[4:7], s65 offen lds\n"
"v_mfma_f32_16x16x32_bf16 v[244:247], a[108:111], v[28:31], v[244:247]\n"
"ds_read_b64_tr_b16 v[42:43], v14 offset:21792\n"
"s_add_u32 m0, s72, 9216\n"
"v_mfma_f32_16x16x32_bf16 v[248:251], a[100:103], v[24:27], v[248:251]\n"
"buffer_load_dwordx4 v11, s[4:7], s65 offen lds\n"
"v_cndmask_b32 v57, 0, v54, vcc\n"
"v_mfma_f32_16x16x32_bf16 v[248:251], a[112:115], v[28:31], v[248:251]\n"
"ds_read_b64_tr_b16 v[44:45], v14 offset:25632\n"
"v_mfma_f32_16x16x32_bf16 v[252:255], a[104:107], v[24:27], v[252:255]\n"
"ds_read_b64_tr_b16 v[46:47], v14 offset:26912\n"
"ds_read_b64_tr_b16 v[16:17], v14 offset:20544\n"
"v_mfma_f32_16x16x32_bf16 v[252:255], a[116:119], v[28:31], v[252:255]\n"
"ds_read_b64_tr_b16 v[18:19], v14 offset:21824\n"
"s_waitcnt lgkmcnt(6)\n"
"v_mfma_f32_16x16x32_bf16 v[136:139], a[120:123], v[32:35], v[136:139]\n"
"ds_read_b64_tr_b16 v[20:21], v14 offset:25664\n"
"s_add_u32 m0, s72, 10240\n"
"v_mfma_f32_16x16x32_bf16 v[136:139], a[132:135], v[36:39], v[136:139]\n"
"ds_read_b64_tr_b16 v[22:23], v14 offset:26944\n"
"buffer_load_dwordx4 v10, s[4:7], s66 offen lds\n"
"v_mfma_f32_16x16x32_bf16 v[140:143], a[124:127], v[32:35], v[140:143]\n"
"s_add_u32 m0, s72, 11264\n"
"v_mfma_f32_16x16x32_bf16 v[140:143], a[136:139], v[36:39], v[140:143]\n"
"ds_read_b64_tr_b16 v[24:25], v14 offset:20576\n"
"v_mfma_f32_16x16x32_bf16 v[144:147], a[128:131], v[32:35], v[144:147]\n"
"ds_read_b64_tr_b16 v[26:27], v14 offset:21856\n"
"buffer_load_dwordx4 v11, s[4:7], s66 offen lds\n"
"v_mfma_f32_16x16x32_bf16 v[144:147], a[140:143], v[36:39], v[144:147]\n"
"s_waitcnt lgkmcnt(6)\n"
"s_mul_i32 s33, s85, 245760\n"
"v_mfma_f32_16x16x32_bf16 v[148:151], a[120:123], v[40:43], v[148:151]\n"
"ds_read_b64_tr_b16 v[28:29], v14 offset:25696\n"
"s_add_u32 s68, s68, s33\n"
"v_mfma_f32_16x16x32_bf16 v[148:151], a[132:135], v[44:47], v[148:151]\n"
"ds_read_b64_tr_b16 v[30:31], v14 offset:26976\n"
"s_add_u32 m0, s71, 61440\n"
"v_mfma_f32_16x16x32_bf16 v[152:155], a[124:127], v[40:43], v[152:155]\n"
"buffer_load_dwordx4 v5, s[8:11], s68 offen offset:0 lds\n"
"v_mfma_f32_16x16x32_bf16 v[152:155], a[136:139], v[44:47], v[152:155]\n"
"ds_read_b64_tr_b16 v[32:33], v14 offset:20608\n"
"v_mfma_f32_16x16x32_bf16 v[156:159], a[128:131], v[40:43], v[156:159]\n"
"ds_read_b64_tr_b16 v[34:35], v14 offset:21888\n"
"v_mfma_f32_16x16x32_bf16 v[156:159], a[140:143], v[44:47], v[156:159]\n"
"s_waitcnt lgkmcnt(6)\n"
"v_mfma_f32_16x16x32_bf16 v[160:163], a[120:123], v[16:19], v[160:163]\n"
"ds_read_b64_tr_b16 v[36:37], v14 offset:25728\n"
"v_mfma_f32_16x16x32_bf16 v[160:163], a[132:135], v[20:23], v[160:163]\n"
"ds_read_b64_tr_b16 v[38:39], v14 offset:27008\n"
"s_waitcnt vmcnt(13)\n"
"v_mfma_f32_16x16x32_bf16 v[164:167], a[124:127], v[16:19], v[164:167]\n"
"ds_read_b128 a[144:147], v12 offset:0\n"
"buffer_load_dwordx4 v6, s[8:11], s68 offen offset:1024 lds\n"
"v_mfma_f32_16x16x32_bf16 v[164:167], a[136:139], v[20:23], v[164:167]\n"
"ds_read_b64_tr_b16 v[40:41], v14 offset:30720\n"
"ds_read_b128 a[148:151], v12 offset:2048\n"
"v_mfma_f32_16x16x32_bf16 v[168:171], a[128:131], v[16:19], v[168:171]\n"
"ds_read_b64_tr_b16 v[42:43], v14 offset:32000\n"
"ds_read_b128 a[152:155], v12 offset:4096\n"
"v_mfma_f32_16x16x32_bf16 v[168:171], a[140:143], v[20:23], v[168:171]\n"
"ds_read_b128 a[156:159], v13 offset:0\n"
"s_waitcnt lgkmcnt(10)\n"
"v_mfma_f32_16x16x32_bf16 v[172:175], a[120:123], v[24:27], v[172:175]\n"
"ds_read_b64_tr_b16 v[44:45], v14 offset:35840\n"
"ds_read_b128 a[160:163], v13 offset:2048\n"
"v_mfma_f32_16x16x32_bf16 v[172:175], a[132:135], v[28:31], v[172:175]\n"
"ds_read_b64_tr_b16 v[46:47], v14 offset:37120\n"
"ds_read_b128 a[164:167], v13 offset:4096\n"
"v_mfma_f32_16x16x32_bf16 v[176:179], a[124:127], v[24:27], v[176:179]\n"
"buffer_load_dwordx4 v7, s[8:11], s68 offen offset:2048 lds\n"
"v_mfma_f32_16x16x32_bf16 v[176:179], a[136:139], v[28:31], v[176:179]\n"
"ds_read_b64_tr_b16 v[16:17], v14 offset:30752\n"
"v_mfma_f32_16x16x32_bf16 v[180:183], a[128:131], v[24:27], v[180:183]\n"
"ds_read_b64_tr_b16 v[18:19], v14 offset:32032\n"
"v_mfma_f32_16x16x32_bf16 v[180:183], a[140:143], v[28:31], v[180:183]\n"
"s_waitcnt lgkmcnt(12)\n"
"v_mfma_f32_16x16x32_bf16 v[184:187], a[120:123], v[32:35], v[184:187]\n"
"ds_read_b64_tr_b16 v[20:21], v14 offset:35872\n"
"v_mfma_f32_16x16x32_bf16 v[184:187], a[132:135], v[36:39], v[184:187]\n"
"ds_read_b64_tr_b16 v[22:23], v14 offset:37152\n"
"v_mfma_f32_16x16x32_bf16 v[188:191], a[124:127], v[32:35], v[188:191]\n"
"buffer_load_dwordx4 v8, s[8:11], s68 offen offset:3072 lds\n"
"v_mfma_f32_16x16x32_bf16 v[188:191], a[136:139], v[36:39], v[188:191]\n"
"ds_read_b64_tr_b16 v[24:25], v14 offset:30784\n"
"ds_read_b64_tr_b16 v[26:27], v14 offset:32064\n"
"v_mfma_f32_16x16x32_bf16 v[192:195], a[128:131], v[32:35], v[192:195]\n"
"v_mfma_f32_16x16x32_bf16 v[192:195], a[140:143], v[36:39], v[192:195]\n"
"s_waitcnt lgkmcnt(7)\n"
"v_mfma_f32_16x16x32_bf16 v[196:199], a[120:123], v[40:43], v[196:199]\n"
"ds_read_b64_tr_b16 v[28:29], v14 offset:35904\n"
"ds_read_b64_tr_b16 v[30:31], v14 offset:37184\n"
"v_mfma_f32_16x16x32_bf16 v[196:199], a[132:135], v[44:47], v[196:199]\n"
"ds_read_b64_tr_b16 v[32:33], v14 offset:30816\n"
"ds_read_b64_tr_b16 v[34:35], v14 offset:32096\n"
"v_mfma_f32_16x16x32_bf16 v[200:203], a[124:127], v[40:43], v[200:203]\n"
"ds_read_b64_tr_b16 v[36:37], v14 offset:35936\n"
"ds_read_b64_tr_b16 v[38:39], v14 offset:37216\n"
"v_mfma_f32_16x16x32_bf16 v[200:203], a[136:139], v[44:47], v[200:203]\n"
"s_add_u32 m0, s71, 65536\n"
"v_mfma_f32_16x16x32_bf16 v[204:207], a[128:131], v[40:43], v[204:207]\n"
"buffer_load_dwordx4 v9, s[8:11], s68 offen offset:0 lds\n"
"v_mfma_f32_16x16x32_bf16 v[204:207], a[140:143], v[44:47], v[204:207]\n"
"s_waitcnt lgkmcnt(8)\n"
"v_mfma_f32_16x16x32_bf16 v[208:211], a[120:123], v[16:19], v[208:211]\n"
"ds_read_b64_tr_b16 v[40:41], v14 offset:30848\n"
"ds_read_b64_tr_b16 v[42:43], v14 offset:32128\n"
"v_mfma_f32_16x16x32_bf16 v[208:211], a[132:135], v[20:23], v[208:211]\n"
"ds_read_b64_tr_b16 v[44:45], v14 offset:35968\n"
"ds_read_b64_tr_b16 v[46:47], v14 offset:37248\n"
"v_mfma_f32_16x16x32_bf16 v[212:215], a[124:127], v[16:19], v[212:215]\n"
"s_cmp_eq_u32 s32, 2\n"
"s_cselect_b32 s85, -9, 1\n"
"v_mfma_f32_16x16x32_bf16 v[212:215], a[136:139], v[20:23], v[212:215]\n"
"v_lshl_add_u32 v10, s85, 7, v10\n"
"v_lshl_add_u32 v11, s85, 7, v11\n"
"v_mfma_f32_16x16x32_bf16 v[216:219], a[128:131], v[16:19], v[216:219]\n"
"s_add_u32 m0, s72, 12288\n"
"v_mfma_f32_16x16x32_bf16 v[216:219], a[140:143], v[20:23], v[216:219]\n"
"buffer_load_dwordx4 v10, s[4:7], s64 offen lds\n"
"s_waitcnt lgkmcnt(8)\n"
"v_mfma_f32_16x16x32_bf16 v[220:223], a[120:123], v[24:27], v[220:223]\n"
"s_add_u32 m0, s72, 13312\n"
"v_mfma_f32_16x16x32_bf16 v[220:223], a[132:135], v[28:31], v[220:223]\n"
"buffer_load_dwordx4 v11, s[4:7], s64 offen lds\n"
"v_mfma_f32_16x16x32_bf16 v[224:227], a[124:127], v[24:27], v[224:227]\n"
"s_add_u32 m0, s72, 14336\n"
"v_mfma_f32_16x16x32_bf16 v[224:227], a[136:139], v[28:31], v[224:227]\n"
"v_mfma_f32_16x16x32_bf16 v[228:231], a[128:131], v[24:27], v[228:231]\n"
"buffer_load_dwordx4 v10, s[4:7], s65 offen lds\n"
"v_mfma_f32_16x16x32_bf16 v[228:231], a[140:143], v[28:31], v[228:231]\n"
"s_waitcnt vmcnt(15) lgkmcnt(0)\n"
"s_barrier\n"
"v_mfma_f32_16x16x32_bf16 v[232:235], a[120:123], v[32:35], v[232:235]\n"
"ds_read_b64_tr_b16 v[16:17], v15 offset:0\n"
"s_add_u32 m0, s72, 15360\n"
"v_mfma_f32_16x16x32_bf16 v[232:235], a[132:135], v[36:39], v[232:235]\n"
"ds_read_b64_tr_b16 v[18:19], v15 offset:1280\n"
"v_mfma_f32_16x16x32_bf16 v[236:239], a[124:127], v[32:35], v[236:239]\n"
"buffer_load_dwordx4 v11, s[4:7], s65 offen lds\n"
"v_mfma_f32_16x16x32_bf16 v[236:239], a[136:139], v[36:39], v[236:239]\n"
"ds_read_b64_tr_b16 v[20:21], v15 offset:5120\n"
"s_add_u32 m0, s72, 16384\n"
"v_mfma_f32_16x16x32_bf16 v[240:243], a[128:131], v[32:35], v[240:243]\n"
"ds_read_b64_tr_b16 v[22:23], v15 offset:6400\n"
"v_mfma_f32_16x16x32_bf16 v[240:243], a[140:143], v[36:39], v[240:243]\n"
"buffer_load_dwordx4 v10, s[4:7], s66 offen lds\n"
"v_mfma_f32_16x16x32_bf16 v[244:247], a[120:123], v[40:43], v[244:247]\n"
"ds_read_b64_tr_b16 v[24:25], v15 offset:32\n"
"v_mfma_f32_16x16x32_bf16 v[244:247], a[132:135], v[44:47], v[244:247]\n"
"ds_read_b64_tr_b16 v[26:27], v15 offset:1312\n"
"s_add_u32 m0, s72, 17408\n"
"v_mfma_f32_16x16x32_bf16 v[248:251], a[124:127], v[40:43], v[248:251]\n"
"v_mfma_f32_16x16x32_bf16 v[248:251], a[136:139], v[44:47], v[248:251]\n"
"ds_read_b64_tr_b16 v[28:29], v15 offset:5152\n"
"buffer_load_dwordx4 v11, s[4:7], s66 offen lds\n"
"v_mfma_f32_16x16x32_bf16 v[252:255], a[128:131], v[40:43], v[252:255]\n"
"ds_read_b64_tr_b16 v[30:31], v15 offset:6432\n"
"ds_read_b64_tr_b16 v[32:33], v15 offset:64\n"
"v_mfma_f32_16x16x32_bf16 v[252:255], a[140:143], v[44:47], v[252:255]\n"
"ds_read_b64_tr_b16 v[34:35], v15 offset:1344\n"
"s_waitcnt lgkmcnt(6)\n"
"v_mfma_f32_16x16x32_bf16 v[136:139], a[144:147], v[16:19], v[136:139]\n"
"ds_read_b64_tr_b16 v[36:37], v15 offset:5184\n"
"s_mul_i32 s33, s85, 245760\n"
"v_mfma_f32_16x16x32_bf16 v[136:139], a[156:159], v[20:23], v[136:139]\n"
"ds_read_b64_tr_b16 v[38:39], v15 offset:6464\n"
"s_add_u32 s68, s68, s33\n"
"v_mfma_f32_16x16x32_bf16 v[140:143], a[148:151], v[16:19], v[140:143]\n"
"s_add_u32 m0, s71, 0\n"
"v_mfma_f32_16x16x32_bf16 v[140:143], a[160:163], v[20:23], v[140:143]\n"
"ds_read_b64_tr_b16 v[40:41], v15 offset:96\n"
"buffer_load_dwordx4 v5, s[8:11], s68 offen offset:0 lds\n"
"v_mfma_f32_16x16x32_bf16 v[144:147], a[152:155], v[16:19], v[144:147]\n"
"ds_read_b64_tr_b16 v[42:43], v15 offset:1376\n"
"v_mfma_f32_16x16x32_bf16 v[144:147], a[164:167], v[20:23], v[144:147]\n"
"s_waitcnt lgkmcnt(6)\n"
"v_mfma_f32_16x16x32_bf16 v[148:151], a[144:147], v[24:27], v[148:151]\n"
"ds_read_b64_tr_b16 v[44:45], v15 offset:5216\n"
"v_mfma_f32_16x16x32_bf16 v[148:151], a[156:159], v[28:31], v[148:151]\n"
"ds_read_b64_tr_b16 v[46:47], v15 offset:6496\n"
"buffer_load_dwordx4 v6, s[8:11], s68 offen offset:1024 lds\n"
"v_mfma_f32_16x16x32_bf16 v[152:155], a[148:151], v[24:27], v[152:155]\n"
"v_mfma_f32_16x16x32_bf16 v[152:155], a[160:163], v[28:31], v[152:155]\n"
"ds_read_b64_tr_b16 v[16:17], v15 offset:128\n"
"v_mfma_f32_16x16x32_bf16 v[156:159], a[152:155], v[24:27], v[156:159]\n"
"ds_read_b64_tr_b16 v[18:19], v15 offset:1408\n"
"v_mfma_f32_16x16x32_bf16 v[156:159], a[164:167], v[28:31], v[156:159]\n"
"s_waitcnt lgkmcnt(6)\n"
"v_mfma_f32_16x16x32_bf16 v[160:163], a[144:147], v[32:35], v[160:163]\n"
"ds_read_b64_tr_b16 v[20:21], v15 offset:5248\n"
"v_mfma_f32_16x16x32_bf16 v[160:163], a[156:159], v[36:39], v[160:163]\n"
"ds_read_b64_tr_b16 v[22:23], v15 offset:6528\n"
"s_waitcnt vmcnt(13)\n"
"v_mfma_f32_16x16x32_bf16 v[164:167], a[148:151], v[32:35], v[164:167]\n"
"ds_read_b128 a[168:171], v12 offset:6144\n"
"buffer_load_dwordx4 v7, s[8:11], s68 offen offset:2048 lds\n"
"v_mfma_f32_16x16x32_bf16 v[164:167], a[160:163], v[36:39], v[164:167]\n"
"ds_read_b64_tr_b16 v[24:25], v15 offset:10240\n"
"ds_read_b128 a[172:175], v12 offset:8192\n"
"v_mfma_f32_16x16x32_bf16 v[168:171], a[152:155], v[32:35], v[168:171]\n"
"ds_read_b64_tr_b16 v[26:27], v15 offset:11520\n"
"ds_read_b128 a[176:179], v12 offset:10240\n"
"v_mfma_f32_16x16x32_bf16 v[168:171], a[164:167], v[36:39], v[168:171]\n"
"ds_read_b128 a[180:183], v13 offset:6144\n"
"s_waitcnt lgkmcnt(10)\n"
"v_mfma_f32_16x16x32_bf16 v[172:175], a[144:147], v[40:43], v[172:175]\n"
"ds_read_b64_tr_b16 v[28:29], v15 offset:15360\n"
"ds_read_b128 a[184:187], v13 offset:8192\n"
"v_mfma_f32_16x16x32_bf16 v[172:175], a[156:159], v[44:47], v[172:175]\n"
"ds_read_b64_tr_b16 v[30:31], v15 offset:16640\n"
"ds_read_b128 a[188:191], v13 offset:10240\n"
"v_mfma_f32_16x16x32_bf16 v[176:179], a[148:151], v[40:43], v[176:179]\n"
"buffer_load_dwordx4 v8, s[8:11], s68 offen offset:3072 lds\n"
"v_mfma_f32_16x16x32_bf16 v[176:179], a[160:163], v[44:47], v[176:179]\n"
"ds_read_b64_tr_b16 v[32:33], v15 offset:10272\n"
"v_mfma_f32_16x16x32_bf16 v[180:183], a[152:155], v[40:43], v[180:183]\n"
"ds_read_b64_tr_b16 v[34:35], v15 offset:11552\n"
"v_mfma_f32_16x16x32_bf16 v[180:183], a[164:167], v[44:47], v[180:183]\n"
"s_waitcnt lgkmcnt(12)\n"
"s_add_u32 m0, s71, 4096\n"
"v_mfma_f32_16x16x32_bf16 v[184:187], a[144:147], v[16:19], v[184:187]\n"
"ds_read_b64_tr_b16 v[36:37], v15 offset:15392\n"
"v_mfma_f32_16x16x32_bf16 v[184:187], a[156:159], v[20:23], v[184:187]\n"
"ds_read_b64_tr_b16 v[38:39], v15 offset:16672\n"
"buffer_load_dwordx4 v9, s[8:11], s68 offen offset:0 lds\n"
"v_mfma_f32_16x16x32_bf16 v[188:191], a[148:151], v[16:19], v[188:191]\n"
"v_mfma_f32_16x16x32_bf16 v[188:191], a[160:163], v[20:23], v[188:191]\n"
"ds_read_b64_tr_b16 v[40:41], v15 offset:10304\n"
"ds_read_b64_tr_b16 v[42:43], v15 offset:11584\n"
"v_mfma_f32_16x16x32_bf16 v[192:195], a[152:155], v[16:19], v[192:195]\n"
"s_cmp_eq_u32 s32, 1\n"
"s_cselect_b32 s85, -9, 1\n"
"v_mfma_f32_16x16x32_bf16 v[192:195], a[164:167], v[20:23], v[192:195]\n"
"v_lshl_add_u32 v10, s85, 7, v10\n"
"s_waitcnt lgkmcnt(7)\n"
"v_mfma_f32_16x16x32_bf16 v[196:199], a[144:147], v[24:27], v[196:199]\n"
"ds_read_b64_tr_b16 v[44:45], v15 offset:15424\n"
"ds_read_b64_tr_b16 v[46:47], v15 offset:16704\n"
"v_mfma_f32_16x16x32_bf16 v[196:199], a[156:159], v[28:31], v[196:199]\n"
"ds_read_b64_tr_b16 v[16:17], v15 offset:10336\n"
"ds_read_b64_tr_b16 v[18:19], v15 offset:11616\n"
"v_mfma_f32_16x16x32_bf16 v[200:203], a[148:151], v[24:27], v[200:203]\n"
"ds_read_b64_tr_b16 v[20:21], v15 offset:15456\n"
"ds_read_b64_tr_b16 v[22:23], v15 offset:16736\n"
"v_mfma_f32_16x16x32_bf16 v[200:203], a[160:163], v[28:31], v[200:203]\n"
"v_lshl_add_u32 v11, s85, 7, v11\n"
"s_add_u32 m0, s72, 0\n"
"v_mfma_f32_16x16x32_bf16 v[204:207], a[152:155], v[24:27], v[204:207]\n"
"buffer_load_dwordx4 v10, s[4:7], s64 offen lds\n"
"s_add_u32 m0, s72, 1024\n"
"v_mfma_f32_16x16x32_bf16 v[204:207], a[164:167], v[28:31], v[204:207]\n"
"buffer_load_dwordx4 v11, s[4:7], s64 offen lds\n"
"s_waitcnt lgkmcnt(8)\n"
"v_mfma_f32_16x16x32_bf16 v[208:211], a[144:147], v[32:35], v[208:211]\n"
"ds_read_b64_tr_b16 v[24:25], v15 offset:10368\n"
"ds_read_b64_tr_b16 v[26:27], v15 offset:11648\n"
"v_mfma_f32_16x16x32_bf16 v[208:211], a[156:159], v[36:39], v[208:211]\n"
"ds_read_b64_tr_b16 v[28:29], v15 offset:15488\n"
"ds_read_b64_tr_b16 v[30:31], v15 offset:16768\n"
"v_mfma_f32_16x16x32_bf16 v[212:215], a[148:151], v[32:35], v[212:215]\n"
"s_add_u32 m0, s72, 2048\n"
"v_mfma_f32_16x16x32_bf16 v[212:215], a[160:163], v[36:39], v[212:215]\n"
"buffer_load_dwordx4 v10, s[4:7], s65 offen lds\n"
"v_mfma_f32_16x16x32_bf16 v[216:219], a[152:155], v[32:35], v[216:219]\n"
"v_mfma_f32_16x16x32_bf16 v[216:219], a[164:167], v[36:39], v[216:219]\n"
"s_waitcnt lgkmcnt(8)\n"
"s_add_u32 m0, s72, 3072\n"
"v_mfma_f32_16x16x32_bf16 v[220:223], a[144:147], v[40:43], v[220:223]\n"
"v_mfma_f32_16x16x32_bf16 v[220:223], a[156:159], v[44:47], v[220:223]\n"
"buffer_load_dwordx4 v11, s[4:7], s65 offen lds\n"
"v_mfma_f32_16x16x32_bf16 v[224:227], a[148:151], v[40:43], v[224:227]\n"
"v_mfma_f32_16x16x32_bf16 v[224:227], a[160:163], v[44:47], v[224:227]\n"
"s_add_u32 m0, s72, 4096\n"
"v_mfma_f32_16x16x32_bf16 v[228:231], a[152:155], v[40:43], v[228:231]\n"
"v_mfma_f32_16x16x32_bf16 v[228:231], a[164:167], v[44:47], v[228:231]\n"
"s_waitcnt vmcnt(15) lgkmcnt(0)\n"
"s_barrier\n"
"v_mfma_f32_16x16x32_bf16 v[232:235], a[144:147], v[16:19], v[232:235]\n"
"ds_read_b64_tr_b16 v[32:33], v15 offset:20480\n"
"buffer_load_dwordx4 v10, s[4:7], s66 offen lds\n"
"v_mfma_f32_16x16x32_bf16 v[232:235], a[156:159], v[20:23], v[232:235]\n"
"ds_read_b64_tr_b16 v[34:35], v15 offset:21760\n"
"s_add_u32 m0, s72, 5120\n"
"v_mfma_f32_16x16x32_bf16 v[236:239], a[148:151], v[16:19], v[236:239]\n"
"buffer_load_dwordx4 v11, s[4:7], s66 offen lds\n"
"v_mfma_f32_16x16x32_bf16 v[236:239], a[160:163], v[20:23], v[236:239]\n"
"ds_read_b64_tr_b16 v[36:37], v15 offset:25600\n"
"s_mul_i32 s33, s85, 245760\n"
"v_mfma_f32_16x16x32_bf16 v[240:243], a[152:155], v[16:19], v[240:243]\n"
"ds_read_b64_tr_b16 v[38:39], v15 offset:26880\n"
"s_add_u32 s68, s68, s33\n"
"v_mfma_f32_16x16x32_bf16 v[240:243], a[164:167], v[20:23], v[240:243]\n"
"s_add_u32 m0, s71, 20480\n"
"v_mfma_f32_16x16x32_bf16 v[244:247], a[144:147], v[24:27], v[244:247]\n"
"ds_read_b64_tr_b16 v[40:41], v15 offset:20512\n"
"buffer_load_dwordx4 v5, s[8:11], s68 offen offset:0 lds\n"
"v_mfma_f32_16x16x32_bf16 v[244:247], a[156:159], v[28:31], v[244:247]\n"
"ds_read_b64_tr_b16 v[42:43], v15 offset:21792\n"
"v_mfma_f32_16x16x32_bf16 v[248:251], a[148:151], v[24:27], v[248:251]\n"
"v_mfma_f32_16x16x32_bf16 v[248:251], a[160:163], v[28:31], v[248:251]\n"
"ds_read_b64_tr_b16 v[44:45], v15 offset:25632\n"
"v_mfma_f32_16x16x32_bf16 v[252:255], a[152:155], v[24:27], v[252:255]\n"
"ds_read_b64_tr_b16 v[46:47], v15 offset:26912\n"
"ds_read_b64_tr_b16 v[16:17], v15 offset:20544\n"
"v_mfma_f32_16x16x32_bf16 v[252:255], a[164:167], v[28:31], v[252:255]\n"
"ds_read_b64_tr_b16 v[18:19], v15 offset:21824\n"
"s_waitcnt lgkmcnt(6)\n"
"v_mfma_f32_16x16x32_bf16 v[136:139], a[168:171], v[32:35], v[136:139]\n"
"ds_read_b64_tr_b16 v[20:21], v15 offset:25664\n"
"buffer_load_dwordx4 v6, s[8:11], s68 offen offset:1024 lds\n"
"v_mfma_f32_16x16x32_bf16 v[136:139], a[180:183], v[36:39], v[136:139]\n"
"ds_read_b64_tr_b16 v[22:23], v15 offset:26944\n"
"v_mfma_f32_16x16x32_bf16 v[140:143], a[172:175], v[32:35], v[140:143]\n"
"v_mfma_f32_16x16x32_bf16 v[140:143], a[184:187], v[36:39], v[140:143]\n"
"ds_read_b64_tr_b16 v[24:25], v15 offset:20576\n"
"v_mfma_f32_16x16x32_bf16 v[144:147], a[176:179], v[32:35], v[144:147]\n"
"ds_read_b64_tr_b16 v[26:27], v15 offset:21856\n"
"v_mfma_f32_16x16x32_bf16 v[144:147], a[188:191], v[36:39], v[144:147]\n"
"s_waitcnt lgkmcnt(6)\n"
"v_mfma_f32_16x16x32_bf16 v[148:151], a[168:171], v[40:43], v[148:151]\n"
"ds_read_b64_tr_b16 v[28:29], v15 offset:25696\n"
"buffer_load_dwordx4 v7, s[8:11], s68 offen offset:2048 lds\n"
"v_mfma_f32_16x16x32_bf16 v[148:151], a[180:183], v[44:47], v[148:151]\n"
"ds_read_b64_tr_b16 v[30:31], v15 offset:26976\n"
"v_mfma_f32_16x16x32_bf16 v[152:155], a[172:175], v[40:43], v[152:155]\n"
"v_mfma_f32_16x16x32_bf16 v[152:155], a[184:187], v[44:47], v[152:155]\n"
"ds_read_b64_tr_b16 v[32:33], v15 offset:20608\n"
"v_mfma_f32_16x16x32_bf16 v[156:159], a[176:179], v[40:43], v[156:159]\n"
"ds_read_b64_tr_b16 v[34:35], v15 offset:21888\n"
"v_mfma_f32_16x16x32_bf16 v[156:159], a[188:191], v[44:47], v[156:159]\n"
"s_waitcnt lgkmcnt(6)\n"
"v_mfma_f32_16x16x32_bf16 v[160:163], a[168:171], v[16:19], v[160:163]\n"
"ds_read_b64_tr_b16 v[36:37], v15 offset:25728\n"
"buffer_load_dwordx4 v8, s[8:11], s68 offen offset:3072 lds\n"
"v_mfma_f32_16x16x32_bf16 v[160:163], a[180:183], v[20:23], v[160:163]\n"
"ds_read_b64_tr_b16 v[38:39], v15 offset:27008\n"
"s_waitcnt vmcnt(15)\n"
"v_mfma_f32_16x16x32_bf16 v[164:167], a[172:175], v[16:19], v[164:167]\n"
"ds_read_b128 a[192:195], v12 offset:12288\n"
"v_mfma_f32_16x16x32_bf16 v[164:167], a[184:187], v[20:23], v[164:167]\n"
"ds_read_b64_tr_b16 v[40:41], v15 offset:30720\n"
"ds_read_b128 a[196:199], v12 offset:14336\n"
"v_mfma_f32_16x16x32_bf16 v[168:171], a[176:179], v[16:19], v[168:171]\n"
"ds_read_b64_tr_b16 v[42:43], v15 offset:32000\n"
"ds_read_b128 a[200:203], v12 offset:16384\n"
"v_mfma_f32_16x16x32_bf16 v[168:171], a[188:191], v[20:23], v[168:171]\n"
"ds_read_b128 a[204:207], v13 offset:12288\n"
"s_waitcnt lgkmcnt(10)\n"
"v_mfma_f32_16x16x32_bf16 v[172:175], a[168:171], v[24:27], v[172:175]\n"
"ds_read_b64_tr_b16 v[44:45], v15 offset:35840\n"
"ds_read_b128 a[208:211], v13 offset:14336\n"
"v_mfma_f32_16x16x32_bf16 v[172:175], a[180:183], v[28:31], v[172:175]\n"
"ds_read_b64_tr_b16 v[46:47], v15 offset:37120\n"
"ds_read_b128 a[212:215], v13 offset:16384\n"
"v_mfma_f32_16x16x32_bf16 v[176:179], a[172:175], v[24:27], v[176:179]\n"
"s_add_u32 m0, s71, 24576\n"
"v_mfma_f32_16x16x32_bf16 v[176:179], a[184:187], v[28:31], v[176:179]\n"
"ds_read_b64_tr_b16 v[16:17], v15 offset:30752\n"
"buffer_load_dwordx4 v9, s[8:11], s68 offen offset:0 lds\n"
"v_mfma_f32_16x16x32_bf16 v[180:183], a[176:179], v[24:27], v[180:183]\n"
"ds_read_b64_tr_b16 v[18:19], v15 offset:32032\n"
"v_mfma_f32_16x16x32_bf16 v[180:183], a[188:191], v[28:31], v[180:183]\n"
"s_waitcnt lgkmcnt(12)\n"
"v_mfma_f32_16x16x32_bf16 v[184:187], a[168:171], v[32:35], v[184:187]\n"
"ds_read_b64_tr_b16 v[20:21], v15 offset:35872\n"
"v_mfma_f32_16x16x32_bf16 v[184:187], a[180:183], v[36:39], v[184:187]\n"
"ds_read_b64_tr_b16 v[22:23], v15 offset:37152\n"
"v_mfma_f32_16x16x32_bf16 v[188:191], a[172:175], v[32:35], v[188:191]\n"
"s_cmp_ge_u32 s3, 2\n"
"s_cselect_b32 s69, 1, 0\n"
"v_mfma_f32_16x16x32_bf16 v[188:191], a[184:187], v[36:39], v[188:191]\n"
"ds_read_b64_tr_b16 v[24:25], v15 offset:30784\n"
"ds_read_b64_tr_b16 v[26:27], v15 offset:32064\n"
"v_mfma_f32_16x16x32_bf16 v[192:195], a[176:179], v[32:35], v[192:195]\n"
"s_add_u32 s69, s69, s32\n"
"v_mfma_f32_16x16x32_bf16 v[192:195], a[188:191], v[36:39], v[192:195]\n"
"s_waitcnt lgkmcnt(7)\n"
"v_mfma_f32_16x16x32_bf16 v[196:199], a[168:171], v[40:43], v[196:199]\n"
"ds_read_b64_tr_b16 v[28:29], v15 offset:35904\n"
"ds_read_b64_tr_b16 v[30:31], v15 offset:37184\n"
"v_mfma_f32_16x16x32_bf16 v[196:199], a[180:183], v[44:47], v[196:199]\n"
"ds_read_b64_tr_b16 v[32:33], v15 offset:30816\n"
"ds_read_b64_tr_b16 v[34:35], v15 offset:32096\n"
"v_mfma_f32_16x16x32_bf16 v[200:203], a[172:175], v[40:43], v[200:203]\n"
"ds_read_b64_tr_b16 v[36:37], v15 offset:35936\n"
"ds_read_b64_tr_b16 v[38:39], v15 offset:37216\n"
"v_mfma_f32_16x16x32_bf16 v[200:203], a[184:187], v[44:47], v[200:203]\n"
"s_cmp_ge_u32 s69, 10\n"
"s_cselect_b32 s33, 10, 0\n"
"v_mfma_f32_16x16x32_bf16 v[204:207], a[176:179], v[40:43], v[204:207]\n"
"s_sub_u32 s69, s69, s33\n"
"s_mul_i32 s69, s69, 245760\n"
"v_mfma_f32_16x16x32_bf16 v[204:207], a[188:191], v[44:47], v[204:207]\n"
"s_waitcnt lgkmcnt(8)\n"
"s_add_u32 s69, s69, s14\n"
"v_mfma_f32_16x16x32_bf16 v[208:211], a[168:171], v[16:19], v[208:211]\n"
"ds_read_b64_tr_b16 v[40:41], v15 offset:30848\n"
"ds_read_b64_tr_b16 v[42:43], v15 offset:32128\n"
"v_mfma_f32_16x16x32_bf16 v[208:211], a[180:183], v[20:23], v[208:211]\n"
"ds_read_b64_tr_b16 v[44:45], v15 offset:35968\n"
"ds_read_b64_tr_b16 v[46:47], v15 offset:37248\n"
"v_mfma_f32_16x16x32_bf16 v[212:215], a[172:175], v[16:19], v[212:215]\n"
"s_add_u32 m0, s71, 40960\n"
"v_mfma_f32_16x16x32_bf16 v[212:215], a[184:187], v[20:23], v[212:215]\n"
"buffer_load_dwordx4 v5, s[8:11], s69 offen offset:0 lds\n"
"v_mfma_f32_16x16x32_bf16 v[216:219], a[176:179], v[16:19], v[216:219]\n"
"v_mfma_f32_16x16x32_bf16 v[216:219], a[188:191], v[20:23], v[216:219]\n"
"s_waitcnt lgkmcnt(8)\n"
"v_mfma_f32_16x16x32_bf16 v[220:223], a[168:171], v[24:27], v[220:223]\n"
"v_mfma_f32_16x16x32_bf16 v[220:223], a[180:183], v[28:31], v[220:223]\n"
"v_mfma_f32_16x16x32_bf16 v[224:227], a[172:175], v[24:27], v[224:227]\n"
"v_mfma_f32_16x16x32_bf16 v[224:227], a[184:187], v[28:31], v[224:227]\n"
"buffer_load_dword v49, v48, s[16:19], 0 offen sc1\n"
"v_mfma_f32_16x16x32_bf16 v[228:231], a[176:179], v[24:27], v[228:231]\n"
"v_mfma_f32_16x16x32_bf16 v[228:231], a[188:191], v[28:31], v[228:231]\n"
"s_waitcnt vmcnt(13) lgkmcnt(0)\n"
"s_barrier\n"
"v_mfma_f32_16x16x32_bf16 v[232:235], a[168:171], v[32:35], v[232:235]\n"
"ds_read_b64_tr_b16 v[16:17], v14 offset:0\n"
"v_mfma_f32_16x16x32_bf16 v[232:235], a[180:183], v[36:39], v[232:235]\n"
"ds_read_b64_tr_b16 v[18:19], v14 offset:1280\n"
"v_mfma_f32_16x16x32_bf16 v[236:239], a[172:175], v[32:35], v[236:239]\n"
"v_mfma_f32_16x16x32_bf16 v[236:239], a[184:187], v[36:39], v[236:239]\n"
"ds_read_b64_tr_b16 v[20:21], v14 offset:5120\n"
"v_mfma_f32_16x16x32_bf16 v[240:243], a[176:179], v[32:35], v[240:243]\n"
"ds_read_b64_tr_b16 v[22:23], v14 offset:6400\n"
"v_mfma_f32_16x16x32_bf16 v[240:243], a[188:191], v[36:39], v[240:243]\n"
"s_add_u32 m0, s71, 40960\n"
"v_mfma_f32_16x16x32_bf16 v[244:247], a[168:171], v[40:43], v[244:247]\n"
"ds_read_b64_tr_b16 v[24:25], v14 offset:32\n"
"v_mfma_f32_16x16x32_bf16 v[244:247], a[180:183], v[44:47], v[244:247]\n"
"ds_read_b64_tr_b16 v[26:27], v14 offset:1312\n"
"buffer_load_dwordx4 v6, s[8:11], s69 offen offset:1024 lds\n"
"v_mfma_f32_16x16x32_bf16 v[248:251], a[172:175], v[40:43], v[248:251]\n"
"v_mfma_f32_16x16x32_bf16 v[248:251], a[184:187], v[44:47], v[248:251]\n"
"ds_read_b64_tr_b16 v[28:29], v14 offset:5152\n"
"v_mfma_f32_16x16x32_bf16 v[252:255], a[176:179], v[40:43], v[252:255]\n"
"ds_read_b64_tr_b16 v[30:31], v14 offset:6432\n"
"ds_read_b64_tr_b16 v[32:33], v14 offset:64\n"
"v_mfma_f32_16x16x32_bf16 v[252:255], a[188:191], v[44:47], v[252:255]\n"
"ds_read_b64_tr_b16 v[34:35], v14 offset:1344\n"
"s_waitcnt lgkmcnt(6)\n"
"v_mfma_f32_16x16x32_bf16 v[136:139], a[192:195], v[16:19], v[136:139]\n"
"ds_read_b64_tr_b16 v[36:37], v14 offset:5184\n"
"v_mfma_f32_16x16x32_bf16 v[136:139], a[204:207], v[20:23], v[136:139]\n"
"ds_read_b64_tr_b16 v[38:39], v14 offset:6464\n"
"v_mfma_f32_16x16x32_bf16 v[140:143], a[196:199], v[16:19], v[140:143]\n"
"s_add_u32 m0, s71, 40960\n"
"v_mfma_f32_16x16x32_bf16 v[140:143], a[208:211], v[20:23], v[140:143]\n"
"ds_read_b64_tr_b16 v[40:41], v14 offset:96\n"
"v_mfma_f32_16x16x32_bf16 v[144:147], a[200:203], v[16:19], v[144:147]\n"
"ds_read_b64_tr_b16 v[42:43], v14 offset:1376\n"
"buffer_load_dwordx4 v7, s[8:11], s69 offen offset:2048 lds\n"
"v_mfma_f32_16x16x32_bf16 v[144:147], a[212:215], v[20:23], v[144:147]\n"
"s_waitcnt lgkmcnt(6)\n"
"v_mfma_f32_16x16x32_bf16 v[148:151], a[192:195], v[24:27], v[148:151]\n"
"ds_read_b64_tr_b16 v[44:45], v14 offset:5216\n"
"v_mfma_f32_16x16x32_bf16 v[148:151], a[204:207], v[28:31], v[148:151]\n"
"ds_read_b64_tr_b16 v[46:47], v14 offset:6496\n"
"v_mfma_f32_16x16x32_bf16 v[152:155], a[196:199], v[24:27], v[152:155]\n"
"v_mfma_f32_16x16x32_bf16 v[152:155], a[208:211], v[28:31], v[152:155]\n"
"ds_read_b64_tr_b16 v[16:17], v14 offset:128\n"
"v_mfma_f32_16x16x32_bf16 v[156:159], a[200:203], v[24:27], v[156:159]\n"
"ds_read_b64_tr_b16 v[18:19], v14 offset:1408\n"
"v_mfma_f32_16x16x32_bf16 v[156:159], a[212:215], v[28:31], v[156:159]\n"
"s_waitcnt lgkmcnt(6)\n"
"v_mfma_f32_16x16x32_bf16 v[160:163], a[192:195], v[32:35], v[160:163]\n"
"ds_read_b64_tr_b16 v[20:21], v14 offset:5248\n"
"s_add_u32 m0, s71, 40960\n"
"v_mfma_f32_16x16x32_bf16 v[160:163], a[204:207], v[36:39], v[160:163]\n"
"ds_read_b64_tr_b16 v[22:23], v14 offset:6528\n"
"s_waitcnt vmcnt(9)\n"
"v_mfma_f32_16x16x32_bf16 v[164:167], a[196:199], v[32:35], v[164:167]\n"
"ds_read_b128 a[216:219], v12 offset:0\n"
"buffer_load_dwordx4 v8, s[8:11], s69 offen offset:3072 lds\n"
"v_mfma_f32_16x16x32_bf16 v[164:167], a[208:211], v[36:39], v[164:167]\n"
"ds_read_b64_tr_b16 v[24:25], v14 offset:10240\n"
"ds_read_b128 a[220:223], v12 offset:2048\n"
"v_mfma_f32_16x16x32_bf16 v[168:171], a[200:203], v[32:35], v[168:171]\n"
"ds_read_b64_tr_b16 v[26:27], v14 offset:11520\n"
"ds_read_b128 a[224:227], v12 offset:4096\n"
"v_mfma_f32_16x16x32_bf16 v[168:171], a[212:215], v[36:39], v[168:171]\n"
"ds_read_b128 a[228:231], v13 offset:0\n"
"s_waitcnt lgkmcnt(10)\n"
"v_mfma_f32_16x16x32_bf16 v[172:175], a[192:195], v[40:43], v[172:175]\n"
"ds_read_b64_tr_b16 v[28:29], v14 offset:15360\n"
"ds_read_b128 a[232:235], v13 offset:2048\n"
"v_mfma_f32_16x16x32_bf16 v[172:175], a[204:207], v[44:47], v[172:175]\n"
"ds_read_b64_tr_b16 v[30:31], v14 offset:16640\n"
"ds_read_b128 a[236:239], v13 offset:4096\n"
"v_mfma_f32_16x16x32_bf16 v[176:179], a[196:199], v[40:43], v[176:179]\n"
"v_mfma_f32_16x16x32_bf16 v[176:179], a[208:211], v[44:47], v[176:179]\n"
"ds_read_b64_tr_b16 v[32:33], v14 offset:10272\n"
"s_add_u32 m0, s71, 45056\n"
"v_mfma_f32_16x16x32_bf16 v[180:183], a[200:203], v[40:43], v[180:183]\n"
"ds_read_b64_tr_b16 v[34:35], v14 offset:11552\n"
"v_mfma_f32_16x16x32_bf16 v[180:183], a[212:215], v[44:47], v[180:183]\n"
"buffer_load_dwordx4 v9, s[8:11], s69 offen offset:0 lds\n"
"s_waitcnt lgkmcnt(12)\n"
"v_mfma_f32_16x16x32_bf16 v[184:187], a[192:195], v[16:19], v[184:187]\n"
"ds_read_b64_tr_b16 v[36:37], v14 offset:15392\n"
"v_mfma_f32_16x16x32_bf16 v[184:187], a[204:207], v[20:23], v[184:187]\n"
"ds_read_b64_tr_b16 v[38:39], v14 offset:16672\n"
"v_mfma_f32_16x16x32_bf16 v[188:191], a[196:199], v[16:19], v[188:191]\n"
"v_mfma_f32_16x16x32_bf16 v[188:191], a[208:211], v[20:23], v[188:191]\n"
"ds_read_b64_tr_b16 v[40:41], v14 offset:10304\n"
"ds_read_b64_tr_b16 v[42:43], v14 offset:11584\n"
"v_mfma_f32_16x16x32_bf16 v[192:195], a[200:203], v[16:19], v[192:195]\n"
"s_cmp_ge_u32 s3, 2\n"
"s_cselect_b32 s69, 3, 2\n"
"v_mfma_f32_16x16x32_bf16 v[192:195], a[212:215], v[20:23], v[192:195]\n"
"s_waitcnt lgkmcnt(7)\n"
"s_add_u32 s69, s69, s32\n"
"v_mfma_f32_16x16x32_bf16 v[196:199], a[192:195], v[24:27], v[196:199]\n"
"ds_read_b64_tr_b16 v[44:45], v14 offset:15424\n"
"ds_read_b64_tr_b16 v[46:47], v14 offset:16704\n"
"v_mfma_f32_16x16x32_bf16 v[196:199], a[204:207], v[28:31], v[196:199]\n"
"ds_read_b64_tr_b16 v[16:17], v14 offset:10336\n"
"ds_read_b64_tr_b16 v[18:19], v14 offset:11616\n"
"v_mfma_f32_16x16x32_bf16 v[200:203], a[196:199], v[24:27], v[200:203]\n"
"ds_read_b64_tr_b16 v[20:21], v14 offset:15456\n"
"ds_read_b64_tr_b16 v[22:23], v14 offset:16736\n"
"v_mfma_f32_16x16x32_bf16 v[200:203], a[208:211], v[28:31], v[200:203]\n"
"s_waitcnt vmcnt(4)\n"
"v_mfma_f32_16x16x32_bf16 v[204:207], a[200:203], v[24:27], v[204:207]\n"
"v_cmp_ne_u32 vcc, s52, v49\n"
"v_mfma_f32_16x16x32_bf16 v[204:207], a[212:215], v[28:31], v[204:207]\n"
"s_waitcnt lgkmcnt(8)\n"
"s_cbranch_vccnz .Lfb_slow_28\n"
".Lfb_back_29:\n"
"v_mfma_f32_16x16x32_bf16 v[208:211], a[192:195], v[32:35], v[208:211]\n"
"ds_read_b64_tr_b16 v[24:25], v14 offset:10368\n"
"ds_read_b64_tr_b16 v[26:27], v14 offset:11648\n"
"v_mfma_f32_16x16x32_bf16 v[208:211], a[204:207], v[36:39], v[208:211]\n"
"ds_read_b64_tr_b16 v[28:29], v14 offset:15488\n"
"ds_read_b64_tr_b16 v[30:31], v14 offset:16768\n"
"v_mfma_f32_16x16x32_bf16 v[212:215], a[196:199], v[32:35], v[212:215]\n"
"s_cmp_ge_u32 s69, 10\n"
"s_cselect_b32 s33, 10, 0\n"
"v_mfma_f32_16x16x32_bf16 v[212:215], a[208:211], v[36:39], v[212:215]\n"
"s_sub_u32 s69, s69, s33\n"
"s_mul_i32 s69, s69, 245760\n"
"v_mfma_f32_16x16x32_bf16 v[216:219], a[200:203], v[32:35], v[216:219]\n"
"s_add_u32 s69, s69, s14\n"
"s_add_u32 m0, s71, 61440\n"
"v_mfma_f32_16x16x32_bf16 v[216:219], a[212:215], v[36:39], v[216:219]\n"
"buffer_load_dwordx4 v5, s[8:11], s69 offen offset:0 lds\n"
"s_waitcnt lgkmcnt(8)\n"
"v_mfma_f32_16x16x32_bf16 v[220:223], a[192:195], v[40:43], v[220:223]\n"
"s_mov_b32 m0, s12\n"
"v_mfma_f32_16x16x32_bf16 v[220:223], a[204:207], v[44:47], v[220:223]\n"
"v_mfma_f32_16x16x32_bf16 v[224:227], a[196:199], v[40:43], v[224:227]\n"
"buffer_load_dwordx4 v60, s[16:19], 0 offen sc1 lds\n"
"v_mfma_f32_16x16x32_bf16 v[224:227], a[208:211], v[44:47], v[224:227]\n"
"s_add_u32 s98, s43, 0\n"
"v_mfma_f32_16x16x32_bf16 v[228:231], a[200:203], v[40:43], v[228:231]\n"
"v_mfma_f32_16x16x32_bf16 v[228:231], a[212:215], v[44:47], v[228:231]\n"
"s_waitcnt lgkmcnt(0)\n"
"s_barrier\n"
"v_mfma_f32_16x16x32_bf16 v[232:235], a[192:195], v[16:19], v[232:235]\n"
"ds_read_b64_tr_b16 v[32:33], v14 offset:20480\n"
"buffer_load_dwordx4 a[240:243], v2, s[24:27], s98 offen offset:0 sc1\n"
"v_mfma_f32_16x16x32_bf16 v[232:235], a[204:207], v[20:23], v[232:235]\n"
"ds_read_b64_tr_b16 v[34:35], v14 offset:21760\n"
"v_mov_b32 v13, v55\n"
"v_mfma_f32_16x16x32_bf16 v[236:239], a[196:199], v[16:19], v[236:239]\n"
"v_mov_b32 v12, v58\n"
"v_mfma_f32_16x16x32_bf16 v[236:239], a[208:211], v[20:23], v[236:239]\n"
"ds_read_b64_tr_b16 v[36:37], v14 offset:25600\n"
"buffer_load_dwordx4 a[244:247], v2, s[24:27], s98 offen offset:1024 sc1\n"
"v_mfma_f32_16x16x32_bf16 v[240:243], a[200:203], v[16:19], v[240:243]\n"
"ds_read_b64_tr_b16 v[38:39], v14 offset:26880\n"
"v_mfma_f32_16x16x32_bf16 v[240:243], a[212:215], v[20:23], v[240:243]\n"
"v_mfma_f32_16x16x32_bf16 v[244:247], a[192:195], v[24:27], v[244:247]\n"
"ds_read_b64_tr_b16 v[40:41], v14 offset:20512\n"
"v_mfma_f32_16x16x32_bf16 v[244:247], a[204:207], v[28:31], v[244:247]\n"
"ds_read_b64_tr_b16 v[42:43], v14 offset:21792\n"
"buffer_load_dwordx4 a[248:251], v2, s[24:27], s98 offen offset:2048 sc1\n"
"v_mfma_f32_16x16x32_bf16 v[248:251], a[196:199], v[24:27], v[248:251]\n"
"v_mfma_f32_16x16x32_bf16 v[248:251], a[208:211], v[28:31], v[248:251]\n"
"ds_read_b64_tr_b16 v[44:45], v14 offset:25632\n"
"v_mfma_f32_16x16x32_bf16 v[252:255], a[200:203], v[24:27], v[252:255]\n"
"ds_read_b64_tr_b16 v[46:47], v14 offset:26912\n"
"ds_read_b64_tr_b16 v[16:17], v14 offset:20544\n"
"v_mfma_f32_16x16x32_bf16 v[252:255], a[212:215], v[28:31], v[252:255]\n"
"ds_read_b64_tr_b16 v[18:19], v14 offset:21824\n"
"s_waitcnt lgkmcnt(6)\n"
"v_mfma_f32_16x16x32_bf16 v[136:139], a[216:219], v[32:35], v[136:139]\n"
"ds_read_b64_tr_b16 v[20:21], v14 offset:25664\n"
"buffer_load_dwordx4 a[252:255], v2, s[24:27], s98 offen offset:3072 sc1\n"
"v_mfma_f32_16x16x32_bf16 v[136:139], a[228:231], v[36:39], v[136:139]\n"
"ds_read_b64_tr_b16 v[22:23], v14 offset:26944\n"
"s_add_u32 m0, s71, 61440\n"
"v_mfma_f32_16x16x32_bf16 v[140:143], a[220:223], v[32:35], v[140:143]\n"
"v_mfma_f32_16x16x32_bf16 v[140:143], a[232:235], v[36:39], v[140:143]\n"
"ds_read_b64_tr_b16 v[24:25], v14 offset:20576\n"
"buffer_load_dwordx4 v6, s[8:11], s69 offen offset:1024 lds\n"
"v_mfma_f32_16x16x32_bf16 v[144:147], a[224:227], v[32:35], v[144:147]\n"
"ds_read_b64_tr_b16 v[26:27], v14 offset:21856\n"
"v_mfma_f32_16x16x32_bf16 v[144:147], a[236:239], v[36:39], v[144:147]\n"
"s_waitcnt lgkmcnt(6)\n"
"v_mfma_f32_16x16x32_bf16 v[148:151], a[216:219], v[40:43], v[148:151]\n"
"ds_read_b64_tr_b16 v[28:29], v14 offset:25696\n"
"v_mfma_f32_16x16x32_bf16 v[148:151], a[228:231], v[44:47], v[148:151]\n"
"ds_read_b64_tr_b16 v[30:31], v14 offset:26976\n"
"s_add_u32 m0, s71, 61440\n"
"v_mfma_f32_16x16x32_bf16 v[152:155], a[220:223], v[40:43], v[152:155]\n"
"v_mfma_f32_16x16x32_bf16 v[152:155], a[232:235], v[44:47], v[152:155]\n"
"ds_read_b64_tr_b16 v[32:33], v14 offset:20608\n"
"buffer_load_dwordx4 v7, s[8:11], s69 offen offset:2048 lds\n"
"v_mfma_f32_16x16x32_bf16 v[156:159], a[224:227], v[40:43], v[156:159]\n"
"ds_read_b64_tr_b16 v[34:35], v14 offset:21888\n"
"v_mfma_f32_16x16x32_bf16 v[156:159], a[236:239], v[44:47], v[156:159]\n"
"s_waitcnt lgkmcnt(6)\n"
"v_mfma_f32_16x16x32_bf16 v[160:163], a[216:219], v[16:19], v[160:163]\n"
"ds_read_b64_tr_b16 v[36:37], v14 offset:25728\n"
"v_mfma_f32_16x16x32_bf16 v[160:163], a[228:231], v[20:23], v[160:163]\n"
"ds_read_b64_tr_b16 v[38:39], v14 offset:27008\n"
"s_add_u32 m0, s71, 61440\n"
"v_mfma_f32_16x16x32_bf16 v[164:167], a[220:223], v[16:19], v[164:167]\n"
"v_mfma_f32_16x16x32_bf16 v[164:167], a[232:235], v[20:23], v[164:167]\n"
"ds_read_b64_tr_b16 v[40:41], v14 offset:30720\n"
"buffer_load_dwordx4 v8, s[8:11], s69 offen offset:3072 lds\n"
"v_mfma_f32_16x16x32_bf16 v[168:171], a[224:227], v[16:19], v[168:171]\n"
"ds_read_b64_tr_b16 v[42:43], v14 offset:32000\n"
"v_mfma_f32_16x16x32_bf16 v[168:171], a[236:239], v[20:23], v[168:171]\n"
"s_waitcnt lgkmcnt(6)\n"
"v_mfma_f32_16x16x32_bf16 v[172:175], a[216:219], v[24:27], v[172:175]\n"
"ds_read_b64_tr_b16 v[44:45], v14 offset:35840\n"
"v_mfma_f32_16x16x32_bf16 v[172:175], a[228:231], v[28:31], v[172:175]\n"
"ds_read_b64_tr_b16 v[46:47], v14 offset:37120\n"
"s_add_u32 m0, s71, 65536\n"
"v_mfma_f32_16x16x32_bf16 v[176:179], a[220:223], v[24:27], v[176:179]\n"
"v_mfma_f32_16x16x32_bf16 v[176:179], a[232:235], v[28:31], v[176:179]\n"
"ds_read_b64_tr_b16 v[16:17], v14 offset:30752\n"
"buffer_load_dwordx4 v9, s[8:11], s69 offen offset:0 lds\n"
"v_mfma_f32_16x16x32_bf16 v[180:183], a[224:227], v[24:27], v[180:183]\n"
"ds_read_b64_tr_b16 v[18:19], v14 offset:32032\n"
"v_mfma_f32_16x16x32_bf16 v[180:183], a[236:239], v[28:31], v[180:183]\n"
"s_waitcnt lgkmcnt(6)\n"
"v_mfma_f32_16x16x32_bf16 v[184:187], a[216:219], v[32:35], v[184:187]\n"
"ds_read_b64_tr_b16 v[20:21], v14 offset:35872\n"
"v_mfma_f32_16x16x32_bf16 v[184:187], a[228:231], v[36:39], v[184:187]\n"
"ds_read_b64_tr_b16 v[22:23], v14 offset:37152\n"
"v_mfma_f32_16x16x32_bf16 v[188:191], a[220:223], v[32:35], v[188:191]\n"
"s_cmp_ge_u32 s3, 2\n"
"s_cselect_b32 s69, 5, 4\n"
"v_mfma_f32_16x16x32_bf16 v[188:191], a[232:235], v[36:39], v[188:191]\n"
"ds_read_b64_tr_b16 v[24:25], v14 offset:30784\n"
"s_add_u32 s69, s69, s32\n"
"v_mfma_f32_16x16x32_bf16 v[192:195], a[224:227], v[32:35], v[192:195]\n"
"ds_read_b64_tr_b16 v[26:27], v14 offset:32064\n"
"v_mfma_f32_16x16x32_bf16 v[192:195], a[236:239], v[36:39], v[192:195]\n"
"s_waitcnt lgkmcnt(6)\n"
"v_mfma_f32_16x16x32_bf16 v[196:199], a[216:219], v[40:43], v[196:199]\n"
"ds_read_b64_tr_b16 v[28:29], v14 offset:35904\n"
"v_mfma_f32_16x16x32_bf16 v[196:199], a[228:231], v[44:47], v[196:199]\n"
"ds_read_b64_tr_b16 v[30:31], v14 offset:37184\n"
"v_mfma_f32_16x16x32_bf16 v[200:203], a[220:223], v[40:43], v[200:203]\n"
"s_cmp_ge_u32 s69, 10\n"
"s_cselect_b32 s33, 10, 0\n"
"v_mfma_f32_16x16x32_bf16 v[200:203], a[232:235], v[44:47], v[200:203]\n"
"ds_read_b64_tr_b16 v[32:33], v14 offset:30816\n"
"s_sub_u32 s69, s69, s33\n"
"v_mfma_f32_16x16x32_bf16 v[204:207], a[224:227], v[40:43], v[204:207]\n"
"ds_read_b64_tr_b16 v[34:35], v14 offset:32096\n"
"s_mul_i32 s69, s69, 245760\n"
"v_mfma_f32_16x16x32_bf16 v[204:207], a[236:239], v[44:47], v[204:207]\n"
"s_waitcnt lgkmcnt(6)\n"
"s_add_u32 s69, s69, s14\n"
"v_mfma_f32_16x16x32_bf16 v[208:211], a[216:219], v[16:19], v[208:211]\n"
"ds_read_b64_tr_b16 v[36:37], v14 offset:35936\n"
"s_add_u32 m0, s71, 0\n"
"v_mfma_f32_16x16x32_bf16 v[208:211], a[228:231], v[20:23], v[208:211]\n"
"ds_read_b64_tr_b16 v[38:39], v14 offset:37216\n"
"buffer_load_dwordx4 v5, s[8:11], s69 offen offset:0 lds\n"
"v_mfma_f32_16x16x32_bf16 v[212:215], a[220:223], v[16:19], v[212:215]\n"
"s_add_u32 m0, s71, 0\n"
"s_waitcnt vmcnt(9)\n"
"v_mfma_f32_16x16x32_bf16 v[212:215], a[232:235], v[20:23], v[212:215]\n"
"ds_read_b64_tr_b16 v[40:41], v14 offset:30848\n"
"ds_read_b128 v[88:91], v132 offset:656\n"
"v_mfma_f32_16x16x32_bf16 v[216:219], a[224:227], v[16:19], v[216:219]\n"
"ds_read_b64_tr_b16 v[42:43], v14 offset:32128\n"
"buffer_load_dwordx4 v6, s[8:11], s69 offen offset:1024 lds\n"
"v_mfma_f32_16x16x32_bf16 v[216:219], a[236:239], v[20:23], v[216:219]\n"
"ds_read_b128 v[92:95], v132 offset:784\n"
"s_waitcnt lgkmcnt(8)\n"
"v_mfma_f32_16x16x32_bf16 v[220:223], a[216:219], v[24:27], v[220:223]\n"
"ds_read_b64_tr_b16 v[44:45], v14 offset:35968\n"
"ds_read_b128 v[96:99], v132 offset:912\n"
"v_mfma_f32_16x16x32_bf16 v[220:223], a[228:231], v[28:31], v[220:223]\n"
"ds_read_b64_tr_b16 v[46:47], v14 offset:37248\n"
"ds_read_b128 v[76:79], v132 offset:640\n"
"v_mfma_f32_16x16x32_bf16 v[224:227], a[220:223], v[24:27], v[224:227]\n"
"ds_read_b128 v[80:83], v132 offset:768\n"
"s_add_u32 m0, s71, 0\n"
"v_mfma_f32_16x16x32_bf16 v[224:227], a[232:235], v[28:31], v[224:227]\n"
"ds_read_b128 v[84:87], v132 offset:896\n"
"ds_read2_b32 v[112:113], v134 offset0:0 offset1:16\n"
"v_mfma_f32_16x16x32_bf16 v[228:231], a[224:227], v[24:27], v[228:231]\n"
"buffer_load_dwordx4 v7, s[8:11], s69 offen offset:2048 lds\n"
"ds_read2_b32 v[114:115], v134 offset0:32 offset1:48\n"
"v_mfma_f32_16x16x32_bf16 v[228:231], a[236:239], v[28:31], v[228:231]\n"
"ds_read2_b32 v[116:117], v134 offset0:64 offset1:80\n"
"s_waitcnt lgkmcnt(13)\n"
"v_mfma_f32_16x16x32_bf16 v[232:235], a[216:219], v[32:35], v[232:235]\n"
"ds_read2_b32 v[118:119], v134 offset0:96 offset1:112\n"
"ds_read2_b32 v[120:121], v134 offset0:128 offset1:144\n"
"v_mfma_f32_16x16x32_bf16 v[232:235], a[228:231], v[36:39], v[232:235]\n"
"s_waitcnt lgkmcnt(13)\n"
"v_mul_f32 v100, 1069066811, v88\n"
"v_mul_f32 v101, 1069066811, v89\n"
"v_mfma_f32_16x16x32_bf16 v[236:239], a[220:223], v[32:35], v[236:239]\n"
"v_mul_f32 v102, 1069066811, v90\n"
"s_add_u32 m0, s71, 0\n"
"v_mfma_f32_16x16x32_bf16 v[236:239], a[232:235], v[36:39], v[236:239]\n"
"v_mul_f32 v103, 1069066811, v91\n"
"s_waitcnt lgkmcnt(11)\n"
"v_mul_f32 v104, 1069066811, v92\n"
"v_mfma_f32_16x16x32_bf16 v[240:243], a[224:227], v[32:35], v[240:243]\n"
"buffer_load_dwordx4 v8, s[8:11], s69 offen offset:3072 lds\n"
"v_mul_f32 v105, 1069066811, v93\n"
"v_mfma_f32_16x16x32_bf16 v[240:243], a[236:239], v[36:39], v[240:243]\n"
"v_mul_f32 v106, 1069066811, v94\n"
"s_waitcnt lgkmcnt(8)\n"
"v_mfma_f32_16x16x32_bf16 v[244:247], a[216:219], v[40:43], v[244:247]\n"
"v_mul_f32 v107, 1069066811, v95\n"
"v_mul_f32 v108, 1069066811, v96\n"
"v_mfma_f32_16x16x32_bf16 v[244:247], a[228:231], v[44:47], v[244:247]\n"
"v_mul_f32 v109, 1069066811, v97\n"
"v_mul_f32 v110, 1069066811, v98\n"
"v_mfma_f32_16x16x32_bf16 v[248:251], a[220:223], v[40:43], v[248:251]\n"
"v_mul_f32 v111, 1069066811, v99\n"
"v_mfma_f32_16x16x32_bf16 v[248:251], a[232:235], v[44:47], v[248:251]\n"
"v_mfma_f32_16x16x32_bf16 v[252:255], a[224:227], v[40:43], v[252:255]\n"
"v_mfma_f32_16x16x32_bf16 v[252:255], a[236:239], v[44:47], v[252:255]\n"
".Lfb_atte:\n"
"ds_write_b64 v53, v[56:57]\n"
"s_waitcnt lgkmcnt(1)\n"
"s_nop 5\n"
"v_pk_fma_f32 v[252:253], v[84:85], v[120:121], v[252:253] op_sel:[0,1,0] op_sel_hi:[1,1,1] neg_lo:[1,0,0] neg_hi:[1,0,0]\n"
"v_pk_fma_f32 v[254:255], v[86:87], v[120:121], v[254:255] op_sel:[0,1,0] op_sel_hi:[1,1,1] neg_lo:[1,0,0] neg_hi:[1,0,0]\n"
"v_pk_mul_f32 v[252:253], v[252:253], v[96:97] op_sel:[0,0] op_sel_hi:[1,1]\n"
"v_pk_mul_f32 v[254:255], v[254:255], v[98:99] op_sel:[0,0] op_sel_hi:[1,1]\n"
"v_cvt_pk_bf16_f32 v36, v252, v253\n"
"v_cvt_pk_bf16_f32 v37, v254, v255\n"
"v_pk_fma_f32 v[248:249], v[80:81], v[120:121], v[248:249] op_sel:[0,1,0] op_sel_hi:[1,1,1] neg_lo:[1,0,0] neg_hi:[1,0,0]\n"
"v_pk_fma_f32 v[250:251], v[82:83], v[120:121], v[250:251] op_sel:[0,1,0] op_sel_hi:[1,1,1] neg_lo:[1,0,0] neg_hi:[1,0,0]\n"
"v_pk_mul_f32 v[248:249], v[248:249], v[92:93] op_sel:[0,0] op_sel_hi:[1,1]\n"
"v_pk_mul_f32 v[250:251], v[250:251], v[94:95] op_sel:[0,0] op_sel_hi:[1,1]\n"
"v_cvt_pk_bf16_f32 v34, v248, v249\n"
"v_cvt_pk_bf16_f32 v35, v250, v251\n"
"v_pk_fma_f32 v[244:245], v[76:77], v[120:121], v[244:245] op_sel:[0,1,0] op_sel_hi:[1,1,1] neg_lo:[1,0,0] neg_hi:[1,0,0]\n"
"v_pk_fma_f32 v[246:247], v[78:79], v[120:121], v[246:247] op_sel:[0,1,0] op_sel_hi:[1,1,1] neg_lo:[1,0,0] neg_hi:[1,0,0]\n"
"v_pk_mul_f32 v[244:245], v[244:245], v[88:89] op_sel:[0,0] op_sel_hi:[1,1]\n"
"v_pk_mul_f32 v[246:247], v[246:247], v[90:91] op_sel:[0,0] op_sel_hi:[1,1]\n"
"v_cvt_pk_bf16_f32 v32, v244, v245\n"
"v_cvt_pk_bf16_f32 v33, v246, v247\n"
"ds_write_b128 v12, v[32:35] offset:59904\n"
"ds_write_b64 v13, v[36:37] offset:59968\n"
"s_add_u32 m0, s71, 4096\n"
"s_nop 0\n"
"buffer_load_dwordx4 v9, s[8:11], s69 offen offset:0 lds\n"
"v_pk_fma_f32 v[240:241], v[84:85], v[120:121], v[240:241] op_sel:[0,0,0] op_sel_hi:[1,0,1] neg_lo:[1,0,0] neg_hi:[1,0,0]\n"
"v_pk_fma_f32 v[242:243], v[86:87], v[120:121], v[242:243] op_sel:[0,0,0] op_sel_hi:[1,0,1] neg_lo:[1,0,0] neg_hi:[1,0,0]\n"
"v_pk_mul_f32 v[240:241], v[240:241], v[96:97] op_sel:[0,0] op_sel_hi:[1,1]\n"
"v_pk_mul_f32 v[242:243], v[242:243], v[98:99] op_sel:[0,0] op_sel_hi:[1,1]\n"
"v_cvt_pk_bf16_f32 v44, v240, v241\n"
"v_cvt_pk_bf16_f32 v45, v242, v243\n"
"v_pk_fma_f32 v[236:237], v[80:81], v[120:121], v[236:237] op_sel:[0,0,0] op_sel_hi:[1,0,1] neg_lo:[1,0,0] neg_hi:[1,0,0]\n"
"v_pk_fma_f32 v[238:239], v[82:83], v[120:121], v[238:239] op_sel:[0,0,0] op_sel_hi:[1,0,1] neg_lo:[1,0,0] neg_hi:[1,0,0]\n"
"v_pk_mul_f32 v[236:237], v[236:237], v[92:93] op_sel:[0,0] op_sel_hi:[1,1]\n"
"v_pk_mul_f32 v[238:239], v[238:239], v[94:95] op_sel:[0,0] op_sel_hi:[1,1]\n"
"v_cvt_pk_bf16_f32 v42, v236, v237\n"
"v_cvt_pk_bf16_f32 v43, v238, v239\n"
"v_pk_fma_f32 v[232:233], v[76:77], v[120:121], v[232:233] op_sel:[0,0,0] op_sel_hi:[1,0,1] neg_lo:[1,0,0] neg_hi:[1,0,0]\n"
"v_pk_fma_f32 v[234:235], v[78:79], v[120:121], v[234:235] op_sel:[0,0,0] op_sel_hi:[1,0,1] neg_lo:[1,0,0] neg_hi:[1,0,0]\n"
"v_pk_mul_f32 v[232:233], v[232:233], v[88:89] op_sel:[0,0] op_sel_hi:[1,1]\n"
"v_pk_mul_f32 v[234:235], v[234:235], v[90:91] op_sel:[0,0] op_sel_hi:[1,1]\n"
"v_cvt_pk_bf16_f32 v40, v232, v233\n"
"v_cvt_pk_bf16_f32 v41, v234, v235\n"
"ds_write_b128 v12, v[40:43] offset:53248\n"
"s_add_u32 s98, s43, 4096\n"
"buffer_load_dwordx4 v[252:255], v2, s[24:27], s98 offen offset:0 sc1\n"
"ds_write_b64 v13, v[44:45] offset:53312\n"
"v_pk_fma_f32 v[228:229], v[84:85], v[118:119], v[228:229] op_sel:[0,1,0] op_sel_hi:[1,1,1] neg_lo:[1,0,0] neg_hi:[1,0,0]\n"
"v_pk_fma_f32 v[230:231], v[86:87], v[118:119], v[230:231] op_sel:[0,1,0] op_sel_hi:[1,1,1] neg_lo:[1,0,0] neg_hi:[1,0,0]\n"
"v_pk_mul_f32 v[228:229], v[228:229], v[96:97] op_sel:[0,0] op_sel_hi:[1,1]\n"
"v_pk_mul_f32 v[230:231], v[230:231], v[98:99] op_sel:[0,0] op_sel_hi:[1,1]\n"
"v_cvt_pk_bf16_f32 v36, v228, v229\n"
"v_cvt_pk_bf16_f32 v37, v230, v231\n"
"v_pk_fma_f32 v[224:225], v[80:81], v[118:119], v[224:225] op_sel:[0,1,0] op_sel_hi:[1,1,1] neg_lo:[1,0,0] neg_hi:[1,0,0]\n"
"v_pk_fma_f32 v[226:227], v[82:83], v[118:119], v[226:227] op_sel:[0,1,0] op_sel_hi:[1,1,1] neg_lo:[1,0,0] neg_hi:[1,0,0]\n"
"v_pk_mul_f32 v[224:225], v[224:225], v[92:93] op_sel:[0,0] op_sel_hi:[1,1]\n"
"v_pk_mul_f32 v[226:227], v[226:227], v[94:95] op_sel:[0,0] op_sel_hi:[1,1]\n"
"v_cvt_pk_bf16_f32 v34, v224, v225\n"
"v_cvt_pk_bf16_f32 v35, v226, v227\n"
"v_pk_fma_f32 v[220:221], v[76:77], v[118:119], v[220:221] op_sel:[0,1,0] op_sel_hi:[1,1,1] neg_lo:[1,0,0] neg_hi:[1,0,0]\n"
"v_pk_fma_f32 v[222:223], v[78:79], v[118:119], v[222:223] op_sel:[0,1,0] op_sel_hi:[1,1,1] neg_lo:[1,0,0] neg_hi:[1,0,0]\n"
"v_pk_mul_f32 v[220:221], v[220:221], v[88:89] op_sel:[0,0] op_sel_hi:[1,1]\n"
"v_pk_mul_f32 v[222:223], v[222:223], v[90:91] op_sel:[0,0] op_sel_hi:[1,1]\n"
"v_cvt_pk_bf16_f32 v32, v220, v221\n"
"v_cvt_pk_bf16_f32 v33, v222, v223\n"
"s_add_u32 s98, s55, 0\n"
"buffer_load_dwordx4 v[248:251], v2, s[24:27], s98 offen offset:0 sc1\n"
"ds_write_b128 v12, v[32:35] offset:46592\n"
"ds_write_b64 v13, v[36:37] offset:46656\n"
"v_pk_fma_f32 v[216:217], v[84:85], v[118:119], v[216:217] op_sel:[0,0,0] op_sel_hi:[1,0,1] neg_lo:[1,0,0] neg_hi:[1,0,0]\n"
"v_pk_fma_f32 v[218:219], v[86:87], v[118:119], v[218:219] op_sel:[0,0,0] op_sel_hi:[1,0,1] neg_lo:[1,0,0] neg_hi:[1,0,0]\n"
"v_pk_mul_f32 v[216:217], v[216:217], v[96:97] op_sel:[0,0] op_sel_hi:[1,1]\n"
"v_pk_mul_f32 v[218:219], v[218:219], v[98:99] op_sel:[0,0] op_sel_hi:[1,1]\n"
"v_cvt_pk_bf16_f32 v44, v216, v217\n"
"v_cvt_pk_bf16_f32 v45, v218, v219\n"
"v_pk_fma_f32 v[212:213], v[80:81], v[118:119], v[212:213] op_sel:[0,0,0] op_sel_hi:[1,0,1] neg_lo:[1,0,0] neg_hi:[1,0,0]\n"
"v_pk_fma_f32 v[214:215], v[82:83], v[118:119], v[214:215] op_sel:[0,0,0] op_sel_hi:[1,0,1] neg_lo:[1,0,0] neg_hi:[1,0,0]\n"
"v_pk_mul_f32 v[212:213], v[212:213], v[92:93] op_sel:[0,0] op_sel_hi:[1,1]\n"
"v_pk_mul_f32 v[214:215], v[214:215], v[94:95] op_sel:[0,0] op_sel_hi:[1,1]\n"
"v_cvt_pk_bf16_f32 v42, v212, v213\n"
"v_cvt_pk_bf16_f32 v43, v214, v215\n"
"v_pk_fma_f32 v[208:209], v[76:77], v[118:119], v[208:209] op_sel:[0,0,0] op_sel_hi:[1,0,1] neg_lo:[1,0,0] neg_hi:[1,0,0]\n"
"v_pk_fma_f32 v[210:211], v[78:79], v[118:119], v[210:211] op_sel:[0,0,0] op_sel_hi:[1,0,1] neg_lo:[1,0,0] neg_hi:[1,0,0]\n"
"buffer_load_dwordx4 v[244:247], v2, s[24:27], s98 offen offset:1024 sc1\n"
"v_pk_mul_f32 v[208:209], v[208:209], v[88:89] op_sel:[0,0] op_sel_hi:[1,1]\n"
"v_pk_mul_f32 v[210:211], v[210:211], v[90:91] op_sel:[0,0] op_sel_hi:[1,1]\n"
"v_cvt_pk_bf16_f32 v40, v208, v209\n"
"v_cvt_pk_bf16_f32 v41, v210, v211\n"
"ds_write_b128 v12, v[40:43] offset:39936\n"
"ds_write_b64 v13, v[44:45] offset:40000\n"
"v_pk_fma_f32 v[204:205], v[84:85], v[116:117], v[204:205] op_sel:[0,1,0] op_sel_hi:[1,1,1] neg_lo:[1,0,0] neg_hi:[1,0,0]\n"
"v_pk_fma_f32 v[206:207], v[86:87], v[116:117], v[206:207] op_sel:[0,1,0] op_sel_hi:[1,1,1] neg_lo:[1,0,0] neg_hi:[1,0,0]\n"
"v_pk_mul_f32 v[204:205], v[204:205], v[96:97] op_sel:[0,0] op_sel_hi:[1,1]\n"
"v_pk_mul_f32 v[206:207], v[206:207], v[98:99] op_sel:[0,0] op_sel_hi:[1,1]\n"
"v_cvt_pk_bf16_f32 v36, v204, v205\n"
"v_cvt_pk_bf16_f32 v37, v206, v207\n"
"v_pk_fma_f32 v[200:201], v[80:81], v[116:117], v[200:201] op_sel:[0,1,0] op_sel_hi:[1,1,1] neg_lo:[1,0,0] neg_hi:[1,0,0]\n"
"v_pk_fma_f32 v[202:203], v[82:83], v[116:117], v[202:203] op_sel:[0,1,0] op_sel_hi:[1,1,1] neg_lo:[1,0,0] neg_hi:[1,0,0]\n"
"v_pk_mul_f32 v[200:201], v[200:201], v[92:93] op_sel:[0,0] op_sel_hi:[1,1]\n"
"v_pk_mul_f32 v[202:203], v[202:203], v[94:95] op_sel:[0,0] op_sel_hi:[1,1]\n"
"buffer_load_dwordx4 v[240:243], v2, s[24:27], s98 offen offset:2048 sc1\n"
"v_cvt_pk_bf16_f32 v34, v200, v201\n"
"v_cvt_pk_bf16_f32 v35, v202, v203\n"
"v_pk_fma_f32 v[196:197], v[76:77], v[116:117], v[196:197] op_sel:[0,1,0] op_sel_hi:[1,1,1] neg_lo:[1,0,0] neg_hi:[1,0,0]\n"
"v_pk_fma_f32 v[198:199], v[78:79], v[116:117], v[198:199] op_sel:[0,1,0] op_sel_hi:[1,1,1] neg_lo:[1,0,0] neg_hi:[1,0,0]\n"
"v_pk_mul_f32 v[196:197], v[196:197], v[88:89] op_sel:[0,0] op_sel_hi:[1,1]\n"
"v_pk_mul_f32 v[198:199], v[198:199], v[90:91] op_sel:[0,0] op_sel_hi:[1,1]\n"
"v_cvt_pk_bf16_f32 v32, v196, v197\n"
"v_cvt_pk_bf16_f32 v33, v198, v199\n"
"ds_write_b128 v12, v[32:35] offset:33280\n"
"ds_write_b64 v13, v[36:37] offset:33344\n"
"v_pk_fma_f32 v[192:193], v[84:85], v[116:117], v[192:193] op_sel:[0,0,0] op_sel_hi:[1,0,1] neg_lo:[1,0,0] neg_hi:[1,0,0]\n"
"v_pk_fma_f32 v[194:195], v[86:87], v[116:117], v[194:195] op_sel:[0,0,0] op_sel_hi:[1,0,1] neg_lo:[1,0,0] neg_hi:[1,0,0]\n"
"v_pk_mul_f32 v[56:57], v[192:193], v[108:109] op_sel:[0,0] op_sel_hi:[1,1]\n"
"v_pk_mul_f32 v[58:59], v[194:195], v[110:111] op_sel:[0,0] op_sel_hi:[1,1]\n"
"v_pk_fma_f32 v[192:193], v[192:193], v[96:97], v[74:75] op_sel:[0,0,1] op_sel_hi:[1,1,1]\n"
"v_pk_fma_f32 v[194:195], v[194:195], v[98:99], v[74:75] op_sel:[0,0,1] op_sel_hi:[1,1,1]\n"
"buffer_load_dwordx4 v[236:239], v2, s[24:27], s98 offen offset:3072 sc1\n"
"v_exp_f32_e64 v56, v56 clamp\n"
"v_exp_f32_e64 v57, v57 clamp\n"
"v_exp_f32_e64 v58, v58 clamp\n"
"v_exp_f32_e64 v59, v59 clamp\n"
"v_max_f32 v192, v192, v75\n"
"v_max_f32 v193, v193, v75\n"
"v_max_f32 v194, v194, v75\n"
"v_max_f32 v195, v195, v75\n"
"v_pk_add_f32 v[192:193], v[192:193], v[56:57] op_sel:[0,0] op_sel_hi:[1,1]\n"
"v_pk_add_f32 v[194:195], v[194:195], v[58:59] op_sel:[0,0] op_sel_hi:[1,1]\n"
"v_cvt_pk_bf16_f32 v44, v192, v193\n"
"v_cvt_pk_bf16_f32 v45, v194, v195\n"
"v_pk_fma_f32 v[188:189], v[80:81], v[116:117], v[188:189] op_sel:[0,0,0] op_sel_hi:[1,0,1] neg_lo:[1,0,0] neg_hi:[1,0,0]\n"
"v_pk_fma_f32 v[190:191], v[82:83], v[116:117], v[190:191] op_sel:[0,0,0] op_sel_hi:[1,0,1] neg_lo:[1,0,0] neg_hi:[1,0,0]\n"
"v_pk_mul_f32 v[52:53], v[188:189], v[104:105] op_sel:[0,0] op_sel_hi:[1,1]\n"
"v_pk_mul_f32 v[54:55], v[190:191], v[106:107] op_sel:[0,0] op_sel_hi:[1,1]\n"
"v_pk_fma_f32 v[188:189], v[188:189], v[92:93], v[74:75] op_sel:[0,0,1] op_sel_hi:[1,1,1]\n"
"v_pk_fma_f32 v[190:191], v[190:191], v[94:95], v[74:75] op_sel:[0,0,1] op_sel_hi:[1,1,1]\n"
"s_add_u32 s98, s55, 4096\n"
"buffer_load_dwordx4 v[232:235], v2, s[24:27], s98 offen offset:0 sc1\n"
"v_exp_f32_e64 v52, v52 clamp\n"
"v_exp_f32_e64 v53, v53 clamp\n"
"v_exp_f32_e64 v54, v54 clamp\n"
"v_exp_f32_e64 v55, v55 clamp\n"
"v_max_f32 v188, v188, v75\n"
"v_max_f32 v189, v189, v75\n"
"v_max_f32 v190, v190, v75\n"
"v_max_f32 v191, v191, v75\n"
"v_pk_add_f32 v[188:189], v[188:189], v[52:53] op_sel:[0,0] op_sel_hi:[1,1]\n"
"v_pk_add_f32 v[190:191], v[190:191], v[54:55] op_sel:[0,0] op_sel_hi:[1,1]\n"
"v_cvt_pk_bf16_f32 v42, v188, v189\n"
"v_cvt_pk_bf16_f32 v43, v190, v191\n"
"v_pk_fma_f32 v[184:185], v[76:77], v[116:117], v[184:185] op_sel:[0,0,0] op_sel_hi:[1,0,1] neg_lo:[1,0,0] neg_hi:[1,0,0]\n"
"v_pk_fma_f32 v[186:187], v[78:79], v[116:117], v[186:187] op_sel:[0,0,0] op_sel_hi:[1,0,1] neg_lo:[1,0,0] neg_hi:[1,0,0]\n"
"v_pk_mul_f32 v[48:49], v[184:185], v[100:101] op_sel:[0,0] op_sel_hi:[1,1]\n"
"v_pk_mul_f32 v[50:51], v[186:187], v[102:103] op_sel:[0,0] op_sel_hi:[1,1]\n"
"v_pk_fma_f32 v[184:185], v[184:185], v[88:89], v[74:75] op_sel:[0,0,1] op_sel_hi:[1,1,1]\n"
"v_pk_fma_f32 v[186:187], v[186:187], v[90:91], v[74:75] op_sel:[0,0,1] op_sel_hi:[1,1,1]\n"
"s_add_u32 s98, s96, 0\n"
"buffer_load_dwordx4 v[228:231], v2, s[24:27], s98 offen offset:0 sc1\n"
"v_exp_f32_e64 v48, v48 clamp\n"
"v_exp_f32_e64 v49, v49 clamp\n"
"v_exp_f32_e64 v50, v50 clamp\n"
"v_exp_f32_e64 v51, v51 clamp\n"
"v_max_f32 v184, v184, v75\n"
"v_max_f32 v185, v185, v75\n"
"v_max_f32 v186, v186, v75\n"
"v_max_f32 v187, v187, v75\n"
"v_pk_add_f32 v[184:185], v[184:185], v[48:49] op_sel:[0,0] op_sel_hi:[1,1]\n"
"v_pk_add_f32 v[186:187], v[186:187], v[50:51] op_sel:[0,0] op_sel_hi:[1,1]\n"
"v_cvt_pk_bf16_f32 v40, v184, v185\n"
"v_cvt_pk_bf16_f32 v41, v186, v187\n"
"ds_write_b128 v12, v[40:43] offset:26624\n"
"ds_write_b64 v13, v[44:45] offset:26688\n"
"buffer_load_dwordx4 v[224:227], v2, s[24:27], s98 offen offset:1024 sc1\n"
"v_pk_fma_f32 v[180:181], v[84:85], v[114:115], v[180:181] op_sel:[0,1,0] op_sel_hi:[1,1,1] neg_lo:[1,0,0] neg_hi:[1,0,0]\n"
"v_pk_fma_f32 v[182:183], v[86:87], v[114:115], v[182:183] op_sel:[0,1,0] op_sel_hi:[1,1,1] neg_lo:[1,0,0] neg_hi:[1,0,0]\n"
"v_pk_mul_f32 v[56:57], v[180:181], v[108:109] op_sel:[0,0] op_sel_hi:[1,1]\n"
"v_pk_mul_f32 v[58:59], v[182:183], v[110:111] op_sel:[0,0] op_sel_hi:[1,1]\n"
"v_pk_fma_f32 v[180:181], v[180:181], v[96:97], v[74:75] op_sel:[0,0,1] op_sel_hi:[1,1,1]\n"
"v_pk_fma_f32 v[182:183], v[182:183], v[98:99], v[74:75] op_sel:[0,0,1] op_sel_hi:[1,1,1]\n"
"v_exp_f32_e64 v56, v56 clamp\n"
"v_exp_f32_e64 v57, v57 clamp\n"
"v_exp_f32_e64 v58, v58 clamp\n"
"v_exp_f32_e64 v59, v59 clamp\n"
"v_max_f32 v180, v180, v75\n"
"v_max_f32 v181, v181, v75\n"
"v_max_f32 v182, v182, v75\n"
"v_max_f32 v183, v183, v75\n"
"v_pk_add_f32 v[180:181], v[180:181], v[56:57] op_sel:[0,0] op_sel_hi:[1,1]\n"
"v_pk_add_f32 v[182:183], v[182:183], v[58:59] op_sel:[0,0] op_sel_hi:[1,1]\n"
"v_cvt_pk_bf16_f32 v36, v180, v181\n"
"v_cvt_pk_bf16_f32 v37, v182, v183\n"
"buffer_load_dwordx4 v[220:223], v2, s[24:27], s98 offen offset:2048 sc1\n"
"v_pk_fma_f32 v[176:177], v[80:81], v[114:115], v[176:177] op_sel:[0,1,0] op_sel_hi:[1,1,1] neg_lo:[1,0,0] neg_hi:[1,0,0]\n"
"v_pk_fma_f32 v[178:179], v[82:83], v[114:115], v[178:179] op_sel:[0,1,0] op_sel_hi:[1,1,1] neg_lo:[1,0,0] neg_hi:[1,0,0]\n"
"v_pk_mul_f32 v[52:53], v[176:177], v[104:105] op_sel:[0,0] op_sel_hi:[1,1]\n"
"v_pk_mul_f32 v[54:55], v[178:179], v[106:107] op_sel:[0,0] op_sel_hi:[1,1]\n"
"v_pk_fma_f32 v[176:177], v[176:177], v[92:93], v[74:75] op_sel:[0,0,1] op_sel_hi:[1,1,1]\n"
"v_pk_fma_f32 v[178:179], v[178:179], v[94:95], v[74:75] op_sel:[0,0,1] op_sel_hi:[1,1,1]\n"
"v_exp_f32_e64 v52, v52 clamp\n"
"v_exp_f32_e64 v53, v53 clamp\n"
"v_exp_f32_e64 v54, v54 clamp\n"
"v_exp_f32_e64 v55, v55 clamp\n"
"v_max_f32 v176, v176, v75\n"
"v_max_f32 v177, v177, v75\n"
"v_max_f32 v178, v178, v75\n"
"v_max_f32 v179, v179, v75\n"
"v_pk_add_f32 v[176:177], v[176:177], v[52:53] op_sel:[0,0] op_sel_hi:[1,1]\n"
"v_pk_add_f32 v[178:179], v[178:179], v[54:55] op_sel:[0,0] op_sel_hi:[1,1]\n"
"v_cvt_pk_bf16_f32 v34, v176, v177\n"
"v_cvt_pk_bf16_f32 v35, v178, v179\n"
"buffer_load_dwordx4 v[216:219], v2, s[24:27], s98 offen offset:3072 sc1\n"
"v_pk_fma_f32 v[172:173], v[76:77], v[114:115], v[172:173] op_sel:[0,1,0] op_sel_hi:[1,1,1] neg_lo:[1,0,0] neg_hi:[1,0,0]\n"
"v_pk_fma_f32 v[174:175], v[78:79], v[114:115], v[174:175] op_sel:[0,1,0] op_sel_hi:[1,1,1] neg_lo:[1,0,0] neg_hi:[1,0,0]\n"
"v_pk_mul_f32 v[48:49], v[172:173], v[100:101] op_sel:[0,0] op_sel_hi:[1,1]\n"
"v_pk_mul_f32 v[50:51], v[174:175], v[102:103] op_sel:[0,0] op_sel_hi:[1,1]\n"
"v_pk_fma_f32 v[172:173], v[172:173], v[88:89], v[74:75] op_sel:[0,0,1] op_sel_hi:[1,1,1]\n"
"v_pk_fma_f32 v[174:175], v[174:175], v[90:91], v[74:75] op_sel:[0,0,1] op_sel_hi:[1,1,1]\n"
"v_exp_f32_e64 v48, v48 clamp\n"
"v_exp_f32_e64 v49, v49 clamp\n"
"v_exp_f32_e64 v50, v50 clamp\n"
"v_exp_f32_e64 v51, v51 clamp\n"
"v_max_f32 v172, v172, v75\n"
"v_max_f32 v173, v173, v75\n"
"v_max_f32 v174, v174, v75\n"
"v_max_f32 v175, v175, v75\n"
"v_pk_add_f32 v[172:173], v[172:173], v[48:49] op_sel:[0,0] op_sel_hi:[1,1]\n"
"v_pk_add_f32 v[174:175], v[174:175], v[50:51] op_sel:[0,0] op_sel_hi:[1,1]\n"
"v_cvt_pk_bf16_f32 v32, v172, v173\n"
"v_cvt_pk_bf16_f32 v33, v174, v175\n"
"s_add_u32 s98, s96, 4096\n"
"buffer_load_dwordx4 v[212:215], v2, s[24:27], s98 offen offset:0 sc1\n"
"ds_write_b128 v12, v[32:35] offset:19968\n"
"ds_write_b64 v13, v[36:37] offset:20032\n"
"v_pk_fma_f32 v[168:169], v[84:85], v[114:115], v[168:169] op_sel:[0,0,0] op_sel_hi:[1,0,1] neg_lo:[1,0,0] neg_hi:[1,0,0]\n"
"v_pk_fma_f32 v[170:171], v[86:87], v[114:115], v[170:171] op_sel:[0,0,0] op_sel_hi:[1,0,1] neg_lo:[1,0,0] neg_hi:[1,0,0]\n"
"v_pk_mul_f32 v[56:57], v[168:169], v[108:109] op_sel:[0,0] op_sel_hi:[1,1]\n"
"v_pk_mul_f32 v[58:59], v[170:171], v[110:111] op_sel:[0,0] op_sel_hi:[1,1]\n"
"v_pk_fma_f32 v[168:169], v[168:169], v[96:97], v[74:75] op_sel:[0,0,1] op_sel_hi:[1,1,1]\n"
"v_pk_fma_f32 v[170:171], v[170:171], v[98:99], v[74:75] op_sel:[0,0,1] op_sel_hi:[1,1,1]\n"
"v_exp_f32_e64 v56, v56 clamp\n"
"v_exp_f32_e64 v57, v57 clamp\n"
"v_exp_f32_e64 v58, v58 clamp\n"
"v_exp_f32_e64 v59, v59 clamp\n"
"buffer_load_dwordx4 v[208:211], v2, s[24:27], s98 offen offset:1024 sc1\n"
"v_max_f32 v168, v168, v75\n"
"v_max_f32 v169, v169, v75\n"
"v_max_f32 v170, v170, v75\n"
"v_max_f32 v171, v171, v75\n"
"v_pk_add_f32 v[168:169], v[168:169], v[56:57] op_sel:[0,0] op_sel_hi:[1,1]\n"
"v_pk_add_f32 v[170:171], v[170:171], v[58:59] op_sel:[0,0] op_sel_hi:[1,1]\n"
"v_cvt_pk_bf16_f32 v44, v168, v169\n"
"v_cvt_pk_bf16_f32 v45, v170, v171\n"
"v_pk_fma_f32 v[164:165], v[80:81], v[114:115], v[164:165] op_sel:[0,0,0] op_sel_hi:[1,0,1] neg_lo:[1,0,0] neg_hi:[1,0,0]\n"
"v_pk_fma_f32 v[166:167], v[82:83], v[114:115], v[166:167] op_sel:[0,0,0] op_sel_hi:[1,0,1] neg_lo:[1,0,0] neg_hi:[1,0,0]\n"
"v_pk_mul_f32 v[52:53], v[164:165], v[104:105] op_sel:[0,0] op_sel_hi:[1,1]\n"
"v_pk_mul_f32 v[54:55], v[166:167], v[106:107] op_sel:[0,0] op_sel_hi:[1,1]\n"
"v_pk_fma_f32 v[164:165], v[164:165], v[92:93], v[74:75] op_sel:[0,0,1] op_sel_hi:[1,1,1]\n"
"v_pk_fma_f32 v[166:167], v[166:167], v[94:95], v[74:75] op_sel:[0,0,1] op_sel_hi:[1,1,1]\n"
"v_exp_f32_e64 v52, v52 clamp\n"
"v_exp_f32_e64 v53, v53 clamp\n"
"v_exp_f32_e64 v54, v54 clamp\n"
"v_exp_f32_e64 v55, v55 clamp\n"
"buffer_load_dwordx4 v[204:207], v2, s[24:27], s98 offen offset:2048 sc1\n"
"v_max_f32 v164, v164, v75\n"
"v_max_f32 v165, v165, v75\n"
"v_max_f32 v166, v166, v75\n"
"v_max_f32 v167, v167, v75\n"
"v_pk_add_f32 v[164:165], v[164:165], v[52:53] op_sel:[0,0] op_sel_hi:[1,1]\n"
"v_pk_add_f32 v[166:167], v[166:167], v[54:55] op_sel:[0,0] op_sel_hi:[1,1]\n"
"v_cvt_pk_bf16_f32 v42, v164, v165\n"
"v_cvt_pk_bf16_f32 v43, v166, v167\n"
"v_pk_fma_f32 v[160:161], v[76:77], v[114:115], v[160:161] op_sel:[0,0,0] op_sel_hi:[1,0,1] neg_lo:[1,0,0] neg_hi:[1,0,0]\n"
"v_pk_fma_f32 v[162:163], v[78:79], v[114:115], v[162:163] op_sel:[0,0,0] op_sel_hi:[1,0,1] neg_lo:[1,0,0] neg_hi:[1,0,0]\n"
"v_pk_mul_f32 v[48:49], v[160:161], v[100:101] op_sel:[0,0] op_sel_hi:[1,1]\n"
"v_pk_mul_f32 v[50:51], v[162:163], v[102:103] op_sel:[0,0] op_sel_hi:[1,1]\n"
"v_pk_fma_f32 v[160:161], v[160:161], v[88:89], v[74:75] op_sel:[0,0,1] op_sel_hi:[1,1,1]\n"
"v_pk_fma_f32 v[162:163], v[162:163], v[90:91], v[74:75] op_sel:[0,0,1] op_sel_hi:[1,1,1]\n"
"v_exp_f32_e64 v48, v48 clamp\n"
"v_exp_f32_e64 v49, v49 clamp\n"
"v_exp_f32_e64 v50, v50 clamp\n"
"v_exp_f32_e64 v51, v51 clamp\n"
"buffer_load_dwordx4 v[200:203], v2, s[24:27], s98 offen offset:3072 sc1\n"
"v_max_f32 v160, v160, v75\n"
"v_max_f32 v161, v161, v75\n"
"v_max_f32 v162, v162, v75\n"
"v_max_f32 v163, v163, v75\n"
"v_pk_add_f32 v[160:161], v[160:161], v[48:49] op_sel:[0,0] op_sel_hi:[1,1]\n"
"v_pk_add_f32 v[162:163], v[162:163], v[50:51] op_sel:[0,0] op_sel_hi:[1,1]\n"
"v_cvt_pk_bf16_f32 v40, v160, v161\n"
"v_cvt_pk_bf16_f32 v41, v162, v163\n"
"ds_write_b128 v12, v[40:43] offset:13312\n"
"ds_write_b64 v13, v[44:45] offset:13376\n"
"v_pk_fma_f32 v[156:157], v[84:85], v[112:113], v[156:157] op_sel:[0,1,0] op_sel_hi:[1,1,1] neg_lo:[1,0,0] neg_hi:[1,0,0]\n"
"v_pk_fma_f32 v[158:159], v[86:87], v[112:113], v[158:159] op_sel:[0,1,0] op_sel_hi:[1,1,1] neg_lo:[1,0,0] neg_hi:[1,0,0]\n"
"v_pk_mul_f32 v[56:57], v[156:157], v[108:109] op_sel:[0,0] op_sel_hi:[1,1]\n"
"v_pk_mul_f32 v[58:59], v[158:159], v[110:111] op_sel:[0,0] op_sel_hi:[1,1]\n"
"v_pk_fma_f32 v[156:157], v[156:157], v[96:97], v[74:75] op_sel:[0,0,1] op_sel_hi:[1,1,1]\n"
"v_pk_fma_f32 v[158:159], v[158:159], v[98:99], v[74:75] op_sel:[0,0,1] op_sel_hi:[1,1,1]\n"
"v_exp_f32_e64 v56, v56 clamp\n"
"v_exp_f32_e64 v57, v57 clamp\n"
"v_exp_f32_e64 v58, v58 clamp\n"
"v_exp_f32_e64 v59, v59 clamp\n"
"v_max_f32 v156, v156, v75\n"
"v_max_f32 v157, v157, v75\n"
"v_max_f32 v158, v158, v75\n"
"v_max_f32 v159, v159, v75\n"
"v_pk_add_f32 v[156:157], v[156:157], v[56:57] op_sel:[0,0] op_sel_hi:[1,1]\n"
"v_pk_add_f32 v[158:159], v[158:159], v[58:59] op_sel:[0,0] op_sel_hi:[1,1]\n"
"v_cvt_pk_bf16_f32 v36, v156, v157\n"
"v_cvt_pk_bf16_f32 v37, v158, v159\n"
"v_pk_fma_f32 v[152:153], v[80:81], v[112:113], v[152:153] op_sel:[0,1,0] op_sel_hi:[1,1,1] neg_lo:[1,0,0] neg_hi:[1,0,0]\n"
"v_pk_fma_f32 v[154:155], v[82:83], v[112:113], v[154:155] op_sel:[0,1,0] op_sel_hi:[1,1,1] neg_lo:[1,0,0] neg_hi:[1,0,0]\n"
"v_pk_mul_f32 v[52:53], v[152:153], v[104:105] op_sel:[0,0] op_sel_hi:[1,1]\n"
"v_pk_mul_f32 v[54:55], v[154:155], v[106:107] op_sel:[0,0] op_sel_hi:[1,1]\n"
"v_pk_fma_f32 v[152:153], v[152:153], v[92:93], v[74:75] op_sel:[0,0,1] op_sel_hi:[1,1,1]\n"
"v_pk_fma_f32 v[154:155], v[154:155], v[94:95], v[74:75] op_sel:[0,0,1] op_sel_hi:[1,1,1]\n"
"v_exp_f32_e64 v52, v52 clamp\n"
"v_exp_f32_e64 v53, v53 clamp\n"
"v_exp_f32_e64 v54, v54 clamp\n"
"v_exp_f32_e64 v55, v55 clamp\n"
"v_max_f32 v152, v152, v75\n"
"v_max_f32 v153, v153, v75\n"
"v_max_f32 v154, v154, v75\n"
"v_max_f32 v155, v155, v75\n"
"v_pk_add_f32 v[152:153], v[152:153], v[52:53] op_sel:[0,0] op_sel_hi:[1,1]\n"
"v_pk_add_f32 v[154:155], v[154:155], v[54:55] op_sel:[0,0] op_sel_hi:[1,1]\n"
"v_cvt_pk_bf16_f32 v34, v152, v153\n"
"v_cvt_pk_bf16_f32 v35, v154, v155\n"
"v_pk_fma_f32 v[148:149], v[76:77], v[112:113], v[148:149] op_sel:[0,1,0] op_sel_hi:[1,1,1] neg_lo:[1,0,0] neg_hi:[1,0,0]\n"
"v_pk_fma_f32 v[150:151], v[78:79], v[112:113], v[150:151] op_sel:[0,1,0] op_sel_hi:[1,1,1] neg_lo:[1,0,0] neg_hi:[1,0,0]\n"
"v_pk_mul_f32 v[48:49], v[148:149], v[100:101] op_sel:[0,0] op_sel_hi:[1,1]\n"
"v_pk_mul_f32 v[50:51], v[150:151], v[102:103] op_sel:[0,0] op_sel_hi:[1,1]\n"
"v_pk_fma_f32 v[148:149], v[148:149], v[88:89], v[74:75] op_sel:[0,0,1] op_sel_hi:[1,1,1]\n"
"v_pk_fma_f32 v[150:151], v[150:151], v[90:91], v[74:75] op_sel:[0,0,1] op_sel_hi:[1,1,1]\n"
"v_exp_f32_e64 v48, v48 clamp\n"
"v_exp_f32_e64 v49, v49 clamp\n"
"v_exp_f32_e64 v50, v50 clamp\n"
"v_exp_f32_e64 v51, v51 clamp\n"
"v_max_f32 v148, v148, v75\n"
"v_max_f32 v149, v149, v75\n"
"v_max_f32 v150, v150, v75\n"
"v_max_f32 v151, v151, v75\n"
"v_pk_add_f32 v[148:149], v[148:149], v[48:49] op_sel:[0,0] op_sel_hi:[1,1]\n"
"v_pk_add_f32 v[150:151], v[150:151], v[50:51] op_sel:[0,0] op_sel_hi:[1,1]\n"
"v_cvt_pk_bf16_f32 v32, v148, v149\n"
"v_cvt_pk_bf16_f32 v33, v150, v151\n"
"ds_write_b128 v12, v[32:35] offset:6656\n"
"ds_write_b64 v13, v[36:37] offset:6720\n"
"s_barrier\n"
"v_pk_fma_f32 v[144:145], v[84:85], v[112:113], v[144:145] op_sel:[0,0,0] op_sel_hi:[1,0,1] neg_lo:[1,0,0] neg_hi:[1,0,0]\n"
"v_pk_fma_f32 v[146:147], v[86:87], v[112:113], v[146:147] op_sel:[0,0,0] op_sel_hi:[1,0,1] neg_lo:[1,0,0] neg_hi:[1,0,0]\n"
"v_pk_mul_f32 v[56:57], v[144:145], v[108:109] op_sel:[0,0] op_sel_hi:[1,1]\n"
"v_pk_mul_f32 v[58:59], v[146:147], v[110:111] op_sel:[0,0] op_sel_hi:[1,1]\n"
"v_pk_fma_f32 v[144:145], v[144:145], v[96:97], v[74:75] op_sel:[0,0,1] op_sel_hi:[1,1,1]\n"
"v_pk_fma_f32 v[146:147], v[146:147], v[98:99], v[74:75] op_sel:[0,0,1] op_sel_hi:[1,1,1]\n"
"v_exp_f32_e64 v56, v56 clamp\n"
"v_exp_f32_e64 v57, v57 clamp\n"
"v_exp_f32_e64 v58, v58 clamp\n"
"v_exp_f32_e64 v59, v59 clamp\n"
"v_max_f32 v144, v144, v75\n"
"v_max_f32 v145, v145, v75\n"
"v_max_f32 v146, v146, v75\n"
"v_max_f32 v147, v147, v75\n"
"v_pk_add_f32 v[144:145], v[144:145], v[56:57] op_sel:[0,0] op_sel_hi:[1,1]\n"
"v_pk_add_f32 v[146:147], v[146:147], v[58:59] op_sel:[0,0] op_sel_hi:[1,1]\n"
"v_cvt_pk_bf16_f32 v44, v144, v145\n"
"v_cvt_pk_bf16_f32 v45, v146, v147\n"
"v_pk_fma_f32 v[140:141], v[80:81], v[112:113], v[140:141] op_sel:[0,0,0] op_sel_hi:[1,0,1] neg_lo:[1,0,0] neg_hi:[1,0,0]\n"
"v_pk_fma_f32 v[142:143], v[82:83], v[112:113], v[142:143] op_sel:[0,0,0] op_sel_hi:[1,0,1] neg_lo:[1,0,0] neg_hi:[1,0,0]\n"
"v_pk_mul_f32 v[52:53], v[140:141], v[104:105] op_sel:[0,0] op_sel_hi:[1,1]\n"
"v_pk_mul_f32 v[54:55], v[142:143], v[106:107] op_sel:[0,0] op_sel_hi:[1,1]\n"
"v_pk_fma_f32 v[140:141], v[140:141], v[92:93], v[74:75] op_sel:[0,0,1] op_sel_hi:[1,1,1]\n"
"v_pk_fma_f32 v[142:143], v[142:143], v[94:95], v[74:75] op_sel:[0,0,1] op_sel_hi:[1,1,1]\n"
"v_exp_f32_e64 v52, v52 clamp\n"
"v_exp_f32_e64 v53, v53 clamp\n"
"v_exp_f32_e64 v54, v54 clamp\n"
"v_exp_f32_e64 v55, v55 clamp\n"
"v_max_f32 v140, v140, v75\n"
"v_max_f32 v141, v141, v75\n"
"v_max_f32 v142, v142, v75\n"
"v_max_f32 v143, v143, v75\n"
"v_pk_add_f32 v[140:141], v[140:141], v[52:53] op_sel:[0,0] op_sel_hi:[1,1]\n"
"v_pk_add_f32 v[142:143], v[142:143], v[54:55] op_sel:[0,0] op_sel_hi:[1,1]\n"
"v_cvt_pk_bf16_f32 v42, v140, v141\n"
"v_cvt_pk_bf16_f32 v43, v142, v143\n"
"v_pk_fma_f32 v[136:137], v[76:77], v[112:113], v[136:137] op_sel:[0,0,0] op_sel_hi:[1,0,1] neg_lo:[1,0,0] neg_hi:[1,0,0]\n"
"v_pk_fma_f32 v[138:139], v[78:79], v[112:113], v[138:139] op_sel:[0,0,0] op_sel_hi:[1,0,1] neg_lo:[1,0,0] neg_hi:[1,0,0]\n"
"v_pk_mul_f32 v[48:49], v[136:137], v[100:101] op_sel:[0,0] op_sel_hi:[1,1]\n"
"v_pk_mul_f32 v[50:51], v[138:139], v[102:103] op_sel:[0,0] op_sel_hi:[1,1]\n"
"v_pk_fma_f32 v[136:137], v[136:137], v[88:89], v[74:75] op_sel:[0,0,1] op_sel_hi:[1,1,1]\n"
"v_pk_fma_f32 v[138:139], v[138:139], v[90:91], v[74:75] op_sel:[0,0,1] op_sel_hi:[1,1,1]\n"
"v_exp_f32_e64 v48, v48 clamp\n"
"v_exp_f32_e64 v49, v49 clamp\n"
"v_exp_f32_e64 v50, v50 clamp\n"
"v_exp_f32_e64 v51, v51 clamp\n"
"v_max_f32 v136, v136, v75\n"
"v_max_f32 v137, v137, v75\n"
"v_max_f32 v138, v138, v75\n"
"v_max_f32 v139, v139, v75\n"
"v_pk_add_f32 v[136:137], v[136:137], v[48:49] op_sel:[0,0] op_sel_hi:[1,1]\n"
"v_pk_add_f32 v[138:139], v[138:139], v[50:51] op_sel:[0,0] op_sel_hi:[1,1]\n"
"v_cvt_pk_bf16_f32 v40, v136, v137\n"
"v_cvt_pk_bf16_f32 v41, v138, v139\n"
"ds_read_b64_tr_b16 v[16:17], v15 offset:0\n"
"ds_read_b64_tr_b16 v[18:19], v15 offset:1280\n"
"ds_read_b64_tr_b16 v[20:21], v15 offset:5120\n"
"ds_read_b64_tr_b16 v[22:23], v15 offset:6400\n"
"ds_read_b64_tr_b16 v[24:25], v15 offset:32\n"
"ds_read_b64_tr_b16 v[26:27], v15 offset:1312\n"
"ds_read_b64_tr_b16 v[28:29], v15 offset:5152\n"
"ds_read_b64_tr_b16 v[30:31], v15 offset:6432\n"
"ds_write_b128 v12, v[40:43] offset:0\n"
"ds_write_b64 v13, v[44:45] offset:64\n"
"s_waitcnt vmcnt(14) lgkmcnt(0)\n"
"s_barrier\n"
"ds_read_b128 v[136:139], v68 offset:0\n"
"ds_read_b128 v[144:147], v68 offset:33280\n"
"ds_read_b128 v[140:143], v69 offset:6656\n"
"ds_read_b128 v[148:151], v70 offset:0\n"
"ds_read_b128 v[152:155], v68 offset:64\n"
"ds_read_b128 v[160:163], v68 offset:33344\n"
"ds_read_b128 v[156:159], v69 offset:6720\n"
"ds_read_b128 v[164:167], v70 offset:64\n"
"v_mfma_f32_16x16x32_bf16 v[76:79], v[16:19], a[0:3], 0\n"
"ds_read_b64_tr_b16 v[32:33], v15 offset:64\n"
"v_mfma_f32_16x16x32_bf16 v[76:79], v[20:23], a[12:15], v[76:79]\n"
"ds_read_b64_tr_b16 v[34:35], v15 offset:1344\n"
"v_mfma_f32_16x16x32_bf16 v[96:99], v[16:19], a[4:7], 0\n"
"ds_read_b128 v[168:171], v68 offset:128\n"
"ds_read_b128 v[176:179], v68 offset:33408\n"
"v_mfma_f32_16x16x32_bf16 v[96:99], v[20:23], a[16:19], v[96:99]\n"
"ds_read_b64_tr_b16 v[36:37], v15 offset:5184\n"
"ds_read_b128 v[172:175], v69 offset:6784\n"
"v_mfma_f32_16x16x32_bf16 v[116:119], v[16:19], a[8:11], 0\n"
"ds_read_b64_tr_b16 v[38:39], v15 offset:6464\n"
"ds_read_b128 v[180:183], v70 offset:128\n"
"v_mfma_f32_16x16x32_bf16 v[116:119], v[20:23], a[20:23], v[116:119]\n"
"ds_read_b128 v[184:187], v68 offset:192\n"
"v_mfma_f32_16x16x32_bf16 v[80:83], v[24:27], a[0:3], 0\n"
"ds_read_b64_tr_b16 v[40:41], v15 offset:96\n"
"ds_read_b128 v[192:195], v68 offset:33472\n"
"v_mfma_f32_16x16x32_bf16 v[80:83], v[28:31], a[12:15], v[80:83]\n"
"ds_read_b64_tr_b16 v[42:43], v15 offset:1376\n"
"ds_read_b128 v[188:191], v69 offset:6848\n"
"v_mfma_f32_16x16x32_bf16 v[100:103], v[24:27], a[4:7], 0\n"
"ds_read_b128 v[196:199], v70 offset:192\n"
"v_mfma_f32_16x16x32_bf16 v[100:103], v[28:31], a[16:19], v[100:103]\n"
"ds_read_b64_tr_b16 v[44:45], v15 offset:5216\n"
"v_mfma_f32_16x16x32_bf16 v[120:123], v[24:27], a[8:11], 0\n"
"ds_read_b64_tr_b16 v[46:47], v15 offset:6496\n"
"ds_read_b64_tr_b16 v[16:17], v15 offset:128\n"
"v_mfma_f32_16x16x32_bf16 v[120:123], v[28:31], a[20:23], v[120:123]\n"
"ds_read_b64_tr_b16 v[18:19], v15 offset:1408\n"
"s_waitcnt lgkmcnt(15)\n"
"v_mfma_f32_16x16x32_bf16 v[48:51], v[136:139], v[144:147], 0\n"
"s_cmp_ge_u32 s3, 2\n"
"s_cselect_b32 s69, 7, 6\n"
"v_mfma_f32_16x16x32_bf16 v[52:55], v[140:143], v[144:147], 0\n"
"s_add_u32 s69, s69, s32\n"
"v_mfma_f32_16x16x32_bf16 v[56:59], v[136:139], v[148:151], 0\n"
"s_cmp_ge_u32 s69, 10\n"
"s_cselect_b32 s33, 10, 0\n"
"v_mfma_f32_16x16x32_bf16 v[60:63], v[140:143], v[148:151], 0\n"
"s_sub_u32 s69, s69, s33\n"
"s_mul_i32 s69, s69, 245760\n"
"v_mfma_f32_16x16x32_bf16 v[48:51], v[152:155], v[160:163], v[48:51]\n"
"ds_read_b128 v[136:139], v68 offset:256\n"
"ds_read_b128 v[144:147], v68 offset:33536\n"
"v_mfma_f32_16x16x32_bf16 v[52:55], v[156:159], v[160:163], v[52:55]\n"
"ds_read_b128 v[140:143], v69 offset:6912\n"
"ds_read_b128 v[148:151], v70 offset:256\n"
"v_mfma_f32_16x16x32_bf16 v[56:59], v[152:155], v[164:167], v[56:59]\n"
"s_add_u32 s69, s69, s14\n"
"s_add_u32 m0, s71, 20480\n"
"v_mfma_f32_16x16x32_bf16 v[60:63], v[156:159], v[164:167], v[60:63]\n"
"buffer_load_dwordx4 v5, s[8:11], s69 offen offset:0 lds\n"
"s_waitcnt lgkmcnt(15)\n"
"v_mfma_f32_16x16x32_bf16 v[84:87], v[32:35], a[0:3], 0\n"
"ds_read_b64_tr_b16 v[20:21], v15 offset:5248\n"
"ds_read_b128 v[152:155], v68 offset:320\n"
"v_mfma_f32_16x16x32_bf16 v[84:87], v[36:39], a[12:15], v[84:87]\n"
"ds_read_b64_tr_b16 v[22:23], v15 offset:6528\n"
"ds_read_b128 v[160:163], v68 offset:33600\n"
"v_mfma_f32_16x16x32_bf16 v[104:107], v[32:35], a[4:7], 0\n"
"ds_read_b128 v[156:159], v69 offset:6976\n"
"ds_read_b128 v[164:167], v70 offset:320\n"
"v_mfma_f32_16x16x32_bf16 v[104:107], v[36:39], a[16:19], v[104:107]\n"
"ds_read_b64_tr_b16 v[24:25], v15 offset:10240\n"
"buffer_load_dwordx4 v6, s[8:11], s69 offen offset:1024 lds\n"
"v_mfma_f32_16x16x32_bf16 v[124:127], v[32:35], a[8:11], 0\n"
"ds_read_b64_tr_b16 v[26:27], v15 offset:11520\n"
"v_and_b32 v10, 15, v1\n"
"v_mfma_f32_16x16x32_bf16 v[124:127], v[36:39], a[20:23], v[124:127]\n"
"v_bfe_u32 v12, v1, 2, 2\n"
"s_waitcnt lgkmcnt(15)\n"
"v_mfma_f32_16x16x32_bf16 v[48:51], v[168:171], v[176:179], v[48:51]\n"
"v_lshl_add_u32 v12, v12, 2, v10\n"
"v_lshl_add_u32 v12, v12, 2, s12\n"
"v_mfma_f32_16x16x32_bf16 v[52:55], v[172:175], v[176:179], v[52:55]\n"
"v_mfma_f32_16x16x32_bf16 v[56:59], v[168:171], v[180:183], v[56:59]\n"
"v_mfma_f32_16x16x32_bf16 v[60:63], v[172:175], v[180:183], v[60:63]\n"
"v_mfma_f32_16x16x32_bf16 v[48:51], v[184:187], v[192:195], v[48:51]\n"
"buffer_load_dwordx4 v7, s[8:11], s69 offen offset:2048 lds\n"
"v_mfma_f32_16x16x32_bf16 v[52:55], v[188:191], v[192:195], v[52:55]\n"
"v_mfma_f32_16x16x32_bf16 v[56:59], v[184:187], v[196:199], v[56:59]\n"
"v_mfma_f32_16x16x32_bf16 v[60:63], v[188:191], v[196:199], v[60:63]\n"
"s_waitcnt lgkmcnt(14)\n"
"v_mfma_f32_16x16x32_bf16 v[88:91], v[40:43], a[0:3], 0\n"
"ds_read_b64_tr_b16 v[28:29], v15 offset:15360\n"
"v_mfma_f32_16x16x32_bf16 v[88:91], v[44:47], a[12:15], v[88:91]\n"
"ds_read_b64_tr_b16 v[30:31], v15 offset:16640\n"
"v_mfma_f32_16x16x32_bf16 v[108:111], v[40:43], a[4:7], 0\n"
"v_mfma_f32_16x16x32_bf16 v[108:111], v[44:47], a[16:19], v[108:111]\n"
"ds_read_b64_tr_b16 v[32:33], v15 offset:10272\n"
"buffer_load_dwordx4 v8, s[8:11], s69 offen offset:3072 lds\n"
"v_mfma_f32_16x16x32_bf16 v[128:131], v[40:43], a[8:11], 0\n"
"ds_read_b64_tr_b16 v[34:35], v15 offset:11552\n"
"v_mfma_f32_16x16x32_bf16 v[128:131], v[44:47], a[20:23], v[128:131]\n"
"s_waitcnt lgkmcnt(6)\n"
"v_mfma_f32_16x16x32_bf16 v[48:51], v[136:139], v[144:147], v[48:51]\n"
"v_mfma_f32_16x16x32_bf16 v[52:55], v[140:143], v[144:147], v[52:55]\n"
"v_mfma_f32_16x16x32_bf16 v[56:59], v[136:139], v[148:151], v[56:59]\n"
"s_add_u32 m0, s71, 24576\n"
"v_mfma_f32_16x16x32_bf16 v[60:63], v[140:143], v[148:151], v[60:63]\n"
"v_mfma_f32_16x16x32_bf16 v[48:51], v[152:155], v[160:163], v[48:51]\n"
"buffer_load_dwordx4 v9, s[8:11], s69 offen offset:0 lds\n"
"v_mfma_f32_16x16x32_bf16 v[52:55], v[156:159], v[160:163], v[52:55]\n"
"v_mfma_f32_16x16x32_bf16 v[56:59], v[152:155], v[164:167], v[56:59]\n"
"v_mfma_f32_16x16x32_bf16 v[60:63], v[156:159], v[164:167], v[60:63]\n"
"v_mfma_f32_16x16x32_bf16 v[92:95], v[16:19], a[0:3], 0\n"
"ds_read_b64_tr_b16 v[36:37], v15 offset:15392\n"
"s_nop 1\n"
"v_cvt_pk_bf16_f32 v48, v48, v49\n"
"v_mfma_f32_16x16x32_bf16 v[92:95], v[20:23], a[12:15], v[92:95]\n"
"ds_read_b64_tr_b16 v[38:39], v15 offset:16672\n"
"v_cvt_pk_bf16_f32 v49, v50, v51\n"
"v_mfma_f32_16x16x32_bf16 v[112:115], v[16:19], a[4:7], 0\n"
"buffer_store_dwordx2 v[48:49], v67, s[16:19], s75 offen offset:0 sc1\n"
"v_mfma_f32_16x16x32_bf16 v[112:115], v[20:23], a[16:19], v[112:115]\n"
"ds_read_b64_tr_b16 v[40:41], v15 offset:10304\n"
"ds_read_b64_tr_b16 v[42:43], v15 offset:11584\n"
"v_cvt_pk_bf16_f32 v52, v52, v53\n"
"v_mfma_f32_16x16x32_bf16 v[132:135], v[16:19], a[8:11], 0\n"
"ds_read2_b32 v[136:137], v12 offset0:160 offset1:164\n"
"v_cvt_pk_bf16_f32 v53, v54, v55\n"
"v_mfma_f32_16x16x32_bf16 v[132:135], v[20:23], a[20:23], v[132:135]\n"
"buffer_store_dwordx2 v[52:53], v71, s[16:19], s75 offen offset:512 sc1\n"
"s_waitcnt lgkmcnt(7)\n"
"v_mfma_f32_16x16x32_bf16 v[76:79], v[24:27], a[24:27], v[76:79]\n"
"ds_read_b64_tr_b16 v[44:45], v15 offset:15424\n"
"ds_read_b64_tr_b16 v[46:47], v15 offset:16704\n"
"v_cvt_pk_bf16_f32 v56, v56, v57\n"
"v_mfma_f32_16x16x32_bf16 v[76:79], v[28:31], a[36:39], v[76:79]\n"
"ds_read_b64_tr_b16 v[16:17], v15 offset:10336\n"
"ds_read_b64_tr_b16 v[18:19], v15 offset:11616\n"
"v_cvt_pk_bf16_f32 v57, v58, v59\n"
"v_mfma_f32_16x16x32_bf16 v[96:99], v[24:27], a[28:31], v[96:99]\n"
"ds_read_b64_tr_b16 v[20:21], v15 offset:15456\n"
"ds_read_b64_tr_b16 v[22:23], v15 offset:16736\n"
"buffer_store_dwordx2 v[56:57], v72, s[16:19], s75 offen offset:640 sc1\n"
"v_mfma_f32_16x16x32_bf16 v[96:99], v[28:31], a[40:43], v[96:99]\n"
"ds_read2_b32 v[140:141], v12 offset0:192 offset1:196\n"
"v_cvt_pk_bf16_f32 v60, v60, v61\n"
"v_mfma_f32_16x16x32_bf16 v[116:119], v[24:27], a[32:35], v[116:119]\n"
"ds_read2_b32 v[144:145], v12 offset0:224 offset1:228\n"
"v_cvt_pk_bf16_f32 v61, v62, v63\n"
"v_mfma_f32_16x16x32_bf16 v[116:119], v[28:31], a[44:47], v[116:119]\n"
"buffer_store_dwordx2 v[60:61], v73, s[16:19], s75 offen offset:800 sc1\n"
"s_waitcnt lgkmcnt(11)\n"
"v_mfma_f32_16x16x32_bf16 v[80:83], v[32:35], a[24:27], v[80:83]\n"
"ds_read_b64_tr_b16 v[24:25], v15 offset:10368\n"
"ds_read_b64_tr_b16 v[26:27], v15 offset:11648\n"
"v_mfma_f32_16x16x32_bf16 v[80:83], v[36:39], a[36:39], v[80:83]\n"
"ds_read_b64_tr_b16 v[28:29], v15 offset:15488\n"
"ds_read_b64_tr_b16 v[30:31], v15 offset:16768\n"
"v_mfma_f32_16x16x32_bf16 v[100:103], v[32:35], a[28:31], v[100:103]\n"
"s_add_u32 s98, s96, 8192\n"
"v_mfma_f32_16x16x32_bf16 v[100:103], v[36:39], a[40:43], v[100:103]\n"
"buffer_load_dwordx4 v[196:199], v2, s[24:27], s98 offen offset:0 sc1\n"
"v_mfma_f32_16x16x32_bf16 v[120:123], v[32:35], a[32:35], v[120:123]\n"
"v_mfma_f32_16x16x32_bf16 v[120:123], v[36:39], a[44:47], v[120:123]\n"
"s_waitcnt lgkmcnt(10)\n"
"v_mfma_f32_16x16x32_bf16 v[84:87], v[40:43], a[24:27], v[84:87]\n"
"buffer_load_dwordx4 v[192:195], v2, s[24:27], s98 offen offset:1024 sc1\n"
"v_mfma_f32_16x16x32_bf16 v[84:87], v[44:47], a[36:39], v[84:87]\n"
"v_mfma_f32_16x16x32_bf16 v[104:107], v[40:43], a[28:31], v[104:107]\n"
"v_mfma_f32_16x16x32_bf16 v[104:107], v[44:47], a[40:43], v[104:107]\n"
"v_mfma_f32_16x16x32_bf16 v[124:127], v[40:43], a[32:35], v[124:127]\n"
"buffer_load_dwordx4 v[188:191], v2, s[24:27], s98 offen offset:2048 sc1\n"
"v_mfma_f32_16x16x32_bf16 v[124:127], v[44:47], a[44:47], v[124:127]\n"
"s_waitcnt lgkmcnt(0)\n"
"s_barrier\n"
"v_mfma_f32_16x16x32_bf16 v[88:91], v[16:19], a[24:27], v[88:91]\n"
"ds_read_b64_tr_b16 v[32:33], v15 offset:20480\n"
"v_mfma_f32_16x16x32_bf16 v[88:91], v[20:23], a[36:39], v[88:91]\n"
"ds_read_b64_tr_b16 v[34:35], v15 offset:21760\n"
"v_mfma_f32_16x16x32_bf16 v[108:111], v[16:19], a[28:31], v[108:111]\n"
"v_lshrrev_b32 v11, 5, v1\n"
"v_lshl_add_u32 v11, v11, 7, s59\n"
"v_mfma_f32_16x16x32_bf16 v[108:111], v[20:23], a[40:43], v[108:111]\n"
"ds_read_b64_tr_b16 v[36:37], v15 offset:25600\n"
"buffer_load_dword v13, v11, s[16:19], 0 offen sc1\n"
"v_mfma_f32_16x16x32_bf16 v[128:131], v[16:19], a[32:35], v[128:131]\n"
"ds_read_b64_tr_b16 v[38:39], v15 offset:26880\n"
"v_mfma_f32_16x16x32_bf16 v[128:131], v[20:23], a[44:47], v[128:131]\n"
"v_mfma_f32_16x16x32_bf16 v[92:95], v[24:27], a[24:27], v[92:95]\n"
"ds_read_b64_tr_b16 v[40:41], v15 offset:20512\n"
"v_mfma_f32_16x16x32_bf16 v[92:95], v[28:31], a[36:39], v[92:95]\n"
"ds_read_b64_tr_b16 v[42:43], v15 offset:21792\n"
"v_mfma_f32_16x16x32_bf16 v[112:115], v[24:27], a[28:31], v[112:115]\n"
"s_cmp_ge_u32 s3, 2\n"
"s_cselect_b32 s69, 9, 8\n"
"v_mfma_f32_16x16x32_bf16 v[112:115], v[28:31], a[40:43], v[112:115]\n"
"ds_read_b64_tr_b16 v[44:45], v15 offset:25632\n"
"s_add_u32 s69, s69, s32\n"
"v_mfma_f32_16x16x32_bf16 v[132:135], v[24:27], a[32:35], v[132:135]\n"
"ds_read_b64_tr_b16 v[46:47], v15 offset:26912\n"
"ds_read_b64_tr_b16 v[16:17], v15 offset:20544\n"
"v_mfma_f32_16x16x32_bf16 v[132:135], v[28:31], a[44:47], v[132:135]\n"
"ds_read_b64_tr_b16 v[18:19], v15 offset:21824\n"
"s_waitcnt lgkmcnt(6)\n"
"v_mfma_f32_16x16x32_bf16 v[76:79], v[32:35], a[48:51], v[76:79]\n"
"ds_read_b64_tr_b16 v[20:21], v15 offset:25664\n"
"v_mfma_f32_16x16x32_bf16 v[76:79], v[36:39], a[60:63], v[76:79]\n"
"ds_read_b64_tr_b16 v[22:23], v15 offset:26944\n"
"v_mfma_f32_16x16x32_bf16 v[96:99], v[32:35], a[52:55], v[96:99]\n"
"s_cmp_ge_u32 s69, 10\n"
"s_cselect_b32 s33, 10, 0\n"
"v_mfma_f32_16x16x32_bf16 v[96:99], v[36:39], a[64:67], v[96:99]\n"
"ds_read_b64_tr_b16 v[24:25], v15 offset:20576\n"
"s_sub_u32 s69, s69, s33\n"
"v_mfma_f32_16x16x32_bf16 v[116:119], v[32:35], a[56:59], v[116:119]\n"
"ds_read_b64_tr_b16 v[26:27], v15 offset:21856\n"
"s_mul_i32 s69, s69, 245760\n"
"v_mfma_f32_16x16x32_bf16 v[116:119], v[36:39], a[68:71], v[116:119]\n"
"s_waitcnt lgkmcnt(6)\n"
"s_add_u32 s69, s69, s14\n"
"v_mfma_f32_16x16x32_bf16 v[80:83], v[40:43], a[48:51], v[80:83]\n"
"ds_read_b64_tr_b16 v[28:29], v15 offset:25696\n"
"s_add_u32 m0, s71, 40960\n"
"v_mfma_f32_16x16x32_bf16 v[80:83], v[44:47], a[60:63], v[80:83]\n"
"ds_read_b64_tr_b16 v[30:31], v15 offset:26976\n"
"buffer_load_dwordx4 v5, s[8:11], s69 offen offset:0 lds\n"
"v_mfma_f32_16x16x32_bf16 v[100:103], v[40:43], a[52:55], v[100:103]\n"
"v_mfma_f32_16x16x32_bf16 v[100:103], v[44:47], a[64:67], v[100:103]\n"
"ds_read_b64_tr_b16 v[32:33], v15 offset:20608\n"
"s_add_u32 m0, s71, 40960\n"
"v_mfma_f32_16x16x32_bf16 v[120:123], v[40:43], a[56:59], v[120:123]\n"
"ds_read_b64_tr_b16 v[34:35], v15 offset:21888\n"
"v_mfma_f32_16x16x32_bf16 v[120:123], v[44:47], a[68:71], v[120:123]\n"
"buffer_load_dwordx4 v6, s[8:11], s69 offen offset:1024 lds\n"
"s_waitcnt lgkmcnt(6)\n"
"v_mfma_f32_16x16x32_bf16 v[84:87], v[16:19], a[48:51], v[84:87]\n"
"ds_read_b64_tr_b16 v[36:37], v15 offset:25728\n"
"v_mfma_f32_16x16x32_bf16 v[84:87], v[20:23], a[60:63], v[84:87]\n"
"ds_read_b64_tr_b16 v[38:39], v15 offset:27008\n"
"v_mfma_f32_16x16x32_bf16 v[104:107], v[16:19], a[52:55], v[104:107]\n"
"v_mfma_f32_16x16x32_bf16 v[104:107], v[20:23], a[64:67], v[104:107]\n"
"ds_read_b64_tr_b16 v[40:41], v15 offset:30720\n"
"v_mfma_f32_16x16x32_bf16 v[124:127], v[16:19], a[56:59], v[124:127]\n"
"ds_read_b64_tr_b16 v[42:43], v15 offset:32000\n"
"s_add_u32 m0, s71, 40960\n"
"v_mfma_f32_16x16x32_bf16 v[124:127], v[20:23], a[68:71], v[124:127]\n"
"s_waitcnt lgkmcnt(6)\n"
"v_mfma_f32_16x16x32_bf16 v[88:91], v[24:27], a[48:51], v[88:91]\n"
"ds_read_b64_tr_b16 v[44:45], v15 offset:35840\n"
"buffer_load_dwordx4 v7, s[8:11], s69 offen offset:2048 lds\n"
"v_mfma_f32_16x16x32_bf16 v[88:91], v[28:31], a[60:63], v[88:91]\n"
"ds_read_b64_tr_b16 v[46:47], v15 offset:37120\n"
"v_mfma_f32_16x16x32_bf16 v[108:111], v[24:27], a[52:55], v[108:111]\n"
"v_mfma_f32_16x16x32_bf16 v[108:111], v[28:31], a[64:67], v[108:111]\n"
"ds_read_b64_tr_b16 v[16:17], v15 offset:30752\n"
"v_mfma_f32_16x16x32_bf16 v[128:131], v[24:27], a[56:59], v[128:131]\n"
"ds_read_b64_tr_b16 v[18:19], v15 offset:32032\n"
"v_mfma_f32_16x16x32_bf16 v[128:131], v[28:31], a[68:71], v[128:131]\n"
"s_waitcnt lgkmcnt(6)\n"
"s_waitcnt vmcnt(7)\n"
"v_mfma_f32_16x16x32_bf16 v[92:95], v[32:35], a[48:51], v[92:95]\n"
"ds_read_b64_tr_b16 v[20:21], v15 offset:35872\n"
"v_mfma_f32_16x16x32_bf16 v[92:95], v[36:39], a[60:63], v[92:95]\n"
"ds_read_b64_tr_b16 v[22:23], v15 offset:37152\n"
"v_mfma_f32_16x16x32_bf16 v[112:115], v[32:35], a[52:55], v[112:115]\n"
"buffer_store_dword v74, v3, s[16:19], s57 offen sc1\n"
"v_mfma_f32_16x16x32_bf16 v[112:115], v[36:39], a[64:67], v[112:115]\n"
"ds_read_b64_tr_b16 v[24:25], v15 offset:30784\n"
"s_add_u32 m0, s71, 40960\n"
"v_mfma_f32_16x16x32_bf16 v[132:135], v[32:35], a[56:59], v[132:135]\n"
"ds_read_b64_tr_b16 v[26:27], v15 offset:32064\n"
"buffer_load_dwordx4 v8, s[8:11], s69 offen offset:3072 lds\n"
"v_mfma_f32_16x16x32_bf16 v[132:135], v[36:39], a[68:71], v[132:135]\n"
"s_waitcnt lgkmcnt(6)\n"
"v_mfma_f32_16x16x32_bf16 v[76:79], v[40:43], a[72:75], v[76:79]\n"
"ds_read_b64_tr_b16 v[28:29], v15 offset:35904\n"
"v_mfma_f32_16x16x32_bf16 v[76:79], v[44:47], a[84:87], v[76:79]\n"
"ds_read_b64_tr_b16 v[30:31], v15 offset:37184\n"
"s_add_u32 m0, s71, 45056\n"
"v_mfma_f32_16x16x32_bf16 v[96:99], v[40:43], a[76:79], v[96:99]\n"
"v_mfma_f32_16x16x32_bf16 v[96:99], v[44:47], a[88:91], v[96:99]\n"
"ds_read_b64_tr_b16 v[32:33], v15 offset:30816\n"
"buffer_load_dwordx4 v9, s[8:11], s69 offen offset:0 lds\n"
"v_mfma_f32_16x16x32_bf16 v[116:119], v[40:43], a[80:83], v[116:119]\n"
"ds_read_b64_tr_b16 v[34:35], v15 offset:32096\n"
"v_mfma_f32_16x16x32_bf16 v[116:119], v[44:47], a[92:95], v[116:119]\n"
"s_waitcnt lgkmcnt(6)\n"
"v_mfma_f32_16x16x32_bf16 v[80:83], v[16:19], a[72:75], v[80:83]\n"
"ds_read_b64_tr_b16 v[36:37], v15 offset:35936\n"
"v_mfma_f32_16x16x32_bf16 v[80:83], v[20:23], a[84:87], v[80:83]\n"
"ds_read_b64_tr_b16 v[38:39], v15 offset:37216\n"
"v_mov_b32 v148, 0\n"
"v_mfma_f32_16x16x32_bf16 v[100:103], v[16:19], a[76:79], v[100:103]\n"
"v_mov_b32 v149, 0\n"
"v_mov_b32 v150, 0\n"
"v_mfma_f32_16x16x32_bf16 v[100:103], v[20:23], a[88:91], v[100:103]\n"
"ds_read_b64_tr_b16 v[40:41], v15 offset:30848\n"
"v_mov_b32 v151, 0\n"
"v_mfma_f32_16x16x32_bf16 v[120:123], v[16:19], a[80:83], v[120:123]\n"
"ds_read_b64_tr_b16 v[42:43], v15 offset:32128\n"
"buffer_load_dwordx4 v[184:187], v2, s[24:27], s98 offen offset:3072 sc1\n"
"v_mfma_f32_16x16x32_bf16 v[120:123], v[20:23], a[92:95], v[120:123]\n"
"s_waitcnt lgkmcnt(6)\n"
"s_lshr_b32 s34, s2, 3\n"
"v_mfma_f32_16x16x32_bf16 v[84:87], v[24:27], a[72:75], v[84:87]\n"
"ds_read_b64_tr_b16 v[44:45], v15 offset:35968\n"
"s_sub_u32 s34, s34, 0\n"
"v_mfma_f32_16x16x32_bf16 v[84:87], v[28:31], a[84:87], v[84:87]\n"
"ds_read_b64_tr_b16 v[46:47], v15 offset:37248\n"
"s_lshl_b32 s34, s34, 4\n"
"v_mfma_f32_16x16x32_bf16 v[104:107], v[24:27], a[76:79], v[104:107]\n"
"v_lshrrev_b32 v68, 4, v0\n"
"v_add_lshl_u32 v68, v68, s34, 7\n"
"v_mfma_f32_16x16x32_bf16 v[104:107], v[28:31], a[88:91], v[104:107]\n"
"ds_read_b64_tr_b16 v[16:17], v14 offset:0\n"
"v_add_u32 v68, .Lfb_attq-.Lfb_pc, v68\n"
"v_mfma_f32_16x16x32_bf16 v[124:127], v[24:27], a[80:83], v[124:127]\n"
"ds_read_b64_tr_b16 v[18:19], v14 offset:1280\n"
"s_waitcnt vmcnt(7)\n"
"v_mfma_f32_16x16x32_bf16 v[124:127], v[28:31], a[92:95], v[124:127]\n"
"v_cmp_ne_u32 vcc, s52, v13\n"
"s_waitcnt lgkmcnt(6)\n"
"v_mfma_f32_16x16x32_bf16 v[88:91], v[32:35], a[72:75], v[88:91]\n"
"ds_read_b64_tr_b16 v[20:21], v14 offset:5120\n"
"s_cbranch_vccnz .Lfb_slow_34\n"
".Lfb_back_35:\n"
"v_mfma_f32_16x16x32_bf16 v[88:91], v[36:39], a[84:87], v[88:91]\n"
"ds_read_b64_tr_b16 v[22:23], v14 offset:6400\n"
"v_min_u32 v68, .Lfb_end-.Lfb_pc-4, v68\n"
"v_mfma_f32_16x16x32_bf16 v[108:111], v[32:35], a[76:79], v[108:111]\n"
"global_load_dword v69, v68, s[86:87]\n"
"s_add_u32 m0, s12, 4294934528\n"
"v_mfma_f32_16x16x32_bf16 v[108:111], v[36:39], a[88:91], v[108:111]\n"
"ds_read_b64_tr_b16 v[24:25], v14 offset:32\n"
"buffer_load_dwordx4 v2, s[16:19], s70 offen sc1 lds\n"
"v_mfma_f32_16x16x32_bf16 v[128:131], v[32:35], a[80:83], v[128:131]\n"
"ds_read_b64_tr_b16 v[26:27], v14 offset:1312\n"
"v_lshlrev_b32 v158, 4, v0\n"
"v_mfma_f32_16x16x32_bf16 v[128:131], v[36:39], a[92:95], v[128:131]\n"
"s_waitcnt lgkmcnt(6)\n"
"s_add_u32 s98, s96, 12288\n"
"v_mfma_f32_16x16x32_bf16 v[92:95], v[40:43], a[72:75], v[92:95]\n"
"ds_read_b64_tr_b16 v[28:29], v14 offset:5152\n"
"v_add_u32 v158, 144976, v158\n"
"v_mfma_f32_16x16x32_bf16 v[92:95], v[44:47], a[84:87], v[92:95]\n"
"ds_read_b64_tr_b16 v[30:31], v14 offset:6432\n"
"buffer_load_dwordx4 v[180:183], v2, s[24:27], s98 offen offset:0 sc1\n"
"v_mfma_f32_16x16x32_bf16 v[112:115], v[40:43], a[76:79], v[112:115]\n"
"ds_write_b128 v158, v[148:151] offset:0\n"
"v_mfma_f32_16x16x32_bf16 v[112:115], v[44:47], a[88:91], v[112:115]\n"
"ds_read_b64_tr_b16 v[32:33], v14 offset:64\n"
"v_min_u32 v10, 10, v1\n"
"v_mfma_f32_16x16x32_bf16 v[132:135], v[40:43], a[80:83], v[132:135]\n"
"ds_read_b64_tr_b16 v[34:35], v14 offset:1344\n"
"v_lshl_add_u32 v10, v10, 7, s73\n"
"v_mfma_f32_16x16x32_bf16 v[132:135], v[44:47], a[92:95], v[132:135]\n"
"v_and_b32 v159, 48, v1\n"
"s_waitcnt lgkmcnt(7)\n"
"v_mfma_f32_16x16x32_bf16 v[76:79], v[16:19], a[96:99], v[76:79]\n"
"ds_read_b64_tr_b16 v[36:37], v14 offset:5184\n"
"buffer_load_dwordx4 v[176:179], v2, s[24:27], s98 offen offset:1024 sc1\n"
"v_mfma_f32_16x16x32_bf16 v[76:79], v[20:23], a[108:111], v[76:79]\n"
"ds_read_b64_tr_b16 v[38:39], v14 offset:6464\n"
"s_mov_b32 s68, 0x3f80\n"
"v_mfma_f32_16x16x32_bf16 v[96:99], v[16:19], a[100:103], v[96:99]\n"
"ds_write_b128 v158, v[148:151] offset:4096\n"
"v_mfma_f32_16x16x32_bf16 v[96:99], v[20:23], a[112:115], v[96:99]\n"
"ds_read_b64_tr_b16 v[40:41], v14 offset:96\n"
"s_mov_b32 s72, 0x3f800000\n"
"v_mfma_f32_16x16x32_bf16 v[116:119], v[16:19], a[104:107], v[116:119]\n"
"ds_read_b64_tr_b16 v[42:43], v14 offset:1376\n"
"buffer_load_dwordx4 v[172:175], v2, s[24:27], s98 offen offset:2048 sc1\n"
"v_mfma_f32_16x16x32_bf16 v[116:119], v[20:23], a[116:119], v[116:119]\n"
"s_waitcnt lgkmcnt(8)\n"
"s_mul_i32 s60, s3, 5\n"
"v_mfma_f32_16x16x32_bf16 v[80:83], v[24:27], a[96:99], v[80:83]\n"
"ds_read_b64_tr_b16 v[44:45], v14 offset:5216\n"
"s_mul_i32 s61, s3, 20\n"
"v_mfma_f32_16x16x32_bf16 v[80:83], v[28:31], a[108:111], v[80:83]\n"
"ds_read_b64_tr_b16 v[46:47], v14 offset:6496\n"
"s_add_u32 s62, s3, 80\n"
"v_mfma_f32_16x16x32_bf16 v[100:103], v[24:27], a[100:103], v[100:103]\n"
"ds_write_b128 v158, v[148:151] offset:8192\n"
"v_mfma_f32_16x16x32_bf16 v[100:103], v[28:31], a[112:115], v[100:103]\n"
"ds_read_b64_tr_b16 v[16:17], v14 offset:128\n"
"buffer_load_dwordx4 v[168:171], v2, s[24:27], s98 offen offset:3072 sc1\n"
"v_mfma_f32_16x16x32_bf16 v[120:123], v[24:27], a[104:107], v[120:123]\n"
"ds_read_b64_tr_b16 v[18:19], v14 offset:1408\n"
"v_and_b32 v70, 15, v1\n"
"v_mfma_f32_16x16x32_bf16 v[120:123], v[28:31], a[116:119], v[120:123]\n"
"s_waitcnt lgkmcnt(8)\n"
"s_add_u32 s98, s96, 16384\n"
"v_mfma_f32_16x16x32_bf16 v[84:87], v[32:35], a[96:99], v[84:87]\n"
"ds_read_b64_tr_b16 v[20:21], v14 offset:5248\n"
"v_lshrrev_b32 v71, 4, v1\n"
"v_mfma_f32_16x16x32_bf16 v[84:87], v[36:39], a[108:111], v[84:87]\n"
"ds_read_b64_tr_b16 v[22:23], v14 offset:6528\n"
"buffer_load_dwordx4 v[164:167], v2, s[24:27], s98 offen offset:0 sc1\n"
"v_mfma_f32_16x16x32_bf16 v[104:107], v[32:35], a[100:103], v[104:107]\n"
"ds_write_b128 v158, v[148:151] offset:12288\n"
"v_mfma_f32_16x16x32_bf16 v[104:107], v[36:39], a[112:115], v[104:107]\n"
"ds_read_b64_tr_b16 v[24:25], v14 offset:10240\n"
"v_add_u32 v71, s60, v71\n"
"v_mfma_f32_16x16x32_bf16 v[124:127], v[32:35], a[104:107], v[124:127]\n"
"ds_read_b64_tr_b16 v[26:27], v14 offset:11520\n"
"v_lshrrev_b32 v156, 3, v71\n"
"v_mfma_f32_16x16x32_bf16 v[124:127], v[36:39], a[116:119], v[124:127]\n"
"buffer_load_dwordx4 v[160:163], v2, s[24:27], s98 offen offset:1024 sc1\n"
"s_waitcnt lgkmcnt(8)\n"
"v_mfma_f32_16x16x32_bf16 v[88:91], v[40:43], a[96:99], v[88:91]\n"
"ds_read_b64_tr_b16 v[28:29], v14 offset:15360\n"
"v_lshlrev_b32 v156, 6, v156\n"
"v_mfma_f32_16x16x32_bf16 v[88:91], v[44:47], a[108:111], v[88:91]\n"
"ds_read_b64_tr_b16 v[30:31], v14 offset:16640\n"
"v_and_b32 v72, 3, v71\n"
"v_mfma_f32_16x16x32_bf16 v[108:111], v[40:43], a[100:103], v[108:111]\n"
"ds_write_b128 v158, v[148:151] offset:14768\n"
"v_mfma_f32_16x16x32_bf16 v[108:111], v[44:47], a[112:115], v[108:111]\n"
"ds_read_b64_tr_b16 v[32:33], v14 offset:10272\n"
"v_lshl_add_u32 v158, v1, 3, s95\n"
"v_mfma_f32_16x16x32_bf16 v[128:131], v[40:43], a[104:107], v[128:131]\n"
"ds_read_b64_tr_b16 v[34:35], v14 offset:11552\n"
"buffer_load_dwordx4 a[0:3], v2, s[24:27], s98 offen offset:2048 sc1\n"
"v_mfma_f32_16x16x32_bf16 v[128:131], v[44:47], a[116:119], v[128:131]\n"
"v_mul_f32 v138, 1069066811, v137\n"
"s_waitcnt lgkmcnt(8)\n"
"v_mfma_f32_16x16x32_bf16 v[92:95], v[16:19], a[96:99], v[92:95]\n"
"ds_read_b64_tr_b16 v[36:37], v14 offset:15392\n"
"v_mul_f32 v142, 1069066811, v141\n"
"v_mfma_f32_16x16x32_bf16 v[92:95], v[20:23], a[108:111], v[92:95]\n"
"ds_read_b64_tr_b16 v[38:39], v14 offset:16672\n"
"v_mul_f32 v146, 1069066811, v145\n"
"v_mfma_f32_16x16x32_bf16 v[112:115], v[16:19], a[100:103], v[112:115]\n"
"buffer_load_dwordx4 a[4:7], v2, s[24:27], s98 offen offset:3072 sc1\n"
"v_lshl_add_u32 v156, v72, 4, v156\n"
"v_mfma_f32_16x16x32_bf16 v[112:115], v[20:23], a[112:115], v[112:115]\n"
"ds_read_b64_tr_b16 v[40:41], v14 offset:10304\n"
"v_bfe_u32 v72, v71, 2, 1\n"
"v_mfma_f32_16x16x32_bf16 v[132:135], v[16:19], a[104:107], v[132:135]\n"
"ds_read_b64_tr_b16 v[42:43], v14 offset:11584\n"
"v_lshl_add_u32 v156, v72, 3, v156\n"
"v_mfma_f32_16x16x32_bf16 v[132:135], v[20:23], a[116:119], v[132:135]\n"
"s_waitcnt lgkmcnt(7)\n"
"s_add_u32 s98, s97, 0\n"
"v_mfma_f32_16x16x32_bf16 v[76:79], v[24:27], a[120:123], v[76:79]\n"
"ds_read_b64_tr_b16 v[44:45], v14 offset:15424\n"
"v_cmp_gt_u32 vcc, 20, v70\n"
"v_mfma_f32_16x16x32_bf16 v[76:79], v[28:31], a[132:135], v[76:79]\n"
"ds_read_b64_tr_b16 v[46:47], v14 offset:16704\n"
"buffer_load_dwordx4 a[8:11], v2, s[24:27], s98 offen offset:0 sc1\n"
"v_mfma_f32_16x16x32_bf16 v[96:99], v[24:27], a[124:127], v[96:99]\n"
"v_add_u32 v70, s61, v70\n"
"v_mov_b32 v72, s62\n"
"v_mfma_f32_16x16x32_bf16 v[96:99], v[28:31], a[136:139], v[96:99]\n"
"ds_read_b64_tr_b16 v[16:17], v14 offset:10336\n"
"v_cndmask_b32 v70, v72, v70, vcc\n"
"v_mfma_f32_16x16x32_bf16 v[116:119], v[24:27], a[128:131], v[116:119]\n"
"ds_read_b64_tr_b16 v[18:19], v14 offset:11616\n"
"v_mul_u32_u24 v70, 224, v70\n"
"v_mfma_f32_16x16x32_bf16 v[116:119], v[28:31], a[140:143], v[116:119]\n"
"buffer_load_dwordx4 a[12:15], v2, s[24:27], s98 offen offset:1024 sc1\n"
"s_waitcnt lgkmcnt(6)\n"
"v_mfma_f32_16x16x32_bf16 v[80:83], v[32:35], a[120:123], v[80:83]\n"
"ds_read_b64_tr_b16 v[20:21], v14 offset:15456\n"
"v_add_u32 v156, v156, v70\n"
"v_mfma_f32_16x16x32_bf16 v[80:83], v[36:39], a[132:135], v[80:83]\n"
"ds_read_b64_tr_b16 v[22:23], v14 offset:16736\n"
"v_add_u32 v156, 144976, v156\n"
"v_mfma_f32_16x16x32_bf16 v[100:103], v[32:35], a[124:127], v[100:103]\n"
"v_subrev_u32 v70, 16, v1\n"
"v_and_b32 v70, 63, v70\n"
"v_mfma_f32_16x16x32_bf16 v[100:103], v[36:39], a[136:139], v[100:103]\n"
"ds_read_b64_tr_b16 v[24:25], v14 offset:10368\n"
"buffer_load_dwordx4 a[16:19], v2, s[24:27], s98 offen offset:2048 sc1\n"
"v_mfma_f32_16x16x32_bf16 v[120:123], v[32:35], a[128:131], v[120:123]\n"
"ds_read_b64_tr_b16 v[26:27], v14 offset:11648\n"
"v_mul_u32_u24 v71, 52, v70\n"
"v_mfma_f32_16x16x32_bf16 v[120:123], v[36:39], a[140:143], v[120:123]\n"
"v_lshrrev_b32 v71, 8, v71\n"
"s_waitcnt lgkmcnt(6)\n"
"v_mfma_f32_16x16x32_bf16 v[84:87], v[40:43], a[120:123], v[84:87]\n"
"ds_read_b64_tr_b16 v[28:29], v14 offset:15488\n"
"v_mul_u32_u24 v72, 5, v71\n"
"v_mfma_f32_16x16x32_bf16 v[84:87], v[44:47], a[132:135], v[84:87]\n"
"ds_read_b64_tr_b16 v[30:31], v14 offset:16768\n"
"v_sub_u32 v70, v70, v72\n"
"v_mfma_f32_16x16x32_bf16 v[104:107], v[40:43], a[124:127], v[104:107]\n"
"buffer_load_dwordx4 a[20:23], v2, s[24:27], s98 offen offset:3072 sc1\n"
"v_add_u32 v70, 16, v70\n"
"v_mfma_f32_16x16x32_bf16 v[104:107], v[44:47], a[136:139], v[104:107]\n"
"v_mov_b32 v72, 4\n"
"v_cndmask_b32 v70, v70, v1, s[88:89]\n"
"v_mfma_f32_16x16x32_bf16 v[124:127], v[40:43], a[128:131], v[124:127]\n"
"v_cndmask_b32 v71, v71, v72, s[88:89]\n"
"v_add_u32 v71, s60, v71\n"
"v_mfma_f32_16x16x32_bf16 v[124:127], v[44:47], a[140:143], v[124:127]\n"
"s_waitcnt vmcnt(15) lgkmcnt(4)\n"
"s_barrier\n"
"v_mfma_f32_16x16x32_bf16 v[88:91], v[16:19], a[120:123], v[88:91]\n"
"ds_read_b64_tr_b16 v[32:33], v14 offset:20480\n"
"buffer_load_dword v11, v10, s[16:19], 0 offen sc1\n"
"v_mfma_f32_16x16x32_bf16 v[88:91], v[20:23], a[132:135], v[88:91]\n"
"ds_read_b64_tr_b16 v[34:35], v14 offset:21760\n"
"v_lshrrev_b32 v157, 3, v71\n"
"v_mfma_f32_16x16x32_bf16 v[108:111], v[16:19], a[124:127], v[108:111]\n"
"v_lshlrev_b32 v157, 6, v157\n"
"v_and_b32 v72, 3, v71\n"
"v_mfma_f32_16x16x32_bf16 v[108:111], v[20:23], a[136:139], v[108:111]\n"
"ds_read_b64_tr_b16 v[36:37], v14 offset:25600\n"
"v_lshl_add_u32 v157, v72, 4, v157\n"
"v_mfma_f32_16x16x32_bf16 v[128:131], v[16:19], a[128:131], v[128:131]\n"
"ds_read_b64_tr_b16 v[38:39], v14 offset:26880\n"
"v_bfe_u32 v72, v71, 2, 1\n"
"v_mfma_f32_16x16x32_bf16 v[128:131], v[20:23], a[140:143], v[128:131]\n"
"s_waitcnt lgkmcnt(4)\n"
"s_add_u32 s98, s97, 4096\n"
"v_mfma_f32_16x16x32_bf16 v[92:95], v[24:27], a[120:123], v[92:95]\n"
"ds_read_b64_tr_b16 v[40:41], v14 offset:20512\n"
"v_lshl_add_u32 v157, v72, 3, v157\n"
"v_mfma_f32_16x16x32_bf16 v[92:95], v[28:31], a[132:135], v[92:95]\n"
"ds_read_b64_tr_b16 v[42:43], v14 offset:21792\n"
"buffer_load_dwordx4 a[24:27], v2, s[24:27], s98 offen offset:0 sc1\n"
"v_mfma_f32_16x16x32_bf16 v[112:115], v[24:27], a[124:127], v[112:115]\n"
"v_cmp_gt_u32 vcc, 20, v70\n"
"v_add_u32 v70, s61, v70\n"
"v_mfma_f32_16x16x32_bf16 v[112:115], v[28:31], a[136:139], v[112:115]\n"
"ds_read_b64_tr_b16 v[44:45], v14 offset:25632\n"
"v_mov_b32 v72, s62\n"
"v_mfma_f32_16x16x32_bf16 v[132:135], v[24:27], a[128:131], v[132:135]\n"
"ds_read_b64_tr_b16 v[46:47], v14 offset:26912\n"
"ds_read_b64_tr_b16 v[16:17], v14 offset:20544\n"
"v_mfma_f32_16x16x32_bf16 v[132:135], v[28:31], a[140:143], v[132:135]\n"
"ds_read_b64_tr_b16 v[18:19], v14 offset:21824\n"
"s_waitcnt lgkmcnt(6)\n"
"v_mfma_f32_16x16x32_bf16 v[76:79], v[32:35], a[144:147], v[76:79]\n"
"ds_read_b64_tr_b16 v[20:21], v14 offset:25664\n"
"buffer_load_dwordx4 a[28:31], v2, s[24:27], s98 offen offset:1024 sc1\n"
"v_mfma_f32_16x16x32_bf16 v[76:79], v[36:39], a[156:159], v[76:79]\n"
"ds_read_b64_tr_b16 v[22:23], v14 offset:26944\n"
"v_cndmask_b32 v70, v72, v70, vcc\n"
"v_mfma_f32_16x16x32_bf16 v[96:99], v[32:35], a[148:151], v[96:99]\n"
"v_mul_u32_u24 v70, 224, v70\n"
"v_add_u32 v157, v157, v70\n"
"v_mfma_f32_16x16x32_bf16 v[96:99], v[36:39], a[160:163], v[96:99]\n"
"ds_read_b64_tr_b16 v[24:25], v14 offset:20576\n"
"v_add_u32 v157, 144976, v157\n"
"v_mfma_f32_16x16x32_bf16 v[116:119], v[32:35], a[152:155], v[116:119]\n"
"ds_read_b64_tr_b16 v[26:27], v14 offset:21856\n"
"buffer_load_dwordx4 a[32:35], v2, s[24:27], s98 offen offset:2048 sc1\n"
"v_mfma_f32_16x16x32_bf16 v[116:119], v[36:39], a[164:167], v[116:119]\n"
"v_cmp_gt_u32 vcc, 41, v1\n"
"s_waitcnt lgkmcnt(6)\n"
"v_mfma_f32_16x16x32_bf16 v[80:83], v[40:43], a[144:147], v[80:83]\n"
"ds_read_b64_tr_b16 v[28:29], v14 offset:25696\n"
"v_mov_b32 v72, 163808\n"
"v_mfma_f32_16x16x32_bf16 v[80:83], v[44:47], a[156:159], v[80:83]\n"
"ds_read_b64_tr_b16 v[30:31], v14 offset:26976\n"
"v_mov_b32 v13, s52\n"
"v_mfma_f32_16x16x32_bf16 v[100:103], v[40:43], a[148:151], v[100:103]\n"
"buffer_load_dwordx4 a[36:39], v2, s[24:27], s98 offen offset:3072 sc1\n"
"v_cndmask_b32 v157, v72, v157, vcc\n"
"v_mfma_f32_16x16x32_bf16 v[100:103], v[44:47], a[160:163], v[100:103]\n"
"ds_read_b64_tr_b16 v[32:33], v14 offset:20608\n"
"v_mfma_f32_16x16x32_bf16 v[120:123], v[40:43], a[152:155], v[120:123]\n"
"ds_read_b64_tr_b16 v[34:35], v14 offset:21888\n"
"s_add_u32 s98, s97, 8192\n"
"v_mfma_f32_16x16x32_bf16 v[120:123], v[44:47], a[164:167], v[120:123]\n"
"s_waitcnt lgkmcnt(6)\n"
"v_mfma_f32_16x16x32_bf16 v[84:87], v[16:19], a[144:147], v[84:87]\n"
"ds_read_b64_tr_b16 v[36:37], v14 offset:25728\n"
"buffer_load_dwordx4 a[40:43], v2, s[24:27], s98 offen offset:0 sc1\n"
"v_mfma_f32_16x16x32_bf16 v[84:87], v[20:23], a[156:159], v[84:87]\n"
"ds_read_b64_tr_b16 v[38:39], v14 offset:27008\n"
"v_mfma_f32_16x16x32_bf16 v[104:107], v[16:19], a[148:151], v[104:107]\n"
"v_mfma_f32_16x16x32_bf16 v[104:107], v[20:23], a[160:163], v[104:107]\n"
"ds_read_b64_tr_b16 v[40:41], v14 offset:30720\n"
"v_mfma_f32_16x16x32_bf16 v[124:127], v[16:19], a[152:155], v[124:127]\n"
"ds_read_b64_tr_b16 v[42:43], v14 offset:32000\n"
"v_mfma_f32_16x16x32_bf16 v[124:127], v[20:23], a[164:167], v[124:127]\n"
"buffer_load_dwordx4 a[44:47], v2, s[24:27], s98 offen offset:1024 sc1\n"
"s_waitcnt lgkmcnt(6)\n"
"v_mfma_f32_16x16x32_bf16 v[88:91], v[24:27], a[144:147], v[88:91]\n"
"ds_read_b64_tr_b16 v[44:45], v14 offset:35840\n"
"v_mfma_f32_16x16x32_bf16 v[88:91], v[28:31], a[156:159], v[88:91]\n"
"ds_read_b64_tr_b16 v[46:47], v14 offset:37120\n"
"v_mfma_f32_16x16x32_bf16 v[108:111], v[24:27], a[148:151], v[108:111]\n"
"v_mfma_f32_16x16x32_bf16 v[108:111], v[28:31], a[160:163], v[108:111]\n"
"ds_read_b64_tr_b16 v[16:17], v14 offset:30752\n"
"buffer_load_dwordx4 a[48:51], v2, s[24:27], s98 offen offset:2048 sc1\n"
"v_mfma_f32_16x16x32_bf16 v[128:131], v[24:27], a[152:155], v[128:131]\n"
"ds_read_b64_tr_b16 v[18:19], v14 offset:32032\n"
"v_mfma_f32_16x16x32_bf16 v[128:131], v[28:31], a[164:167], v[128:131]\n"
"s_waitcnt lgkmcnt(6)\n"
"v_mfma_f32_16x16x32_bf16 v[92:95], v[32:35], a[144:147], v[92:95]\n"
"ds_read_b64_tr_b16 v[20:21], v14 offset:35872\n"
"v_mfma_f32_16x16x32_bf16 v[92:95], v[36:39], a[156:159], v[92:95]\n"
"ds_read_b64_tr_b16 v[22:23], v14 offset:37152\n"
"v_mfma_f32_16x16x32_bf16 v[112:115], v[32:35], a[148:151], v[112:115]\n"
"buffer_load_dwordx4 a[52:55], v2, s[24:27], s98 offen offset:3072 sc1\n"
"v_mfma_f32_16x16x32_bf16 v[112:115], v[36:39], a[160:163], v[112:115]\n"
"ds_read_b64_tr_b16 v[24:25], v14 offset:30784\n"
"v_mfma_f32_16x16x32_bf16 v[132:135], v[32:35], a[152:155], v[132:135]\n"
"ds_read_b64_tr_b16 v[26:27], v14 offset:32064\n"
"s_add_u32 s98, s97, 12288\n"
"v_mfma_f32_16x16x32_bf16 v[132:135], v[36:39], a[164:167], v[132:135]\n"
"s_waitcnt lgkmcnt(6)\n"
"v_mfma_f32_16x16x32_bf16 v[76:79], v[40:43], a[168:171], v[76:79]\n"
"ds_read_b64_tr_b16 v[28:29], v14 offset:35904\n"
"buffer_load_dwordx4 a[56:59], v2, s[24:27], s98 offen offset:0 sc1\n"
"v_mfma_f32_16x16x32_bf16 v[76:79], v[44:47], a[180:183], v[76:79]\n"
"ds_read_b64_tr_b16 v[30:31], v14 offset:37184\n"
"v_mfma_f32_16x16x32_bf16 v[96:99], v[40:43], a[172:175], v[96:99]\n"
"v_mfma_f32_16x16x32_bf16 v[96:99], v[44:47], a[184:187], v[96:99]\n"
"ds_read_b64_tr_b16 v[32:33], v14 offset:30816\n"
"v_mfma_f32_16x16x32_bf16 v[116:119], v[40:43], a[176:179], v[116:119]\n"
"ds_read_b64_tr_b16 v[34:35], v14 offset:32096\n"
"buffer_load_dwordx4 a[60:63], v2, s[24:27], s98 offen offset:1024 sc1\n"
"v_mfma_f32_16x16x32_bf16 v[116:119], v[44:47], a[188:191], v[116:119]\n"
"s_waitcnt lgkmcnt(6)\n"
"v_mfma_f32_16x16x32_bf16 v[80:83], v[16:19], a[168:171], v[80:83]\n"
"ds_read_b64_tr_b16 v[36:37], v14 offset:35936\n"
"v_mfma_f32_16x16x32_bf16 v[80:83], v[20:23], a[180:183], v[80:83]\n"
"ds_read_b64_tr_b16 v[38:39], v14 offset:37216\n"
"v_mfma_f32_16x16x32_bf16 v[100:103], v[16:19], a[172:175], v[100:103]\n"
"v_mfma_f32_16x16x32_bf16 v[100:103], v[20:23], a[184:187], v[100:103]\n"
"ds_read_b64_tr_b16 v[40:41], v14 offset:30848\n"
"buffer_load_dwordx4 a[64:67], v2, s[24:27], s98 offen offset:2048 sc1\n"
"v_mfma_f32_16x16x32_bf16 v[120:123], v[16:19], a[176:179], v[120:123]\n"
"ds_read_b64_tr_b16 v[42:43], v14 offset:32128\n"
"v_mfma_f32_16x16x32_bf16 v[120:123], v[20:23], a[188:191], v[120:123]\n"
"s_waitcnt lgkmcnt(6)\n"
"s_waitcnt vmcnt(11)\n"
"v_mfma_f32_16x16x32_bf16 v[84:87], v[24:27], a[168:171], v[84:87]\n"
"ds_read_b64_tr_b16 v[44:45], v14 offset:35968\n"
"v_cmp_ne_u32 vcc, s52, v11\n"
"v_mfma_f32_16x16x32_bf16 v[84:87], v[28:31], a[180:183], v[84:87]\n"
"ds_read_b64_tr_b16 v[46:47], v14 offset:37248\n"
"s_cbranch_vccnz .Lfb_slow_31\n"
".Lfb_back_32:\n"
"v_mfma_f32_16x16x32_bf16 v[104:107], v[24:27], a[172:175], v[104:107]\n"
"buffer_load_dwordx4 a[68:71], v2, s[24:27], s98 offen offset:3072 sc1\n"
"v_and_b32 v10, 15, v1\n"
"v_mfma_f32_16x16x32_bf16 v[104:107], v[28:31], a[184:187], v[104:107]\n"
"ds_read_b64_tr_b16 v[16:17], v15 offset:0\n"
"s_add_u32 s35, s74, 0\n"
"v_mfma_f32_16x16x32_bf16 v[124:127], v[24:27], a[176:179], v[124:127]\n"
"ds_read_b64_tr_b16 v[18:19], v15 offset:1280\n"
"s_add_u32 m0, s95, 0\n"
"v_mfma_f32_16x16x32_bf16 v[124:127], v[28:31], a[188:191], v[124:127]\n"
"v_mul_u32_u24 v10, 224, v10\n"
"s_waitcnt lgkmcnt(6)\n"
"v_mfma_f32_16x16x32_bf16 v[88:91], v[32:35], a[168:171], v[88:91]\n"
"ds_read_b64_tr_b16 v[20:21], v15 offset:5120\n"
"buffer_load_dwordx4 v2, s[16:19], s35 offen offset:0 sc1 lds\n"
"v_mfma_f32_16x16x32_bf16 v[88:91], v[36:39], a[180:183], v[88:91]\n"
"ds_read_b64_tr_b16 v[22:23], v15 offset:6400\n"
"v_add_u32 v12, 144976, v159\n"
"v_mfma_f32_16x16x32_bf16 v[108:111], v[32:35], a[172:175], v[108:111]\n"
"v_add_u32 v10, v10, v12\n"
"v_and_b32 v11, 3, v1\n"
"v_mfma_f32_16x16x32_bf16 v[108:111], v[36:39], a[184:187], v[108:111]\n"
"ds_read_b64_tr_b16 v[24:25], v15 offset:32\n"
"v_mul_u32_u24 v11, 224, v11\n"
"v_mfma_f32_16x16x32_bf16 v[128:131], v[32:35], a[176:179], v[128:131]\n"
"ds_read_b64_tr_b16 v[26:27], v15 offset:1312\n"
"buffer_load_dwordx4 v2, s[16:19], s35 offen offset:1024 sc1 lds\n"
"v_mfma_f32_16x16x32_bf16 v[128:131], v[36:39], a[188:191], v[128:131]\n"
"v_add_u32 v11, v11, v12\n"
"s_waitcnt lgkmcnt(6)\n"
"v_mfma_f32_16x16x32_bf16 v[92:95], v[40:43], a[168:171], v[92:95]\n"
"ds_read_b64_tr_b16 v[28:29], v15 offset:5152\n"
"v_mov_b32 v12, 1073741824\n"
"v_mfma_f32_16x16x32_bf16 v[92:95], v[44:47], a[180:183], v[92:95]\n"
"ds_read_b64_tr_b16 v[30:31], v15 offset:6432\n"
"v_cndmask_b32 v12, v12, v2, s[90:91]\n"
"v_mfma_f32_16x16x32_bf16 v[112:115], v[40:43], a[172:175], v[112:115]\n"
"buffer_load_dwordx4 v2, s[16:19], s35 offen offset:2048 sc1 lds\n"
"s_add_u32 s63, s12, 4294934528\n"
"v_mfma_f32_16x16x32_bf16 v[112:115], v[44:47], a[184:187], v[112:115]\n"
"ds_read_b64_tr_b16 v[32:33], v15 offset:64\n"
"v_add_u32 v159, s63, v159\n"
"v_mfma_f32_16x16x32_bf16 v[132:135], v[40:43], a[176:179], v[132:135]\n"
"ds_read_b64_tr_b16 v[34:35], v15 offset:1344\n"
"v_and_b32 v72, 31, v1\n"
"v_mfma_f32_16x16x32_bf16 v[132:135], v[44:47], a[188:191], v[132:135]\n"
"v_min_u32 v72, 5, v72\n"
"s_waitcnt lgkmcnt(6)\n"
"v_mfma_f32_16x16x32_bf16 v[76:79], v[16:19], a[192:195], v[76:79]\n"
"ds_read_b64_tr_b16 v[36:37], v15 offset:5184\n"
"v_lshrrev_b32 v9, 5, v1\n"
"v_mfma_f32_16x16x32_bf16 v[76:79], v[20:23], a[204:207], v[76:79]\n"
"ds_read_b64_tr_b16 v[38:39], v15 offset:6464\n"
"buffer_load_dwordx4 v2, s[16:19], s35 offen offset:3072 sc1 lds\n"
"v_mfma_f32_16x16x32_bf16 v[96:99], v[16:19], a[196:199], v[96:99]\n"
"s_add_u32 s35, s74, 4096\n"
"s_add_u32 m0, s95, 4096\n"
"v_mfma_f32_16x16x32_bf16 v[96:99], v[20:23], a[208:211], v[96:99]\n"
"ds_read_b64_tr_b16 v[40:41], v15 offset:96\n"
"v_mul_u32_u24 v9, s46, v9\n"
"v_mfma_f32_16x16x32_bf16 v[116:119], v[16:19], a[200:203], v[116:119]\n"
"ds_read_b64_tr_b16 v[42:43], v15 offset:1376\n"
"v_add3_u32 v72, v72, v9, s41\n"
"v_mfma_f32_16x16x32_bf16 v[116:119], v[20:23], a[212:215], v[116:119]\n"
"buffer_load_dwordx4 v2, s[16:19], s35 offen offset:0 sc1 lds\n"
"s_waitcnt lgkmcnt(6)\n"
"v_mfma_f32_16x16x32_bf16 v[80:83], v[24:27], a[192:195], v[80:83]\n"
"ds_read_b64_tr_b16 v[44:45], v15 offset:5216\n"
"v_lshlrev_b32 v72, 7, v72\n"
"v_mfma_f32_16x16x32_bf16 v[80:83], v[28:31], a[204:207], v[80:83]\n"
"ds_read_b64_tr_b16 v[46:47], v15 offset:6496\n"
"v_add_u32 v9, 0, v1\n"
"v_mfma_f32_16x16x32_bf16 v[100:103], v[24:27], a[196:199], v[100:103]\n"
"v_mul_u32_u24 v9, 6554, v9\n"
"v_lshrrev_b32 v9, 16, v9\n"
"v_mfma_f32_16x16x32_bf16 v[100:103], v[28:31], a[208:211], v[100:103]\n"
"ds_read_b64_tr_b16 v[16:17], v15 offset:128\n"
"v_mul_u32_u24 v9, 1120, v9\n"
"v_mfma_f32_16x16x32_bf16 v[120:123], v[24:27], a[200:203], v[120:123]\n"
"ds_read_b64_tr_b16 v[18:19], v15 offset:1408\n"
"buffer_load_dwordx4 v2, s[16:19], s35 offen offset:1024 sc1 lds\n"
"v_mfma_f32_16x16x32_bf16 v[120:123], v[28:31], a[212:215], v[120:123]\n"
"v_add_u32 v5, v9, v2\n"
"s_waitcnt lgkmcnt(6)\n"
"v_mfma_f32_16x16x32_bf16 v[84:87], v[32:35], a[192:195], v[84:87]\n"
"ds_read_b64_tr_b16 v[20:21], v15 offset:5248\n"
"v_add_u32 v9, 64, v1\n"
"v_mfma_f32_16x16x32_bf16 v[84:87], v[36:39], a[204:207], v[84:87]\n"
"ds_read_b64_tr_b16 v[22:23], v15 offset:6528\n"
"v_mul_u32_u24 v9, 6554, v9\n"
"v_mfma_f32_16x16x32_bf16 v[104:107], v[32:35], a[196:199], v[104:107]\n"
"buffer_load_dwordx4 v2, s[16:19], s35 offen offset:2048 sc1 lds\n"
"v_lshrrev_b32 v9, 16, v9\n"
"v_mfma_f32_16x16x32_bf16 v[104:107], v[36:39], a[208:211], v[104:107]\n"
"ds_read_b64_tr_b16 v[24:25], v15 offset:10240\n"
"v_mul_u32_u24 v9, 1120, v9\n"
"v_mfma_f32_16x16x32_bf16 v[124:127], v[32:35], a[200:203], v[124:127]\n"
"ds_read_b64_tr_b16 v[26:27], v15 offset:11520\n"
"v_add_u32 v6, v9, v2\n"
"v_mfma_f32_16x16x32_bf16 v[124:127], v[36:39], a[212:215], v[124:127]\n"
"v_add_u32 v9, 128, v1\n"
"s_waitcnt lgkmcnt(6)\n"
"v_mfma_f32_16x16x32_bf16 v[88:91], v[40:43], a[192:195], v[88:91]\n"
"ds_read_b64_tr_b16 v[28:29], v15 offset:15360\n"
"buffer_load_dwordx4 v2, s[16:19], s35 offen offset:3072 sc1 lds\n"
"v_mfma_f32_16x16x32_bf16 v[88:91], v[44:47], a[204:207], v[88:91]\n"
"ds_read_b64_tr_b16 v[30:31], v15 offset:16640\n"
"v_mul_u32_u24 v9, 6554, v9\n"
"v_mfma_f32_16x16x32_bf16 v[108:111], v[40:43], a[196:199], v[108:111]\n"
"s_add_u32 s35, s74, 8192\n"
"s_add_u32 m0, s95, 8192\n"
"v_mfma_f32_16x16x32_bf16 v[108:111], v[44:47], a[208:211], v[108:111]\n"
"ds_read_b64_tr_b16 v[32:33], v15 offset:10272\n"
"v_lshrrev_b32 v9, 16, v9\n"
"v_mfma_f32_16x16x32_bf16 v[128:131], v[40:43], a[200:203], v[128:131]\n"
"ds_read_b64_tr_b16 v[34:35], v15 offset:11552\n"
"v_mul_u32_u24 v9, 1120, v9\n"
"v_mfma_f32_16x16x32_bf16 v[128:131], v[44:47], a[212:215], v[128:131]\n"
"buffer_load_dwordx4 v2, s[16:19], s35 offen offset:0 sc1 lds\n"
"s_waitcnt lgkmcnt(6)\n"
"v_mfma_f32_16x16x32_bf16 v[92:95], v[16:19], a[192:195], v[92:95]\n"
"ds_read_b64_tr_b16 v[36:37], v15 offset:15392\n"
"v_add_u32 v7, v9, v2\n"
"v_mfma_f32_16x16x32_bf16 v[92:95], v[20:23], a[204:207], v[92:95]\n"
"ds_read_b64_tr_b16 v[38:39], v15 offset:16672\n"
"v_lshrrev_b32 v9, 5, v1\n"
"v_mfma_f32_16x16x32_bf16 v[112:115], v[16:19], a[196:199], v[112:115]\n"
"v_lshl_add_u32 v7, v9, 30, v7\n"
"v_mfma_f32_16x16x32_bf16 v[112:115], v[20:23], a[208:211], v[112:115]\n"
"ds_read_b64_tr_b16 v[40:41], v15 offset:10304\n"
"buffer_load_dwordx4 v2, s[16:19], s35 offen offset:1024 sc1 lds\n"
"v_mfma_f32_16x16x32_bf16 v[132:135], v[16:19], a[200:203], v[132:135]\n"
"ds_read_b64_tr_b16 v[42:43], v15 offset:11584\n"
"v_mfma_f32_16x16x32_bf16 v[132:135], v[20:23], a[212:215], v[132:135]\n"
"s_waitcnt lgkmcnt(6)\n"
"v_mfma_f32_16x16x32_bf16 v[76:79], v[24:27], a[216:219], v[76:79]\n"
"ds_read_b64_tr_b16 v[44:45], v15 offset:15424\n"
"v_mfma_f32_16x16x32_bf16 v[76:79], v[28:31], a[228:231], v[76:79]\n"
"ds_read_b64_tr_b16 v[46:47], v15 offset:16704\n"
"v_mfma_f32_16x16x32_bf16 v[96:99], v[24:27], a[220:223], v[96:99]\n"
"v_mfma_f32_16x16x32_bf16 v[96:99], v[28:31], a[232:235], v[96:99]\n"
"ds_read_b64_tr_b16 v[16:17], v15 offset:10336\n"
"v_mfma_f32_16x16x32_bf16 v[116:119], v[24:27], a[224:227], v[116:119]\n"
"ds_read_b64_tr_b16 v[18:19], v15 offset:11616\n"
"v_mfma_f32_16x16x32_bf16 v[116:119], v[28:31], a[236:239], v[116:119]\n"
"s_waitcnt lgkmcnt(6)\n"
"v_mfma_f32_16x16x32_bf16 v[80:83], v[32:35], a[216:219], v[80:83]\n"
"ds_read_b64_tr_b16 v[20:21], v15 offset:15456\n"
"v_mfma_f32_16x16x32_bf16 v[80:83], v[36:39], a[228:231], v[80:83]\n"
"ds_read_b64_tr_b16 v[22:23], v15 offset:16736\n"
"v_mfma_f32_16x16x32_bf16 v[100:103], v[32:35], a[220:223], v[100:103]\n"
"v_mfma_f32_16x16x32_bf16 v[100:103], v[36:39], a[232:235], v[100:103]\n"
"ds_read_b64_tr_b16 v[24:25], v15 offset:10368\n"
"v_mfma_f32_16x16x32_bf16 v[120:123], v[32:35], a[224:227], v[120:123]\n"
"ds_read_b64_tr_b16 v[26:27], v15 offset:11648\n"
"v_mfma_f32_16x16x32_bf16 v[120:123], v[36:39], a[236:239], v[120:123]\n"
"s_waitcnt lgkmcnt(6)\n"
"v_mfma_f32_16x16x32_bf16 v[84:87], v[40:43], a[216:219], v[84:87]\n"
"ds_read_b64_tr_b16 v[28:29], v15 offset:15488\n"
"v_mfma_f32_16x16x32_bf16 v[84:87], v[44:47], a[228:231], v[84:87]\n"
"ds_read_b64_tr_b16 v[30:31], v15 offset:16768\n"
"v_mfma_f32_16x16x32_bf16 v[104:107], v[40:43], a[220:223], v[104:107]\n"
"v_mfma_f32_16x16x32_bf16 v[104:107], v[44:47], a[232:235], v[104:107]\n"
"v_mfma_f32_16x16x32_bf16 v[124:127], v[40:43], a[224:227], v[124:127]\n"
"v_mfma_f32_16x16x32_bf16 v[124:127], v[44:47], a[236:239], v[124:127]\n"
"ds_read_b128 v[48:51], v159 offset:0\n"
"s_waitcnt lgkmcnt(5)\n"
"v_mfma_f32_16x16x32_bf16 v[88:91], v[16:19], a[216:219], v[88:91]\n"
"ds_read_b128 v[52:55], v159 offset:64\n"
"ds_read_b128 v[56:59], v159 offset:128\n"
"v_mfma_f32_16x16x32_bf16 v[88:91], v[20:23], a[228:231], v[88:91]\n"
"ds_read_b128 v[60:63], v159 offset:192\n"
"ds_read_b128 v[64:67], v159 offset:256\n"
"v_mfma_f32_16x16x32_bf16 v[108:111], v[16:19], a[220:223], v[108:111]\n"
"v_mfma_f32_16x16x32_bf16 v[108:111], v[20:23], a[232:235], v[108:111]\n"
"v_mfma_f32_16x16x32_bf16 v[128:131], v[16:19], a[224:227], v[128:131]\n"
"v_mfma_f32_16x16x32_bf16 v[128:131], v[20:23], a[236:239], v[128:131]\n"
"s_waitcnt lgkmcnt(5)\n"
"v_mfma_f32_16x16x32_bf16 v[92:95], v[24:27], a[216:219], v[92:95]\n"
"v_mfma_f32_16x16x32_bf16 v[92:95], v[28:31], a[228:231], v[92:95]\n"
"v_mfma_f32_16x16x32_bf16 v[112:115], v[24:27], a[220:223], v[112:115]\n"
"v_mfma_f32_16x16x32_bf16 v[112:115], v[28:31], a[232:235], v[112:115]\n"
"v_mfma_f32_16x16x32_bf16 v[132:135], v[24:27], a[224:227], v[132:135]\n"
"v_mfma_f32_16x16x32_bf16 v[132:135], v[28:31], a[236:239], v[132:135]\n"
".Lfb_attq:\n"
"s_mul_i32 s58, s3, 3072\n"
"s_add_u32 m0, s58, 126976\n"
"s_nop 0\n"
"buffer_load_dwordx4 v5, s[4:7], s79 offen offset:0 lds\n"
"s_waitcnt lgkmcnt(4)\n"
"v_pk_fma_f32 v[76:77], v[48:49], v[136:137], v[76:77] op_sel:[0,0,0] op_sel_hi:[1,0,1] neg_lo:[0,1,0] neg_hi:[0,1,0]\n"
"v_pk_fma_f32 v[78:79], v[50:51], v[136:137], v[78:79] op_sel:[0,0,0] op_sel_hi:[1,0,1] neg_lo:[0,1,0] neg_hi:[0,1,0]\n"
"v_pk_mul_f32 v[44:45], v[76:77], v[138:139] op_sel:[0,0] op_sel_hi:[1,0]\n"
"v_pk_mul_f32 v[46:47], v[78:79], v[138:139] op_sel:[0,0] op_sel_hi:[1,0]\n"
"v_pk_fma_f32 v[76:77], v[76:77], v[136:137], v[74:75] op_sel:[0,1,1] op_sel_hi:[1,1,1]\n"
"v_pk_fma_f32 v[78:79], v[78:79], v[136:137], v[74:75] op_sel:[0,1,1] op_sel_hi:[1,1,1]\n"
"v_exp_f32_e64 v44, v44 clamp\n"
"v_exp_f32_e64 v45, v45 clamp\n"
"v_exp_f32_e64 v46, v46 clamp\n"
"v_exp_f32_e64 v47, v47 clamp\n"
"v_max_f32 v76, v76, v75\n"
"v_max_f32 v77, v77, v75\n"
"v_max_f32 v78, v78, v75\n"
"v_max_f32 v79, v79, v75\n"
"v_pk_add_f32 v[76:77], v[76:77], v[44:45] op_sel:[0,0] op_sel_hi:[1,1]\n"
"v_pk_add_f32 v[78:79], v[78:79], v[46:47] op_sel:[0,0] op_sel_hi:[1,1]\n"
"v_cvt_pk_bf16_f32 v16, v76, v77\n"
"buffer_load_dwordx4 v6, s[4:7], s79 offen offset:1024 lds\n"
"v_cvt_pk_bf16_f32 v17, v78, v79\n"
"s_waitcnt lgkmcnt(3)\n"
"v_pk_fma_f32 v[80:81], v[52:53], v[136:137], v[80:81] op_sel:[0,0,0] op_sel_hi:[1,0,1] neg_lo:[0,1,0] neg_hi:[0,1,0]\n"
"v_pk_fma_f32 v[82:83], v[54:55], v[136:137], v[82:83] op_sel:[0,0,0] op_sel_hi:[1,0,1] neg_lo:[0,1,0] neg_hi:[0,1,0]\n"
"v_pk_mul_f32 v[68:69], v[80:81], v[138:139] op_sel:[0,0] op_sel_hi:[1,0]\n"
"v_pk_mul_f32 v[70:71], v[82:83], v[138:139] op_sel:[0,0] op_sel_hi:[1,0]\n"
"v_pk_fma_f32 v[80:81], v[80:81], v[136:137], v[74:75] op_sel:[0,1,1] op_sel_hi:[1,1,1]\n"
"v_pk_fma_f32 v[82:83], v[82:83], v[136:137], v[74:75] op_sel:[0,1,1] op_sel_hi:[1,1,1]\n"
"v_exp_f32_e64 v68, v68 clamp\n"
"v_exp_f32_e64 v69, v69 clamp\n"
"v_exp_f32_e64 v70, v70 clamp\n"
"v_exp_f32_e64 v71, v71 clamp\n"
"v_max_f32 v80, v80, v75\n"
"v_max_f32 v81, v81, v75\n"
"v_max_f32 v82, v82, v75\n"
"v_max_f32 v83, v83, v75\n"
"v_pk_add_f32 v[80:81], v[80:81], v[68:69] op_sel:[0,0] op_sel_hi:[1,1]\n"
"v_pk_add_f32 v[82:83], v[82:83], v[70:71] op_sel:[0,0] op_sel_hi:[1,1]\n"
"buffer_load_dwordx4 v7, s[4:7], s79 offen offset:2048 lds\n"
"v_cvt_pk_bf16_f32 v18, v80, v81\n"
"v_cvt_pk_bf16_f32 v19, v82, v83\n"
"s_waitcnt lgkmcnt(2)\n"
"v_pk_fma_f32 v[84:85], v[56:57], v[136:137], v[84:85] op_sel:[0,0,0] op_sel_hi:[1,0,1] neg_lo:[0,1,0] neg_hi:[0,1,0]\n"
"v_pk_fma_f32 v[86:87], v[58:59], v[136:137], v[86:87] op_sel:[0,0,0] op_sel_hi:[1,0,1] neg_lo:[0,1,0] neg_hi:[0,1,0]\n"
"v_pk_mul_f32 v[44:45], v[84:85], v[138:139] op_sel:[0,0] op_sel_hi:[1,0]\n"
"v_pk_mul_f32 v[46:47], v[86:87], v[138:139] op_sel:[0,0] op_sel_hi:[1,0]\n"
"v_pk_fma_f32 v[84:85], v[84:85], v[136:137], v[74:75] op_sel:[0,1,1] op_sel_hi:[1,1,1]\n"
"v_pk_fma_f32 v[86:87], v[86:87], v[136:137], v[74:75] op_sel:[0,1,1] op_sel_hi:[1,1,1]\n"
"v_exp_f32_e64 v44, v44 clamp\n"
"v_exp_f32_e64 v45, v45 clamp\n"
"v_exp_f32_e64 v46, v46 clamp\n"
"v_exp_f32_e64 v47, v47 clamp\n"
"v_max_f32 v84, v84, v75\n"
"v_max_f32 v85, v85, v75\n"
"v_max_f32 v86, v86, v75\n"
"v_max_f32 v87, v87, v75\n"
"v_pk_add_f32 v[84:85], v[84:85], v[44:45] op_sel:[0,0] op_sel_hi:[1,1]\n"
"s_add_u32 s98, s97, 16384\n"
"buffer_load_dwordx4 a[72:75], v2, s[24:27], s98 offen offset:0 sc1\n"
"v_pk_add_f32 v[86:87], v[86:87], v[46:47] op_sel:[0,0] op_sel_hi:[1,1]\n"
"v_cvt_pk_bf16_f32 v20, v84, v85\n"
"v_cvt_pk_bf16_f32 v21, v86, v87\n"
"s_waitcnt lgkmcnt(1)\n"
"v_pk_fma_f32 v[88:89], v[60:61], v[136:137], v[88:89] op_sel:[0,0,0] op_sel_hi:[1,0,1] neg_lo:[0,1,0] neg_hi:[0,1,0]\n"
"v_pk_fma_f32 v[90:91], v[62:63], v[136:137], v[90:91] op_sel:[0,0,0] op_sel_hi:[1,0,1] neg_lo:[0,1,0] neg_hi:[0,1,0]\n"
"v_pk_mul_f32 v[68:69], v[88:89], v[138:139] op_sel:[0,0] op_sel_hi:[1,0]\n"
"v_pk_mul_f32 v[70:71], v[90:91], v[138:139] op_sel:[0,0] op_sel_hi:[1,0]\n"
"v_pk_fma_f32 v[88:89], v[88:89], v[136:137], v[74:75] op_sel:[0,1,1] op_sel_hi:[1,1,1]\n"
"v_pk_fma_f32 v[90:91], v[90:91], v[136:137], v[74:75] op_sel:[0,1,1] op_sel_hi:[1,1,1]\n"
"v_exp_f32_e64 v68, v68 clamp\n"
"v_exp_f32_e64 v69, v69 clamp\n"
"v_exp_f32_e64 v70, v70 clamp\n"
"v_exp_f32_e64 v71, v71 clamp\n"
"v_max_f32 v88, v88, v75\n"
"v_max_f32 v89, v89, v75\n"
"v_max_f32 v90, v90, v75\n"
"v_max_f32 v91, v91, v75\n"
"buffer_load_dwordx4 a[76:79], v2, s[24:27], s98 offen offset:1024 sc1\n"
"v_pk_add_f32 v[88:89], v[88:89], v[68:69] op_sel:[0,0] op_sel_hi:[1,1]\n"
"v_pk_add_f32 v[90:91], v[90:91], v[70:71] op_sel:[0,0] op_sel_hi:[1,1]\n"
"v_cvt_pk_bf16_f32 v22, v88, v89\n"
"v_cvt_pk_bf16_f32 v23, v90, v91\n"
"v_mov_b32 v26, 0\n"
"v_mov_b32 v27, 0\n"
"s_waitcnt lgkmcnt(0)\n"
"v_pk_fma_f32 v[92:93], v[64:65], v[136:137], v[92:93] op_sel:[0,0,0] op_sel_hi:[1,0,1] neg_lo:[0,1,0] neg_hi:[0,1,0]\n"
"v_pk_fma_f32 v[94:95], v[66:67], v[136:137], v[94:95] op_sel:[0,0,0] op_sel_hi:[1,0,1] neg_lo:[0,1,0] neg_hi:[0,1,0]\n"
"v_pk_mul_f32 v[44:45], v[92:93], v[138:139] op_sel:[0,0] op_sel_hi:[1,0]\n"
"v_pk_mul_f32 v[46:47], v[94:95], v[138:139] op_sel:[0,0] op_sel_hi:[1,0]\n"
"v_pk_fma_f32 v[92:93], v[92:93], v[136:137], v[74:75] op_sel:[0,1,1] op_sel_hi:[1,1,1]\n"
"v_pk_fma_f32 v[94:95], v[94:95], v[136:137], v[74:75] op_sel:[0,1,1] op_sel_hi:[1,1,1]\n"
"v_exp_f32_e64 v44, v44 clamp\n"
"v_exp_f32_e64 v45, v45 clamp\n"
"v_exp_f32_e64 v46, v46 clamp\n"
"v_exp_f32_e64 v47, v47 clamp\n"
"v_max_f32 v92, v92, v75\n"
"buffer_load_dwordx4 a[80:83], v2, s[24:27], s98 offen offset:2048 sc1\n"
"v_max_f32 v93, v93, v75\n"
"v_max_f32 v94, v94, v75\n"
"v_max_f32 v95, v95, v75\n"
"v_pk_add_f32 v[92:93], v[92:93], v[44:45] op_sel:[0,0] op_sel_hi:[1,1]\n"
"v_pk_add_f32 v[94:95], v[94:95], v[46:47] op_sel:[0,0] op_sel_hi:[1,1]\n"
"v_cvt_pk_bf16_f32 v24, v92, v93\n"
"v_cvt_pk_bf16_f32 v25, v94, v95\n"
"s_waitcnt vmcnt(12)\n"
"ds_read_b64 v[28:29], v158 offset:0\n"
"ds_read_b64 v[30:31], v158 offset:512\n"
"ds_read_b64 v[32:33], v158 offset:896\n"
"ds_read_b64 v[34:35], v158 offset:1408\n"
"buffer_load_dwordx4 a[84:87], v2, s[24:27], s98 offen offset:3072 sc1\n"
"ds_read_b64 v[36:37], v158 offset:1792\n"
"ds_read_b64 v[38:39], v158 offset:2304\n"
"ds_read_b64 v[40:41], v158 offset:2688\n"
"ds_read_b64 v[42:43], v158 offset:3200\n"
"v_pk_fma_f32 v[96:97], v[48:49], v[140:141], v[96:97] op_sel:[0,0,0] op_sel_hi:[1,0,1] neg_lo:[0,1,0] neg_hi:[0,1,0]\n"
"v_pk_fma_f32 v[98:99], v[50:51], v[140:141], v[98:99] op_sel:[0,0,0] op_sel_hi:[1,0,1] neg_lo:[0,1,0] neg_hi:[0,1,0]\n"
"v_pk_mul_f32 v[44:45], v[96:97], v[142:143] op_sel:[0,0] op_sel_hi:[1,0]\n"
"v_pk_mul_f32 v[46:47], v[98:99], v[142:143] op_sel:[0,0] op_sel_hi:[1,0]\n"
"v_pk_fma_f32 v[96:97], v[96:97], v[140:141], v[74:75] op_sel:[0,1,1] op_sel_hi:[1,1,1]\n"
"s_add_u32 s98, s97, 20480\n"
"buffer_load_dwordx4 a[88:91], v2, s[24:27], s98 offen offset:0 sc1\n"
"v_pk_fma_f32 v[98:99], v[98:99], v[140:141], v[74:75] op_sel:[0,1,1] op_sel_hi:[1,1,1]\n"
"v_exp_f32_e64 v44, v44 clamp\n"
"v_exp_f32_e64 v45, v45 clamp\n"
"v_exp_f32_e64 v46, v46 clamp\n"
"v_exp_f32_e64 v47, v47 clamp\n"
"v_max_f32 v96, v96, v75\n"
"v_max_f32 v97, v97, v75\n"
"v_max_f32 v98, v98, v75\n"
"v_max_f32 v99, v99, v75\n"
"v_pk_add_f32 v[96:97], v[96:97], v[44:45] op_sel:[0,0] op_sel_hi:[1,1]\n"
"v_pk_add_f32 v[98:99], v[98:99], v[46:47] op_sel:[0,0] op_sel_hi:[1,1]\n"
"v_cvt_pk_bf16_f32 v76, v96, v97\n"
"v_cvt_pk_bf16_f32 v77, v98, v99\n"
"s_waitcnt lgkmcnt(7)\n"
"v_dot2_f32_bf16 v148, v28, s68, 0\n"
"v_dot2_f32_bf16 v149, v28, s72, 0\n"
"v_dot2_f32_bf16 v150, v29, s68, 0\n"
"v_dot2_f32_bf16 v151, v29, s72, 0\n"
"buffer_load_dwordx4 a[92:95], v2, s[24:27], s98 offen offset:1024 sc1\n"
"s_waitcnt lgkmcnt(6)\n"
"v_dot2_f32_bf16 v152, v30, s68, 0\n"
"v_dot2_f32_bf16 v153, v30, s72, 0\n"
"v_dot2_f32_bf16 v154, v31, s68, 0\n"
"v_dot2_f32_bf16 v155, v31, s72, 0\n"
"s_waitcnt lgkmcnt(5)\n"
"v_dot2_f32_bf16 v148, v32, s68, v148\n"
"v_dot2_f32_bf16 v149, v32, s72, v149\n"
"v_dot2_f32_bf16 v150, v33, s68, v150\n"
"v_dot2_f32_bf16 v151, v33, s72, v151\n"
"s_waitcnt lgkmcnt(4)\n"
"v_dot2_f32_bf16 v152, v34, s68, v152\n"
"v_dot2_f32_bf16 v153, v34, s72, v153\n"
"v_dot2_f32_bf16 v154, v35, s68, v154\n"
"v_dot2_f32_bf16 v155, v35, s72, v155\n"
"s_waitcnt lgkmcnt(3)\n"
"v_dot2_f32_bf16 v148, v36, s68, v148\n"
"v_dot2_f32_bf16 v149, v36, s72, v149\n"
"v_dot2_f32_bf16 v150, v37, s68, v150\n"
"v_dot2_f32_bf16 v151, v37, s72, v151\n"
"s_waitcnt lgkmcnt(2)\n"
"v_dot2_f32_bf16 v152, v38, s68, v152\n"
"v_dot2_f32_bf16 v153, v38, s72, v153\n"
"v_dot2_f32_bf16 v154, v39, s68, v154\n"
"v_dot2_f32_bf16 v155, v39, s72, v155\n"
"s_waitcnt lgkmcnt(1)\n"
"v_dot2_f32_bf16 v148, v40, s68, v148\n"
"buffer_load_dwordx4 a[96:99], v2, s[24:27], s98 offen offset:2048 sc1\n"
"v_dot2_f32_bf16 v149, v40, s72, v149\n"
"v_dot2_f32_bf16 v150, v41, s68, v150\n"
"v_dot2_f32_bf16 v151, v41, s72, v151\n"
"s_waitcnt lgkmcnt(0)\n"
"v_dot2_f32_bf16 v152, v42, s68, v152\n"
"v_dot2_f32_bf16 v153, v42, s72, v153\n"
"v_dot2_f32_bf16 v154, v43, s68, v154\n"
"v_dot2_f32_bf16 v155, v43, s72, v155\n"
"s_waitcnt vmcnt(13)\n"
"ds_read_b64 v[28:29], v158 offset:3584\n"
"ds_read_b64 v[30:31], v158 offset:4096\n"
"ds_read_b64 v[32:33], v158 offset:4480\n"
"ds_read_b64 v[34:35], v158 offset:4992\n"
"buffer_load_dwordx4 a[100:103], v2, s[24:27], s98 offen offset:3072 sc1\n"
"ds_read_b64 v[36:37], v158 offset:5376\n"
"ds_read_b64 v[38:39], v158 offset:5888\n"
"ds_read_b64 v[40:41], v158 offset:6272\n"
"ds_read_b64 v[42:43], v158 offset:6784\n"
"v_pk_fma_f32 v[100:101], v[52:53], v[140:141], v[100:101] op_sel:[0,0,0] op_sel_hi:[1,0,1] neg_lo:[0,1,0] neg_hi:[0,1,0]\n"
"v_pk_fma_f32 v[102:103], v[54:55], v[140:141], v[102:103] op_sel:[0,0,0] op_sel_hi:[1,0,1] neg_lo:[0,1,0] neg_hi:[0,1,0]\n"
"v_pk_mul_f32 v[68:69], v[100:101], v[142:143] op_sel:[0,0] op_sel_hi:[1,0]\n"
"v_pk_mul_f32 v[70:71], v[102:103], v[142:143] op_sel:[0,0] op_sel_hi:[1,0]\n"
"v_pk_fma_f32 v[100:101], v[100:101], v[140:141], v[74:75] op_sel:[0,1,1] op_sel_hi:[1,1,1]\n"
"s_add_u32 s98, s97, 24576\n"
"buffer_load_dwordx4 a[104:107], v2, s[24:27], s98 offen offset:0 sc1\n"
"v_pk_fma_f32 v[102:103], v[102:103], v[140:141], v[74:75] op_sel:[0,1,1] op_sel_hi:[1,1,1]\n"
"v_exp_f32_e64 v68, v68 clamp\n"
"v_exp_f32_e64 v69, v69 clamp\n"
"v_exp_f32_e64 v70, v70 clamp\n"
"v_exp_f32_e64 v71, v71 clamp\n"
"v_max_f32 v100, v100, v75\n"
"v_max_f32 v101, v101, v75\n"
"v_max_f32 v102, v102, v75\n"
"v_max_f32 v103, v103, v75\n"
"v_pk_add_f32 v[100:101], v[100:101], v[68:69] op_sel:[0,0] op_sel_hi:[1,1]\n"
"v_pk_add_f32 v[102:103], v[102:103], v[70:71] op_sel:[0,0] op_sel_hi:[1,1]\n"
"v_cvt_pk_bf16_f32 v78, v100, v101\n"
"v_cvt_pk_bf16_f32 v79, v102, v103\n"
"s_waitcnt lgkmcnt(7)\n"
"v_dot2_f32_bf16 v148, v28, s68, v148\n"
"v_dot2_f32_bf16 v149, v28, s72, v149\n"
"v_dot2_f32_bf16 v150, v29, s68, v150\n"
"v_dot2_f32_bf16 v151, v29, s72, v151\n"
"s_waitcnt lgkmcnt(6)\n"
"v_dot2_f32_bf16 v152, v30, s68, v152\n"
"v_dot2_f32_bf16 v153, v30, s72, v153\n"
"v_dot2_f32_bf16 v154, v31, s68, v154\n"
"v_dot2_f32_bf16 v155, v31, s72, v155\n"
"s_waitcnt lgkmcnt(5)\n"
"v_dot2_f32_bf16 v148, v32, s68, v148\n"
"v_dot2_f32_bf16 v149, v32, s72, v149\n"
"v_dot2_f32_bf16 v150, v33, s68, v150\n"
"v_dot2_f32_bf16 v151, v33, s72, v151\n"
"s_waitcnt lgkmcnt(4)\n"
"v_dot2_f32_bf16 v152, v34, s68, v152\n"
"v_dot2_f32_bf16 v153, v34, s72, v153\n"
"v_dot2_f32_bf16 v154, v35, s68, v154\n"
"v_dot2_f32_bf16 v155, v35, s72, v155\n"
"s_waitcnt lgkmcnt(3)\n"
"v_dot2_f32_bf16 v148, v36, s68, v148\n"
"v_dot2_f32_bf16 v149, v36, s72, v149\n"
"v_dot2_f32_bf16 v150, v37, s68, v150\n"
"v_dot2_f32_bf16 v151, v37, s72, v151\n"
"s_waitcnt lgkmcnt(2)\n"
"v_dot2_f32_bf16 v152, v38, s68, v152\n"
"v_dot2_f32_bf16 v153, v38, s72, v153\n"
"v_dot2_f32_bf16 v154, v39, s68, v154\n"
"v_dot2_f32_bf16 v155, v39, s72, v155\n"
"s_waitcnt lgkmcnt(1)\n"
"v_dot2_f32_bf16 v148, v40, s68, v148\n"
"v_dot2_f32_bf16 v149, v40, s72, v149\n"
"v_dot2_f32_bf16 v150, v41, s68, v150\n"
"v_dot2_f32_bf16 v151, v41, s72, v151\n"
"s_waitcnt lgkmcnt(0)\n"
"v_dot2_f32_bf16 v152, v42, s68, v152\n"
"v_dot2_f32_bf16 v153, v42, s72, v153\n"
"v_dot2_f32_bf16 v154, v43, s68, v154\n"
"v_dot2_f32_bf16 v155, v43, s72, v155\n"
"s_waitcnt vmcnt(12)\n"
"ds_read_b64 v[28:29], v158 offset:7168\n"
"ds_read_b64 v[30:31], v158 offset:7680\n"
"ds_read_b64 v[32:33], v158 offset:8064\n"
"ds_read_b64 v[34:35], v158 offset:8576\n"
"ds_read_b64 v[36:37], v158 offset:8960\n"
"ds_read_b64 v[38:39], v158 offset:9472\n"
"v_pk_fma_f32 v[104:105], v[56:57], v[140:141], v[104:105] op_sel:[0,0,0] op_sel_hi:[1,0,1] neg_lo:[0,1,0] neg_hi:[0,1,0]\n"
"v_pk_fma_f32 v[106:107], v[58:59], v[140:141], v[106:107] op_sel:[0,0,0] op_sel_hi:[1,0,1] neg_lo:[0,1,0] neg_hi:[0,1,0]\n"
"v_pk_mul_f32 v[44:45], v[104:105], v[142:143] op_sel:[0,0] op_sel_hi:[1,0]\n"
"v_pk_mul_f32 v[46:47], v[106:107], v[142:143] op_sel:[0,0] op_sel_hi:[1,0]\n"
"v_pk_fma_f32 v[104:105], v[104:105], v[140:141], v[74:75] op_sel:[0,1,1] op_sel_hi:[1,1,1]\n"
"v_pk_fma_f32 v[106:107], v[106:107], v[140:141], v[74:75] op_sel:[0,1,1] op_sel_hi:[1,1,1]\n"
"v_exp_f32_e64 v44, v44 clamp\n"
"v_exp_f32_e64 v45, v45 clamp\n"
"v_exp_f32_e64 v46, v46 clamp\n"
"v_exp_f32_e64 v47, v47 clamp\n"
"v_max_f32 v104, v104, v75\n"
"v_max_f32 v105, v105, v75\n"
"v_max_f32 v106, v106, v75\n"
"v_max_f32 v107, v107, v75\n"
"v_pk_add_f32 v[104:105], v[104:105], v[44:45] op_sel:[0,0] op_sel_hi:[1,1]\n"
"v_pk_add_f32 v[106:107], v[106:107], v[46:47] op_sel:[0,0] op_sel_hi:[1,1]\n"
"v_cvt_pk_bf16_f32 v80, v104, v105\n"
"v_cvt_pk_bf16_f32 v81, v106, v107\n"
"s_waitcnt lgkmcnt(5)\n"
"v_dot2_f32_bf16 v148, v28, s68, v148\n"
"v_dot2_f32_bf16 v149, v28, s72, v149\n"
"v_dot2_f32_bf16 v150, v29, s68, v150\n"
"v_dot2_f32_bf16 v151, v29, s72, v151\n"
"s_waitcnt lgkmcnt(4)\n"
"v_dot2_f32_bf16 v152, v30, s68, v152\n"
"v_dot2_f32_bf16 v153, v30, s72, v153\n"
"v_dot2_f32_bf16 v154, v31, s68, v154\n"
"v_dot2_f32_bf16 v155, v31, s72, v155\n"
"s_waitcnt lgkmcnt(3)\n"
"v_dot2_f32_bf16 v148, v32, s68, v148\n"
"v_dot2_f32_bf16 v149, v32, s72, v149\n"
"v_dot2_f32_bf16 v150, v33, s68, v150\n"
"v_dot2_f32_bf16 v151, v33, s72, v151\n"
"s_waitcnt lgkmcnt(2)\n"
"v_dot2_f32_bf16 v152, v34, s68, v152\n"
"v_dot2_f32_bf16 v153, v34, s72, v153\n"
"v_dot2_f32_bf16 v154, v35, s68, v154\n"
"v_dot2_f32_bf16 v155, v35, s72, v155\n"
"s_waitcnt lgkmcnt(1)\n"
"v_dot2_f32_bf16 v148, v36, s68, v148\n"
"v_dot2_f32_bf16 v149, v36, s72, v149\n"
"v_dot2_f32_bf16 v150, v37, s68, v150\n"
"v_dot2_f32_bf16 v151, v37, s72, v151\n"
"s_waitcnt lgkmcnt(0)\n"
"v_dot2_f32_bf16 v152, v38, s68, v152\n"
"v_dot2_f32_bf16 v153, v38, s72, v153\n"
"v_dot2_f32_bf16 v154, v39, s68, v154\n"
"v_dot2_f32_bf16 v155, v39, s72, v155\n"
"v_cvt_pk_bf16_f32 v28, v148, v149\n"
"v_cvt_pk_bf16_f32 v29, v150, v151\n"
"v_cvt_pk_bf16_f32 v30, v152, v153\n"
"v_cvt_pk_bf16_f32 v31, v154, v155\n"
"ds_write_b64 v156, v[28:29]\n"
"ds_write_b64 v157, v[30:31]\n"
"v_pk_fma_f32 v[108:109], v[60:61], v[140:141], v[108:109] op_sel:[0,0,0] op_sel_hi:[1,0,1] neg_lo:[0,1,0] neg_hi:[0,1,0]\n"
"v_pk_fma_f32 v[110:111], v[62:63], v[140:141], v[110:111] op_sel:[0,0,0] op_sel_hi:[1,0,1] neg_lo:[0,1,0] neg_hi:[0,1,0]\n"
"v_pk_mul_f32 v[68:69], v[108:109], v[142:143] op_sel:[0,0] op_sel_hi:[1,0]\n"
"v_pk_mul_f32 v[70:71], v[110:111], v[142:143] op_sel:[0,0] op_sel_hi:[1,0]\n"
"v_pk_fma_f32 v[108:109], v[108:109], v[140:141], v[74:75] op_sel:[0,1,1] op_sel_hi:[1,1,1]\n"
"v_pk_fma_f32 v[110:111], v[110:111], v[140:141], v[74:75] op_sel:[0,1,1] op_sel_hi:[1,1,1]\n"
"v_exp_f32_e64 v68, v68 clamp\n"
"v_exp_f32_e64 v69, v69 clamp\n"
"v_exp_f32_e64 v70, v70 clamp\n"
"v_exp_f32_e64 v71, v71 clamp\n"
"v_max_f32 v108, v108, v75\n"
"v_max_f32 v109, v109, v75\n"
"v_max_f32 v110, v110, v75\n"
"v_max_f32 v111, v111, v75\n"
"v_pk_add_f32 v[108:109], v[108:109], v[68:69] op_sel:[0,0] op_sel_hi:[1,1]\n"
"v_pk_add_f32 v[110:111], v[110:111], v[70:71] op_sel:[0,0] op_sel_hi:[1,1]\n"
"v_cvt_pk_bf16_f32 v82, v108, v109\n"
"v_cvt_pk_bf16_f32 v83, v110, v111\n"
"buffer_load_dword v73, v72, s[16:19], 0 offen sc1\n"
"s_waitcnt lgkmcnt(0)\n"
"s_barrier\n"
"ds_read_b128 v[88:91], v11 offset:17920\n"
"ds_read_b128 v[92:95], v11 offset:17984\n"
"ds_read_b128 v[28:31], v11 offset:18048\n"
"ds_read_b128 v[32:35], v10 offset:0\n"
"v_mov_b32 v86, 0\n"
"v_mov_b32 v87, 0\n"
"v_pk_fma_f32 v[112:113], v[64:65], v[140:141], v[112:113] op_sel:[0,0,0] op_sel_hi:[1,0,1] neg_lo:[0,1,0] neg_hi:[0,1,0]\n"
"v_pk_fma_f32 v[114:115], v[66:67], v[140:141], v[114:115] op_sel:[0,0,0] op_sel_hi:[1,0,1] neg_lo:[0,1,0] neg_hi:[0,1,0]\n"
"v_pk_mul_f32 v[44:45], v[112:113], v[142:143] op_sel:[0,0] op_sel_hi:[1,0]\n"
"v_pk_mul_f32 v[46:47], v[114:115], v[142:143] op_sel:[0,0] op_sel_hi:[1,0]\n"
"v_pk_fma_f32 v[112:113], v[112:113], v[140:141], v[74:75] op_sel:[0,1,1] op_sel_hi:[1,1,1]\n"
"v_pk_fma_f32 v[114:115], v[114:115], v[140:141], v[74:75] op_sel:[0,1,1] op_sel_hi:[1,1,1]\n"
"v_exp_f32_e64 v44, v44 clamp\n"
"v_exp_f32_e64 v45, v45 clamp\n"
"v_exp_f32_e64 v46, v46 clamp\n"
"v_exp_f32_e64 v47, v47 clamp\n"
"v_max_f32 v112, v112, v75\n"
"v_max_f32 v113, v113, v75\n"
"v_max_f32 v114, v114, v75\n"
"v_max_f32 v115, v115, v75\n"
"v_pk_add_f32 v[112:113], v[112:113], v[44:45] op_sel:[0,0] op_sel_hi:[1,1]\n"
"v_pk_add_f32 v[114:115], v[114:115], v[46:47] op_sel:[0,0] op_sel_hi:[1,1]\n"
"v_cvt_pk_bf16_f32 v84, v112, v113\n"
"v_cvt_pk_bf16_f32 v85, v114, v115\n"
"v_pk_fma_f32 v[116:117], v[48:49], v[144:145], v[116:117] op_sel:[0,0,0] op_sel_hi:[1,0,1] neg_lo:[0,1,0] neg_hi:[0,1,0]\n"
"v_pk_fma_f32 v[118:119], v[50:51], v[144:145], v[118:119] op_sel:[0,0,0] op_sel_hi:[1,0,1] neg_lo:[0,1,0] neg_hi:[0,1,0]\n"
"v_pk_mul_f32 v[44:45], v[116:117], v[146:147] op_sel:[0,0] op_sel_hi:[1,0]\n"
"v_pk_mul_f32 v[46:47], v[118:119], v[146:147] op_sel:[0,0] op_sel_hi:[1,0]\n"
"v_pk_fma_f32 v[116:117], v[116:117], v[144:145], v[74:75] op_sel:[0,1,1] op_sel_hi:[1,1,1]\n"
"v_pk_fma_f32 v[118:119], v[118:119], v[144:145], v[74:75] op_sel:[0,1,1] op_sel_hi:[1,1,1]\n"
"v_exp_f32_e64 v44, v44 clamp\n"
"v_exp_f32_e64 v45, v45 clamp\n"
"v_exp_f32_e64 v46, v46 clamp\n"
"v_exp_f32_e64 v47, v47 clamp\n"
"v_max_f32 v116, v116, v75\n"
"v_max_f32 v117, v117, v75\n"
"v_max_f32 v118, v118, v75\n"
"v_max_f32 v119, v119, v75\n"
"v_pk_add_f32 v[116:117], v[116:117], v[44:45] op_sel:[0,0] op_sel_hi:[1,1]\n"
"v_pk_add_f32 v[118:119], v[118:119], v[46:47] op_sel:[0,0] op_sel_hi:[1,1]\n"
"v_cvt_pk_bf16_f32 v96, v116, v117\n"
"v_cvt_pk_bf16_f32 v97, v118, v119\n"
"v_pk_fma_f32 v[120:121], v[52:53], v[144:145], v[120:121] op_sel:[0,0,0] op_sel_hi:[1,0,1] neg_lo:[0,1,0] neg_hi:[0,1,0]\n"
"v_pk_fma_f32 v[122:123], v[54:55], v[144:145], v[122:123] op_sel:[0,0,0] op_sel_hi:[1,0,1] neg_lo:[0,1,0] neg_hi:[0,1,0]\n"
"v_pk_mul_f32 v[68:69], v[120:121], v[146:147] op_sel:[0,0] op_sel_hi:[1,0]\n"
"v_pk_mul_f32 v[70:71], v[122:123], v[146:147] op_sel:[0,0] op_sel_hi:[1,0]\n"
"v_pk_fma_f32 v[120:121], v[120:121], v[144:145], v[74:75] op_sel:[0,1,1] op_sel_hi:[1,1,1]\n"
"v_pk_fma_f32 v[122:123], v[122:123], v[144:145], v[74:75] op_sel:[0,1,1] op_sel_hi:[1,1,1]\n"
"v_exp_f32_e64 v68, v68 clamp\n"
"v_exp_f32_e64 v69, v69 clamp\n"
"v_exp_f32_e64 v70, v70 clamp\n"
"v_exp_f32_e64 v71, v71 clamp\n"
"v_max_f32 v120, v120, v75\n"
"v_max_f32 v121, v121, v75\n"
"v_max_f32 v122, v122, v75\n"
"v_max_f32 v123, v123, v75\n"
"v_pk_add_f32 v[120:121], v[120:121], v[68:69] op_sel:[0,0] op_sel_hi:[1,1]\n"
"v_pk_add_f32 v[122:123], v[122:123], v[70:71] op_sel:[0,0] op_sel_hi:[1,1]\n"
"v_cvt_pk_bf16_f32 v98, v120, v121\n"
"v_cvt_pk_bf16_f32 v99, v122, v123\n"
"v_pk_fma_f32 v[124:125], v[56:57], v[144:145], v[124:125] op_sel:[0,0,0] op_sel_hi:[1,0,1] neg_lo:[0,1,0] neg_hi:[0,1,0]\n"
"v_pk_fma_f32 v[126:127], v[58:59], v[144:145], v[126:127] op_sel:[0,0,0] op_sel_hi:[1,0,1] neg_lo:[0,1,0] neg_hi:[0,1,0]\n"
"v_pk_mul_f32 v[44:45], v[124:125], v[146:147] op_sel:[0,0] op_sel_hi:[1,0]\n"
"v_pk_mul_f32 v[46:47], v[126:127], v[146:147] op_sel:[0,0] op_sel_hi:[1,0]\n"
"v_pk_fma_f32 v[124:125], v[124:125], v[144:145], v[74:75] op_sel:[0,1,1] op_sel_hi:[1,1,1]\n"
"v_pk_fma_f32 v[126:127], v[126:127], v[144:145], v[74:75] op_sel:[0,1,1] op_sel_hi:[1,1,1]\n"
"v_exp_f32_e64 v44, v44 clamp\n"
"v_exp_f32_e64 v45, v45 clamp\n"
"v_exp_f32_e64 v46, v46 clamp\n"
"v_exp_f32_e64 v47, v47 clamp\n"
"v_max_f32 v124, v124, v75\n"
"v_max_f32 v125, v125, v75\n"
"v_max_f32 v126, v126, v75\n"
"v_max_f32 v127, v127, v75\n"
"v_pk_add_f32 v[124:125], v[124:125], v[44:45] op_sel:[0,0] op_sel_hi:[1,1]\n"
"v_pk_add_f32 v[126:127], v[126:127], v[46:47] op_sel:[0,0] op_sel_hi:[1,1]\n"
"v_cvt_pk_bf16_f32 v100, v124, v125\n"
"v_cvt_pk_bf16_f32 v101, v126, v127\n"
"v_pk_fma_f32 v[128:129], v[60:61], v[144:145], v[128:129] op_sel:[0,0,0] op_sel_hi:[1,0,1] neg_lo:[0,1,0] neg_hi:[0,1,0]\n"
"v_pk_fma_f32 v[130:131], v[62:63], v[144:145], v[130:131] op_sel:[0,0,0] op_sel_hi:[1,0,1] neg_lo:[0,1,0] neg_hi:[0,1,0]\n"
"v_pk_mul_f32 v[68:69], v[128:129], v[146:147] op_sel:[0,0] op_sel_hi:[1,0]\n"
"v_pk_mul_f32 v[70:71], v[130:131], v[146:147] op_sel:[0,0] op_sel_hi:[1,0]\n"
"v_pk_fma_f32 v[128:129], v[128:129], v[144:145], v[74:75] op_sel:[0,1,1] op_sel_hi:[1,1,1]\n"
"v_pk_fma_f32 v[130:131], v[130:131], v[144:145], v[74:75] op_sel:[0,1,1] op_sel_hi:[1,1,1]\n"
"v_exp_f32_e64 v68, v68 clamp\n"
"v_exp_f32_e64 v69, v69 clamp\n"
"v_exp_f32_e64 v70, v70 clamp\n"
"v_exp_f32_e64 v71, v71 clamp\n"
"v_max_f32 v128, v128, v75\n"
"v_max_f32 v129, v129, v75\n"
"v_max_f32 v130, v130, v75\n"
"v_max_f32 v131, v131, v75\n"
"v_pk_add_f32 v[128:129], v[128:129], v[68:69] op_sel:[0,0] op_sel_hi:[1,1]\n"
"v_pk_add_f32 v[130:131], v[130:131], v[70:71] op_sel:[0,0] op_sel_hi:[1,1]\n"
"v_cvt_pk_bf16_f32 v102, v128, v129\n"
"v_cvt_pk_bf16_f32 v103, v130, v131\n"
"s_waitcnt vmcnt(0)\n"
"v_cmp_ne_u32 vcc, s52, v73\n"
"s_nop 0\n"
"s_cbranch_vccnz .Lfb_slow_37\n"
".Lfb_back_38:\n"
"s_xor_b32 s85, s3, 3\n"
"s_mul_i32 s85, s85, 116736\n"
"s_add_u32 s85, s85, s45\n"
"buffer_load_dwordx4 a[128:131], v2, s[20:23], s85 offen offset:0 sc1\n"
"v_mov_b32 v106, 0\n"
"v_mov_b32 v107, 0\n"
"v_pk_fma_f32 v[132:133], v[64:65], v[144:145], v[132:133] op_sel:[0,0,0] op_sel_hi:[1,0,1] neg_lo:[0,1,0] neg_hi:[0,1,0]\n"
"v_pk_fma_f32 v[134:135], v[66:67], v[144:145], v[134:135] op_sel:[0,0,0] op_sel_hi:[1,0,1] neg_lo:[0,1,0] neg_hi:[0,1,0]\n"
"v_pk_mul_f32 v[44:45], v[132:133], v[146:147] op_sel:[0,0] op_sel_hi:[1,0]\n"
"v_pk_mul_f32 v[46:47], v[134:135], v[146:147] op_sel:[0,0] op_sel_hi:[1,0]\n"
"v_pk_fma_f32 v[132:133], v[132:133], v[144:145], v[74:75] op_sel:[0,1,1] op_sel_hi:[1,1,1]\n"
"v_pk_fma_f32 v[134:135], v[134:135], v[144:145], v[74:75] op_sel:[0,1,1] op_sel_hi:[1,1,1]\n"
"v_exp_f32_e64 v44, v44 clamp\n"
"v_exp_f32_e64 v45, v45 clamp\n"
"v_exp_f32_e64 v46, v46 clamp\n"
"v_exp_f32_e64 v47, v47 clamp\n"
"buffer_load_dwordx4 a[132:135], v2, s[20:23], s85 offen offset:1024 sc1\n"
"v_max_f32 v132, v132, v75\n"
"v_max_f32 v133, v133, v75\n"
"v_max_f32 v134, v134, v75\n"
"v_max_f32 v135, v135, v75\n"
"v_pk_add_f32 v[132:133], v[132:133], v[44:45] op_sel:[0,0] op_sel_hi:[1,1]\n"
"v_pk_add_f32 v[134:135], v[134:135], v[46:47] op_sel:[0,0] op_sel_hi:[1,1]\n"
"v_cvt_pk_bf16_f32 v104, v132, v133\n"
"v_cvt_pk_bf16_f32 v105, v134, v135\n"
"ds_read_b128 v[52:55], v10 offset:3584\n"
"ds_read_b128 v[56:59], v10 offset:3648\n"
"ds_read_b128 v[60:63], v10 offset:7168\n"
"s_waitcnt lgkmcnt(6)\n"
"v_mfma_f32_16x16x32_bf16 v[132:135], v[88:91], v[16:19], 0\n"
"buffer_load_dwordx4 a[136:139], v2, s[20:23], s85 offen offset:2048 sc1\n"
"v_mfma_f32_16x16x32_bf16 v[68:71], v[88:91], v[76:79], 0\n"
"v_mfma_f32_16x16x32_bf16 v[156:159], v[88:91], v[96:99], 0\n"
"s_waitcnt lgkmcnt(5)\n"
"v_mfma_f32_16x16x32_bf16 v[132:135], v[92:95], v[20:23], v[132:135]\n"
"ds_read_b128 v[88:91], v10 offset:7232\n"
"v_mfma_f32_16x16x32_bf16 v[68:71], v[92:95], v[80:83], v[68:71]\n"
"buffer_load_dwordx4 a[140:143], v2, s[20:23], s85 offen offset:3072 sc1\n"
"v_mfma_f32_16x16x32_bf16 v[156:159], v[92:95], v[100:103], v[156:159]\n"
"s_waitcnt lgkmcnt(5)\n"
"v_mfma_f32_16x16x32_bf16 v[132:135], v[28:31], v[24:27], v[132:135]\n"
"ds_read_b128 v[92:95], v10 offset:10816\n"
"s_add_u32 s85, s85, 4096\n"
"v_mfma_f32_16x16x32_bf16 v[68:71], v[28:31], v[84:87], v[68:71]\n"
"v_mfma_f32_16x16x32_bf16 v[156:159], v[28:31], v[104:107], v[156:159]\n"
"buffer_load_dwordx4 a[144:147], v2, s[20:23], s85 offen offset:0 sc1\n"
"s_waitcnt lgkmcnt(5)\n"
"v_mfma_f32_16x16x32_bf16 v[112:115], v[32:35], v[16:19], 0\n"
"ds_read_b128 v[28:31], v10 offset:10880\n"
"v_mfma_f32_16x16x32_bf16 v[36:39], v[32:35], v[76:79], 0\n"
"v_mfma_f32_16x16x32_bf16 v[136:139], v[32:35], v[96:99], 0\n"
"s_waitcnt lgkmcnt(5)\n"
"v_mfma_f32_16x16x32_bf16 v[116:119], v[52:55], v[16:19], 0\n"
"ds_read_b128 v[32:35], v10 offset:14400\n"
"buffer_load_dwordx4 a[148:151], v2, s[20:23], s85 offen offset:1024 sc1\n"
"v_mfma_f32_16x16x32_bf16 v[40:43], v[52:55], v[76:79], 0\n"
"s_xor_b32 s85, s3, 2\n"
"s_mul_i32 s85, s85, 116736\n"
"v_mfma_f32_16x16x32_bf16 v[140:143], v[52:55], v[96:99], 0\n"
"v_add_f32 v132, v132, v75\n"
"s_add_u32 s85, s85, s45\n"
"s_waitcnt lgkmcnt(5)\n"
"v_mfma_f32_16x16x32_bf16 v[116:119], v[56:59], v[20:23], v[116:119]\n"
"ds_read_b128 v[52:55], v10 offset:14464\n"
"v_add_f32 v133, v133, v75\n"
"v_mfma_f32_16x16x32_bf16 v[40:43], v[56:59], v[80:83], v[40:43]\n"
"buffer_load_dwordx4 a[152:155], v2, s[20:23], s85 offen offset:0 sc1\n"
"v_add_f32 v134, v134, v75\n"
"v_mfma_f32_16x16x32_bf16 v[140:143], v[56:59], v[100:103], v[140:143]\n"
"v_add_f32 v135, v135, v75\n"
"v_add_f32 v68, v68, v75\n"
"s_waitcnt lgkmcnt(5)\n"
"v_mfma_f32_16x16x32_bf16 v[120:123], v[60:63], v[16:19], 0\n"
"v_rcp_f32 v132, v132\n"
"v_mfma_f32_16x16x32_bf16 v[44:47], v[60:63], v[76:79], 0\n"
"v_rcp_f32 v133, v133\n"
"v_mfma_f32_16x16x32_bf16 v[144:147], v[60:63], v[96:99], 0\n"
"buffer_load_dwordx4 a[156:159], v2, s[20:23], s85 offen offset:1024 sc1\n"
"v_cndmask_b32 v72, v133, v132, s[88:89]\n"
"s_waitcnt lgkmcnt(4)\n"
"v_mfma_f32_16x16x32_bf16 v[120:123], v[88:91], v[20:23], v[120:123]\n"
"v_rcp_f32 v134, v134\n"
"v_mfma_f32_16x16x32_bf16 v[44:47], v[88:91], v[80:83], v[44:47]\n"
"v_rcp_f32 v135, v135\n"
"v_mfma_f32_16x16x32_bf16 v[144:147], v[88:91], v[100:103], v[144:147]\n"
"v_cndmask_b32 v73, v134, v133, s[90:91]\n"
"v_cndmask_b32 v74, v135, v134, s[92:93]\n"
"s_waitcnt lgkmcnt(3)\n"
"v_mfma_f32_16x16x32_bf16 v[124:127], v[92:95], v[20:23], 0\n"
"buffer_load_dwordx4 a[160:163], v2, s[20:23], s85 offen offset:2048 sc1\n"
"v_add_f32 v69, v69, v75\n"
"v_mfma_f32_16x16x32_bf16 v[48:51], v[92:95], v[80:83], 0\n"
"v_add_f32 v70, v70, v75\n"
"v_add_f32 v71, v71, v75\n"
"v_mfma_f32_16x16x32_bf16 v[148:151], v[92:95], v[100:103], 0\n"
"s_waitcnt lgkmcnt(0)\n"
"s_barrier\n"
"v_mfma_f32_16x16x32_bf16 v[124:127], v[28:31], v[24:27], v[124:127]\n"
"v_rcp_f32 v68, v68\n"
"v_mfma_f32_16x16x32_bf16 v[48:51], v[28:31], v[84:87], v[48:51]\n"
"buffer_load_dwordx4 a[164:167], v2, s[20:23], s85 offen offset:3072 sc1\n"
"v_add_f32 v156, v156, v75\n"
"v_mfma_f32_16x16x32_bf16 v[148:151], v[28:31], v[104:107], v[148:151]\n"
"v_rcp_f32 v69, v69\n"
"v_mfma_f32_16x16x32_bf16 v[128:131], v[32:35], v[20:23], 0\n"
"v_cndmask_b32 v14, v69, v68, s[88:89]\n"
"s_add_u32 s85, s85, 4096\n"
"v_mfma_f32_16x16x32_bf16 v[64:67], v[32:35], v[80:83], 0\n"
"v_rcp_f32 v70, v70\n"
"v_mfma_f32_16x16x32_bf16 v[152:155], v[32:35], v[100:103], 0\n"
"buffer_load_dwordx4 a[168:171], v2, s[20:23], s85 offen offset:0 sc1\n"
"v_cndmask_b32 v15, v70, v69, s[90:91]\n"
"v_mfma_f32_16x16x32_bf16 v[128:131], v[52:55], v[24:27], v[128:131]\n"
"v_rcp_f32 v71, v71\n"
"v_mfma_f32_16x16x32_bf16 v[64:67], v[52:55], v[84:87], v[64:67]\n"
"v_cndmask_b32 v108, v71, v70, s[92:93]\n"
"v_add_f32 v157, v157, v75\n"
"v_mfma_f32_16x16x32_bf16 v[152:155], v[52:55], v[104:107], v[152:155]\n"
"v_add_f32 v158, v158, v75\n"
"v_add_f32 v159, v159, v75\n"
"v_rcp_f32 v156, v156\n"
"v_rcp_f32 v157, v157\n"
"v_rcp_f32 v158, v158\n"
"v_rcp_f32 v159, v159\n"
"v_cndmask_b32 v109, v157, v156, s[88:89]\n"
"v_cndmask_b32 v110, v158, v157, s[90:91]\n"
"v_cndmask_b32 v111, v159, v158, s[92:93]\n"
"v_pk_mul_f32 v[112:113], v[112:113], v[132:133] op_sel:[0,0] op_sel_hi:[1,0]\n"
"v_pk_mul_f32 v[114:115], v[114:115], v[132:133] op_sel:[0,0] op_sel_hi:[1,0]\n"
"v_cvt_pk_bf16_f32 v16, v112, v113\n"
"v_cvt_pk_bf16_f32 v17, v114, v115\n"
"v_pk_mul_f32 v[116:117], v[116:117], v[72:73] op_sel:[0,0] op_sel_hi:[1,0]\n"
"v_pk_mul_f32 v[118:119], v[118:119], v[72:73] op_sel:[0,0] op_sel_hi:[1,0]\n"
"v_cvt_pk_bf16_f32 v18, v116, v117\n"
"v_cvt_pk_bf16_f32 v19, v118, v119\n"
"buffer_store_dwordx4 v[16:19], v2, s[20:23], s77 offen sc1\n"
"v_pk_mul_f32 v[120:121], v[120:121], v[72:73] op_sel:[0,1] op_sel_hi:[1,1]\n"
"v_pk_mul_f32 v[122:123], v[122:123], v[72:73] op_sel:[0,1] op_sel_hi:[1,1]\n"
"v_cvt_pk_bf16_f32 v20, v120, v121\n"
"v_cvt_pk_bf16_f32 v21, v122, v123\n"
"v_pk_mul_f32 v[124:125], v[124:125], v[74:75] op_sel:[0,0] op_sel_hi:[1,0]\n"
"v_pk_mul_f32 v[126:127], v[126:127], v[74:75] op_sel:[0,0] op_sel_hi:[1,0]\n"
"v_cvt_pk_bf16_f32 v22, v124, v125\n"
"v_cvt_pk_bf16_f32 v23, v126, v127\n"
"buffer_store_dwordx4 v[20:23], v2, s[20:23], s77 offen offset:1024 sc1\n"
"v_pk_mul_f32 v[128:129], v[128:129], v[134:135] op_sel:[0,1] op_sel_hi:[1,1]\n"
"v_pk_mul_f32 v[130:131], v[130:131], v[134:135] op_sel:[0,1] op_sel_hi:[1,1]\n"
"v_cvt_pk_bf16_f32 v24, v128, v129\n"
"v_cvt_pk_bf16_f32 v25, v130, v131\n"
"s_nop 0\n"
"v_permlane32_swap_b32 v24, v26\n"
"v_permlane32_swap_b32 v25, v27\n"
"buffer_store_dwordx4 v[24:27], v12, s[20:23], s94 offen sc1\n"
"s_add_u32 s77, s77, 116736\n"
"s_add_u32 s94, s94, 116736\n"
"v_pk_mul_f32 v[36:37], v[36:37], v[68:69] op_sel:[0,0] op_sel_hi:[1,0]\n"
"v_pk_mul_f32 v[38:39], v[38:39], v[68:69] op_sel:[0,0] op_sel_hi:[1,0]\n"
"v_cvt_pk_bf16_f32 v76, v36, v37\n"
"v_cvt_pk_bf16_f32 v77, v38, v39\n"
"v_pk_mul_f32 v[40:41], v[40:41], v[14:15] op_sel:[0,0] op_sel_hi:[1,0]\n"
"v_pk_mul_f32 v[42:43], v[42:43], v[14:15] op_sel:[0,0] op_sel_hi:[1,0]\n"
"v_cvt_pk_bf16_f32 v78, v40, v41\n"
"v_cvt_pk_bf16_f32 v79, v42, v43\n"
"buffer_store_dwordx4 v[76:79], v2, s[20:23], s77 offen sc1\n"
"v_pk_mul_f32 v[44:45], v[44:45], v[14:15] op_sel:[0,1] op_sel_hi:[1,1]\n"
"v_pk_mul_f32 v[46:47], v[46:47], v[14:15] op_sel:[0,1] op_sel_hi:[1,1]\n"
"v_cvt_pk_bf16_f32 v80, v44, v45\n"
"v_cvt_pk_bf16_f32 v81, v46, v47\n"
"v_pk_mul_f32 v[48:49], v[48:49], v[108:109] op_sel:[0,0] op_sel_hi:[1,0]\n"
"v_pk_mul_f32 v[50:51], v[50:51], v[108:109] op_sel:[0,0] op_sel_hi:[1,0]\n"
"v_cvt_pk_bf16_f32 v82, v48, v49\n"
"v_cvt_pk_bf16_f32 v83, v50, v51\n"
"buffer_store_dwordx4 v[80:83], v2, s[20:23], s77 offen offset:1024 sc1\n"
"v_pk_mul_f32 v[64:65], v[64:65], v[70:71] op_sel:[0,1] op_sel_hi:[1,1]\n"
"v_pk_mul_f32 v[66:67], v[66:67], v[70:71] op_sel:[0,1] op_sel_hi:[1,1]\n"
"v_cvt_pk_bf16_f32 v84, v64, v65\n"
"v_cvt_pk_bf16_f32 v85, v66, v67\n"
"s_nop 0\n"
"v_permlane32_swap_b32 v84, v86\n"
"v_permlane32_swap_b32 v85, v87\n"
"buffer_store_dwordx4 v[84:87], v12, s[20:23], s94 offen sc1\n"
"s_add_u32 s77, s77, 116736\n"
"s_add_u32 s94, s94, 116736\n"
"v_pk_mul_f32 v[136:137], v[136:137], v[156:157] op_sel:[0,0] op_sel_hi:[1,0]\n"
"v_pk_mul_f32 v[138:139], v[138:139], v[156:157] op_sel:[0,0] op_sel_hi:[1,0]\n"
"v_cvt_pk_bf16_f32 v96, v136, v137\n"
"v_cvt_pk_bf16_f32 v97, v138, v139\n"
"v_pk_mul_f32 v[140:141], v[140:141], v[108:109] op_sel:[0,1] op_sel_hi:[1,1]\n"
"v_pk_mul_f32 v[142:143], v[142:143], v[108:109] op_sel:[0,1] op_sel_hi:[1,1]\n"
"v_cvt_pk_bf16_f32 v98, v140, v141\n"
"v_cvt_pk_bf16_f32 v99, v142, v143\n"
"buffer_store_dwordx4 v[96:99], v2, s[20:23], s77 offen sc1\n"
"v_pk_mul_f32 v[144:145], v[144:145], v[110:111] op_sel:[0,0] op_sel_hi:[1,0]\n"
"v_pk_mul_f32 v[146:147], v[146:147], v[110:111] op_sel:[0,0] op_sel_hi:[1,0]\n"
"v_cvt_pk_bf16_f32 v100, v144, v145\n"
"v_cvt_pk_bf16_f32 v101, v146, v147\n"
"v_pk_mul_f32 v[148:149], v[148:149], v[110:111] op_sel:[0,1] op_sel_hi:[1,1]\n"
"v_pk_mul_f32 v[150:151], v[150:151], v[110:111] op_sel:[0,1] op_sel_hi:[1,1]\n"
"v_cvt_pk_bf16_f32 v102, v148, v149\n"
"v_cvt_pk_bf16_f32 v103, v150, v151\n"
"buffer_store_dwordx4 v[100:103], v2, s[20:23], s77 offen offset:1024 sc1\n"
"v_pk_mul_f32 v[152:153], v[152:153], v[158:159] op_sel:[0,1] op_sel_hi:[1,1]\n"
"v_pk_mul_f32 v[154:155], v[154:155], v[158:159] op_sel:[0,1] op_sel_hi:[1,1]\n"
"v_cvt_pk_bf16_f32 v104, v152, v153\n"
"v_cvt_pk_bf16_f32 v105, v154, v155\n"
"s_nop 0\n"
"v_permlane32_swap_b32 v104, v106\n"
"v_permlane32_swap_b32 v105, v107\n"
"buffer_store_dwordx4 v[104:107], v12, s[20:23], s94 offen sc1\n"
"buffer_load_dwordx4 a[172:175], v2, s[20:23], s85 offen offset:1024 sc1\n"
"s_waitcnt vmcnt(18)\n"
"v_mfma_f32_16x16x32_bf16 a[188:191], a[240:243], a[128:131], 0\n"
"s_xor_b32 s85, s3, 1\n"
"s_mul_i32 s85, s85, 116736\n"
"v_mfma_f32_16x16x32_bf16 a[188:191], v[248:251], a[132:135], a[188:191]\n"
"s_add_u32 s85, s85, s45\n"
"s_mul_i32 s62, s3, 3072\n"
"v_mfma_f32_16x16x32_bf16 a[188:191], v[228:231], a[136:139], a[188:191]\n"
"buffer_load_dwordx4 a[176:179], v2, s[20:23], s85 offen offset:0 sc1\n"
"s_add_u32 s62, s62, 126976\n"
"v_mfma_f32_16x16x32_bf16 a[192:195], a[244:247], a[128:131], 0\n"
"v_and_b32 v11, 15, v1\n"
"v_mul_u32_u24 v10, 160, v11\n"
"v_mfma_f32_16x16x32_bf16 a[192:195], v[244:247], a[132:135], a[192:195]\n"
"buffer_load_dwordx4 a[180:183], v2, s[20:23], s85 offen offset:1024 sc1\n"
"v_lshrrev_b32 v14, 4, v1\n"
"v_mfma_f32_16x16x32_bf16 a[192:195], v[224:227], a[136:139], a[192:195]\n"
"v_lshl_add_u32 v10, v14, 3, v10\n"
"v_add_u32 v10, s62, v10\n"
"v_mfma_f32_16x16x32_bf16 a[196:199], a[248:251], a[128:131], 0\n"
"v_mfma_f32_16x16x32_bf16 a[196:199], v[240:243], a[132:135], a[196:199]\n"
"ds_read_b64 v[44:45], v10 offset:0\n"
"ds_read_b64 v[46:47], v10 offset:32\n"
"v_mfma_f32_16x16x32_bf16 a[196:199], v[220:223], a[136:139], a[196:199]\n"
"buffer_load_dwordx4 a[184:187], v2, s[20:23], s85 offen offset:2048 sc1\n"
"ds_read_b64 v[48:49], v10 offset:64\n"
"v_mfma_f32_16x16x32_bf16 a[200:203], a[252:255], a[128:131], 0\n"
"ds_read_b64 v[50:51], v10 offset:96\n"
"ds_read_b64 v[52:53], v10 offset:128\n"
"v_mfma_f32_16x16x32_bf16 a[200:203], v[236:239], a[132:135], a[200:203]\n"
"v_lshl_add_u32 v10, v11, 4, v10\n"
"v_add_u32 v10, 16384, v10\n"
"v_mfma_f32_16x16x32_bf16 a[200:203], v[216:219], a[136:139], a[200:203]\n"
"s_add_u32 s62, s62, 16384\n"
"v_mfma_f32_16x16x32_bf16 a[204:207], v[252:255], a[128:131], 0\n"
"buffer_load_dwordx4 a[228:231], v2, s[20:23], s85 offen offset:3072 sc1\n"
"v_bfe_u32 v11, v1, 4, 1\n"
"v_mfma_f32_16x16x32_bf16 a[204:207], v[232:235], a[132:135], a[204:207]\n"
"v_mul_u32_u24 v11, 44, v11\n"
"v_lshrrev_b32 v14, 5, v1\n"
"v_mfma_f32_16x16x32_bf16 a[204:207], v[212:215], a[136:139], a[204:207]\n"
"v_add3_u32 v11, v11, v14, s47\n"
"s_waitcnt vmcnt(19)\n"
"v_mfma_f32_16x16x32_bf16 a[188:191], v[208:211], a[140:143], a[188:191]\n"
"v_lshlrev_b32 v11, 7, v11\n"
"s_add_u32 s85, s85, 4096\n"
"v_mfma_f32_16x16x32_bf16 a[188:191], v[188:191], a[144:147], a[188:191]\n"
"s_lshl_b32 s34, s40, 7\n"
"s_add_u32 s34, s34, 1325056\n"
"v_mfma_f32_16x16x32_bf16 a[188:191], v[168:171], a[148:151], a[188:191]\n"
"buffer_load_dwordx4 a[232:235], v2, s[20:23], s85 offen offset:0 sc1\n"
"s_xor_b32 s60, s40, 32\n"
"v_mfma_f32_16x16x32_bf16 a[192:195], v[204:207], a[140:143], a[192:195]\n"
"s_lshl_b32 s61, s60, 7\n"
"s_add_u32 s61, s61, 1325056\n"
"v_mfma_f32_16x16x32_bf16 a[192:195], v[184:187], a[144:147], a[192:195]\n"
"v_mov_b32 v8, s61\n"
"s_mul_i32 s63, s56, 5120\n"
"v_mfma_f32_16x16x32_bf16 a[192:195], v[164:167], a[148:151], a[192:195]\n"
"v_lshlrev_b32 v15, 4, v0\n"
"v_add_u32 v15, 81920, v15\n"
"v_mfma_f32_16x16x32_bf16 a[196:199], v[200:203], a[140:143], a[196:199]\n"
"v_xor_b32 v156, 3072, v15\n"
"v_xor_b32 v157, 2048, v15\n"
"v_mfma_f32_16x16x32_bf16 a[196:199], v[180:183], a[144:147], a[196:199]\n"
"buffer_load_dwordx4 a[236:239], v2, s[20:23], s85 offen offset:1024 sc1\n"
"v_xor_b32 v158, 1024, v15\n"
"v_mfma_f32_16x16x32_bf16 a[196:199], v[160:163], a[148:151], a[196:199]\n"
"s_xor_b32 s85, s3, 0\n"
"s_mul_i32 s85, s85, 116736\n"
"v_mfma_f32_16x16x32_bf16 a[200:203], v[196:199], a[140:143], a[200:203]\n"
"ds_write_b128 v15, a[188:191] offset:0\n"
"v_mfma_f32_16x16x32_bf16 a[200:203], v[176:179], a[144:147], a[200:203]\n"
"s_add_u32 s85, s85, s45\n"
"s_add_u32 s63, s63, 75776\n"
"v_mfma_f32_16x16x32_bf16 a[200:203], a[0:3], a[148:151], a[200:203]\n"
"buffer_load_dwordx4 v[20:23], v2, s[20:23], s85 offen offset:0 sc1\n"
"s_lshr_b32 s60, s60, 3\n"
"v_mfma_f32_16x16x32_bf16 a[204:207], v[192:195], a[140:143], a[204:207]\n"
"ds_write_b128 v15, a[192:195] offset:4096\n"
"v_mfma_f32_16x16x32_bf16 a[204:207], v[172:175], a[144:147], a[204:207]\n"
"s_mul_i32 s60, s60, 116736\n"
"s_add_u32 s60, s60, s63\n"
"v_mfma_f32_16x16x32_bf16 a[204:207], a[4:7], a[148:151], a[204:207]\n"
"s_waitcnt vmcnt(19)\n"
"s_add_u32 s61, s60, 4096\n"
"v_mfma_f32_16x16x32_bf16 a[208:211], a[240:243], a[152:155], 0\n"
"ds_write_b128 v15, a[196:199] offset:8192\n"
"v_mfma_f32_16x16x32_bf16 a[208:211], v[248:251], a[156:159], a[208:211]\n"
"buffer_load_dwordx4 v[24:27], v2, s[20:23], s85 offen offset:1024 sc1\n"
"s_lshr_b32 s40, s40, 3\n"
"v_mfma_f32_16x16x32_bf16 a[208:211], v[228:231], a[160:163], a[208:211]\n"
"s_mul_i32 s40, s40, 116736\n"
"s_add_u32 s40, s40, s63\n"
"v_mfma_f32_16x16x32_bf16 a[212:215], a[244:247], a[152:155], 0\n"
"ds_write_b128 v15, a[200:203] offset:12288\n"
"v_mfma_f32_16x16x32_bf16 a[212:215], v[244:247], a[156:159], a[212:215]\n"
"buffer_load_dwordx4 v[28:31], v2, s[20:23], s85 offen offset:2048 sc1\n"
"s_add_u32 s63, s40, 4096\n"
"v_mfma_f32_16x16x32_bf16 a[212:215], v[224:227], a[160:163], a[212:215]\n"
"s_and_b32 s35, s52, 0x1fff\n"
"s_or_b32 s35, s35, 0x4000\n"
"v_mfma_f32_16x16x32_bf16 a[216:219], a[248:251], a[152:155], 0\n"
"ds_write_b128 v15, a[204:207] offset:16384\n"
"v_mfma_f32_16x16x32_bf16 a[216:219], v[240:243], a[156:159], a[216:219]\n"
"s_lshl_b32 s35, s35, 16\n"
"v_mfma_f32_16x16x32_bf16 a[216:219], v[220:223], a[160:163], a[216:219]\n"
"buffer_load_dwordx4 v[32:35], v2, s[20:23], s85 offen offset:3072 sc1\n"
"v_mov_b32 v16, s35\n"
"v_mfma_f32_16x16x32_bf16 a[220:223], a[252:255], a[152:155], 0\n"
"v_lshlrev_b32 v9, 3, v1\n"
"v_mfma_f32_16x16x32_bf16 a[220:223], v[236:239], a[156:159], a[220:223]\n"
"s_add_u32 s85, s85, 4096\n"
"v_mfma_f32_16x16x32_bf16 a[220:223], v[216:219], a[160:163], a[220:223]\n"
"v_mfma_f32_16x16x32_bf16 a[224:227], v[252:255], a[152:155], 0\n"
"buffer_load_dwordx4 v[36:39], v2, s[20:23], s85 offen offset:0 sc1\n"
"v_mfma_f32_16x16x32_bf16 a[224:227], v[232:235], a[156:159], a[224:227]\n"
"v_mfma_f32_16x16x32_bf16 a[224:227], v[212:215], a[160:163], a[224:227]\n"
"s_waitcnt vmcnt(11)\n"
"v_mfma_f32_16x16x32_bf16 a[208:211], v[208:211], a[164:167], a[208:211]\n"
"v_mfma_f32_16x16x32_bf16 a[208:211], v[188:191], a[168:171], a[208:211]\n"
"s_waitcnt lgkmcnt(0)\n"
"v_mfma_f32_16x16x32_bf16 a[208:211], v[168:171], a[172:175], a[208:211]\n"
"buffer_load_dwordx4 v[40:43], v2, s[20:23], s85 offen offset:1024 sc1\n"
"v_mfma_f32_16x16x32_bf16 a[212:215], v[204:207], a[164:167], a[212:215]\n"
"v_mfma_f32_16x16x32_bf16 a[212:215], v[184:187], a[168:171], a[212:215]\n"
"v_mfma_f32_16x16x32_bf16 a[212:215], v[164:167], a[172:175], a[212:215]\n"
"v_mfma_f32_16x16x32_bf16 a[216:219], v[200:203], a[164:167], a[216:219]\n"
"buffer_store_dword v13, v3, s[16:19], s76 offen sc1\n"
"v_mfma_f32_16x16x32_bf16 a[216:219], v[180:183], a[168:171], a[216:219]\n"
"s_xor_b32 s85, s3, 3\n"
"s_mul_i32 s85, s85, 116736\n"
"v_mfma_f32_16x16x32_bf16 a[216:219], v[160:163], a[172:175], a[216:219]\n"
"s_add_u32 s85, s85, s44\n"
"v_mfma_f32_16x16x32_bf16 a[220:223], v[196:199], a[164:167], a[220:223]\n"
"ds_write_b128 v15, a[208:211] offset:20480\n"
"v_mfma_f32_16x16x32_bf16 a[220:223], v[176:179], a[168:171], a[220:223]\n"
"buffer_load_dwordx4 a[148:151], v2, s[20:23], s85 offen offset:0 sc1\n"
"v_mfma_f32_16x16x32_bf16 a[220:223], a[0:3], a[172:175], a[220:223]\n"
"v_mfma_f32_16x16x32_bf16 a[224:227], v[192:195], a[164:167], a[224:227]\n"
"ds_write_b128 v15, a[212:215] offset:24576\n"
"v_mfma_f32_16x16x32_bf16 a[224:227], v[172:175], a[168:171], a[224:227]\n"
"buffer_load_dwordx4 a[152:155], v2, s[20:23], s85 offen offset:1024 sc1\n"
"v_mfma_f32_16x16x32_bf16 a[224:227], a[4:7], a[172:175], a[224:227]\n"
"s_waitcnt vmcnt(12)\n"
"v_mfma_f32_16x16x32_bf16 a[188:191], a[240:243], a[176:179], 0\n"
"ds_write_b128 v15, a[216:219] offset:28672\n"
"v_mfma_f32_16x16x32_bf16 a[188:191], v[248:251], a[180:183], a[188:191]\n"
"buffer_load_dwordx4 a[156:159], v2, s[20:23], s85 offen offset:2048 sc1\n"
"v_mfma_f32_16x16x32_bf16 a[188:191], v[228:231], a[184:187], a[188:191]\n"
"v_mfma_f32_16x16x32_bf16 a[192:195], a[244:247], a[176:179], 0\n"
"ds_write_b128 v15, a[220:223] offset:32768\n"
"v_mfma_f32_16x16x32_bf16 a[192:195], v[244:247], a[180:183], a[192:195]\n"
"v_mfma_f32_16x16x32_bf16 a[192:195], v[224:227], a[184:187], a[192:195]\n"
"buffer_load_dwordx4 a[160:163], v2, s[20:23], s85 offen offset:3072 sc1\n"
"v_mfma_f32_16x16x32_bf16 a[196:199], a[248:251], a[176:179], 0\n"
"ds_write_b128 v15, a[224:227] offset:36864\n"
"v_mfma_f32_16x16x32_bf16 a[196:199], v[240:243], a[180:183], a[196:199]\n"
"v_mfma_f32_16x16x32_bf16 a[196:199], v[220:223], a[184:187], a[196:199]\n"
"s_add_u32 s85, s85, 4096\n"
"v_mfma_f32_16x16x32_bf16 a[200:203], a[252:255], a[176:179], 0\n"
"v_mfma_f32_16x16x32_bf16 a[200:203], v[236:239], a[180:183], a[200:203]\n"
"buffer_load_dwordx4 a[164:167], v2, s[20:23], s85 offen offset:0 sc1\n"
"v_mfma_f32_16x16x32_bf16 a[200:203], v[216:219], a[184:187], a[200:203]\n"
"v_mfma_f32_16x16x32_bf16 a[204:207], v[252:255], a[176:179], 0\n"
"v_mfma_f32_16x16x32_bf16 a[204:207], v[232:235], a[180:183], a[204:207]\n"
"v_mfma_f32_16x16x32_bf16 a[204:207], v[212:215], a[184:187], a[204:207]\n"
"buffer_load_dwordx4 a[168:171], v2, s[20:23], s85 offen offset:1024 sc1\n"
"s_waitcnt vmcnt(13)\n"
"v_mfma_f32_16x16x32_bf16 a[188:191], v[208:211], a[228:231], a[188:191]\n"
"s_xor_b32 s85, s3, 2\n"
"s_mul_i32 s85, s85, 116736\n"
"v_mfma_f32_16x16x32_bf16 a[188:191], v[188:191], a[232:235], a[188:191]\n"
"s_waitcnt lgkmcnt(0)\n"
"s_barrier\n"
"v_mfma_f32_16x16x32_bf16 a[188:191], v[168:171], a[236:239], a[188:191]\n"
"ds_read_b128 v[116:119], v156 offset:0\n"
"ds_read_b128 v[120:123], v156 offset:4096\n"
"v_mfma_f32_16x16x32_bf16 a[192:195], v[204:207], a[228:231], a[192:195]\n"
"ds_read_b128 v[124:127], v156 offset:8192\n"
"ds_read_b128 v[128:131], v156 offset:12288\n"
"v_mfma_f32_16x16x32_bf16 a[192:195], v[184:187], a[232:235], a[192:195]\n"
"ds_read_b128 v[132:135], v156 offset:16384\n"
"s_add_u32 s85, s85, s44\n"
"v_mfma_f32_16x16x32_bf16 a[192:195], v[164:167], a[236:239], a[192:195]\n"
"ds_read_b128 v[136:139], v157 offset:20480\n"
"ds_read_b128 v[140:143], v157 offset:24576\n"
"v_mfma_f32_16x16x32_bf16 a[196:199], v[200:203], a[228:231], a[196:199]\n"
"ds_read_b128 v[144:147], v157 offset:28672\n"
"ds_read_b128 v[148:151], v157 offset:32768\n"
"v_mfma_f32_16x16x32_bf16 a[196:199], v[180:183], a[232:235], a[196:199]\n"
"ds_read_b128 v[152:155], v157 offset:36864\n"
"buffer_load_dwordx4 a[172:175], v2, s[20:23], s85 offen offset:0 sc1\n"
"v_mfma_f32_16x16x32_bf16 a[196:199], v[160:163], a[236:239], a[196:199]\n"
"buffer_load_dwordx4 a[176:179], v2, s[20:23], s85 offen offset:1024 sc1\n"
"v_mfma_f32_16x16x32_bf16 a[200:203], v[196:199], a[228:231], a[200:203]\n"
"ds_write_b128 v15, a[188:191] offset:40960\n"
"v_mfma_f32_16x16x32_bf16 a[200:203], v[176:179], a[232:235], a[200:203]\n"
"v_mfma_f32_16x16x32_bf16 a[200:203], a[0:3], a[236:239], a[200:203]\n"
"v_mfma_f32_16x16x32_bf16 a[204:207], v[192:195], a[228:231], a[204:207]\n"
"ds_write_b128 v15, a[192:195] offset:45056\n"
"v_mfma_f32_16x16x32_bf16 a[204:207], v[172:175], a[232:235], a[204:207]\n"
"buffer_load_dwordx4 a[180:183], v2, s[20:23], s85 offen offset:2048 sc1\n"
"v_mfma_f32_16x16x32_bf16 a[204:207], a[4:7], a[236:239], a[204:207]\n"
"s_waitcnt lgkmcnt(6)\n"
"v_add_f32 v116, v116, v136\n"
"s_waitcnt vmcnt(13)\n"
"v_mfma_f32_16x16x32_bf16 v[76:79], a[240:243], v[20:23], 0\n"
"ds_write_b128 v15, a[196:199] offset:49152\n"
"v_mfma_f32_16x16x32_bf16 v[76:79], v[248:251], v[24:27], v[76:79]\n"
"buffer_load_dwordx4 a[184:187], v2, s[20:23], s85 offen offset:3072 sc1\n"
"v_add_f32 v117, v117, v137\n"
"v_mfma_f32_16x16x32_bf16 v[76:79], v[228:231], v[28:31], v[76:79]\n"
"v_add_f32 v118, v118, v138\n"
"v_add_f32 v119, v119, v139\n"
"v_mfma_f32_16x16x32_bf16 v[80:83], a[244:247], v[20:23], 0\n"
"ds_write_b128 v15, a[200:203] offset:53248\n"
"v_mfma_f32_16x16x32_bf16 v[80:83], v[244:247], v[24:27], v[80:83]\n"
"s_waitcnt lgkmcnt(7)\n"
"v_add_f32 v120, v120, v140\n"
"s_add_u32 s85, s85, 4096\n"
"v_mfma_f32_16x16x32_bf16 v[80:83], v[224:227], v[28:31], v[80:83]\n"
"v_add_f32 v121, v121, v141\n"
"v_add_f32 v122, v122, v142\n"
"v_mfma_f32_16x16x32_bf16 v[84:87], a[248:251], v[20:23], 0\n"
"ds_write_b128 v15, a[204:207] offset:57344\n"
"v_mfma_f32_16x16x32_bf16 v[84:87], v[240:243], v[24:27], v[84:87]\n"
"buffer_load_dwordx4 a[228:231], v2, s[20:23], s85 offen offset:0 sc1\n"
"v_add_f32 v123, v123, v143\n"
"v_mfma_f32_16x16x32_bf16 v[84:87], v[220:223], v[28:31], v[84:87]\n"
"s_waitcnt lgkmcnt(7)\n"
"v_add_f32 v124, v124, v144\n"
"v_add_f32 v125, v125, v145\n"
"v_mfma_f32_16x16x32_bf16 v[88:91], a[252:255], v[20:23], 0\n"
"v_add_f32 v126, v126, v146\n"
"v_add_f32 v127, v127, v147\n"
"v_mfma_f32_16x16x32_bf16 v[88:91], v[236:239], v[24:27], v[88:91]\n"
"buffer_load_dwordx4 a[232:235], v2, s[20:23], s85 offen offset:1024 sc1\n"
"s_waitcnt lgkmcnt(6)\n"
"v_add_f32 v128, v128, v148\n"
"v_mfma_f32_16x16x32_bf16 v[88:91], v[216:219], v[28:31], v[88:91]\n"
"s_xor_b32 s85, s3, 1\n"
"s_mul_i32 s85, s85, 116736\n"
"v_mfma_f32_16x16x32_bf16 v[92:95], v[252:255], v[20:23], 0\n"
"v_add_f32 v129, v129, v149\n"
"s_add_u32 s85, s85, s44\n"
"v_mfma_f32_16x16x32_bf16 v[92:95], v[232:235], v[24:27], v[92:95]\n"
"v_add_f32 v130, v130, v150\n"
"v_add_f32 v131, v131, v151\n"
"v_mfma_f32_16x16x32_bf16 v[92:95], v[212:215], v[28:31], v[92:95]\n"
"buffer_load_dwordx4 a[236:239], v2, s[20:23], s85 offen offset:0 sc1\n"
"s_waitcnt vmcnt(14)\n"
"v_mfma_f32_16x16x32_bf16 v[76:79], v[208:211], v[32:35], v[76:79]\n"
"s_waitcnt lgkmcnt(0)\n"
"s_barrier\n"
"v_mfma_f32_16x16x32_bf16 v[76:79], v[188:191], v[36:39], v[76:79]\n"
"ds_read_b128 v[56:59], v158 offset:40960\n"
"ds_read_b128 v[60:63], v158 offset:45056\n"
"v_mfma_f32_16x16x32_bf16 v[76:79], v[168:171], v[40:43], v[76:79]\n"
"ds_read_b128 v[64:67], v158 offset:49152\n"
"ds_read_b128 v[68:71], v158 offset:53248\n"
"v_mfma_f32_16x16x32_bf16 v[80:83], v[204:207], v[32:35], v[80:83]\n"
"ds_read_b128 v[72:75], v158 offset:57344\n"
"v_add_f32 v132, v132, v152\n"
"v_mfma_f32_16x16x32_bf16 v[80:83], v[184:187], v[36:39], v[80:83]\n"
"buffer_load_dwordx4 a[208:211], v2, s[20:23], s85 offen offset:1024 sc1\n"
"v_add_f32 v133, v133, v153\n"
"v_mfma_f32_16x16x32_bf16 v[80:83], v[164:167], v[40:43], v[80:83]\n"
"v_add_f32 v134, v134, v154\n"
"v_add_f32 v135, v135, v155\n"
"v_mfma_f32_16x16x32_bf16 v[84:87], v[200:203], v[32:35], v[84:87]\n"
"v_mfma_f32_16x16x32_bf16 v[84:87], v[180:183], v[36:39], v[84:87]\n"
"v_add_f32 v116, v116, v76\n"
"v_add_f32 v117, v117, v77\n"
"v_mfma_f32_16x16x32_bf16 v[84:87], v[160:163], v[40:43], v[84:87]\n"
"buffer_load_dwordx4 a[212:215], v2, s[20:23], s85 offen offset:2048 sc1\n"
"v_add_f32 v118, v118, v78\n"
"v_mfma_f32_16x16x32_bf16 v[88:91], v[196:199], v[32:35], v[88:91]\n"
"v_add_f32 v119, v119, v79\n"
"v_mfma_f32_16x16x32_bf16 v[88:91], v[176:179], v[36:39], v[88:91]\n"
"s_waitcnt lgkmcnt(4)\n"
"v_add_f32 v116, v116, v56\n"
"v_add_f32 v117, v117, v57\n"
"v_mfma_f32_16x16x32_bf16 v[88:91], a[0:3], v[40:43], v[88:91]\n"
"v_add_f32 v118, v118, v58\n"
"v_add_f32 v119, v119, v59\n"
"v_mfma_f32_16x16x32_bf16 v[92:95], v[192:195], v[32:35], v[92:95]\n"
"v_cvt_pk_bf16_f32 v96, v116, v117\n"
"v_cvt_pk_bf16_f32 v97, v118, v16\n"
"v_mfma_f32_16x16x32_bf16 v[92:95], v[172:175], v[36:39], v[92:95]\n"
"v_add_f32 v120, v120, v80\n"
"v_add_f32 v121, v121, v81\n"
"v_mfma_f32_16x16x32_bf16 v[92:95], a[4:7], v[40:43], v[92:95]\n"
"v_add_f32 v122, v122, v82\n"
"s_waitcnt vmcnt(12)\n"
"v_mfma_f32_16x16x32_bf16 a[128:131], a[240:243], a[148:151], 0\n"
"v_add_f32 v123, v123, v83\n"
"s_waitcnt lgkmcnt(3)\n"
"v_add_f32 v120, v120, v60\n"
"v_mfma_f32_16x16x32_bf16 a[128:131], v[248:251], a[152:155], a[128:131]\n"
"v_add_f32 v121, v121, v61\n"
"v_add_f32 v122, v122, v62\n"
"v_mfma_f32_16x16x32_bf16 a[128:131], v[228:231], a[156:159], a[128:131]\n"
"v_add_f32 v123, v123, v63\n"
"v_cvt_pk_bf16_f32 v98, v119, v120\n"
"v_mfma_f32_16x16x32_bf16 a[132:135], a[244:247], a[148:151], 0\n"
"v_cvt_pk_bf16_f32 v99, v121, v16\n"
"v_add_f32 v124, v124, v84\n"
"v_mfma_f32_16x16x32_bf16 a[132:135], v[244:247], a[152:155], a[132:135]\n"
"buffer_store_dwordx4 v[96:99], v2, s[20:23], s60 offen offset:0 sc1\n"
"v_mfma_f32_16x16x32_bf16 a[132:135], v[224:227], a[156:159], a[132:135]\n"
"v_add_f32 v125, v125, v85\n"
"v_add_f32 v126, v126, v86\n"
"v_mfma_f32_16x16x32_bf16 a[136:139], a[248:251], a[148:151], 0\n"
"v_add_f32 v127, v127, v87\n"
"s_waitcnt lgkmcnt(2)\n"
"v_add_f32 v124, v124, v64\n"
"v_mfma_f32_16x16x32_bf16 a[136:139], v[240:243], a[152:155], a[136:139]\n"
"v_add_f32 v125, v125, v65\n"
"v_add_f32 v126, v126, v66\n"
"v_mfma_f32_16x16x32_bf16 a[136:139], v[220:223], a[156:159], a[136:139]\n"
"v_add_f32 v127, v127, v67\n"
"v_cvt_pk_bf16_f32 v100, v122, v123\n"
"v_mfma_f32_16x16x32_bf16 a[140:143], a[252:255], a[148:151], 0\n"
"v_cvt_pk_bf16_f32 v101, v124, v16\n"
"v_cvt_pk_bf16_f32 v102, v125, v126\n"
"v_mfma_f32_16x16x32_bf16 a[140:143], v[236:239], a[152:155], a[140:143]\n"
"buffer_load_dwordx4 a[216:219], v2, s[20:23], s85 offen offset:3072 sc1\n"
"v_cvt_pk_bf16_f32 v103, v127, v16\n"
"v_mfma_f32_16x16x32_bf16 a[140:143], v[216:219], a[156:159], a[140:143]\n"
"buffer_store_dwordx4 v[100:103], v2, s[20:23], s60 offen offset:1024 sc1\n"
"v_mfma_f32_16x16x32_bf16 a[144:147], v[252:255], a[148:151], 0\n"
"v_add_f32 v128, v128, v88\n"
"v_add_f32 v129, v129, v89\n"
"v_mfma_f32_16x16x32_bf16 a[144:147], v[232:235], a[152:155], a[144:147]\n"
"v_add_f32 v130, v130, v90\n"
"v_add_f32 v131, v131, v91\n"
"v_mfma_f32_16x16x32_bf16 a[144:147], v[212:215], a[156:159], a[144:147]\n"
"s_waitcnt lgkmcnt(1)\n"
"v_add_f32 v128, v128, v68\n"
"s_waitcnt vmcnt(12)\n"
"v_mfma_f32_16x16x32_bf16 a[128:131], v[208:211], a[160:163], a[128:131]\n"
"v_add_f32 v129, v129, v69\n"
"v_add_f32 v130, v130, v70\n"
"v_mfma_f32_16x16x32_bf16 a[128:131], v[188:191], a[164:167], a[128:131]\n"
"v_add_f32 v131, v131, v71\n"
"v_cvt_pk_bf16_f32 v104, v128, v129\n"
"v_mfma_f32_16x16x32_bf16 a[128:131], v[168:171], a[168:171], a[128:131]\n"
"v_cvt_pk_bf16_f32 v105, v130, v16\n"
"v_add_f32 v132, v132, v92\n"
"v_mfma_f32_16x16x32_bf16 a[132:135], v[204:207], a[160:163], a[132:135]\n"
"v_add_f32 v133, v133, v93\n"
"v_add_f32 v134, v134, v94\n"
"v_mfma_f32_16x16x32_bf16 a[132:135], v[184:187], a[164:167], a[132:135]\n"
"buffer_load_dword v12, v11, s[16:19], 0 offen sc1\n"
"s_add_u32 s85, s85, 4096\n"
"v_mfma_f32_16x16x32_bf16 a[132:135], v[164:167], a[168:171], a[132:135]\n"
"v_add_f32 v135, v135, v95\n"
"s_waitcnt lgkmcnt(0)\n"
"v_add_f32 v132, v132, v72\n"
"v_mfma_f32_16x16x32_bf16 a[136:139], v[200:203], a[160:163], a[136:139]\n"
"buffer_load_dwordx4 a[220:223], v2, s[20:23], s85 offen offset:0 sc1\n"
"v_add_f32 v133, v133, v73\n"
"v_mfma_f32_16x16x32_bf16 a[136:139], v[180:183], a[164:167], a[136:139]\n"
"v_add_f32 v134, v134, v74\n"
"v_add_f32 v135, v135, v75\n"
"v_mfma_f32_16x16x32_bf16 a[136:139], v[160:163], a[168:171], a[136:139]\n"
"v_cvt_pk_bf16_f32 v106, v131, v132\n"
"v_cvt_pk_bf16_f32 v107, v133, v16\n"
"v_mfma_f32_16x16x32_bf16 a[140:143], v[196:199], a[160:163], a[140:143]\n"
"buffer_store_dwordx4 v[104:107], v2, s[20:23], s60 offen offset:2048 sc1\n"
"v_mfma_f32_16x16x32_bf16 a[140:143], v[176:179], a[164:167], a[140:143]\n"
"v_cvt_pk_bf16_f32 v108, v134, v135\n"
"v_cvt_pk_bf16_f32 v109, v135, v16\n"
"v_mfma_f32_16x16x32_bf16 a[140:143], a[0:3], a[168:171], a[140:143]\n"
"buffer_load_dwordx4 a[224:227], v2, s[20:23], s85 offen offset:1024 sc1\n"
"v_lshlrev_b32 v76, 16, v44\n"
"v_mfma_f32_16x16x32_bf16 a[144:147], v[192:195], a[160:163], a[144:147]\n"
"s_xor_b32 s85, s3, 0\n"
"s_mul_i32 s85, s85, 116736\n"
"v_mfma_f32_16x16x32_bf16 a[144:147], v[172:175], a[164:167], a[144:147]\n"
"v_lshlrev_b32 v78, 16, v45\n"
"s_add_u32 s85, s85, s44\n"
"v_mfma_f32_16x16x32_bf16 a[144:147], a[4:7], a[168:171], a[144:147]\n"
"v_lshlrev_b32 v80, 16, v46\n"
"s_waitcnt vmcnt(13)\n"
"v_mfma_f32_16x16x32_bf16 a[148:151], a[240:243], a[172:175], 0\n"
"buffer_load_dwordx4 v[20:23], v2, s[20:23], s85 offen offset:0 sc1\n"
"v_lshlrev_b32 v82, 16, v47\n"
"v_mfma_f32_16x16x32_bf16 a[148:151], v[248:251], a[176:179], a[148:151]\n"
"buffer_store_dwordx2 v[108:109], v9, s[20:23], s60 offen offset:3072 sc1\n"
"v_mfma_f32_16x16x32_bf16 a[148:151], v[228:231], a[180:183], a[148:151]\n"
"v_lshlrev_b32 v84, 16, v48\n"
"v_lshlrev_b32 v86, 16, v49\n"
"v_mfma_f32_16x16x32_bf16 a[152:155], a[244:247], a[172:175], 0\n"
"v_lshlrev_b32 v88, 16, v50\n"
"v_lshlrev_b32 v90, 16, v51\n"
"v_mfma_f32_16x16x32_bf16 a[152:155], v[244:247], a[176:179], a[152:155]\n"
"buffer_load_dwordx4 v[24:27], v2, s[20:23], s85 offen offset:1024 sc1\n"
"v_lshlrev_b32 v92, 16, v52\n"
"v_mfma_f32_16x16x32_bf16 a[152:155], v[224:227], a[180:183], a[152:155]\n"
"v_lshlrev_b32 v94, 16, v53\n"
"v_and_b32 v77, 0xffff0000, v44\n"
"v_mfma_f32_16x16x32_bf16 a[156:159], a[248:251], a[172:175], 0\n"
"v_and_b32 v79, 0xffff0000, v45\n"
"v_and_b32 v81, 0xffff0000, v46\n"
"v_mfma_f32_16x16x32_bf16 a[156:159], v[240:243], a[176:179], a[156:159]\n"
"v_and_b32 v83, 0xffff0000, v47\n"
"v_and_b32 v85, 0xffff0000, v48\n"
"v_mfma_f32_16x16x32_bf16 a[156:159], v[220:223], a[180:183], a[156:159]\n"
"buffer_load_dwordx4 v[28:31], v2, s[20:23], s85 offen offset:2048 sc1\n"
"v_and_b32 v87, 0xffff0000, v49\n"
"v_mfma_f32_16x16x32_bf16 a[160:163], a[252:255], a[172:175], 0\n"
"v_and_b32 v89, 0xffff0000, v50\n"
"v_and_b32 v91, 0xffff0000, v51\n"
"v_mfma_f32_16x16x32_bf16 a[160:163], v[236:239], a[176:179], a[160:163]\n"
"v_and_b32 v93, 0xffff0000, v52\n"
"v_and_b32 v95, 0xffff0000, v53\n"
"v_mfma_f32_16x16x32_bf16 a[160:163], v[216:219], a[180:183], a[160:163]\n"
"v_add_u32 v14, 0, v1\n"
"v_mul_u32_u24 v17, 6554, v14\n"
"v_mfma_f32_16x16x32_bf16 a[164:167], v[252:255], a[172:175], 0\n"
"v_lshrrev_b32 v17, 16, v17\n"
"v_add_u32 v14, v14, v17\n"
"v_mfma_f32_16x16x32_bf16 a[164:167], v[232:235], a[176:179], a[164:167]\n"
"buffer_load_dwordx4 a[188:191], v2, s[20:23], s85 offen offset:3072 sc1\n"
"v_lshl_add_u32 v17, v14, 4, s62\n"
"v_mfma_f32_16x16x32_bf16 a[164:167], v[212:215], a[180:183], a[164:167]\n"
"v_add_u32 v14, 64, v1\n"
"s_waitcnt vmcnt(15)\n"
"v_mfma_f32_16x16x32_bf16 a[148:151], v[208:211], a[184:187], a[148:151]\n"
"v_mul_u32_u24 v18, 6554, v14\n"
"s_add_u32 s85, s85, 4096\n"
"v_mfma_f32_16x16x32_bf16 a[148:151], v[188:191], a[228:231], a[148:151]\n"
"v_lshrrev_b32 v18, 16, v18\n"
"v_add_u32 v14, v14, v18\n"
"v_mfma_f32_16x16x32_bf16 a[148:151], v[168:171], a[232:235], a[148:151]\n"
"buffer_load_dwordx4 a[192:195], v2, s[20:23], s85 offen offset:0 sc1\n"
"v_lshl_add_u32 v18, v14, 4, s62\n"
"v_mfma_f32_16x16x32_bf16 a[152:155], v[204:207], a[184:187], a[152:155]\n"
"v_add_u32 v14, 128, v1\n"
"v_mul_u32_u24 v19, 6554, v14\n"
"v_mfma_f32_16x16x32_bf16 a[152:155], v[184:187], a[228:231], a[152:155]\n"
"v_lshrrev_b32 v19, 16, v19\n"
"v_add_u32 v14, v14, v19\n"
"v_mfma_f32_16x16x32_bf16 a[152:155], v[164:167], a[232:235], a[152:155]\n"
"v_lshl_add_u32 v19, v14, 4, s62\n"
"v_mfma_f32_16x16x32_bf16 a[156:159], v[200:203], a[184:187], a[156:159]\n"
"buffer_load_dwordx4 a[196:199], v2, s[20:23], s85 offen offset:1024 sc1\n"
"v_mfma_f32_16x16x32_bf16 a[156:159], v[180:183], a[228:231], a[156:159]\n"
"v_mfma_f32_16x16x32_bf16 a[156:159], v[160:163], a[232:235], a[156:159]\n"
"v_mfma_f32_16x16x32_bf16 a[160:163], v[196:199], a[184:187], a[160:163]\n"
"v_mfma_f32_16x16x32_bf16 a[160:163], v[176:179], a[228:231], a[160:163]\n"
"v_mfma_f32_16x16x32_bf16 a[160:163], a[0:3], a[232:235], a[160:163]\n"
"v_mfma_f32_16x16x32_bf16 a[164:167], v[192:195], a[184:187], a[164:167]\n"
"v_mfma_f32_16x16x32_bf16 a[164:167], v[172:175], a[228:231], a[164:167]\n"
"v_mfma_f32_16x16x32_bf16 a[164:167], a[4:7], a[232:235], a[164:167]\n"
"s_waitcnt vmcnt(14)\n"
"v_mfma_f32_16x16x32_bf16 a[168:171], a[240:243], a[236:239], 0\n"
"v_mfma_f32_16x16x32_bf16 a[168:171], v[248:251], a[208:211], a[168:171]\n"
"v_mfma_f32_16x16x32_bf16 a[168:171], v[228:231], a[212:215], a[168:171]\n"
"v_mfma_f32_16x16x32_bf16 a[172:175], a[244:247], a[236:239], 0\n"
"v_mfma_f32_16x16x32_bf16 a[172:175], v[244:247], a[208:211], a[172:175]\n"
"v_mfma_f32_16x16x32_bf16 a[172:175], v[224:227], a[212:215], a[172:175]\n"
"v_mfma_f32_16x16x32_bf16 a[176:179], a[248:251], a[236:239], 0\n"
"v_mfma_f32_16x16x32_bf16 a[176:179], v[240:243], a[208:211], a[176:179]\n"
"v_mfma_f32_16x16x32_bf16 a[176:179], v[220:223], a[212:215], a[176:179]\n"
"s_waitcnt vmcnt(10)\n"
"v_mfma_f32_16x16x32_bf16 a[180:183], a[252:255], a[236:239], 0\n"
"v_cmp_ne_u32 vcc, s52, v12\n"
"v_mfma_f32_16x16x32_bf16 a[180:183], v[236:239], a[208:211], a[180:183]\n"
"s_cbranch_vccnz .Lfb_slow_40\n"
".Lfb_back_41:\n"
"v_mfma_f32_16x16x32_bf16 a[180:183], v[216:219], a[212:215], a[180:183]\n"
"s_xor_b32 s85, s3, 3\n"
"s_mul_i32 s85, s85, 116736\n"
"v_mfma_f32_16x16x32_bf16 a[184:187], v[252:255], a[236:239], 0\n"
"s_add_u32 s85, s85, s84\n"
"v_mfma_f32_16x16x32_bf16 a[184:187], v[232:235], a[208:211], a[184:187]\n"
"buffer_load_dwordx4 a[200:203], v2, s[20:23], s85 offen offset:0 sc1\n"
"v_mfma_f32_16x16x32_bf16 a[184:187], v[212:215], a[212:215], a[184:187]\n"
"s_waitcnt vmcnt(8)\n"
"v_mfma_f32_16x16x32_bf16 a[168:171], v[208:211], a[216:219], a[168:171]\n"
"buffer_load_dwordx4 a[204:207], v2, s[20:23], s85 offen offset:1024 sc1\n"
"v_mfma_f32_16x16x32_bf16 a[168:171], v[188:191], a[220:223], a[168:171]\n"
"v_mfma_f32_16x16x32_bf16 a[168:171], v[168:171], a[224:227], a[168:171]\n"
"v_mfma_f32_16x16x32_bf16 a[172:175], v[204:207], a[216:219], a[172:175]\n"
"v_mfma_f32_16x16x32_bf16 a[172:175], v[184:187], a[220:223], a[172:175]\n"
"buffer_load_dwordx4 v[32:35], v2, s[20:23], s85 offen offset:2048 sc1\n"
"v_mfma_f32_16x16x32_bf16 a[172:175], v[164:167], a[224:227], a[172:175]\n"
"v_mfma_f32_16x16x32_bf16 a[176:179], v[200:203], a[216:219], a[176:179]\n"
"buffer_load_dwordx4 v[36:39], v2, s[20:23], s85 offen offset:3072 sc1\n"
"s_add_u32 s85, s85, 4096\n"
"v_mfma_f32_16x16x32_bf16 a[176:179], v[180:183], a[220:223], a[176:179]\n"
"v_mfma_f32_16x16x32_bf16 a[176:179], v[160:163], a[224:227], a[176:179]\n"
"buffer_load_dwordx4 v[40:43], v2, s[20:23], s85 offen offset:0 sc1\n"
"v_mfma_f32_16x16x32_bf16 a[180:183], v[196:199], a[216:219], a[180:183]\n"
"s_xor_b32 s85, s3, 2\n"
"s_mul_i32 s85, s85, 116736\n"
"v_mfma_f32_16x16x32_bf16 a[180:183], v[176:179], a[220:223], a[180:183]\n"
"s_add_u32 s85, s85, s84\n"
"v_mfma_f32_16x16x32_bf16 a[180:183], a[0:3], a[224:227], a[180:183]\n"
"v_mfma_f32_16x16x32_bf16 a[184:187], v[192:195], a[216:219], a[184:187]\n"
"buffer_load_dwordx4 a[228:231], v2, s[20:23], s85 offen offset:0 sc1\n"
"v_mfma_f32_16x16x32_bf16 a[184:187], v[172:175], a[220:223], a[184:187]\n"
"v_mfma_f32_16x16x32_bf16 a[184:187], a[4:7], a[224:227], a[184:187]\n"
"s_waitcnt vmcnt(9)\n"
"v_mfma_f32_16x16x32_bf16 v[96:99], a[240:243], v[20:23], 0\n"
"v_mfma_f32_16x16x32_bf16 v[96:99], v[248:251], v[24:27], v[96:99]\n"
"buffer_load_dwordx4 a[232:235], v2, s[20:23], s85 offen offset:1024 sc1\n"
"v_mfma_f32_16x16x32_bf16 v[96:99], v[228:231], v[28:31], v[96:99]\n"
"v_mfma_f32_16x16x32_bf16 v[100:103], a[244:247], v[20:23], 0\n"
"v_mfma_f32_16x16x32_bf16 v[100:103], v[244:247], v[24:27], v[100:103]\n"
"v_mfma_f32_16x16x32_bf16 v[100:103], v[224:227], v[28:31], v[100:103]\n"
"buffer_load_dwordx4 a[236:239], v2, s[20:23], s85 offen offset:2048 sc1\n"
"v_mfma_f32_16x16x32_bf16 v[104:107], a[248:251], v[20:23], 0\n"
"v_mfma_f32_16x16x32_bf16 v[104:107], v[240:243], v[24:27], v[104:107]\n"
"v_mfma_f32_16x16x32_bf16 v[104:107], v[220:223], v[28:31], v[104:107]\n"
"v_mfma_f32_16x16x32_bf16 v[108:111], a[252:255], v[20:23], 0\n"
"v_mfma_f32_16x16x32_bf16 v[108:111], v[236:239], v[24:27], v[108:111]\n"
"buffer_load_dwordx4 a[208:211], v2, s[20:23], s85 offen offset:3072 sc1\n"
"v_mfma_f32_16x16x32_bf16 v[108:111], v[216:219], v[28:31], v[108:111]\n"
"v_mfma_f32_16x16x32_bf16 v[112:115], v[252:255], v[20:23], 0\n"
"s_add_u32 s85, s85, 4096\n"
"v_mfma_f32_16x16x32_bf16 v[112:115], v[232:235], v[24:27], v[112:115]\n"
"v_mfma_f32_16x16x32_bf16 v[112:115], v[212:215], v[28:31], v[112:115]\n"
"buffer_load_dwordx4 a[212:215], v2, s[20:23], s85 offen offset:0 sc1\n"
"s_waitcnt vmcnt(10)\n"
"v_mfma_f32_16x16x32_bf16 v[96:99], v[208:211], a[188:191], v[96:99]\n"
"s_xor_b32 s85, s3, 1\n"
"s_mul_i32 s85, s85, 116736\n"
"v_mfma_f32_16x16x32_bf16 v[96:99], v[188:191], a[192:195], v[96:99]\n"
"s_add_u32 s85, s85, s84\n"
"v_mfma_f32_16x16x32_bf16 v[96:99], v[168:171], a[196:199], v[96:99]\n"
"v_mfma_f32_16x16x32_bf16 v[100:103], v[204:207], a[188:191], v[100:103]\n"
"buffer_load_dwordx4 a[216:219], v2, s[20:23], s85 offen offset:0 sc1\n"
"v_mfma_f32_16x16x32_bf16 v[100:103], v[184:187], a[192:195], v[100:103]\n"
"v_mfma_f32_16x16x32_bf16 v[100:103], v[164:167], a[196:199], v[100:103]\n"
"v_mfma_f32_16x16x32_bf16 v[104:107], v[200:203], a[188:191], v[104:107]\n"
"v_mfma_f32_16x16x32_bf16 v[104:107], v[180:183], a[192:195], v[104:107]\n"
"v_mfma_f32_16x16x32_bf16 v[104:107], v[160:163], a[196:199], v[104:107]\n"
"buffer_load_dwordx4 a[220:223], v2, s[20:23], s85 offen offset:1024 sc1\n"
"v_mfma_f32_16x16x32_bf16 v[108:111], v[196:199], a[188:191], v[108:111]\n"
"v_mfma_f32_16x16x32_bf16 v[108:111], v[176:179], a[192:195], v[108:111]\n"
"v_mfma_f32_16x16x32_bf16 v[108:111], a[0:3], a[196:199], v[108:111]\n"
"v_mfma_f32_16x16x32_bf16 v[112:115], v[192:195], a[188:191], v[112:115]\n"
"buffer_load_dwordx4 a[224:227], v2, s[20:23], s85 offen offset:2048 sc1\n"
"v_mfma_f32_16x16x32_bf16 v[112:115], v[172:175], a[192:195], v[112:115]\n"
"v_mfma_f32_16x16x32_bf16 v[112:115], a[4:7], a[196:199], v[112:115]\n"
"s_waitcnt vmcnt(11)\n"
"v_mfma_f32_16x16x32_bf16 a[128:131], a[8:11], a[200:203], a[128:131]\n"
"v_mfma_f32_16x16x32_bf16 a[128:131], a[28:31], a[204:207], a[128:131]\n"
"buffer_load_dwordx4 v[20:23], v2, s[20:23], s85 offen offset:3072 sc1\n"
"v_mfma_f32_16x16x32_bf16 a[132:135], a[12:15], a[200:203], a[132:135]\n"
"v_mfma_f32_16x16x32_bf16 a[132:135], a[32:35], a[204:207], a[132:135]\n"
"v_mfma_f32_16x16x32_bf16 a[136:139], a[16:19], a[200:203], a[136:139]\n"
"s_add_u32 s85, s85, 4096\n"
"v_mfma_f32_16x16x32_bf16 a[136:139], a[36:39], a[204:207], a[136:139]\n"
"v_mfma_f32_16x16x32_bf16 a[140:143], a[20:23], a[200:203], a[140:143]\n"
"buffer_load_dwordx4 v[24:27], v2, s[20:23], s85 offen offset:0 sc1\n"
"v_mfma_f32_16x16x32_bf16 a[140:143], a[40:43], a[204:207], a[140:143]\n"
"s_xor_b32 s85, s3, 0\n"
"s_mul_i32 s85, s85, 116736\n"
"v_mfma_f32_16x16x32_bf16 a[144:147], a[24:27], a[200:203], a[144:147]\n"
"s_add_u32 s85, s85, s84\n"
"v_mfma_f32_16x16x32_bf16 a[144:147], a[44:47], a[204:207], a[144:147]\n"
"s_waitcnt vmcnt(10)\n"
"v_mfma_f32_16x16x32_bf16 a[128:131], a[48:51], v[32:35], a[128:131]\n"
"buffer_load_dwordx4 v[28:31], v2, s[20:23], s85 offen offset:0 sc1\n"
"v_mfma_f32_16x16x32_bf16 a[128:131], a[68:71], v[36:39], a[128:131]\n"
"v_mfma_f32_16x16x32_bf16 a[128:131], a[88:91], v[40:43], a[128:131]\n"
"v_mfma_f32_16x16x32_bf16 a[132:135], a[52:55], v[32:35], a[132:135]\n"
"v_mfma_f32_16x16x32_bf16 a[132:135], a[72:75], v[36:39], a[132:135]\n"
"buffer_load_dwordx4 a[188:191], v2, s[20:23], s85 offen offset:1024 sc1\n"
"v_mfma_f32_16x16x32_bf16 a[132:135], a[92:95], v[40:43], a[132:135]\n"
"v_mfma_f32_16x16x32_bf16 a[136:139], a[56:59], v[32:35], a[136:139]\n"
"v_mfma_f32_16x16x32_bf16 a[136:139], a[76:79], v[36:39], a[136:139]\n"
"v_mfma_f32_16x16x32_bf16 a[136:139], a[96:99], v[40:43], a[136:139]\n"
"buffer_load_dwordx4 v[116:119], v2, s[20:23], s40 offen offset:0 sc1\n"
"v_mfma_f32_16x16x32_bf16 a[140:143], a[60:63], v[32:35], a[140:143]\n"
"ds_write_b128 v15, a[128:131] offset:0\n"
"v_mfma_f32_16x16x32_bf16 a[140:143], a[80:83], v[36:39], a[140:143]\n"
"v_mfma_f32_16x16x32_bf16 a[140:143], a[100:103], v[40:43], a[140:143]\n"
"v_mfma_f32_16x16x32_bf16 a[144:147], a[64:67], v[32:35], a[144:147]\n"
"ds_write_b128 v15, a[132:135] offset:4096\n"
"v_mfma_f32_16x16x32_bf16 a[144:147], a[84:87], v[36:39], a[144:147]\n"
"buffer_load_dwordx4 v[120:123], v2, s[20:23], s40 offen offset:1024 sc1\n"
"v_mfma_f32_16x16x32_bf16 a[144:147], a[104:107], v[40:43], a[144:147]\n"
"s_waitcnt vmcnt(12)\n"
"v_mfma_f32_16x16x32_bf16 a[148:151], a[8:11], a[228:231], a[148:151]\n"
"ds_write_b128 v15, a[136:139] offset:8192\n"
"v_mfma_f32_16x16x32_bf16 a[148:151], a[28:31], a[232:235], a[148:151]\n"
"v_mfma_f32_16x16x32_bf16 a[152:155], a[12:15], a[228:231], a[152:155]\n"
"buffer_load_dwordx4 v[124:127], v2, s[20:23], s40 offen offset:2048 sc1\n"
"v_mfma_f32_16x16x32_bf16 a[152:155], a[32:35], a[232:235], a[152:155]\n"
"ds_write_b128 v15, a[140:143] offset:12288\n"
"v_mfma_f32_16x16x32_bf16 a[156:159], a[16:19], a[228:231], a[156:159]\n"
"v_mfma_f32_16x16x32_bf16 a[156:159], a[36:39], a[232:235], a[156:159]\n"
"v_mfma_f32_16x16x32_bf16 a[160:163], a[20:23], a[228:231], a[160:163]\n"
"ds_write_b128 v15, a[144:147] offset:16384\n"
"v_mfma_f32_16x16x32_bf16 a[160:163], a[40:43], a[232:235], a[160:163]\n"
"buffer_load_dwordx2 v[128:129], v9, s[20:23], s40 offen offset:3072 sc1\n"
"v_mfma_f32_16x16x32_bf16 a[164:167], a[24:27], a[228:231], a[164:167]\n"
"v_mfma_f32_16x16x32_bf16 a[164:167], a[44:47], a[232:235], a[164:167]\n"
"s_waitcnt vmcnt(11)\n"
"v_mfma_f32_16x16x32_bf16 a[148:151], a[48:51], a[236:239], a[148:151]\n"
"v_mfma_f32_16x16x32_bf16 a[148:151], a[68:71], a[208:211], a[148:151]\n"
"buffer_load_dwordx4 a[192:195], v2, s[20:23], s85 offen offset:2048 sc1\n"
"s_waitcnt lgkmcnt(0)\n"
"v_mfma_f32_16x16x32_bf16 a[148:151], a[88:91], a[212:215], a[148:151]\n"
"v_mfma_f32_16x16x32_bf16 a[152:155], a[52:55], a[236:239], a[152:155]\n"
"buffer_load_dwordx4 a[196:199], v2, s[20:23], s85 offen offset:3072 sc1\n"
"s_add_u32 s85, s85, 4096\n"
"v_mfma_f32_16x16x32_bf16 a[152:155], a[72:75], a[208:211], a[152:155]\n"
"v_mfma_f32_16x16x32_bf16 a[152:155], a[92:95], a[212:215], a[152:155]\n"
"buffer_load_dwordx4 a[200:203], v2, s[20:23], s85 offen offset:0 sc1\n"
"v_mfma_f32_16x16x32_bf16 a[156:159], a[56:59], a[236:239], a[156:159]\n"
"v_mfma_f32_16x16x32_bf16 a[156:159], a[76:79], a[208:211], a[156:159]\n"
"v_mfma_f32_16x16x32_bf16 a[156:159], a[96:99], a[212:215], a[156:159]\n"
"v_mfma_f32_16x16x32_bf16 a[160:163], a[60:63], a[236:239], a[160:163]\n"
"ds_write_b128 v15, a[148:151] offset:20480\n"
"v_mfma_f32_16x16x32_bf16 a[160:163], a[80:83], a[208:211], a[160:163]\n"
"v_mfma_f32_16x16x32_bf16 a[160:163], a[100:103], a[212:215], a[160:163]\n"
"v_mfma_f32_16x16x32_bf16 a[164:167], a[64:67], a[236:239], a[164:167]\n"
"ds_write_b128 v15, a[152:155] offset:24576\n"
"v_mfma_f32_16x16x32_bf16 a[164:167], a[84:87], a[208:211], a[164:167]\n"
"v_mfma_f32_16x16x32_bf16 a[164:167], a[104:107], a[212:215], a[164:167]\n"
"s_waitcnt vmcnt(12)\n"
"v_mfma_f32_16x16x32_bf16 a[168:171], a[8:11], a[216:219], a[168:171]\n"
"ds_write_b128 v15, a[156:159] offset:28672\n"
"v_mfma_f32_16x16x32_bf16 a[168:171], a[28:31], a[220:223], a[168:171]\n"
"v_mfma_f32_16x16x32_bf16 a[172:175], a[12:15], a[216:219], a[172:175]\n"
"v_mfma_f32_16x16x32_bf16 a[172:175], a[32:35], a[220:223], a[172:175]\n"
"ds_write_b128 v15, a[160:163] offset:32768\n"
"v_mfma_f32_16x16x32_bf16 a[176:179], a[16:19], a[216:219], a[176:179]\n"
"v_mfma_f32_16x16x32_bf16 a[176:179], a[36:39], a[220:223], a[176:179]\n"
"v_mfma_f32_16x16x32_bf16 a[180:183], a[20:23], a[216:219], a[180:183]\n"
"ds_write_b128 v15, a[164:167] offset:36864\n"
"v_mfma_f32_16x16x32_bf16 a[180:183], a[40:43], a[220:223], a[180:183]\n"
"v_mfma_f32_16x16x32_bf16 a[184:187], a[24:27], a[216:219], a[184:187]\n"
"v_mfma_f32_16x16x32_bf16 a[184:187], a[44:47], a[220:223], a[184:187]\n"
"s_waitcnt vmcnt(9)\n"
"v_mfma_f32_16x16x32_bf16 a[168:171], a[48:51], a[224:227], a[168:171]\n"
"v_mfma_f32_16x16x32_bf16 a[168:171], a[68:71], v[20:23], a[168:171]\n"
"s_waitcnt lgkmcnt(0)\n"
"s_barrier\n"
"v_mfma_f32_16x16x32_bf16 a[168:171], a[88:91], v[24:27], a[168:171]\n"
"ds_read_b128 v[136:139], v156 offset:0\n"
"ds_read_b128 v[140:143], v156 offset:4096\n"
"v_mfma_f32_16x16x32_bf16 a[172:175], a[52:55], a[224:227], a[172:175]\n"
"ds_read_b128 v[144:147], v156 offset:8192\n"
"ds_read_b128 v[148:151], v156 offset:12288\n"
"v_mfma_f32_16x16x32_bf16 a[172:175], a[72:75], v[20:23], a[172:175]\n"
"ds_read_b128 v[152:155], v156 offset:16384\n"
"v_mfma_f32_16x16x32_bf16 a[172:175], a[92:95], v[24:27], a[172:175]\n"
"ds_read_b128 v[220:223], v157 offset:20480\n"
"ds_read_b128 v[224:227], v157 offset:24576\n"
"v_mfma_f32_16x16x32_bf16 a[176:179], a[56:59], a[224:227], a[176:179]\n"
"ds_read_b128 v[228:231], v157 offset:28672\n"
"ds_read_b128 v[232:235], v157 offset:32768\n"
"v_mfma_f32_16x16x32_bf16 a[176:179], a[76:79], v[20:23], a[176:179]\n"
"ds_read_b128 v[236:239], v157 offset:36864\n"
"v_mfma_f32_16x16x32_bf16 a[176:179], a[96:99], v[24:27], a[176:179]\n"
"v_mfma_f32_16x16x32_bf16 a[180:183], a[60:63], a[224:227], a[180:183]\n"
"ds_write_b128 v15, a[168:171] offset:40960\n"
"v_mfma_f32_16x16x32_bf16 a[180:183], a[80:83], v[20:23], a[180:183]\n"
"v_mfma_f32_16x16x32_bf16 a[180:183], a[100:103], v[24:27], a[180:183]\n"
"s_waitcnt lgkmcnt(10)\n"
"v_add_f32 v76, v76, v136\n"
"v_add_f32 v77, v77, v137\n"
"v_mfma_f32_16x16x32_bf16 a[184:187], a[64:67], a[224:227], a[184:187]\n"
"ds_write_b128 v15, a[172:175] offset:45056\n"
"v_mfma_f32_16x16x32_bf16 a[184:187], a[84:87], v[20:23], a[184:187]\n"
"v_add_f32 v78, v78, v138\n"
"v_add_f32 v79, v79, v139\n"
"v_mfma_f32_16x16x32_bf16 a[184:187], a[104:107], v[24:27], a[184:187]\n"
"s_waitcnt lgkmcnt(10)\n"
"v_add_f32 v80, v80, v140\n"
"s_waitcnt vmcnt(7)\n"
"v_mfma_f32_16x16x32_bf16 v[96:99], a[8:11], v[28:31], v[96:99]\n"
"ds_write_b128 v15, a[176:179] offset:49152\n"
"v_mfma_f32_16x16x32_bf16 v[96:99], a[28:31], a[188:191], v[96:99]\n"
"v_add_f32 v81, v81, v141\n"
"v_add_f32 v82, v82, v142\n"
"v_mfma_f32_16x16x32_bf16 v[100:103], a[12:15], v[28:31], v[100:103]\n"
"v_add_f32 v83, v83, v143\n"
"s_waitcnt vmcnt(3)\n"
"v_mfma_f32_16x16x32_bf16 v[100:103], a[32:35], a[188:191], v[100:103]\n"
"ds_write_b128 v15, a[180:183] offset:53248\n"
"v_mfma_f32_16x16x32_bf16 v[104:107], a[16:19], v[28:31], v[104:107]\n"
"v_xor_b32 v14, s35, v117\n"
"v_bitop3_b32 v14, v119, s35, v14 bitop3:0xbe\n"
"v_mfma_f32_16x16x32_bf16 v[104:107], a[36:39], a[188:191], v[104:107]\n"
"v_bitop3_b32 v14, v121, s35, v14 bitop3:0xbe\n"
"v_bitop3_b32 v14, v123, s35, v14 bitop3:0xbe\n"
"v_mfma_f32_16x16x32_bf16 v[108:111], a[20:23], v[28:31], v[108:111]\n"
"ds_write_b128 v15, a[184:187] offset:57344\n"
"v_mfma_f32_16x16x32_bf16 v[108:111], a[40:43], a[188:191], v[108:111]\n"
"v_bitop3_b32 v14, v125, s35, v14 bitop3:0xbe\n"
"v_bitop3_b32 v14, v127, s35, v14 bitop3:0xbe\n"
"v_mfma_f32_16x16x32_bf16 v[112:115], a[24:27], v[28:31], v[112:115]\n"
"v_bitop3_b32 v14, v129, s35, v14 bitop3:0xbe\n"
"v_cmp_lt_u32 vcc, 0xffff, v14\n"
"v_mfma_f32_16x16x32_bf16 v[112:115], a[44:47], a[188:191], v[112:115]\n"
"s_waitcnt vmcnt(0)\n"
"s_cbranch_vccnz .Lfb_slow_43\n"
".Lfb_back_44:\n"
"v_mfma_f32_16x16x32_bf16 v[96:99], a[48:51], a[192:195], v[96:99]\n"
"v_lshlrev_b32 v240, 16, v116\n"
"v_and_b32 v241, 0xffff0000, v116\n"
"v_mfma_f32_16x16x32_bf16 v[96:99], a[68:71], a[196:199], v[96:99]\n"
"s_waitcnt lgkmcnt(0)\n"
"s_barrier\n"
"v_mfma_f32_16x16x32_bf16 v[96:99], a[88:91], a[200:203], v[96:99]\n"
"ds_read_b128 v[56:59], v158 offset:40960\n"
"ds_read_b128 v[60:63], v158 offset:45056\n"
"v_mfma_f32_16x16x32_bf16 v[100:103], a[52:55], a[192:195], v[100:103]\n"
"ds_read_b128 v[64:67], v158 offset:49152\n"
"ds_read_b128 v[68:71], v158 offset:53248\n"
"v_mfma_f32_16x16x32_bf16 v[100:103], a[72:75], a[196:199], v[100:103]\n"
"ds_read_b128 v[72:75], v158 offset:57344\n"
"v_lshlrev_b32 v242, 16, v117\n"
"v_mfma_f32_16x16x32_bf16 v[100:103], a[92:95], a[200:203], v[100:103]\n"
"v_lshlrev_b32 v243, 16, v118\n"
"v_and_b32 v244, 0xffff0000, v118\n"
"v_mfma_f32_16x16x32_bf16 v[104:107], a[56:59], a[192:195], v[104:107]\n"
"v_lshlrev_b32 v245, 16, v119\n"
"v_lshlrev_b32 v246, 16, v120\n"
"v_mfma_f32_16x16x32_bf16 v[104:107], a[76:79], a[196:199], v[104:107]\n"
"v_and_b32 v247, 0xffff0000, v120\n"
"v_lshlrev_b32 v248, 16, v121\n"
"v_mfma_f32_16x16x32_bf16 v[104:107], a[96:99], a[200:203], v[104:107]\n"
"v_lshlrev_b32 v249, 16, v122\n"
"v_and_b32 v250, 0xffff0000, v122\n"
"v_mfma_f32_16x16x32_bf16 v[108:111], a[60:63], a[192:195], v[108:111]\n"
"v_lshlrev_b32 v251, 16, v123\n"
"v_lshlrev_b32 v252, 16, v124\n"
"v_mfma_f32_16x16x32_bf16 v[108:111], a[80:83], a[196:199], v[108:111]\n"
"v_and_b32 v253, 0xffff0000, v124\n"
"v_lshlrev_b32 v254, 16, v125\n"
"v_mfma_f32_16x16x32_bf16 v[108:111], a[100:103], a[200:203], v[108:111]\n"
"v_lshlrev_b32 v255, 16, v126\n"
"v_and_b32 v44, 0xffff0000, v126\n"
"v_mfma_f32_16x16x32_bf16 v[112:115], a[64:67], a[192:195], v[112:115]\n"
"v_lshlrev_b32 v45, 16, v127\n"
"v_lshlrev_b32 v46, 16, v128\n"
"v_mfma_f32_16x16x32_bf16 v[112:115], a[84:87], a[196:199], v[112:115]\n"
"v_and_b32 v47, 0xffff0000, v128\n"
"v_mfma_f32_16x16x32_bf16 v[112:115], a[104:107], a[200:203], v[112:115]\n"
"v_pk_add_f32 v[76:77], v[76:77], v[220:221] op_sel:[0,0] op_sel_hi:[1,1]\n"
"v_pk_add_f32 v[78:79], v[78:79], v[222:223] op_sel:[0,0] op_sel_hi:[1,1]\n"
"v_pk_add_f32 v[76:77], v[76:77], v[240:241] op_sel:[0,0] op_sel_hi:[1,1]\n"
"v_pk_add_f32 v[78:79], v[78:79], v[242:243] op_sel:[0,0] op_sel_hi:[1,1]\n"
"v_pk_add_f32 v[76:77], v[76:77], v[96:97] op_sel:[0,0] op_sel_hi:[1,1]\n"
"v_pk_add_f32 v[78:79], v[78:79], v[98:99] op_sel:[0,0] op_sel_hi:[1,1]\n"
"s_waitcnt lgkmcnt(4)\n"
"v_pk_add_f32 v[76:77], v[76:77], v[56:57] op_sel:[0,0] op_sel_hi:[1,1]\n"
"v_pk_add_f32 v[78:79], v[78:79], v[58:59] op_sel:[0,0] op_sel_hi:[1,1]\n"
"v_cvt_pk_bf16_f32 v76, v76, v77\n"
"v_cvt_pk_bf16_f32 v77, v78, v79\n"
"ds_write_b64 v10, v[76:77] offset:0\n"
"v_pk_add_f32 v[80:81], v[80:81], v[224:225] op_sel:[0,0] op_sel_hi:[1,1]\n"
"v_pk_add_f32 v[82:83], v[82:83], v[226:227] op_sel:[0,0] op_sel_hi:[1,1]\n"
"v_pk_add_f32 v[80:81], v[80:81], v[244:245] op_sel:[0,0] op_sel_hi:[1,1]\n"
"v_pk_add_f32 v[82:83], v[82:83], v[246:247] op_sel:[0,0] op_sel_hi:[1,1]\n"
"v_pk_add_f32 v[80:81], v[80:81], v[100:101] op_sel:[0,0] op_sel_hi:[1,1]\n"
"v_pk_add_f32 v[82:83], v[82:83], v[102:103] op_sel:[0,0] op_sel_hi:[1,1]\n"
"s_waitcnt lgkmcnt(4)\n"
"v_pk_add_f32 v[80:81], v[80:81], v[60:61] op_sel:[0,0] op_sel_hi:[1,1]\n"
"v_pk_add_f32 v[82:83], v[82:83], v[62:63] op_sel:[0,0] op_sel_hi:[1,1]\n"
"v_cvt_pk_bf16_f32 v80, v80, v81\n"
"v_cvt_pk_bf16_f32 v81, v82, v83\n"
"ds_write_b64 v10, v[80:81] offset:32\n"
"v_pk_add_f32 v[84:85], v[84:85], v[144:145] op_sel:[0,0] op_sel_hi:[1,1]\n"
"v_pk_add_f32 v[86:87], v[86:87], v[146:147] op_sel:[0,0] op_sel_hi:[1,1]\n"
"v_pk_add_f32 v[84:85], v[84:85], v[228:229] op_sel:[0,0] op_sel_hi:[1,1]\n"
"v_pk_add_f32 v[86:87], v[86:87], v[230:231] op_sel:[0,0] op_sel_hi:[1,1]\n"
"v_pk_add_f32 v[84:85], v[84:85], v[248:249] op_sel:[0,0] op_sel_hi:[1,1]\n"
"v_pk_add_f32 v[86:87], v[86:87], v[250:251] op_sel:[0,0] op_sel_hi:[1,1]\n"
"s_waitcnt lgkmcnt(4)\n"
"v_pk_add_f32 v[84:85], v[84:85], v[64:65] op_sel:[0,0] op_sel_hi:[1,1]\n"
"v_pk_add_f32 v[86:87], v[86:87], v[66:67] op_sel:[0,0] op_sel_hi:[1,1]\n"
"v_pk_add_f32 v[84:85], v[84:85], v[104:105] op_sel:[0,0] op_sel_hi:[1,1]\n"
"v_pk_add_f32 v[86:87], v[86:87], v[106:107] op_sel:[0,0] op_sel_hi:[1,1]\n"
"v_cvt_pk_bf16_f32 v84, v84, v85\n"
"v_cvt_pk_bf16_f32 v85, v86, v87\n"
"ds_write_b64 v10, v[84:85] offset:64\n"
"v_pk_add_f32 v[88:89], v[88:89], v[148:149] op_sel:[0,0] op_sel_hi:[1,1]\n"
"v_pk_add_f32 v[90:91], v[90:91], v[150:151] op_sel:[0,0] op_sel_hi:[1,1]\n"
"v_pk_add_f32 v[88:89], v[88:89], v[232:233] op_sel:[0,0] op_sel_hi:[1,1]\n"
"v_pk_add_f32 v[90:91], v[90:91], v[234:235] op_sel:[0,0] op_sel_hi:[1,1]\n"
"v_pk_add_f32 v[88:89], v[88:89], v[252:253] op_sel:[0,0] op_sel_hi:[1,1]\n"
"v_pk_add_f32 v[90:91], v[90:91], v[254:255] op_sel:[0,0] op_sel_hi:[1,1]\n"
"s_waitcnt lgkmcnt(4)\n"
"v_pk_add_f32 v[88:89], v[88:89], v[68:69] op_sel:[0,0] op_sel_hi:[1,1]\n"
"v_pk_add_f32 v[90:91], v[90:91], v[70:71] op_sel:[0,0] op_sel_hi:[1,1]\n"
"v_pk_add_f32 v[88:89], v[88:89], v[108:109] op_sel:[0,0] op_sel_hi:[1,1]\n"
"v_pk_add_f32 v[90:91], v[90:91], v[110:111] op_sel:[0,0] op_sel_hi:[1,1]\n"
"v_cvt_pk_bf16_f32 v88, v88, v89\n"
"v_cvt_pk_bf16_f32 v89, v90, v91\n"
"ds_write_b64 v10, v[88:89] offset:96\n"
"v_pk_add_f32 v[92:93], v[92:93], v[152:153] op_sel:[0,0] op_sel_hi:[1,1]\n"
"v_pk_add_f32 v[94:95], v[94:95], v[154:155] op_sel:[0,0] op_sel_hi:[1,1]\n"
"v_pk_add_f32 v[92:93], v[92:93], v[236:237] op_sel:[0,0] op_sel_hi:[1,1]\n"
"v_pk_add_f32 v[94:95], v[94:95], v[238:239] op_sel:[0,0] op_sel_hi:[1,1]\n"
"v_pk_add_f32 v[92:93], v[92:93], v[44:45] op_sel:[0,0] op_sel_hi:[1,1]\n"
"v_pk_add_f32 v[94:95], v[94:95], v[46:47] op_sel:[0,0] op_sel_hi:[1,1]\n"
"s_waitcnt lgkmcnt(4)\n"
"v_pk_add_f32 v[92:93], v[92:93], v[72:73] op_sel:[0,0] op_sel_hi:[1,1]\n"
"v_pk_add_f32 v[94:95], v[94:95], v[74:75] op_sel:[0,0] op_sel_hi:[1,1]\n"
"v_pk_add_f32 v[92:93], v[92:93], v[112:113] op_sel:[0,0] op_sel_hi:[1,1]\n"
"v_pk_add_f32 v[94:95], v[94:95], v[114:115] op_sel:[0,0] op_sel_hi:[1,1]\n"
"v_cvt_pk_bf16_f32 v92, v92, v93\n"
"v_cvt_pk_bf16_f32 v93, v94, v95\n"
"ds_write_b64 v10, v[92:93] offset:128\n"
"ds_read_b128 v[20:23], v17\n"
"ds_read_b128 v[24:27], v18\n"
"ds_read_b128 v[28:31], v19\n"
"s_waitcnt lgkmcnt(2)\n"
"buffer_store_dwordx4 v[20:23], v5, s[48:51], s79 offen offset:0 \n"
"s_waitcnt lgkmcnt(1)\n"
"buffer_store_dwordx4 v[24:27], v6, s[48:51], s79 offen offset:1024 \n"
"s_waitcnt lgkmcnt(0)\n"
"buffer_store_dwordx4 v[28:31], v7, s[48:51], s79 offen offset:2048 \n"
"s_endpgm\n"
".Lfb_ffnm:\n"
"s_bfe_u32 s53, s34, 0x50016\n"
"s_lshr_b32 s32, s34, 27\n"
"s_lshr_b32 s54, s35, 22\n"
"v_mov_b32 v40, v5\n"
"s_cmp_eq_u32 s32, 9\n"
"s_cselect_b32 s85, -9, 1\n"
"s_mul_i32 s33, s85, 221184\n"
"s_add_u32 s75, s75, s33\n"
"s_add_u32 s33, s75, 110592\n"
"s_add_u32 m0, s59, 0\n"
"s_nop 0\n"
"buffer_load_dwordx4 v6, s[8:11], s75 offen lds\n"
"s_mov_b32 s70, 122880\n"
"s_mov_b32 s71, 143360\n"
"s_mov_b32 s72, 163840\n"
"s_mov_b32 s73, 184320\n"
"s_mov_b32 s74, 204800\n"
"v_bfe_u32 v52, v1, 1, 3\n"
"v_lshrrev_b32 v7, 4, v1\n"
"v_xor_b32 v52, v52, v7\n"
"v_and_b32 v7, 15, v1\n"
"v_lshlrev_b32 v7, 7, v7\n"
"v_lshl_add_u32 v7, v52, 4, v7\n"
"s_mul_i32 s62, s61, 2112\n"
"v_xor_b32 v7, s62, v7\n"
"v_add_u32 v7, 73728, v7\n"
"v_xor_b32 v8, 64, v7\n"
"v_add_u32 v9, 45056, v7\n"
"v_add_u32 v10, 45056, v8\n"
"v_bfe_u32 v52, v1, 3, 2\n"
"s_add_u32 m0, s59, 4096\n"
"s_nop 0\n"
"buffer_load_dwordx4 v6, s[8:11], s33 offen lds\n"
"s_lshl_b32 s62, s60, 1\n"
"v_xor_b32 v52, s62, v52\n"
"v_lshlrev_b32 v52, 5, v52\n"
"v_lshrrev_b32 v53, 4, v1\n"
"v_lshl_add_u32 v52, v53, 10, v52\n"
"v_bfe_u32 v53, v1, 2, 2\n"
"v_lshl_add_u32 v52, v53, 7, v52\n"
"v_and_b32 v53, 3, v1\n"
"v_lshl_add_u32 v52, v53, 3, v52\n"
"s_lshl_b32 s62, s61, 12\n"
"v_add_u32 v11, s62, v52\n"
"v_xor_b32 v12, 4096, v11\n"
"v_xor_b32 v13, 32, v11\n"
"v_xor_b32 v14, 32, v12\n"
"s_lshl_b32 s62, s54, 1\n"
"s_add_u32 s62, s62, s61\n"
"s_lshl_b32 s63, s53, 1\n"
"s_add_u32 s63, s63, s60\n"
"s_add_u32 m0, s59, 98304\n"
"s_nop 0\n"
"buffer_load_dwordx4 v5, s[4:7], s70 offen lds\n"
"s_mul_i32 s57, s62, 54\n"
"s_add_u32 s57, s57, s63\n"
"s_lshl_b32 s57, s57, 7\n"
"s_add_u32 s57, s57, 8192\n"
"s_mul_i32 s76, s62, 1254\n"
"s_add_u32 s76, s76, s63\n"
"s_lshl_b32 s76, s76, 10\n"
"s_mul_i32 s77, s62, 2816\n"
"s_mul_i32 s94, s63, 5120\n"
"s_add_u32 s94, s94, 1048576\n"
"s_mul_i32 s95, s53, 6\n"
"s_lshl_b32 s92, s32, 1\n"
"s_add_u32 s92, s92, s61\n"
"s_lshl_b32 s92, s92, 8\n"
"s_add_u32 s92, s92, s94\n"
"s_cmp_eq_u32 s32, 9\n"
"s_cselect_b32 s93, -9, 1\n"
"s_lshl_b32 s93, s93, 9\n"
"s_add_u32 m0, s59, 102400\n"
"s_nop 0\n"
"buffer_load_dwordx4 v5, s[4:7], s71 offen lds\n"
"s_add_u32 s93, s93, s92\n"
"s_load_dword s52, s[50:51], 0\n"
"v_lshlrev_b32 v2, 4, v1\n"
"v_mov_b32 v52, 1073741824\n"
"v_cmp_eq_u32 vcc, 0, v1\n"
"s_mov_b32 s16, s50\n"
"s_and_b32 s17, s51, 0xffff\n"
"s_mov_b32 s18, 1456128\n"
"s_mov_b32 s19, 0x20000\n"
"v_cndmask_b32 v3, v52, v1, vcc\n"
"s_add_u32 s24, s50, 20971520\n"
"s_addc_u32 s25, s51, 0\n"
"s_and_b32 s25, s25, 0xffff\n"
"s_mov_b32 s26, 3031040\n"
"s_mov_b32 s27, 0x20000\n"
"s_add_u32 s20, s50, 4194304\n"
"s_addc_u32 s21, s51, 0\n"
"s_and_b32 s21, s21, 0xffff\n"
"s_add_u32 m0, s59, 106496\n"
"s_nop 0\n"
"buffer_load_dwordx4 v5, s[4:7], s72 offen lds\n"
"s_mov_b32 s22, 14942208\n"
"s_mov_b32 s23, 0x20000\n"
"s_add_u32 s80, s50, 262144\n"
"s_addc_u32 s81, s51, 0\n"
"s_and_b32 s81, s81, 0xffff\n"
"s_mov_b32 s82, 32768\n"
"s_mov_b32 s83, 0x20000\n"
"s_mul_i32 s42, s53, 6\n"
"s_add_u32 s42, s42, s54\n"
"s_lshl_b32 s42, s42, 7\n"
"s_add_u32 s42, s42, 212992\n"
"s_cmp_eq_u32 s3, 0\n"
"s_cselect_b32 s42, s42, 1073741824\n"
"s_mul_i32 s91, s53, 6\n"
"s_add_u32 s91, s91, s54\n"
"s_lshl_b32 s91, s91, 2\n"
"s_add_u32 s91, s91, s3\n"
"v_lshrrev_b32 v15, 4, v1\n"
"s_add_u32 m0, s59, 110592\n"
"s_nop 0\n"
"buffer_load_dwordx4 v5, s[4:7], s73 offen lds\n"
"v_lshl_add_u32 v15, s91, 2, v15\n"
"v_mul_u32_u24 v15, 1280, v15\n"
"v_and_b32 v52, 15, v1\n"
"v_lshl_add_u32 v15, v52, 4, v15\n"
"s_mul_i32 s35, s3, 5120\n"
"s_add_u32 s35, s35, 49152\n"
"s_lshr_b32 s61, s91, 3\n"
"s_and_b32 s62, s91, 7\n"
"s_mul_i32 s63, s62, 74\n"
"s_add_u32 s63, s63, s61\n"
"s_mul_i32 s63, s63, 5120\n"
"s_cmp_lt_u32 s61, 74\n"
"s_cselect_b32 s89, s63, 1073741824\n"
"s_add_u32 s90, s89, 4096\n"
"s_cmp_lt_u32 s61, 54\n"
"s_cselect_b32 s28, s46, s40\n"
"s_cselect_b32 s29, s47, s41\n"
"s_cselect_b32 s30, 27, 10\n"
"s_add_u32 m0, s59, 114688\n"
"s_nop 0\n"
"buffer_load_dwordx4 v5, s[4:7], s74 offen lds\n"
"s_mul_i32 s30, s30, 81920\n"
"s_and_b32 s29, s29, 0xffff\n"
"s_mov_b32 s31, 0x20000\n"
"s_sub_u32 s63, s61, 54\n"
"s_and_b32 s63, s63, 63\n"
"s_mul_i32 s34, s63, 52\n"
"s_lshr_b32 s34, s34, 8\n"
"s_mul_i32 s88, s34, 5\n"
"s_sub_u32 s63, s63, s88\n"
"s_mul_i32 s34, s34, 160\n"
"s_lshl_b32 s88, s63, 3\n"
"s_lshr_b32 s88, 0x70502000, s88\n"
"s_and_b32 s88, s88, 255\n"
"s_add_u32 s34, s34, s88\n"
"s_lshl_b32 s88, s61, 5\n"
"s_cmp_lt_u32 s61, 54\n"
"s_cselect_b32 s34, s88, s34\n"
"s_cselect_b32 s63, 0, s63\n"
"s_add_u32 m0, s59, 8192\n"
"s_nop 0\n"
"buffer_load_dwordx4 v6, s[12:15], s75 offen lds\n"
"s_mov_b32 s61, 0x18100800\n"
"s_cmp_eq_u32 s63, 4\n"
"s_cselect_b32 s61, 0x98489040, s61\n"
"s_lshl_b32 s63, s3, 3\n"
"s_lshr_b32 s61, s61, s63\n"
"s_and_b32 s61, s61, 255\n"
"s_add_u32 s34, s34, s61\n"
"s_mul_i32 s34, s34, 1280\n"
"s_lshr_b32 s62, s62, 2\n"
"s_mul_i32 s62, s62, 640\n"
"s_add_u32 s88, s34, s62\n"
"v_add_u32 v52, 0, v1\n"
"v_mul_u32_u24 v53, 1639, v52\n"
"v_lshrrev_b32 v53, 16, v53\n"
"v_mul_u32_u24 v53, 640, v53\n"
"v_add_u32 v75, v53, v2\n"
"v_add_u32 v52, 64, v1\n"
"v_mul_u32_u24 v53, 1639, v52\n"
"s_add_u32 m0, s59, 12288\n"
"s_nop 0\n"
"buffer_load_dwordx4 v6, s[12:15], s33 offen lds\n"
"v_lshrrev_b32 v53, 16, v53\n"
"v_mul_u32_u24 v53, 640, v53\n"
"v_add_u32 v76, v53, v2\n"
"v_add_u32 v52, 128, v1\n"
"v_mul_u32_u24 v53, 1639, v52\n"
"v_lshrrev_b32 v53, 16, v53\n"
"v_mul_u32_u24 v53, 640, v53\n"
"v_add_u32 v77, v53, v2\n"
"v_add_u32 v52, 192, v1\n"
"v_mul_u32_u24 v53, 1639, v52\n"
"v_lshrrev_b32 v53, 16, v53\n"
"v_mul_u32_u24 v53, 640, v53\n"
"v_add_u32 v78, v53, v2\n"
"v_add_u32 v52, 256, v1\n"
"v_mul_u32_u24 v53, 1639, v52\n"
"v_lshrrev_b32 v53, 16, v53\n"
"v_mul_u32_u24 v53, 640, v53\n"
"v_lshl_add_u32 v79, v52, 4, v53\n"
"s_cmp_eq_u32 s32, 9\n"
"s_cselect_b32 s85, -9, 1\n"
"v_lshl_add_u32 v5, s85, 7, v5\n"
"s_add_u32 m0, s59, 118784\n"
"s_nop 0\n"
"buffer_load_dwordx4 v5, s[4:7], s64 offen lds\n"
"v_lshrrev_b32 v57, 2, v1\n"
"v_mul_u32_u24 v57, 640, v57\n"
"s_mul_i32 s61, s3, 160\n"
"s_add_u32 s61, s61, 49152\n"
"v_and_b32 v52, 3, v1\n"
"v_lshl_add_u32 v57, v52, 3, v57\n"
"v_add_u32 v57, s61, v57\n"
"v_bfe_u32 v52, v1, 2, 2\n"
"v_mov_b32 v53, 0x3f803f80\n"
"v_lshlrev_b32 v54, 2, v1\n"
"v_cmp_eq_u32 vcc, 0, v52\n"
"s_nop 1\n"
"v_cndmask_b32 v16, 0, v53, vcc\n"
"v_cndmask_b32 v17, 0, v53, vcc\n"
"v_cndmask_b32 v18, 0, v53, vcc\n"
"v_cndmask_b32 v19, 0, v53, vcc\n"
"v_cmp_eq_u32 vcc, 1, v52\n"
"s_nop 1\n"
"v_cndmask_b32 v20, 0, v53, vcc\n"
"v_cndmask_b32 v21, 0, v53, vcc\n"
"s_add_u32 m0, s59, 122880\n"
"s_nop 0\n"
"buffer_load_dwordx4 v5, s[4:7], s65 offen lds\n"
"v_cndmask_b32 v22, 0, v53, vcc\n"
"v_cndmask_b32 v23, 0, v53, vcc\n"
"v_cmp_eq_u32 vcc, 2, v52\n"
"s_nop 1\n"
"v_cndmask_b32 v24, 0, v53, vcc\n"
"v_cndmask_b32 v25, 0, v53, vcc\n"
"v_cndmask_b32 v26, 0, v53, vcc\n"
"v_cndmask_b32 v27, 0, v53, vcc\n"
"v_cmp_eq_u32 vcc, 3, v52\n"
"s_nop 1\n"
"v_cndmask_b32 v28, 0, v53, vcc\n"
"v_cndmask_b32 v29, 0, v53, vcc\n"
"v_cndmask_b32 v30, 0, v53, vcc\n"
"v_cndmask_b32 v31, 0, v53, vcc\n"
"s_cmp_ge_u32 s32, 3\n"
"s_cselect_b32 s60, -3, 7\n"
"v_lshl_add_u32 v52, s60, 7, v40\n"
"s_cmp_ge_u32 s32, 2\n"
"s_cselect_b32 s60, -2, 8\n"
"v_lshl_add_u32 v53, s60, 7, v40\n"
"s_add_u32 m0, s59, 126976\n"
"s_nop 0\n"
"buffer_load_dwordx4 v5, s[4:7], s66 offen lds\n"
"s_cmp_ge_u32 s32, 1\n"
"s_cselect_b32 s60, -1, 9\n"
"v_lshl_add_u32 v55, s60, 7, v40\n"
"s_waitcnt lgkmcnt(0)\n"
"v_mov_b32 v56, s52\n"
"s_add_u32 s52, s52, 1\n"
"s_add_u32 m0, s59, 131072\n"
"s_nop 0\n"
"buffer_load_dwordx4 v5, s[4:7], s67 offen lds\n"
"s_add_u32 m0, s59, 135168\n"
"s_nop 0\n"
"buffer_load_dwordx4 v5, s[4:7], s68 offen lds\n"
"buffer_store_dword v56, v3, s[16:19], s57 offen sc1\n"
"s_waitcnt vmcnt(13)\n"
"s_barrier\n"
"ds_read_b128 a[232:235], v7 offset:0\n"
"ds_read_b128 a[236:239], v8 offset:0\n"
"ds_read_b64_tr_b16 a[104:105], v11 offset:32768\n"
"ds_read_b64_tr_b16 a[106:107], v11 offset:33280\n"
"ds_read_b64_tr_b16 a[120:121], v12 offset:32768\n"
"ds_read_b64_tr_b16 a[122:123], v12 offset:33280\n"
"ds_read_b64_tr_b16 a[108:109], v13 offset:32768\n"
"ds_read_b64_tr_b16 a[110:111], v13 offset:33280\n"
"ds_read_b64_tr_b16 a[124:125], v14 offset:32768\n"
"ds_read_b64_tr_b16 a[126:127], v14 offset:33280\n"
"ds_read_b64_tr_b16 a[112:113], v11 offset:40960\n"
"ds_read_b64_tr_b16 a[114:115], v11 offset:41472\n"
"ds_read_b64_tr_b16 a[128:129], v12 offset:40960\n"
"ds_read_b64_tr_b16 a[130:131], v12 offset:41472\n"
"ds_read_b64_tr_b16 a[116:117], v13 offset:40960\n"
"ds_read_b64_tr_b16 a[118:119], v13 offset:41472\n"
"ds_read_b64_tr_b16 a[132:133], v14 offset:40960\n"
"ds_read_b64_tr_b16 a[134:135], v14 offset:41472\n"
"ds_read_b128 a[240:243], v7 offset:4096\n"
"ds_read_b128 a[244:247], v8 offset:4096\n"
"s_waitcnt lgkmcnt(15)\n"
"v_mfma_f32_16x16x32_bf16 v[240:243], a[104:107], a[232:235], 0\n"
"ds_read_b128 a[248:251], v7 offset:8192\n"
"ds_read_b64_tr_b16 a[136:137], v11 offset:0\n"
"s_waitcnt lgkmcnt(15)\n"
"v_mfma_f32_16x16x32_bf16 v[240:243], a[120:123], a[236:239], v[240:243]\n"
"ds_read_b128 a[252:255], v8 offset:8192\n"
"s_add_u32 m0, s59, 139264\n"
"s_waitcnt lgkmcnt(15)\n"
"v_mfma_f32_16x16x32_bf16 v[244:247], a[108:111], a[232:235], 0\n"
"ds_read_b64_tr_b16 a[138:139], v11 offset:512\n"
"buffer_load_dwordx4 v5, s[4:7], s69 offen lds\n"
"s_waitcnt lgkmcnt(14)\n"
"v_mfma_f32_16x16x32_bf16 v[244:247], a[124:127], a[236:239], v[244:247]\n"
"s_cmp_eq_u32 s32, 8\n"
"s_cselect_b32 s85, -9, 1\n"
"s_waitcnt lgkmcnt(12)\n"
"v_mfma_f32_16x16x32_bf16 v[248:251], a[112:115], a[232:235], 0\n"
"ds_read_b64_tr_b16 a[152:153], v12 offset:0\n"
"s_mul_i32 s33, s85, 221184\n"
"s_waitcnt lgkmcnt(11)\n"
"v_mfma_f32_16x16x32_bf16 v[248:251], a[128:131], a[236:239], v[248:251]\n"
"s_add_u32 s75, s75, s33\n"
"s_add_u32 s33, s75, 110592\n"
"s_waitcnt lgkmcnt(9)\n"
"v_mfma_f32_16x16x32_bf16 v[252:255], a[116:119], a[232:235], 0\n"
"ds_read_b64_tr_b16 a[154:155], v12 offset:512\n"
"s_add_u32 m0, s59, 16384\n"
"s_waitcnt lgkmcnt(8)\n"
"v_mfma_f32_16x16x32_bf16 v[252:255], a[132:135], a[236:239], v[252:255]\n"
"buffer_load_dwordx4 v6, s[8:11], s75 offen lds\n"
"s_waitcnt lgkmcnt(6)\n"
"v_mfma_f32_16x16x32_bf16 v[224:227], a[104:107], a[240:243], 0\n"
"ds_read_b128 a[232:235], v7 offset:12288\n"
"ds_read_b64_tr_b16 a[140:141], v13 offset:0\n"
"v_mfma_f32_16x16x32_bf16 v[224:227], a[120:123], a[244:247], v[224:227]\n"
"ds_read_b128 a[236:239], v8 offset:12288\n"
"s_add_u32 m0, s59, 20480\n"
"v_mfma_f32_16x16x32_bf16 v[228:231], a[108:111], a[240:243], 0\n"
"ds_read_b64_tr_b16 a[142:143], v13 offset:512\n"
"buffer_load_dwordx4 v6, s[8:11], s33 offen lds\n"
"v_mfma_f32_16x16x32_bf16 v[228:231], a[124:127], a[244:247], v[228:231]\n"
"s_add_u32 m0, s59, 143360\n"
"s_lshr_b32 s60, s2, 3\n"
"v_mfma_f32_16x16x32_bf16 v[232:235], a[112:115], a[240:243], 0\n"
"ds_read_b64_tr_b16 a[156:157], v14 offset:0\n"
"buffer_load_dwordx4 v5, s[4:7], s70 offen lds\n"
"v_mfma_f32_16x16x32_bf16 v[232:235], a[128:131], a[244:247], v[232:235]\n"
"s_add_u32 m0, s59, 147456\n"
"s_sub_u32 s60, s60, 11\n"
"v_mfma_f32_16x16x32_bf16 v[236:239], a[116:119], a[240:243], 0\n"
"ds_read_b64_tr_b16 a[158:159], v14 offset:512\n"
"buffer_load_dwordx4 v5, s[4:7], s71 offen lds\n"
"v_mfma_f32_16x16x32_bf16 v[236:239], a[132:135], a[244:247], v[236:239]\n"
"s_waitcnt lgkmcnt(9)\n"
"s_add_u32 m0, s59, 151552\n"
"v_mfma_f32_16x16x32_bf16 v[208:211], a[104:107], a[248:251], 0\n"
"ds_read_b128 a[240:243], v7 offset:16384\n"
"buffer_load_dwordx4 v5, s[4:7], s72 offen lds\n"
"v_mfma_f32_16x16x32_bf16 v[208:211], a[120:123], a[252:255], v[208:211]\n"
"ds_read_b128 a[244:247], v8 offset:16384\n"
"s_add_u32 m0, s59, 155648\n"
"v_mfma_f32_16x16x32_bf16 v[212:215], a[108:111], a[248:251], 0\n"
"buffer_load_dwordx4 v5, s[4:7], s73 offen lds\n"
"s_add_u32 m0, s59, 159744\n"
"v_mfma_f32_16x16x32_bf16 v[212:215], a[124:127], a[252:255], v[212:215]\n"
"buffer_load_dwordx4 v5, s[4:7], s74 offen lds\n"
"s_add_u32 m0, s59, 24576\n"
"v_mfma_f32_16x16x32_bf16 v[48:51], v[16:19], a[104:107], 0\n"
"v_mfma_f32_16x16x32_bf16 v[48:51], v[20:23], a[108:111], v[48:51]\n"
"v_mfma_f32_16x16x32_bf16 v[48:51], v[24:27], a[112:115], v[48:51]\n"
"v_mfma_f32_16x16x32_bf16 v[48:51], v[28:31], a[116:119], v[48:51]\n"
"v_mfma_f32_16x16x32_bf16 v[216:219], a[112:115], a[248:251], 0\n"
"buffer_load_dwordx4 v6, s[12:15], s75 offen lds\n"
"s_add_u32 m0, s59, 28672\n"
"v_mfma_f32_16x16x32_bf16 v[216:219], a[128:131], a[252:255], v[216:219]\n"
"buffer_load_dwordx4 v6, s[12:15], s33 offen lds\n"
"s_add_u32 s62, s60, 11\n"
"v_mfma_f32_16x16x32_bf16 v[220:223], a[116:119], a[248:251], 0\n"
"s_and_b32 s61, s62, 1\n"
"s_lshr_b32 s62, s62, 1\n"
"v_mfma_f32_16x16x32_bf16 v[220:223], a[132:135], a[252:255], v[220:223]\n"
"s_waitcnt lgkmcnt(5)\n"
"s_mul_i32 s63, s61, 454761243\n"
"v_mfma_f32_16x16x32_bf16 v[192:195], a[104:107], a[232:235], 0\n"
"ds_read_b128 a[248:251], v7 offset:20480\n"
"s_add_u32 s63, s63, 2475509504\n"
"v_mfma_f32_16x16x32_bf16 v[192:195], a[120:123], a[236:239], v[192:195]\n"
"ds_read_b128 a[252:255], v8 offset:20480\n"
"v_mfma_f32_16x16x32_bf16 v[196:199], a[108:111], a[232:235], 0\n"
"buffer_store_dword v48, v54, s[16:19], s92 offen sc1\n"
"v_mfma_f32_16x16x32_bf16 v[196:199], a[124:127], a[236:239], v[196:199]\n"
"s_cmp_lt_u32 s60, 11\n"
"s_cselect_b32 s60, s60, s62\n"
"s_cselect_b32 s61, 0, s61\n"
"s_cselect_b32 s63, 403703808, s63\n"
"v_mfma_f32_16x16x32_bf16 v[200:203], a[112:115], a[232:235], 0\n"
"s_lshr_b32 s56, s60, 1\n"
"s_and_b32 s60, s60, 1\n"
"v_mfma_f32_16x16x32_bf16 v[200:203], a[128:131], a[236:239], v[200:203]\n"
"s_lshl_b32 s60, s60, 1\n"
"s_add_u32 s60, s60, s61\n"
"v_mfma_f32_16x16x32_bf16 v[204:207], a[116:119], a[232:235], 0\n"
"v_lshrrev_b32 v34, 4, v1\n"
"v_lshl_add_u32 v34, s91, 2, v34\n"
"v_mfma_f32_16x16x32_bf16 v[204:207], a[132:135], a[236:239], v[204:207]\n"
"s_waitcnt vmcnt(17) lgkmcnt(0)\n"
"s_barrier\n"
"v_mfma_f32_16x16x32_bf16 v[176:179], a[104:107], a[240:243], 0\n"
"ds_read_b128 a[232:235], v7 offset:24576\n"
"ds_read_b64_tr_b16 a[144:145], v11 offset:8192\n"
"v_mfma_f32_16x16x32_bf16 v[176:179], a[120:123], a[244:247], v[176:179]\n"
"ds_read_b128 a[236:239], v8 offset:24576\n"
"v_lshlrev_b32 v32, 4, v34\n"
"v_mfma_f32_16x16x32_bf16 v[180:183], a[108:111], a[240:243], 0\n"
"ds_read_b64_tr_b16 a[146:147], v11 offset:8704\n"
"v_add_u32 v32, 262144, v32\n"
"v_mfma_f32_16x16x32_bf16 v[180:183], a[124:127], a[244:247], v[180:183]\n"
"s_cmp_eq_u32 s32, 8\n"
"s_cselect_b32 s85, -9, 1\n"
"v_mfma_f32_16x16x32_bf16 v[184:187], a[112:115], a[240:243], 0\n"
"ds_read_b64_tr_b16 a[160:161], v12 offset:8192\n"
"v_lshl_add_u32 v5, s85, 7, v5\n"
"v_mfma_f32_16x16x32_bf16 v[184:187], a[128:131], a[244:247], v[184:187]\n"
"v_lshrrev_b32 v33, 2, v1\n"
"s_add_u32 m0, s59, 73728\n"
"v_mfma_f32_16x16x32_bf16 v[188:191], a[116:119], a[240:243], 0\n"
"ds_read_b64_tr_b16 a[162:163], v12 offset:8704\n"
"buffer_load_dwordx4 v5, s[4:7], s64 offen lds\n"
"v_mfma_f32_16x16x32_bf16 v[188:191], a[132:135], a[244:247], v[188:191]\n"
"s_add_u32 m0, s59, 77824\n"
"v_mfma_f32_16x16x32_bf16 v[160:163], a[104:107], a[248:251], 0\n"
"ds_read_b128 a[240:243], v7 offset:28672\n"
"ds_read_b64_tr_b16 a[148:149], v13 offset:8192\n"
"v_mfma_f32_16x16x32_bf16 v[160:163], a[120:123], a[252:255], v[160:163]\n"
"ds_read_b128 a[244:247], v8 offset:28672\n"
"buffer_load_dwordx4 v5, s[4:7], s65 offen lds\n"
"v_mfma_f32_16x16x32_bf16 v[164:167], a[108:111], a[248:251], 0\n"
"ds_read_b64_tr_b16 a[150:151], v13 offset:8704\n"
"s_add_u32 m0, s59, 81920\n"
"v_mfma_f32_16x16x32_bf16 v[164:167], a[124:127], a[252:255], v[164:167]\n"
"buffer_load_dwordx4 v5, s[4:7], s66 offen lds\n"
"s_add_u32 m0, s59, 86016\n"
"v_mfma_f32_16x16x32_bf16 v[168:171], a[112:115], a[248:251], 0\n"
"ds_read_b64_tr_b16 a[164:165], v14 offset:8192\n"
"buffer_load_dwordx4 v5, s[4:7], s67 offen lds\n"
"v_mfma_f32_16x16x32_bf16 v[168:171], a[128:131], a[252:255], v[168:171]\n"
"v_lshl_add_u32 v33, s91, 5, v33\n"
"s_add_u32 m0, s59, 90112\n"
"v_mfma_f32_16x16x32_bf16 v[172:175], a[116:119], a[248:251], 0\n"
"ds_read_b64_tr_b16 a[166:167], v14 offset:8704\n"
"buffer_load_dwordx4 v5, s[4:7], s68 offen lds\n"
"v_mfma_f32_16x16x32_bf16 v[172:175], a[132:135], a[252:255], v[172:175]\n"
"s_waitcnt lgkmcnt(9)\n"
"s_add_u32 m0, s59, 94208\n"
"v_mfma_f32_16x16x32_bf16 v[144:147], a[104:107], a[232:235], 0\n"
"ds_read_b128 a[248:251], v7 offset:32768\n"
"buffer_load_dwordx4 v5, s[4:7], s69 offen lds\n"
"v_mfma_f32_16x16x32_bf16 v[144:147], a[120:123], a[236:239], v[144:147]\n"
"ds_read_b128 a[252:255], v8 offset:32768\n"
"v_add_u32 v33, 294912, v33\n"
"v_mfma_f32_16x16x32_bf16 v[148:151], a[108:111], a[232:235], 0\n"
"s_cmp_eq_u32 s32, 7\n"
"s_cselect_b32 s85, -9, 1\n"
"v_mfma_f32_16x16x32_bf16 v[148:151], a[124:127], a[236:239], v[148:151]\n"
"s_mul_i32 s33, s85, 221184\n"
"s_add_u32 s75, s75, s33\n"
"v_mfma_f32_16x16x32_bf16 v[152:155], a[112:115], a[232:235], 0\n"
"s_add_u32 s33, s75, 110592\n"
"s_add_u32 m0, s59, 0\n"
"v_mfma_f32_16x16x32_bf16 v[152:155], a[128:131], a[236:239], v[152:155]\n"
"buffer_load_dwordx4 v6, s[8:11], s75 offen lds\n"
"s_add_u32 m0, s59, 4096\n"
"v_mfma_f32_16x16x32_bf16 v[156:159], a[116:119], a[232:235], 0\n"
"buffer_load_dwordx4 v6, s[8:11], s33 offen lds\n"
"v_and_b32 v35, 15, v1\n"
"v_mfma_f32_16x16x32_bf16 v[156:159], a[132:135], a[236:239], v[156:159]\n"
"v_lshrrev_b32 v34, 11, v34\n"
"s_waitcnt lgkmcnt(5)\n"
"v_mfma_f32_16x16x32_bf16 v[128:131], a[104:107], a[240:243], 0\n"
"ds_read_b128 a[232:235], v7 offset:36864\n"
"v_or_b32 v35, v35, v34\n"
"v_mfma_f32_16x16x32_bf16 v[128:131], a[120:123], a[244:247], v[128:131]\n"
"ds_read_b128 a[236:239], v8 offset:36864\n"
"v_cmp_eq_u32 vcc, 0, v35\n"
"v_mfma_f32_16x16x32_bf16 v[132:135], a[108:111], a[240:243], 0\n"
"v_mov_b32 v34, 1073741824\n"
"s_nop 0\n"
"v_mfma_f32_16x16x32_bf16 v[132:135], a[124:127], a[244:247], v[132:135]\n"
"v_cndmask_b32 v32, v34, v32, vcc\n"
"v_cndmask_b32 v33, v34, v33, vcc\n"
"v_mfma_f32_16x16x32_bf16 v[136:139], a[112:115], a[240:243], 0\n"
"s_and_b32 s62, s2, 7\n"
"s_lshl_b32 s62, s62, 2\n"
"v_mfma_f32_16x16x32_bf16 v[136:139], a[128:131], a[244:247], v[136:139]\n"
"s_add_u32 s55, s62, s60\n"
"s_lshl_b32 s62, s3, 3\n"
"v_mfma_f32_16x16x32_bf16 v[140:143], a[116:119], a[240:243], 0\n"
"s_lshr_b32 s63, s63, s62\n"
"s_and_b32 s78, s63, 63\n"
"v_mfma_f32_16x16x32_bf16 v[140:143], a[132:135], a[244:247], v[140:143]\n"
"s_waitcnt lgkmcnt(2)\n"
"s_add_u32 m0, s35, 0\n"
"v_mfma_f32_16x16x32_bf16 v[112:115], a[104:107], a[248:251], 0\n"
"ds_read_b128 a[240:243], v7 offset:40960\n"
"buffer_load_dwordx4 v75, s[28:31], s88 offen offset:0 lds\n"
"v_mfma_f32_16x16x32_bf16 v[112:115], a[120:123], a[252:255], v[112:115]\n"
"ds_read_b128 a[244:247], v8 offset:40960\n"
"s_bfe_u32 s58, s63, 0x20006\n"
"v_mfma_f32_16x16x32_bf16 v[116:119], a[108:111], a[248:251], 0\n"
"buffer_load_dwordx4 v76, s[28:31], s88 offen offset:1024 lds\n"
"s_and_b32 s49, s49, 0xffff\n"
"v_mfma_f32_16x16x32_bf16 v[116:119], a[124:127], a[252:255], v[116:119]\n"
"s_mov_b32 s50, 2621440\n"
"s_mov_b32 s51, 0x20000\n"
"v_mfma_f32_16x16x32_bf16 v[120:123], a[112:115], a[248:251], 0\n"
"buffer_load_dwordx4 v77, s[28:31], s88 offen offset:2048 lds\n"
"s_mul_i32 s60, s55, 456\n"
"v_mfma_f32_16x16x32_bf16 v[120:123], a[128:131], a[252:255], v[120:123]\n"
"s_add_u32 s44, s60, s78\n"
"s_lshl_b32 s44, s44, 10\n"
"v_mfma_f32_16x16x32_bf16 v[124:127], a[116:119], a[248:251], 0\n"
"s_mul_i32 s62, s3, 5\n"
"s_add_u32 s62, s62, 54\n"
"v_mfma_f32_16x16x32_bf16 v[124:127], a[132:135], a[252:255], v[124:127]\n"
"s_waitcnt vmcnt(19) lgkmcnt(0)\n"
"s_barrier\n"
"v_mfma_f32_16x16x32_bf16 v[96:99], a[104:107], a[232:235], 0\n"
"ds_read_b128 a[248:251], v9 offset:0\n"
"ds_read_b64_tr_b16 a[168:169], v11 offset:16384\n"
"v_mfma_f32_16x16x32_bf16 v[96:99], a[120:123], a[236:239], v[96:99]\n"
"ds_read_b128 a[252:255], v10 offset:0\n"
"buffer_load_dwordx4 v78, s[28:31], s88 offen offset:3072 lds\n"
"v_mfma_f32_16x16x32_bf16 v[100:103], a[108:111], a[232:235], 0\n"
"ds_read_b64_tr_b16 a[170:171], v11 offset:16896\n"
"s_add_u32 m0, s35, 4096\n"
"v_mfma_f32_16x16x32_bf16 v[100:103], a[124:127], a[236:239], v[100:103]\n"
"buffer_load_dwordx4 v79, s[28:31], s88 offen offset:0 lds\n"
"s_add_u32 s84, s60, s62\n"
"v_mfma_f32_16x16x32_bf16 v[104:107], a[112:115], a[232:235], 0\n"
"ds_read_b64_tr_b16 a[184:185], v12 offset:16384\n"
"buffer_load_dwordx4 v[60:63], v15, s[4:7], 0 offen offset:0\n"
"v_mfma_f32_16x16x32_bf16 v[104:107], a[128:131], a[236:239], v[104:107]\n"
"s_lshl_b32 s84, s84, 10\n"
"s_xor_b32 s60, s55, 1\n"
"v_mfma_f32_16x16x32_bf16 v[108:111], a[116:119], a[232:235], 0\n"
"ds_read_b64_tr_b16 a[186:187], v12 offset:16896\n"
"buffer_load_dwordx4 v[64:67], v15, s[4:7], 0 offen offset:256\n"
"v_mfma_f32_16x16x32_bf16 v[108:111], a[132:135], a[236:239], v[108:111]\n"
"s_mul_i32 s60, s60, 456\n"
"v_mfma_f32_16x16x32_bf16 v[80:83], a[104:107], a[240:243], 0\n"
"ds_read_b128 a[232:235], v9 offset:4096\n"
"ds_read_b64_tr_b16 a[172:173], v13 offset:16384\n"
"v_mfma_f32_16x16x32_bf16 v[80:83], a[120:123], a[244:247], v[80:83]\n"
"ds_read_b128 a[236:239], v10 offset:4096\n"
"buffer_load_dwordx4 v[68:71], v15, s[4:7], 0 offen offset:512\n"
"v_mfma_f32_16x16x32_bf16 v[84:87], a[108:111], a[240:243], 0\n"
"ds_read_b64_tr_b16 a[174:175], v13 offset:16896\n"
"s_add_u32 s45, s60, s78\n"
"v_mfma_f32_16x16x32_bf16 v[84:87], a[124:127], a[244:247], v[84:87]\n"
"buffer_load_dwordx4 v[72:75], v15, s[4:7], 0 offen offset:768\n"
"s_lshl_b32 s45, s45, 10\n"
"v_mfma_f32_16x16x32_bf16 v[88:91], a[112:115], a[240:243], 0\n"
"ds_read_b64_tr_b16 a[188:189], v14 offset:16384\n"
"s_mul_i32 s61, s56, 74\n"
"v_mfma_f32_16x16x32_bf16 v[88:91], a[128:131], a[244:247], v[88:91]\n"
"buffer_load_dwordx4 v[76:79], v15, s[4:7], 0 offen offset:1024\n"
"s_add_u32 s62, s62, s61\n"
"v_mfma_f32_16x16x32_bf16 v[92:95], a[116:119], a[240:243], 0\n"
"ds_read_b64_tr_b16 a[190:191], v14 offset:16896\n"
"s_mul_i32 s97, s62, 5120\n"
"v_mfma_f32_16x16x32_bf16 v[92:95], a[132:135], a[244:247], v[92:95]\n"
"s_waitcnt lgkmcnt(9)\n"
"s_add_u32 s61, s61, s78\n"
"v_mfma_f32_16x16x32_bf16 v[240:243], a[136:139], a[248:251], v[240:243]\n"
"ds_read_b128 a[240:243], v9 offset:8192\n"
"s_mul_i32 s43, s61, 5120\n"
"v_mfma_f32_16x16x32_bf16 v[240:243], a[152:155], a[252:255], v[240:243]\n"
"ds_read_b128 a[244:247], v10 offset:8192\n"
"s_add_u32 s96, s43, 10240\n"
"v_mfma_f32_16x16x32_bf16 v[244:247], a[140:143], a[248:251], v[244:247]\n"
"s_cmp_ge_u32 s58, 1\n"
"s_cselect_b32 s43, 1073741824, s43\n"
"v_mfma_f32_16x16x32_bf16 v[244:247], a[156:159], a[252:255], v[244:247]\n"
"s_lshl_b32 s60, s55, 2\n"
"s_add_u32 s60, s60, s3\n"
"v_mfma_f32_16x16x32_bf16 v[248:251], a[144:147], a[248:251], v[248:251]\n"
"s_lshl_b32 s60, s60, 3\n"
"s_add_u32 s40, s60, s56\n"
"v_mfma_f32_16x16x32_bf16 v[248:251], a[160:163], a[252:255], v[248:251]\n"
"s_lshr_b32 s61, s55, 1\n"
"s_lshl_b32 s61, s61, 3\n"
"v_mfma_f32_16x16x32_bf16 v[252:255], a[148:151], a[248:251], v[252:255]\n"
"s_mul_i32 s62, s61, 373\n"
"s_lshr_b32 s62, s62, 12\n"
"v_mfma_f32_16x16x32_bf16 v[252:255], a[164:167], a[252:255], v[252:255]\n"
"s_waitcnt lgkmcnt(5)\n"
"s_mul_i32 s62, s62, 54\n"
"v_mfma_f32_16x16x32_bf16 v[224:227], a[136:139], a[232:235], v[224:227]\n"
"ds_read_b128 a[248:251], v9 offset:12288\n"
"s_add_u32 s63, s61, 7\n"
"v_mfma_f32_16x16x32_bf16 v[224:227], a[152:155], a[236:239], v[224:227]\n"
"ds_read_b128 a[252:255], v10 offset:12288\n"
"s_mul_i32 s63, s63, 373\n"
"v_mfma_f32_16x16x32_bf16 v[228:231], a[140:143], a[232:235], v[228:231]\n"
"s_lshr_b32 s63, s63, 12\n"
"s_mul_i32 s63, s63, 54\n"
"v_mfma_f32_16x16x32_bf16 v[228:231], a[156:159], a[236:239], v[228:231]\n"
"s_sub_u32 s46, s63, s62\n"
"s_add_u32 s41, s62, s78\n"
"v_mfma_f32_16x16x32_bf16 v[232:235], a[144:147], a[232:235], v[232:235]\n"
"s_add_u32 s41, s41, 64\n"
"s_lshl_b32 s61, s55, 2\n"
"v_mfma_f32_16x16x32_bf16 v[232:235], a[160:163], a[236:239], v[232:235]\n"
"s_mul_i32 s62, s61, 1366\n"
"s_lshr_b32 s62, s62, 12\n"
"v_mfma_f32_16x16x32_bf16 v[236:239], a[148:151], a[232:235], v[236:239]\n"
"s_mul_i32 s63, s3, 88\n"
"s_add_u32 s62, s62, s63\n"
"v_mfma_f32_16x16x32_bf16 v[236:239], a[164:167], a[236:239], v[236:239]\n"
"s_waitcnt lgkmcnt(2)\n"
"s_add_u32 s47, s62, 768\n"
"v_mfma_f32_16x16x32_bf16 v[208:211], a[136:139], a[240:243], v[208:211]\n"
"ds_read_b128 a[232:235], v9 offset:16384\n"
"s_add_u32 s61, s61, s3\n"
"v_mfma_f32_16x16x32_bf16 v[208:211], a[152:155], a[244:247], v[208:211]\n"
"ds_read_b128 a[236:239], v10 offset:16384\n"
"s_mul_i32 s60, s61, 20480\n"
"v_mfma_f32_16x16x32_bf16 v[212:215], a[140:143], a[240:243], v[212:215]\n"
"s_mul_i32 s61, s56, 160\n"
"s_add_u32 s79, s60, s61\n"
"v_mfma_f32_16x16x32_bf16 v[212:215], a[156:159], a[244:247], v[212:215]\n"
"v_mfma_f32_16x16x32_bf16 v[44:47], v[16:19], a[136:139], 0\n"
"v_mfma_f32_16x16x32_bf16 v[44:47], v[20:23], a[140:143], v[44:47]\n"
"v_mfma_f32_16x16x32_bf16 v[44:47], v[24:27], a[144:147], v[44:47]\n"
"v_mfma_f32_16x16x32_bf16 v[44:47], v[28:31], a[148:151], v[44:47]\n"
"v_mfma_f32_16x16x32_bf16 v[216:219], a[144:147], a[240:243], v[216:219]\n"
"s_sub_u32 s55, s96, 5120\n"
"v_mfma_f32_16x16x32_bf16 v[216:219], a[160:163], a[244:247], v[216:219]\n"
"s_cmp_ge_u32 s58, 2\n"
"s_cselect_b32 s55, 1073741824, s55\n"
"v_mfma_f32_16x16x32_bf16 v[220:223], a[148:151], a[240:243], v[220:223]\n"
"v_mfma_f32_16x16x32_bf16 v[220:223], a[164:167], a[244:247], v[220:223]\n"
"s_waitcnt lgkmcnt(2)\n"
"v_mfma_f32_16x16x32_bf16 v[192:195], a[136:139], a[248:251], v[192:195]\n"
"ds_read_b128 a[240:243], v9 offset:20480\n"
"v_mfma_f32_16x16x32_bf16 v[192:195], a[152:155], a[252:255], v[192:195]\n"
"ds_read_b128 a[244:247], v10 offset:20480\n"
"v_mfma_f32_16x16x32_bf16 v[196:199], a[140:143], a[248:251], v[196:199]\n"
"buffer_store_dword v44, v54, s[16:19], s93 offen sc1\n"
"v_mfma_f32_16x16x32_bf16 v[196:199], a[156:159], a[252:255], v[196:199]\n"
"v_mfma_f32_16x16x32_bf16 v[200:203], a[144:147], a[248:251], v[200:203]\n"
"v_mfma_f32_16x16x32_bf16 v[200:203], a[160:163], a[252:255], v[200:203]\n"
"v_mfma_f32_16x16x32_bf16 v[204:207], a[148:151], a[248:251], v[204:207]\n"
"v_mfma_f32_16x16x32_bf16 v[204:207], a[164:167], a[252:255], v[204:207]\n"
"s_waitcnt vmcnt(20) lgkmcnt(0)\n"
"s_barrier\n"
"v_mfma_f32_16x16x32_bf16 v[176:179], a[136:139], a[232:235], v[176:179]\n"
"ds_read_b128 a[248:251], v9 offset:24576\n"
"ds_read_b64_tr_b16 a[176:177], v11 offset:24576\n"
"v_mfma_f32_16x16x32_bf16 v[176:179], a[152:155], a[236:239], v[176:179]\n"
"ds_read_b128 a[252:255], v10 offset:24576\n"
"s_add_u32 m0, s59, 98304\n"
"v_mfma_f32_16x16x32_bf16 v[180:183], a[140:143], a[232:235], v[180:183]\n"
"ds_read_b64_tr_b16 a[178:179], v11 offset:25088\n"
"buffer_load_dwordx4 v5, s[4:7], s70 offen lds\n"
"v_mfma_f32_16x16x32_bf16 v[180:183], a[156:159], a[236:239], v[180:183]\n"
"s_add_u32 m0, s59, 102400\n"
"v_mfma_f32_16x16x32_bf16 v[184:187], a[144:147], a[232:235], v[184:187]\n"
"ds_read_b64_tr_b16 a[192:193], v12 offset:24576\n"
"v_mfma_f32_16x16x32_bf16 v[184:187], a[160:163], a[236:239], v[184:187]\n"
"buffer_load_dwordx4 v5, s[4:7], s71 offen lds\n"
"v_mfma_f32_16x16x32_bf16 v[188:191], a[148:151], a[232:235], v[188:191]\n"
"ds_read_b64_tr_b16 a[194:195], v12 offset:25088\n"
"v_mfma_f32_16x16x32_bf16 v[188:191], a[164:167], a[236:239], v[188:191]\n"
"s_add_u32 m0, s59, 106496\n"
"v_mfma_f32_16x16x32_bf16 v[160:163], a[136:139], a[240:243], v[160:163]\n"
"ds_read_b128 a[232:235], v9 offset:28672\n"
"ds_read_b64_tr_b16 a[180:181], v13 offset:24576\n"
"v_mfma_f32_16x16x32_bf16 v[160:163], a[152:155], a[244:247], v[160:163]\n"
"ds_read_b128 a[236:239], v10 offset:28672\n"
"buffer_load_dwordx4 v5, s[4:7], s72 offen lds\n"
"v_mfma_f32_16x16x32_bf16 v[164:167], a[140:143], a[240:243], v[164:167]\n"
"ds_read_b64_tr_b16 a[182:183], v13 offset:25088\n"
"v_mfma_f32_16x16x32_bf16 v[164:167], a[156:159], a[244:247], v[164:167]\n"
"s_add_u32 m0, s59, 110592\n"
"v_mfma_f32_16x16x32_bf16 v[168:171], a[144:147], a[240:243], v[168:171]\n"
"ds_read_b64_tr_b16 a[196:197], v14 offset:24576\n"
"v_mfma_f32_16x16x32_bf16 v[168:171], a[160:163], a[244:247], v[168:171]\n"
"buffer_load_dwordx4 v5, s[4:7], s73 offen lds\n"
"v_mfma_f32_16x16x32_bf16 v[172:175], a[148:151], a[240:243], v[172:175]\n"
"ds_read_b64_tr_b16 a[198:199], v14 offset:25088\n"
"v_mfma_f32_16x16x32_bf16 v[172:175], a[164:167], a[244:247], v[172:175]\n"
"s_waitcnt lgkmcnt(9)\n"
"s_add_u32 m0, s59, 114688\n"
"v_mfma_f32_16x16x32_bf16 v[144:147], a[136:139], a[248:251], v[144:147]\n"
"ds_read_b128 a[240:243], v9 offset:32768\n"
"v_mfma_f32_16x16x32_bf16 v[144:147], a[152:155], a[252:255], v[144:147]\n"
"ds_read_b128 a[244:247], v10 offset:32768\n"
"buffer_load_dwordx4 v5, s[4:7], s74 offen lds\n"
"v_mfma_f32_16x16x32_bf16 v[148:151], a[140:143], a[248:251], v[148:151]\n"
"v_mfma_f32_16x16x32_bf16 v[148:151], a[156:159], a[252:255], v[148:151]\n"
"s_add_u32 m0, s59, 8192\n"
"v_mfma_f32_16x16x32_bf16 v[152:155], a[144:147], a[248:251], v[152:155]\n"
"v_mfma_f32_16x16x32_bf16 v[152:155], a[160:163], a[252:255], v[152:155]\n"
"buffer_load_dwordx4 v6, s[12:15], s75 offen lds\n"
"v_mfma_f32_16x16x32_bf16 v[156:159], a[148:151], a[248:251], v[156:159]\n"
"v_mfma_f32_16x16x32_bf16 v[156:159], a[164:167], a[252:255], v[156:159]\n"
"s_waitcnt lgkmcnt(5)\n"
"s_add_u32 m0, s59, 12288\n"
"v_mfma_f32_16x16x32_bf16 v[128:131], a[136:139], a[232:235], v[128:131]\n"
"ds_read_b128 a[248:251], v9 offset:36864\n"
"v_mfma_f32_16x16x32_bf16 v[128:131], a[152:155], a[236:239], v[128:131]\n"
"ds_read_b128 a[252:255], v10 offset:36864\n"
"buffer_load_dwordx4 v6, s[12:15], s33 offen lds\n"
"v_mfma_f32_16x16x32_bf16 v[132:135], a[140:143], a[232:235], v[132:135]\n"
"s_cmp_eq_u32 s32, 7\n"
"s_cselect_b32 s85, -9, 1\n"
"v_mfma_f32_16x16x32_bf16 v[132:135], a[156:159], a[236:239], v[132:135]\n"
"v_lshl_add_u32 v5, s85, 7, v5\n"
"s_add_u32 m0, s59, 118784\n"
"v_mfma_f32_16x16x32_bf16 v[136:139], a[144:147], a[232:235], v[136:139]\n"
"v_mfma_f32_16x16x32_bf16 v[136:139], a[160:163], a[236:239], v[136:139]\n"
"buffer_load_dwordx4 v5, s[4:7], s64 offen lds\n"
"v_mfma_f32_16x16x32_bf16 v[140:143], a[148:151], a[232:235], v[140:143]\n"
"s_waitcnt vmcnt(9)\n"
"s_mov_b32 s99, 0x3f803f80\n"
"v_dot2_f32_bf16 v58, v60, s99, 0\n"
"v_dot2_f32_bf16 v59, v60, v60, 0\n"
"v_dot2_f32_bf16 v58, v61, s99, v58\n"
"v_dot2_f32_bf16 v59, v61, v61, v59\n"
"v_dot2_f32_bf16 v58, v62, s99, v58\n"
"v_dot2_f32_bf16 v59, v62, v62, v59\n"
"v_dot2_f32_bf16 v58, v63, s99, v58\n"
"v_dot2_f32_bf16 v59, v63, v63, v59\n"
"v_dot2_f32_bf16 v58, v64, s99, v58\n"
"v_dot2_f32_bf16 v59, v64, v64, v59\n"
"v_dot2_f32_bf16 v58, v65, s99, v58\n"
"v_dot2_f32_bf16 v59, v65, v65, v59\n"
"v_dot2_f32_bf16 v58, v66, s99, v58\n"
"v_dot2_f32_bf16 v59, v66, v66, v59\n"
"v_dot2_f32_bf16 v58, v67, s99, v58\n"
"v_dot2_f32_bf16 v59, v67, v67, v59\n"
"v_dot2_f32_bf16 v58, v68, s99, v58\n"
"v_dot2_f32_bf16 v59, v68, v68, v59\n"
"v_dot2_f32_bf16 v58, v69, s99, v58\n"
"v_dot2_f32_bf16 v59, v69, v69, v59\n"
"v_dot2_f32_bf16 v58, v70, s99, v58\n"
"v_dot2_f32_bf16 v59, v70, v70, v59\n"
"v_dot2_f32_bf16 v58, v71, s99, v58\n"
"v_dot2_f32_bf16 v59, v71, v71, v59\n"
"v_dot2_f32_bf16 v58, v72, s99, v58\n"
"v_dot2_f32_bf16 v59, v72, v72, v59\n"
"v_dot2_f32_bf16 v58, v73, s99, v58\n"
"v_dot2_f32_bf16 v59, v73, v73, v59\n"
"v_dot2_f32_bf16 v58, v74, s99, v58\n"
"v_dot2_f32_bf16 v59, v74, v74, v59\n"
"v_dot2_f32_bf16 v58, v75, s99, v58\n"
"v_dot2_f32_bf16 v59, v75, v75, v59\n"
"v_dot2_f32_bf16 v58, v76, s99, v58\n"
"v_dot2_f32_bf16 v59, v76, v76, v59\n"
"v_dot2_f32_bf16 v58, v77, s99, v58\n"
"v_dot2_f32_bf16 v59, v77, v77, v59\n"
"v_dot2_f32_bf16 v58, v78, s99, v58\n"
"v_dot2_f32_bf16 v59, v78, v78, v59\n"
"v_dot2_f32_bf16 v58, v79, s99, v58\n"
"v_dot2_f32_bf16 v59, v79, v79, v59\n"
".Lfb_b_4:\n"
"v_mfma_f32_16x16x32_bf16 v[140:143], a[164:167], a[236:239], v[140:143]\n"
"s_waitcnt lgkmcnt(2)\n"
"s_add_u32 m0, s59, 122880\n"
"v_mfma_f32_16x16x32_bf16 v[112:115], a[136:139], a[240:243], v[112:115]\n"
"ds_read_b128 a[232:235], v9 offset:40960\n"
"v_add_f32_dpp v58, v58, v58 row_ror:8 row_mask:0xf bank_mask:0xf\n"
"v_mfma_f32_16x16x32_bf16 v[112:115], a[152:155], a[244:247], v[112:115]\n"
"ds_read_b128 a[236:239], v10 offset:40960\n"
"buffer_load_dwordx4 v5, s[4:7], s65 offen lds\n"
"v_mfma_f32_16x16x32_bf16 v[116:119], a[140:143], a[240:243], v[116:119]\n"
"v_add_f32_dpp v59, v59, v59 row_ror:8 row_mask:0xf bank_mask:0xf\n"
"v_add_f32_dpp v58, v58, v58 row_ror:4 row_mask:0xf bank_mask:0xf\n"
"v_mfma_f32_16x16x32_bf16 v[116:119], a[156:159], a[244:247], v[116:119]\n"
"v_add_f32_dpp v59, v59, v59 row_ror:4 row_mask:0xf bank_mask:0xf\n"
"s_add_u32 m0, s59, 126976\n"
"v_mfma_f32_16x16x32_bf16 v[120:123], a[144:147], a[240:243], v[120:123]\n"
"v_add_f32_dpp v58, v58, v58 row_ror:2 row_mask:0xf bank_mask:0xf\n"
"v_add_f32_dpp v59, v59, v59 row_ror:2 row_mask:0xf bank_mask:0xf\n"
"v_mfma_f32_16x16x32_bf16 v[120:123], a[160:163], a[244:247], v[120:123]\n"
"buffer_load_dwordx4 v5, s[4:7], s66 offen lds\n"
"v_add_f32_dpp v58, v58, v58 row_ror:1 row_mask:0xf bank_mask:0xf\n"
"v_mfma_f32_16x16x32_bf16 v[124:127], a[148:151], a[240:243], v[124:127]\n"
"v_add_f32_dpp v59, v59, v59 row_ror:1 row_mask:0xf bank_mask:0xf\n"
"v_mul_f32 v60, 0x3acccccd, v58\n"
"v_mfma_f32_16x16x32_bf16 v[124:127], a[164:167], a[244:247], v[124:127]\n"
"s_waitcnt lgkmcnt(0)\n"
"s_barrier\n"
"v_mfma_f32_16x16x32_bf16 v[96:99], a[136:139], a[248:251], v[96:99]\n"
"ds_read_b128 a[240:243], v7 offset:0\n"
"ds_read_b64_tr_b16 a[200:201], v11 offset:0\n"
"v_mfma_f32_16x16x32_bf16 v[96:99], a[152:155], a[252:255], v[96:99]\n"
"ds_read_b128 a[244:247], v8 offset:0\n"
"ds_read_b64_tr_b16 a[0:1], v57 offset:0\n"
"v_mfma_f32_16x16x32_bf16 v[100:103], a[140:143], a[248:251], v[100:103]\n"
"ds_read_b64_tr_b16 a[202:203], v11 offset:512\n"
"ds_read_b64_tr_b16 a[2:3], v57 offset:10240\n"
"v_mfma_f32_16x16x32_bf16 v[100:103], a[156:159], a[252:255], v[100:103]\n"
"ds_read_b64_tr_b16 a[4:5], v57 offset:32\n"
"ds_read_b64_tr_b16 a[6:7], v57 offset:10272\n"
"v_mfma_f32_16x16x32_bf16 v[104:107], a[144:147], a[248:251], v[104:107]\n"
"ds_read_b64_tr_b16 a[216:217], v12 offset:0\n"
"ds_read_b64_tr_b16 a[8:9], v57 offset:64\n"
"v_mfma_f32_16x16x32_bf16 v[104:107], a[160:163], a[252:255], v[104:107]\n"
"ds_read_b64_tr_b16 a[10:11], v57 offset:10304\n"
"ds_read_b64_tr_b16 a[12:13], v57 offset:96\n"
"v_mfma_f32_16x16x32_bf16 v[108:111], a[148:151], a[248:251], v[108:111]\n"
"ds_read_b64_tr_b16 a[218:219], v12 offset:512\n"
"ds_read_b64_tr_b16 a[14:15], v57 offset:10336\n"
"v_mfma_f32_16x16x32_bf16 v[108:111], a[164:167], a[252:255], v[108:111]\n"
"ds_read_b64_tr_b16 a[16:17], v57 offset:128\n"
"v_mfma_f32_16x16x32_bf16 v[80:83], a[136:139], a[232:235], v[80:83]\n"
"ds_read_b128 a[248:251], v7 offset:4096\n"
"ds_read_b64_tr_b16 a[204:205], v13 offset:0\n"
"v_mfma_f32_16x16x32_bf16 v[80:83], a[152:155], a[236:239], v[80:83]\n"
"ds_read_b128 a[252:255], v8 offset:4096\n"
"ds_read_b64_tr_b16 a[18:19], v57 offset:10368\n"
"v_mfma_f32_16x16x32_bf16 v[84:87], a[140:143], a[232:235], v[84:87]\n"
"ds_read_b64_tr_b16 a[206:207], v13 offset:512\n"
"s_add_u32 m0, s59, 131072\n"
"v_mfma_f32_16x16x32_bf16 v[84:87], a[156:159], a[236:239], v[84:87]\n"
"buffer_load_dwordx4 v5, s[4:7], s67 offen lds\n"
"s_add_u32 m0, s59, 135168\n"
"v_mfma_f32_16x16x32_bf16 v[88:91], a[144:147], a[232:235], v[88:91]\n"
"ds_read_b64_tr_b16 a[220:221], v14 offset:0\n"
"buffer_load_dwordx4 v5, s[4:7], s68 offen lds\n"
"v_mfma_f32_16x16x32_bf16 v[88:91], a[160:163], a[236:239], v[88:91]\n"
"v_mul_f32 v59, 0x3acccccd, v59\n"
"v_fma_f32 v59, -v60, v60, v59\n"
"v_mfma_f32_16x16x32_bf16 v[92:95], a[148:151], a[232:235], v[92:95]\n"
"ds_read_b64_tr_b16 a[222:223], v14 offset:512\n"
"v_max_f32 v59, 0, v59\n"
"v_mfma_f32_16x16x32_bf16 v[92:95], a[164:167], a[236:239], v[92:95]\n"
"v_add_f32 v62, 0x3727c5ac, v59\n"
"s_waitcnt lgkmcnt(15)\n"
"v_mfma_f32_16x16x32_bf16 v[240:243], a[168:171], a[240:243], v[240:243]\n"
"ds_read_b128 a[232:235], v7 offset:8192\n"
"v_mfma_f32_16x16x32_bf16 v[240:243], a[184:187], a[244:247], v[240:243]\n"
"ds_read_b128 a[236:239], v8 offset:8192\n"
"v_mfma_f32_16x16x32_bf16 v[244:247], a[172:175], a[240:243], v[244:247]\n"
"buffer_store_dwordx4 a[0:3], v2, s[24:27], s89 offen offset:0 sc1\n"
"v_mfma_f32_16x16x32_bf16 v[244:247], a[188:191], a[244:247], v[244:247]\n"
"s_add_u32 m0, s59, 139264\n"
"v_mfma_f32_16x16x32_bf16 v[248:251], a[176:179], a[240:243], v[248:251]\n"
"buffer_load_dwordx4 v5, s[4:7], s69 offen lds\n"
"v_mfma_f32_16x16x32_bf16 v[248:251], a[192:195], a[244:247], v[248:251]\n"
"v_rsq_f32 v63, v62\n"
"v_mfma_f32_16x16x32_bf16 v[252:255], a[180:183], a[240:243], v[252:255]\n"
"v_mul_f32 v61, 0xbfb8aa3b, v63\n"
"v_mfma_f32_16x16x32_bf16 v[252:255], a[196:199], a[244:247], v[252:255]\n"
"buffer_store_dwordx4 v[60:63], v32, s[16:19], 0 offen sc1\n"
"s_waitcnt lgkmcnt(6)\n"
"v_mfma_f32_16x16x32_bf16 v[224:227], a[168:171], a[248:251], v[224:227]\n"
"ds_read_b128 a[240:243], v7 offset:12288\n"
"buffer_store_dword v60, v33, s[16:19], 0 offen sc1\n"
"v_mfma_f32_16x16x32_bf16 v[224:227], a[184:187], a[252:255], v[224:227]\n"
"ds_read_b128 a[244:247], v8 offset:12288\n"
"buffer_store_dword v63, v33, s[16:19], 0 offen offset:16 sc1\n"
"v_mfma_f32_16x16x32_bf16 v[228:231], a[172:175], a[248:251], v[228:231]\n"
"buffer_store_dwordx4 a[4:7], v2, s[24:27], s89 offen offset:1024 sc1\n"
"v_mfma_f32_16x16x32_bf16 v[228:231], a[188:191], a[252:255], v[228:231]\n"
"buffer_store_dwordx4 a[8:11], v2, s[24:27], s89 offen offset:2048 sc1\n"
"v_mfma_f32_16x16x32_bf16 v[232:235], a[176:179], a[248:251], v[232:235]\n"
"v_mfma_f32_16x16x32_bf16 v[232:235], a[192:195], a[252:255], v[232:235]\n"
"buffer_store_dwordx4 a[12:15], v2, s[24:27], s89 offen offset:3072 sc1\n"
"v_mfma_f32_16x16x32_bf16 v[236:239], a[180:183], a[248:251], v[236:239]\n"
"v_mfma_f32_16x16x32_bf16 v[236:239], a[196:199], a[252:255], v[236:239]\n"
"s_waitcnt lgkmcnt(2)\n"
"v_mfma_f32_16x16x32_bf16 v[208:211], a[168:171], a[232:235], v[208:211]\n"
"ds_read_b128 a[248:251], v7 offset:16384\n"
"v_mfma_f32_16x16x32_bf16 v[208:211], a[184:187], a[236:239], v[208:211]\n"
"ds_read_b128 a[252:255], v8 offset:16384\n"
"v_mfma_f32_16x16x32_bf16 v[212:215], a[172:175], a[232:235], v[212:215]\n"
"buffer_store_dwordx4 a[16:19], v2, s[24:27], s90 offen offset:0 sc1\n"
"v_mfma_f32_16x16x32_bf16 v[212:215], a[188:191], a[236:239], v[212:215]\n"
"s_cmp_eq_u32 s32, 6\n"
"s_cselect_b32 s85, -9, 1\n"
"v_mfma_f32_16x16x32_bf16 v[216:219], a[176:179], a[232:235], v[216:219]\n"
"s_mul_i32 s33, s85, 221184\n"
"s_add_u32 s75, s75, s33\n"
"v_mfma_f32_16x16x32_bf16 v[216:219], a[192:195], a[236:239], v[216:219]\n"
"s_add_u32 s33, s75, 110592\n"
"s_add_u32 m0, s59, 16384\n"
"v_mfma_f32_16x16x32_bf16 v[220:223], a[180:183], a[232:235], v[220:223]\n"
"buffer_load_dwordx4 v6, s[8:11], s75 offen lds\n"
"s_add_u32 m0, s59, 20480\n"
"v_mfma_f32_16x16x32_bf16 v[220:223], a[196:199], a[236:239], v[220:223]\n"
"buffer_load_dwordx4 v6, s[8:11], s33 offen lds\n"
"s_waitcnt lgkmcnt(2)\n"
"v_mfma_f32_16x16x32_bf16 v[192:195], a[168:171], a[240:243], v[192:195]\n"
"ds_read_b128 a[232:235], v7 offset:20480\n"
"v_mfma_f32_16x16x32_bf16 v[192:195], a[184:187], a[244:247], v[192:195]\n"
"ds_read_b128 a[236:239], v8 offset:20480\n"
"s_add_u32 m0, s59, 143360\n"
"v_mfma_f32_16x16x32_bf16 v[196:199], a[172:175], a[240:243], v[196:199]\n"
"v_mfma_f32_16x16x32_bf16 v[196:199], a[188:191], a[244:247], v[196:199]\n"
"buffer_load_dwordx4 v5, s[4:7], s70 offen lds\n"
"v_mfma_f32_16x16x32_bf16 v[200:203], a[176:179], a[240:243], v[200:203]\n"
"v_mfma_f32_16x16x32_bf16 v[200:203], a[192:195], a[244:247], v[200:203]\n"
"s_add_u32 m0, s59, 147456\n"
"v_mfma_f32_16x16x32_bf16 v[204:207], a[180:183], a[240:243], v[204:207]\n"
"v_mfma_f32_16x16x32_bf16 v[204:207], a[196:199], a[244:247], v[204:207]\n"
"s_waitcnt vmcnt(17) lgkmcnt(0)\n"
"s_barrier\n"
"v_mfma_f32_16x16x32_bf16 v[176:179], a[168:171], a[248:251], v[176:179]\n"
"ds_read_b128 a[240:243], v7 offset:24576\n"
"ds_read_b64_tr_b16 a[208:209], v11 offset:8192\n"
"v_mfma_f32_16x16x32_bf16 v[176:179], a[184:187], a[252:255], v[176:179]\n"
"ds_read_b128 a[244:247], v8 offset:24576\n"
"buffer_load_dwordx4 v5, s[4:7], s71 offen lds\n"
"v_mfma_f32_16x16x32_bf16 v[180:183], a[172:175], a[248:251], v[180:183]\n"
"ds_read_b64_tr_b16 a[210:211], v11 offset:8704\n"
"s_add_u32 m0, s59, 151552\n"
"v_mfma_f32_16x16x32_bf16 v[180:183], a[188:191], a[252:255], v[180:183]\n"
"buffer_load_dwordx4 v5, s[4:7], s72 offen lds\n"
"v_mfma_f32_16x16x32_bf16 v[184:187], a[176:179], a[248:251], v[184:187]\n"
"ds_read_b64_tr_b16 a[224:225], v12 offset:8192\n"
"v_mfma_f32_16x16x32_bf16 v[184:187], a[192:195], a[252:255], v[184:187]\n"
"s_add_u32 m0, s59, 155648\n"
"v_mfma_f32_16x16x32_bf16 v[188:191], a[180:183], a[248:251], v[188:191]\n"
"ds_read_b64_tr_b16 a[226:227], v12 offset:8704\n"
"v_mfma_f32_16x16x32_bf16 v[188:191], a[196:199], a[252:255], v[188:191]\n"
"buffer_load_dwordx4 v5, s[4:7], s73 offen lds\n"
"v_mfma_f32_16x16x32_bf16 v[160:163], a[168:171], a[232:235], v[160:163]\n"
"ds_read_b128 a[248:251], v7 offset:28672\n"
"ds_read_b64_tr_b16 a[212:213], v13 offset:8192\n"
"v_mfma_f32_16x16x32_bf16 v[160:163], a[184:187], a[236:239], v[160:163]\n"
"ds_read_b128 a[252:255], v8 offset:28672\n"
"s_add_u32 m0, s59, 159744\n"
"v_mfma_f32_16x16x32_bf16 v[164:167], a[172:175], a[232:235], v[164:167]\n"
"ds_read_b64_tr_b16 a[214:215], v13 offset:8704\n"
"s_lshr_b32 s62, s77, 8\n"
"v_mfma_f32_16x16x32_bf16 v[164:167], a[188:191], a[236:239], v[164:167]\n"
"buffer_load_dwordx4 v5, s[4:7], s74 offen lds\n"
"s_mul_i32 s61, s3, 5\n"
"v_mfma_f32_16x16x32_bf16 v[168:171], a[176:179], a[232:235], v[168:171]\n"
"ds_read_b64_tr_b16 a[228:229], v14 offset:8192\n"
"s_add_u32 s61, s61, 46\n"
"v_mfma_f32_16x16x32_bf16 v[168:171], a[192:195], a[236:239], v[168:171]\n"
"v_min_u32 v36, 12, v1\n"
"s_add_u32 m0, s59, 24576\n"
"v_mfma_f32_16x16x32_bf16 v[172:175], a[180:183], a[232:235], v[172:175]\n"
"ds_read_b64_tr_b16 a[230:231], v14 offset:8704\n"
"v_cmp_gt_u32 vcc, 8, v36\n"
"v_mfma_f32_16x16x32_bf16 v[172:175], a[196:199], a[236:239], v[172:175]\n"
"buffer_load_dwordx4 v6, s[12:15], s75 offen lds\n"
"s_waitcnt lgkmcnt(9)\n"
"v_mfma_f32_16x16x32_bf16 v[144:147], a[168:171], a[240:243], v[144:147]\n"
"ds_read_b128 a[232:235], v7 offset:32768\n"
"v_add_u32 v37, s61, v36\n"
"v_mfma_f32_16x16x32_bf16 v[144:147], a[184:187], a[244:247], v[144:147]\n"
"ds_read_b128 a[236:239], v8 offset:32768\n"
"s_add_u32 m0, s59, 28672\n"
"v_mfma_f32_16x16x32_bf16 v[148:151], a[172:175], a[240:243], v[148:151]\n"
"v_add_u32 v36, s78, v36\n"
"s_lshr_b32 s60, s56, 2\n"
"v_mfma_f32_16x16x32_bf16 v[148:151], a[188:191], a[244:247], v[148:151]\n"
"buffer_load_dwordx4 v6, s[12:15], s33 offen lds\n"
"v_cndmask_b32 v36, v37, v36, vcc\n"
"v_mfma_f32_16x16x32_bf16 v[152:155], a[176:179], a[240:243], v[152:155]\n"
"s_cmp_eq_u32 s32, 6\n"
"s_cselect_b32 s85, -9, 1\n"
"v_mfma_f32_16x16x32_bf16 v[152:155], a[192:195], a[244:247], v[152:155]\n"
"v_lshl_add_u32 v5, s85, 7, v5\n"
"s_add_u32 m0, s59, 73728\n"
"v_mfma_f32_16x16x32_bf16 v[156:159], a[180:183], a[240:243], v[156:159]\n"
"v_lshl_add_u32 v36, v36, 1, s60\n"
"v_and_b32 v37, 15, v1\n"
"v_mfma_f32_16x16x32_bf16 v[156:159], a[196:199], a[244:247], v[156:159]\n"
"buffer_load_dwordx4 v5, s[4:7], s64 offen lds\n"
"s_waitcnt lgkmcnt(5)\n"
"v_mfma_f32_16x16x32_bf16 v[128:131], a[168:171], a[248:251], v[128:131]\n"
"ds_read_b128 a[240:243], v7 offset:36864\n"
"v_min_u32 v37, 10, v37\n"
"v_mfma_f32_16x16x32_bf16 v[128:131], a[184:187], a[252:255], v[128:131]\n"
"ds_read_b128 a[244:247], v8 offset:36864\n"
"s_add_u32 m0, s59, 77824\n"
"v_mfma_f32_16x16x32_bf16 v[132:135], a[172:175], a[248:251], v[132:135]\n"
"v_add_u32 v37, s62, v37\n"
"v_cmp_gt_u32 vcc, 32, v1\n"
"v_mfma_f32_16x16x32_bf16 v[132:135], a[188:191], a[252:255], v[132:135]\n"
"buffer_load_dwordx4 v5, s[4:7], s65 offen lds\n"
"v_min_u32 v37, 127, v37\n"
"v_mfma_f32_16x16x32_bf16 v[136:139], a[176:179], a[248:251], v[136:139]\n"
"s_nop 0\n"
"v_mfma_f32_16x16x32_bf16 v[136:139], a[192:195], a[252:255], v[136:139]\n"
"v_cndmask_b32 v36, v37, v36, vcc\n"
"s_add_u32 m0, s59, 81920\n"
"v_mfma_f32_16x16x32_bf16 v[140:143], a[180:183], a[248:251], v[140:143]\n"
"v_and_b32 v37, 15, v1\n"
"v_cmp_gt_u32 vcc, 48, v1\n"
"v_mfma_f32_16x16x32_bf16 v[140:143], a[196:199], a[252:255], v[140:143]\n"
"buffer_load_dwordx4 v5, s[4:7], s66 offen lds\n"
"s_waitcnt lgkmcnt(2)\n"
"v_mfma_f32_16x16x32_bf16 v[112:115], a[168:171], a[232:235], v[112:115]\n"
"ds_read_b128 a[248:251], v7 offset:40960\n"
"v_min_u32 v37, 5, v37\n"
"v_mfma_f32_16x16x32_bf16 v[112:115], a[184:187], a[236:239], v[112:115]\n"
"ds_read_b128 a[252:255], v8 offset:40960\n"
"s_add_u32 m0, s59, 86016\n"
"v_mfma_f32_16x16x32_bf16 v[116:119], a[172:175], a[232:235], v[116:119]\n"
"v_add_u32 v37, s95, v37\n"
"v_cndmask_b32 v36, v37, v36, vcc\n"
"v_mfma_f32_16x16x32_bf16 v[116:119], a[188:191], a[236:239], v[116:119]\n"
"buffer_load_dwordx4 v5, s[4:7], s67 offen lds\n"
"v_lshlrev_b32 v36, 7, v36\n"
"v_mfma_f32_16x16x32_bf16 v[120:123], a[176:179], a[232:235], v[120:123]\n"
"v_add_u32 v36, 212992, v36\n"
"v_and_b32 v15, 15, v1\n"
"v_mfma_f32_16x16x32_bf16 v[120:123], a[192:195], a[236:239], v[120:123]\n"
"v_lshlrev_b32 v15, 4, v15\n"
"s_add_u32 m0, s59, 90112\n"
"v_mfma_f32_16x16x32_bf16 v[124:127], a[180:183], a[232:235], v[124:127]\n"
"s_lshr_b32 s60, s3, 1\n"
"s_mul_i32 s60, s60, 3072\n"
"v_mfma_f32_16x16x32_bf16 v[124:127], a[196:199], a[236:239], v[124:127]\n"
"s_waitcnt vmcnt(11) lgkmcnt(0)\n"
"s_barrier\n"
"v_mfma_f32_16x16x32_bf16 v[96:99], a[168:171], a[240:243], v[96:99]\n"
"ds_read_b128 a[232:235], v9 offset:0\n"
"ds_read_b64_tr_b16 a[104:105], v11 offset:16384\n"
"v_mfma_f32_16x16x32_bf16 v[96:99], a[184:187], a[244:247], v[96:99]\n"
"ds_read_b128 a[236:239], v10 offset:0\n"
"buffer_load_dwordx4 v5, s[4:7], s68 offen lds\n"
"v_mfma_f32_16x16x32_bf16 v[100:103], a[172:175], a[240:243], v[100:103]\n"
"ds_read_b64_tr_b16 a[106:107], v11 offset:16896\n"
"v_mov_b32 v56, s52\n"
"v_mfma_f32_16x16x32_bf16 v[100:103], a[188:191], a[244:247], v[100:103]\n"
"buffer_store_dword v56, v3, s[16:19], s42 offen sc1\n"
"v_mfma_f32_16x16x32_bf16 v[104:107], a[176:179], a[240:243], v[104:107]\n"
"ds_read_b64_tr_b16 a[120:121], v12 offset:16384\n"
"s_add_u32 m0, s59, 94208\n"
"v_mfma_f32_16x16x32_bf16 v[104:107], a[192:195], a[244:247], v[104:107]\n"
"buffer_load_dwordx4 v5, s[4:7], s69 offen lds\n"
"s_add_u32 s60, s60, 65536\n"
"v_mfma_f32_16x16x32_bf16 v[108:111], a[180:183], a[240:243], v[108:111]\n"
"ds_read_b64_tr_b16 a[122:123], v12 offset:16896\n"
"v_add_u32 v57, s60, v15\n"
"v_mfma_f32_16x16x32_bf16 v[108:111], a[196:199], a[244:247], v[108:111]\n"
"s_and_b32 s61, s59, 1024\n"
"v_mfma_f32_16x16x32_bf16 v[80:83], a[168:171], a[248:251], v[80:83]\n"
"ds_read_b128 a[240:243], v9 offset:4096\n"
"ds_read_b64_tr_b16 a[108:109], v13 offset:16384\n"
"v_mfma_f32_16x16x32_bf16 v[80:83], a[184:187], a[252:255], v[80:83]\n"
"ds_read_b128 a[244:247], v10 offset:4096\n"
"s_add_u32 s63, s60, s61\n"
"v_mfma_f32_16x16x32_bf16 v[84:87], a[172:175], a[248:251], v[84:87]\n"
"ds_read_b64_tr_b16 a[110:111], v13 offset:16896\n"
"s_add_u32 s77, s77, s61\n"
"v_mfma_f32_16x16x32_bf16 v[84:87], a[188:191], a[252:255], v[84:87]\n"
"s_cmp_eq_u32 s32, 5\n"
"s_cselect_b32 s85, -9, 1\n"
"v_mfma_f32_16x16x32_bf16 v[88:91], a[176:179], a[248:251], v[88:91]\n"
"ds_read_b64_tr_b16 a[124:125], v14 offset:16384\n"
"s_mul_i32 s33, s85, 221184\n"
"v_mfma_f32_16x16x32_bf16 v[88:91], a[192:195], a[252:255], v[88:91]\n"
"s_add_u32 s75, s75, s33\n"
"s_add_u32 s33, s75, 110592\n"
"v_mfma_f32_16x16x32_bf16 v[92:95], a[180:183], a[248:251], v[92:95]\n"
"ds_read_b64_tr_b16 a[126:127], v14 offset:16896\n"
"s_add_u32 m0, s59, 0\n"
"v_mfma_f32_16x16x32_bf16 v[92:95], a[196:199], a[252:255], v[92:95]\n"
"buffer_load_dwordx4 v6, s[8:11], s75 offen lds\n"
"s_waitcnt lgkmcnt(9)\n"
"v_mfma_f32_16x16x32_bf16 v[240:243], a[200:203], a[232:235], v[240:243]\n"
"ds_read_b128 a[248:251], v9 offset:8192\n"
"s_add_u32 m0, s59, 4096\n"
"v_mfma_f32_16x16x32_bf16 v[240:243], a[216:219], a[236:239], v[240:243]\n"
"ds_read_b128 a[252:255], v10 offset:8192\n"
"buffer_load_dwordx4 v6, s[8:11], s33 offen lds\n"
"v_mfma_f32_16x16x32_bf16 v[244:247], a[204:207], a[232:235], v[244:247]\n"
"s_add_u32 m0, s59, 32768\n"
"v_mfma_f32_16x16x32_bf16 v[244:247], a[220:223], a[236:239], v[244:247]\n"
"v_mfma_f32_16x16x32_bf16 v[248:251], a[208:211], a[232:235], v[248:251]\n"
"buffer_load_dwordx4 v6, s[12:15], s75 offen lds\n"
"v_mfma_f32_16x16x32_bf16 v[248:251], a[224:227], a[236:239], v[248:251]\n"
"v_mfma_f32_16x16x32_bf16 v[252:255], a[212:215], a[232:235], v[252:255]\n"
"s_add_u32 m0, s59, 36864\n"
"v_mfma_f32_16x16x32_bf16 v[252:255], a[228:231], a[236:239], v[252:255]\n"
"s_waitcnt lgkmcnt(5)\n"
"v_mfma_f32_16x16x32_bf16 v[224:227], a[200:203], a[240:243], v[224:227]\n"
"ds_read_b128 a[232:235], v9 offset:12288\n"
"buffer_load_dwordx4 v6, s[12:15], s33 offen lds\n"
"v_mfma_f32_16x16x32_bf16 v[224:227], a[216:219], a[244:247], v[224:227]\n"
"ds_read_b128 a[236:239], v10 offset:12288\n"
"v_mfma_f32_16x16x32_bf16 v[228:231], a[204:207], a[240:243], v[228:231]\n"
"s_add_u32 m0, s59, 98304\n"
"v_mfma_f32_16x16x32_bf16 v[228:231], a[220:223], a[244:247], v[228:231]\n"
"v_mfma_f32_16x16x32_bf16 v[232:235], a[208:211], a[240:243], v[232:235]\n"
"buffer_load_dwordx4 v5, s[4:7], s70 offen lds\n"
"v_mfma_f32_16x16x32_bf16 v[232:235], a[224:227], a[244:247], v[232:235]\n"
"v_mfma_f32_16x16x32_bf16 v[236:239], a[212:215], a[240:243], v[236:239]\n"
"s_add_u32 m0, s59, 102400\n"
"v_mfma_f32_16x16x32_bf16 v[236:239], a[228:231], a[244:247], v[236:239]\n"
"s_waitcnt lgkmcnt(2)\n"
"v_mfma_f32_16x16x32_bf16 v[208:211], a[200:203], a[248:251], v[208:211]\n"
"ds_read_b128 a[240:243], v9 offset:16384\n"
"buffer_load_dwordx4 v5, s[4:7], s71 offen lds\n"
"v_mfma_f32_16x16x32_bf16 v[208:211], a[216:219], a[252:255], v[208:211]\n"
"ds_read_b128 a[244:247], v10 offset:16384\n"
"v_mfma_f32_16x16x32_bf16 v[212:215], a[204:207], a[248:251], v[212:215]\n"
"s_add_u32 m0, s59, 106496\n"
"v_mfma_f32_16x16x32_bf16 v[212:215], a[220:223], a[252:255], v[212:215]\n"
"v_mfma_f32_16x16x32_bf16 v[216:219], a[208:211], a[248:251], v[216:219]\n"
"buffer_load_dwordx4 v5, s[4:7], s72 offen lds\n"
"v_mfma_f32_16x16x32_bf16 v[216:219], a[224:227], a[252:255], v[216:219]\n"
"v_mfma_f32_16x16x32_bf16 v[220:223], a[212:215], a[248:251], v[220:223]\n"
"s_add_u32 m0, s59, 110592\n"
"v_mfma_f32_16x16x32_bf16 v[220:223], a[228:231], a[252:255], v[220:223]\n"
"s_waitcnt lgkmcnt(2)\n"
"v_mfma_f32_16x16x32_bf16 v[192:195], a[200:203], a[232:235], v[192:195]\n"
"ds_read_b128 a[248:251], v9 offset:20480\n"
"buffer_load_dwordx4 v5, s[4:7], s73 offen lds\n"
"v_mfma_f32_16x16x32_bf16 v[192:195], a[216:219], a[236:239], v[192:195]\n"
"ds_read_b128 a[252:255], v10 offset:20480\n"
"v_mfma_f32_16x16x32_bf16 v[196:199], a[204:207], a[232:235], v[196:199]\n"
"s_add_u32 m0, s59, 114688\n"
"v_mfma_f32_16x16x32_bf16 v[196:199], a[220:223], a[236:239], v[196:199]\n"
"v_mfma_f32_16x16x32_bf16 v[200:203], a[208:211], a[232:235], v[200:203]\n"
"buffer_load_dwordx4 v5, s[4:7], s74 offen lds\n"
"v_mfma_f32_16x16x32_bf16 v[200:203], a[224:227], a[236:239], v[200:203]\n"
"s_cmp_eq_u32 s32, 4\n"
"s_cselect_b32 s85, -9, 1\n"
"v_mfma_f32_16x16x32_bf16 v[204:207], a[212:215], a[232:235], v[204:207]\n"
"s_mul_i32 s33, s85, 221184\n"
"s_add_u32 s75, s75, s33\n"
"v_mfma_f32_16x16x32_bf16 v[204:207], a[228:231], a[236:239], v[204:207]\n"
"s_waitcnt vmcnt(16) lgkmcnt(0)\n"
"s_barrier\n"
"v_mfma_f32_16x16x32_bf16 v[176:179], a[200:203], a[240:243], v[176:179]\n"
"ds_read_b128 a[232:235], v9 offset:24576\n"
"ds_read_b64_tr_b16 a[112:113], v11 offset:24576\n"
"v_mfma_f32_16x16x32_bf16 v[176:179], a[216:219], a[244:247], v[176:179]\n"
"ds_read_b128 a[236:239], v10 offset:24576\n"
"s_add_u32 s33, s75, 110592\n"
"v_mfma_f32_16x16x32_bf16 v[180:183], a[204:207], a[240:243], v[180:183]\n"
"ds_read_b64_tr_b16 a[114:115], v11 offset:25088\n"
"s_add_u32 m0, s59, 8192\n"
"v_mfma_f32_16x16x32_bf16 v[180:183], a[220:223], a[244:247], v[180:183]\n"
"buffer_load_dwordx4 v6, s[8:11], s75 offen lds\n"
"s_add_u32 m0, s59, 12288\n"
"v_mfma_f32_16x16x32_bf16 v[184:187], a[208:211], a[240:243], v[184:187]\n"
"ds_read_b64_tr_b16 a[128:129], v12 offset:24576\n"
"buffer_load_dwordx4 v6, s[8:11], s33 offen lds\n"
"v_mfma_f32_16x16x32_bf16 v[184:187], a[224:227], a[244:247], v[184:187]\n"
"v_mfma_f32_16x16x32_bf16 v[188:191], a[212:215], a[240:243], v[188:191]\n"
"ds_read_b64_tr_b16 a[130:131], v12 offset:25088\n"
"s_add_u32 m0, s59, 40960\n"
"v_mfma_f32_16x16x32_bf16 v[188:191], a[228:231], a[244:247], v[188:191]\n"
"v_mfma_f32_16x16x32_bf16 v[160:163], a[200:203], a[248:251], v[160:163]\n"
"ds_read_b128 a[240:243], v9 offset:28672\n"
"ds_read_b64_tr_b16 a[116:117], v13 offset:24576\n"
"v_mfma_f32_16x16x32_bf16 v[160:163], a[216:219], a[252:255], v[160:163]\n"
"ds_read_b128 a[244:247], v10 offset:28672\n"
"buffer_load_dwordx4 v6, s[12:15], s75 offen lds\n"
"v_mfma_f32_16x16x32_bf16 v[164:167], a[204:207], a[248:251], v[164:167]\n"
"ds_read_b64_tr_b16 a[118:119], v13 offset:25088\n"
"s_add_u32 m0, s59, 45056\n"
"v_mfma_f32_16x16x32_bf16 v[164:167], a[220:223], a[252:255], v[164:167]\n"
"v_mfma_f32_16x16x32_bf16 v[168:171], a[208:211], a[248:251], v[168:171]\n"
"ds_read_b64_tr_b16 a[132:133], v14 offset:24576\n"
"buffer_load_dwordx4 v6, s[12:15], s33 offen lds\n"
"v_mfma_f32_16x16x32_bf16 v[168:171], a[224:227], a[252:255], v[168:171]\n"
"s_cmp_eq_u32 s32, 5\n"
"s_cselect_b32 s85, -9, 1\n"
"v_mfma_f32_16x16x32_bf16 v[172:175], a[212:215], a[248:251], v[172:175]\n"
"ds_read_b64_tr_b16 a[134:135], v14 offset:25088\n"
"v_lshl_add_u32 v5, s85, 7, v5\n"
"v_mfma_f32_16x16x32_bf16 v[172:175], a[228:231], a[252:255], v[172:175]\n"
"s_waitcnt lgkmcnt(9)\n"
"s_add_u32 m0, s59, 118784\n"
"v_mfma_f32_16x16x32_bf16 v[144:147], a[200:203], a[232:235], v[144:147]\n"
"ds_read_b128 a[248:251], v9 offset:32768\n"
"buffer_load_dwordx4 v5, s[4:7], s64 offen lds\n"
"v_mfma_f32_16x16x32_bf16 v[144:147], a[216:219], a[236:239], v[144:147]\n"
"ds_read_b128 a[252:255], v10 offset:32768\n"
"v_mfma_f32_16x16x32_bf16 v[148:151], a[204:207], a[232:235], v[148:151]\n"
"s_add_u32 m0, s59, 122880\n"
"v_mfma_f32_16x16x32_bf16 v[148:151], a[220:223], a[236:239], v[148:151]\n"
"v_mfma_f32_16x16x32_bf16 v[152:155], a[208:211], a[232:235], v[152:155]\n"
"buffer_load_dwordx4 v5, s[4:7], s65 offen lds\n"
"v_mfma_f32_16x16x32_bf16 v[152:155], a[224:227], a[236:239], v[152:155]\n"
"v_mfma_f32_16x16x32_bf16 v[156:159], a[212:215], a[232:235], v[156:159]\n"
"s_add_u32 m0, s59, 126976\n"
"v_mfma_f32_16x16x32_bf16 v[156:159], a[228:231], a[236:239], v[156:159]\n"
"s_waitcnt lgkmcnt(5)\n"
"v_mfma_f32_16x16x32_bf16 v[128:131], a[200:203], a[240:243], v[128:131]\n"
"ds_read_b128 a[232:235], v9 offset:36864\n"
"buffer_load_dwordx4 v5, s[4:7], s66 offen lds\n"
"v_mfma_f32_16x16x32_bf16 v[128:131], a[216:219], a[244:247], v[128:131]\n"
"ds_read_b128 a[236:239], v10 offset:36864\n"
"v_mfma_f32_16x16x32_bf16 v[132:135], a[204:207], a[240:243], v[132:135]\n"
"s_add_u32 m0, s59, 131072\n"
"v_mfma_f32_16x16x32_bf16 v[132:135], a[220:223], a[244:247], v[132:135]\n"
"v_mfma_f32_16x16x32_bf16 v[136:139], a[208:211], a[240:243], v[136:139]\n"
"buffer_load_dwordx4 v5, s[4:7], s67 offen lds\n"
"v_mfma_f32_16x16x32_bf16 v[136:139], a[224:227], a[244:247], v[136:139]\n"
"v_mfma_f32_16x16x32_bf16 v[140:143], a[212:215], a[240:243], v[140:143]\n"
"s_add_u32 m0, s59, 135168\n"
"v_mfma_f32_16x16x32_bf16 v[140:143], a[228:231], a[244:247], v[140:143]\n"
"s_waitcnt lgkmcnt(2)\n"
"v_mfma_f32_16x16x32_bf16 v[112:115], a[200:203], a[248:251], v[112:115]\n"
"ds_read_b128 a[240:243], v9 offset:40960\n"
"buffer_load_dwordx4 v5, s[4:7], s68 offen lds\n"
"v_mfma_f32_16x16x32_bf16 v[112:115], a[216:219], a[252:255], v[112:115]\n"
"ds_read_b128 a[244:247], v10 offset:40960\n"
"v_mfma_f32_16x16x32_bf16 v[116:119], a[204:207], a[248:251], v[116:119]\n"
"s_add_u32 m0, s59, 139264\n"
"v_mfma_f32_16x16x32_bf16 v[116:119], a[220:223], a[252:255], v[116:119]\n"
"v_mfma_f32_16x16x32_bf16 v[120:123], a[208:211], a[248:251], v[120:123]\n"
"buffer_load_dwordx4 v5, s[4:7], s69 offen lds\n"
"v_mfma_f32_16x16x32_bf16 v[120:123], a[224:227], a[252:255], v[120:123]\n"
"v_mfma_f32_16x16x32_bf16 v[124:127], a[212:215], a[248:251], v[124:127]\n"
"v_mfma_f32_16x16x32_bf16 v[124:127], a[228:231], a[252:255], v[124:127]\n"
"s_waitcnt vmcnt(15) lgkmcnt(0)\n"
"s_barrier\n"
".Lfb_ffnh:\n"
"v_mfma_f32_16x16x32_bf16 v[96:99], a[200:203], a[232:235], v[96:99]\n"
"ds_read_b128 a[248:251], v7 offset:0\n"
"ds_read_b64_tr_b16 a[136:137], v11 offset:0\n"
"v_mfma_f32_16x16x32_bf16 v[96:99], a[216:219], a[236:239], v[96:99]\n"
"ds_read_b128 a[252:255], v8 offset:0\n"
"s_lshr_b32 s34, s2, 3\n"
"v_mfma_f32_16x16x32_bf16 v[100:103], a[204:207], a[232:235], v[100:103]\n"
"ds_read_b64_tr_b16 a[138:139], v11 offset:512\n"
"s_sub_u32 s34, s34, 11\n"
"v_mfma_f32_16x16x32_bf16 v[100:103], a[220:223], a[236:239], v[100:103]\n"
"s_lshl_b32 s34, s34, 3\n"
"v_mfma_f32_16x16x32_bf16 v[104:107], a[208:211], a[232:235], v[104:107]\n"
"ds_read_b64_tr_b16 a[152:153], v12 offset:0\n"
"v_lshrrev_b32 v38, 5, v0\n"
"v_mfma_f32_16x16x32_bf16 v[104:107], a[224:227], a[236:239], v[104:107]\n"
"v_add_lshl_u32 v38, v38, s34, 7\n"
"v_add_u32 v38, .Lfb_ffne-.Lfb_pc, v38\n"
"v_mfma_f32_16x16x32_bf16 v[108:111], a[212:215], a[232:235], v[108:111]\n"
"ds_read_b64_tr_b16 a[154:155], v12 offset:512\n"
"v_min_u32 v38, .Lfb_end-.Lfb_pc-4, v38\n"
"v_mfma_f32_16x16x32_bf16 v[108:111], a[228:231], a[236:239], v[108:111]\n"
"global_load_dword v39, v38, s[86:87]\n"
"v_mfma_f32_16x16x32_bf16 v[80:83], a[200:203], a[240:243], v[80:83]\n"
"ds_read_b128 a[232:235], v7 offset:4096\n"
"ds_read_b64_tr_b16 a[140:141], v13 offset:0\n"
"v_mfma_f32_16x16x32_bf16 v[80:83], a[216:219], a[244:247], v[80:83]\n"
"ds_read_b128 a[236:239], v8 offset:4096\n"
"v_mfma_f32_16x16x32_bf16 v[84:87], a[204:207], a[240:243], v[84:87]\n"
"ds_read_b64_tr_b16 a[142:143], v13 offset:512\n"
"v_mfma_f32_16x16x32_bf16 v[84:87], a[220:223], a[244:247], v[84:87]\n"
"s_cmp_eq_u32 s32, 3\n"
"s_cselect_b32 s85, -9, 1\n"
"v_mfma_f32_16x16x32_bf16 v[88:91], a[208:211], a[240:243], v[88:91]\n"
"ds_read_b64_tr_b16 a[156:157], v14 offset:0\n"
"s_mul_i32 s33, s85, 221184\n"
"v_mfma_f32_16x16x32_bf16 v[88:91], a[224:227], a[244:247], v[88:91]\n"
"s_add_u32 s75, s75, s33\n"
"s_add_u32 s33, s75, 110592\n"
"v_mfma_f32_16x16x32_bf16 v[92:95], a[212:215], a[240:243], v[92:95]\n"
"ds_read_b64_tr_b16 a[158:159], v14 offset:512\n"
"s_add_u32 m0, s59, 16384\n"
"v_mfma_f32_16x16x32_bf16 v[92:95], a[228:231], a[244:247], v[92:95]\n"
"ds_read_b64_tr_b16 a[144:145], v11 offset:32768\n"
"s_waitcnt lgkmcnt(10)\n"
"v_mfma_f32_16x16x32_bf16 v[240:243], a[104:107], a[248:251], v[240:243]\n"
"ds_read_b128 a[240:243], v7 offset:8192\n"
"buffer_load_dwordx4 v6, s[8:11], s75 offen lds\n"
"v_mfma_f32_16x16x32_bf16 v[240:243], a[120:123], a[252:255], v[240:243]\n"
"ds_read_b128 a[244:247], v8 offset:8192\n"
"ds_read_b64_tr_b16 a[146:147], v11 offset:33280\n"
"v_mfma_f32_16x16x32_bf16 v[244:247], a[108:111], a[248:251], v[244:247]\n"
"s_add_u32 m0, s59, 20480\n"
"v_mfma_f32_16x16x32_bf16 v[244:247], a[124:127], a[252:255], v[244:247]\n"
"ds_read_b64_tr_b16 a[160:161], v12 offset:32768\n"
"buffer_load_dwordx4 v6, s[8:11], s33 offen lds\n"
"v_mfma_f32_16x16x32_bf16 v[248:251], a[112:115], a[248:251], v[248:251]\n"
"s_add_u32 m0, s59, 49152\n"
"v_mfma_f32_16x16x32_bf16 v[248:251], a[128:131], a[252:255], v[248:251]\n"
"ds_read_b64_tr_b16 a[162:163], v12 offset:33280\n"
"buffer_load_dwordx4 v6, s[12:15], s75 offen lds\n"
"v_mfma_f32_16x16x32_bf16 v[252:255], a[116:119], a[248:251], v[252:255]\n"
"v_mfma_f32_16x16x32_bf16 v[252:255], a[132:135], a[252:255], v[252:255]\n"
"ds_read_b64_tr_b16 a[148:149], v13 offset:32768\n"
"s_waitcnt lgkmcnt(10)\n"
"v_mfma_f32_16x16x32_bf16 v[224:227], a[104:107], a[232:235], v[224:227]\n"
"ds_read_b128 a[248:251], v7 offset:12288\n"
"s_add_u32 m0, s59, 53248\n"
"v_mfma_f32_16x16x32_bf16 v[224:227], a[120:123], a[236:239], v[224:227]\n"
"ds_read_b128 a[252:255], v8 offset:12288\n"
"ds_read_b64_tr_b16 a[150:151], v13 offset:33280\n"
"v_mfma_f32_16x16x32_bf16 v[228:231], a[108:111], a[232:235], v[228:231]\n"
"buffer_load_dwordx4 v6, s[12:15], s33 offen lds\n"
"v_mfma_f32_16x16x32_bf16 v[228:231], a[124:127], a[236:239], v[228:231]\n"
"ds_read_b64_tr_b16 a[164:165], v14 offset:32768\n"
"s_add_u32 m0, s59, 143360\n"
"v_mfma_f32_16x16x32_bf16 v[232:235], a[112:115], a[232:235], v[232:235]\n"
"v_mfma_f32_16x16x32_bf16 v[232:235], a[128:131], a[236:239], v[232:235]\n"
"ds_read_b64_tr_b16 a[166:167], v14 offset:33280\n"
"buffer_load_dwordx4 v5, s[4:7], s70 offen lds\n"
"v_mfma_f32_16x16x32_bf16 v[236:239], a[116:119], a[232:235], v[236:239]\n"
"v_mfma_f32_16x16x32_bf16 v[236:239], a[132:135], a[236:239], v[236:239]\n"
"s_waitcnt lgkmcnt(9)\n"
"v_mfma_f32_16x16x32_bf16 v[208:211], a[104:107], a[240:243], v[208:211]\n"
"ds_read_b128 a[232:235], v7 offset:16384\n"
"v_mfma_f32_16x16x32_bf16 v[208:211], a[120:123], a[244:247], v[208:211]\n"
"ds_read_b128 a[236:239], v8 offset:16384\n"
"buffer_load_dword v37, v36, s[16:19], 0 offen sc1\n"
"v_mfma_f32_16x16x32_bf16 v[212:215], a[108:111], a[240:243], v[212:215]\n"
"v_mfma_f32_16x16x32_bf16 v[212:215], a[124:127], a[244:247], v[212:215]\n"
"v_mfma_f32_16x16x32_bf16 v[216:219], a[112:115], a[240:243], v[216:219]\n"
"v_mfma_f32_16x16x32_bf16 v[216:219], a[128:131], a[244:247], v[216:219]\n"
"v_mfma_f32_16x16x32_bf16 v[220:223], a[116:119], a[240:243], v[220:223]\n"
"v_mfma_f32_16x16x32_bf16 v[220:223], a[132:135], a[244:247], v[220:223]\n"
"s_waitcnt lgkmcnt(5)\n"
"v_mfma_f32_16x16x32_bf16 v[192:195], a[104:107], a[248:251], v[192:195]\n"
"ds_read_b128 a[240:243], v7 offset:20480\n"
"v_mfma_f32_16x16x32_bf16 v[192:195], a[120:123], a[252:255], v[192:195]\n"
"ds_read_b128 a[244:247], v8 offset:20480\n"
"v_mfma_f32_16x16x32_bf16 v[196:199], a[108:111], a[248:251], v[196:199]\n"
"v_mfma_f32_16x16x32_bf16 v[196:199], a[124:127], a[252:255], v[196:199]\n"
"s_add_u32 m0, s59, 147456\n"
"v_mfma_f32_16x16x32_bf16 v[200:203], a[112:115], a[248:251], v[200:203]\n"
"v_mfma_f32_16x16x32_bf16 v[200:203], a[128:131], a[252:255], v[200:203]\n"
"buffer_load_dwordx4 v5, s[4:7], s71 offen lds\n"
"v_mfma_f32_16x16x32_bf16 v[204:207], a[116:119], a[248:251], v[204:207]\n"
"v_mfma_f32_16x16x32_bf16 v[204:207], a[132:135], a[252:255], v[204:207]\n"
"s_waitcnt vmcnt(14) lgkmcnt(0)\n"
"s_barrier\n"
"v_mfma_f32_16x16x32_bf16 v[176:179], a[104:107], a[232:235], v[176:179]\n"
"ds_read_b128 a[248:251], v7 offset:24576\n"
"ds_read_b64_tr_b16 a[168:169], v11 offset:8192\n"
"v_mfma_f32_16x16x32_bf16 v[176:179], a[120:123], a[236:239], v[176:179]\n"
"ds_read_b128 a[252:255], v8 offset:24576\n"
"s_add_u32 m0, s59, 151552\n"
"v_mfma_f32_16x16x32_bf16 v[180:183], a[108:111], a[232:235], v[180:183]\n"
"ds_read_b64_tr_b16 a[170:171], v11 offset:8704\n"
"buffer_load_dwordx4 v5, s[4:7], s72 offen lds\n"
"v_mfma_f32_16x16x32_bf16 v[180:183], a[124:127], a[236:239], v[180:183]\n"
"s_add_u32 m0, s59, 155648\n"
"v_mfma_f32_16x16x32_bf16 v[184:187], a[112:115], a[232:235], v[184:187]\n"
"ds_read_b64_tr_b16 a[184:185], v12 offset:8192\n"
"v_mfma_f32_16x16x32_bf16 v[184:187], a[128:131], a[236:239], v[184:187]\n"
"buffer_load_dwordx4 v5, s[4:7], s73 offen lds\n"
"v_mfma_f32_16x16x32_bf16 v[188:191], a[116:119], a[232:235], v[188:191]\n"
"ds_read_b64_tr_b16 a[186:187], v12 offset:8704\n"
"v_mfma_f32_16x16x32_bf16 v[188:191], a[132:135], a[236:239], v[188:191]\n"
"s_add_u32 m0, s59, 159744\n"
"v_mfma_f32_16x16x32_bf16 v[160:163], a[104:107], a[240:243], v[160:163]\n"
"ds_read_b128 a[232:235], v7 offset:28672\n"
"ds_read_b64_tr_b16 a[172:173], v13 offset:8192\n"
"v_mfma_f32_16x16x32_bf16 v[160:163], a[120:123], a[244:247], v[160:163]\n"
"ds_read_b128 a[236:239], v8 offset:28672\n"
"buffer_load_dwordx4 v5, s[4:7], s74 offen lds\n"
"v_mfma_f32_16x16x32_bf16 v[164:167], a[108:111], a[240:243], v[164:167]\n"
"ds_read_b64_tr_b16 a[174:175], v13 offset:8704\n"
"v_mfma_f32_16x16x32_bf16 v[164:167], a[124:127], a[244:247], v[164:167]\n"
"s_cmp_eq_u32 s32, 2\n"
"s_cselect_b32 s85, -9, 1\n"
"v_mfma_f32_16x16x32_bf16 v[168:171], a[112:115], a[240:243], v[168:171]\n"
"ds_read_b64_tr_b16 a[188:189], v14 offset:8192\n"
"s_mul_i32 s33, s85, 221184\n"
"v_mfma_f32_16x16x32_bf16 v[168:171], a[128:131], a[244:247], v[168:171]\n"
"s_add_u32 s75, s75, s33\n"
"s_add_u32 s33, s75, 110592\n"
"v_mfma_f32_16x16x32_bf16 v[172:175], a[116:119], a[240:243], v[172:175]\n"
"ds_read_b64_tr_b16 a[190:191], v14 offset:8704\n"
"s_add_u32 m0, s59, 24576\n"
"v_mfma_f32_16x16x32_bf16 v[172:175], a[132:135], a[244:247], v[172:175]\n"
"ds_read_b64_tr_b16 a[176:177], v11 offset:40960\n"
"s_waitcnt lgkmcnt(10)\n"
"v_mfma_f32_16x16x32_bf16 v[144:147], a[104:107], a[248:251], v[144:147]\n"
"ds_read_b128 a[240:243], v7 offset:32768\n"
"buffer_load_dwordx4 v6, s[8:11], s75 offen lds\n"
"v_mfma_f32_16x16x32_bf16 v[144:147], a[120:123], a[252:255], v[144:147]\n"
"ds_read_b128 a[244:247], v8 offset:32768\n"
"ds_read_b64_tr_b16 a[178:179], v11 offset:41472\n"
"v_mfma_f32_16x16x32_bf16 v[148:151], a[108:111], a[248:251], v[148:151]\n"
"s_add_u32 m0, s59, 28672\n"
"v_mfma_f32_16x16x32_bf16 v[148:151], a[124:127], a[252:255], v[148:151]\n"
"ds_read_b64_tr_b16 a[192:193], v12 offset:40960\n"
"buffer_load_dwordx4 v6, s[8:11], s33 offen lds\n"
"v_mfma_f32_16x16x32_bf16 v[152:155], a[112:115], a[248:251], v[152:155]\n"
"s_add_u32 m0, s59, 57344\n"
"v_mfma_f32_16x16x32_bf16 v[152:155], a[128:131], a[252:255], v[152:155]\n"
"ds_read_b64_tr_b16 a[194:195], v12 offset:41472\n"
"buffer_load_dwordx4 v6, s[12:15], s75 offen lds\n"
"v_mfma_f32_16x16x32_bf16 v[156:159], a[116:119], a[248:251], v[156:159]\n"
"v_mfma_f32_16x16x32_bf16 v[156:159], a[132:135], a[252:255], v[156:159]\n"
"ds_read_b64_tr_b16 a[180:181], v13 offset:40960\n"
"s_waitcnt lgkmcnt(10)\n"
"v_mfma_f32_16x16x32_bf16 v[128:131], a[104:107], a[232:235], v[128:131]\n"
"ds_read_b128 a[248:251], v7 offset:36864\n"
"s_add_u32 m0, s59, 61440\n"
"v_mfma_f32_16x16x32_bf16 v[128:131], a[120:123], a[236:239], v[128:131]\n"
"ds_read_b128 a[252:255], v8 offset:36864\n"
"ds_read_b64_tr_b16 a[182:183], v13 offset:41472\n"
"v_mfma_f32_16x16x32_bf16 v[132:135], a[108:111], a[232:235], v[132:135]\n"
"buffer_load_dwordx4 v6, s[12:15], s33 offen lds\n"
"v_mfma_f32_16x16x32_bf16 v[132:135], a[124:127], a[236:239], v[132:135]\n"
"ds_read_b64_tr_b16 a[196:197], v14 offset:40960\n"
"v_mfma_f32_16x16x32_bf16 v[136:139], a[112:115], a[232:235], v[136:139]\n"
"s_cmp_eq_u32 s32, 4\n"
"s_cselect_b32 s85, -9, 1\n"
"v_mfma_f32_16x16x32_bf16 v[136:139], a[128:131], a[236:239], v[136:139]\n"
"ds_read_b64_tr_b16 a[198:199], v14 offset:41472\n"
"v_lshl_add_u32 v5, s85, 7, v5\n"
"v_mfma_f32_16x16x32_bf16 v[140:143], a[116:119], a[232:235], v[140:143]\n"
"s_add_u32 m0, s59, 73728\n"
"v_mfma_f32_16x16x32_bf16 v[140:143], a[132:135], a[236:239], v[140:143]\n"
"buffer_load_dwordx4 v5, s[4:7], s64 offen lds\n"
"s_waitcnt lgkmcnt(9)\n"
"v_mfma_f32_16x16x32_bf16 v[112:115], a[104:107], a[240:243], v[112:115]\n"
"ds_read_b128 a[232:235], v7 offset:40960\n"
"s_add_u32 m0, s59, 77824\n"
"v_mfma_f32_16x16x32_bf16 v[112:115], a[120:123], a[244:247], v[112:115]\n"
"ds_read_b128 a[236:239], v8 offset:40960\n"
"buffer_load_dwordx4 v52, s[4:7], s64 offen lds\n"
"v_mfma_f32_16x16x32_bf16 v[116:119], a[108:111], a[240:243], v[116:119]\n"
"v_mfma_f32_16x16x32_bf16 v[116:119], a[124:127], a[244:247], v[116:119]\n"
"s_add_u32 m0, s59, 81920\n"
"v_mfma_f32_16x16x32_bf16 v[120:123], a[112:115], a[240:243], v[120:123]\n"
"v_mfma_f32_16x16x32_bf16 v[120:123], a[128:131], a[244:247], v[120:123]\n"
"buffer_load_dwordx4 v53, s[4:7], s64 offen lds\n"
"v_mfma_f32_16x16x32_bf16 v[124:127], a[116:119], a[240:243], v[124:127]\n"
"v_mfma_f32_16x16x32_bf16 v[124:127], a[132:135], a[244:247], v[124:127]\n"
"s_waitcnt vmcnt(13) lgkmcnt(0)\n"
"s_barrier\n"
"v_mfma_f32_16x16x32_bf16 v[96:99], a[104:107], a[248:251], v[96:99]\n"
"ds_read_b128 a[240:243], v9 offset:0\n"
"ds_read_b64_tr_b16 a[200:201], v11 offset:16384\n"
"v_mfma_f32_16x16x32_bf16 v[96:99], a[120:123], a[252:255], v[96:99]\n"
"ds_read_b128 a[244:247], v10 offset:0\n"
"s_waitcnt vmcnt(11)\n"
"v_mfma_f32_16x16x32_bf16 v[100:103], a[108:111], a[248:251], v[100:103]\n"
"ds_read_b64_tr_b16 a[202:203], v11 offset:16896\n"
"v_cmp_ne_u32 vcc, s52, v37\n"
"v_mfma_f32_16x16x32_bf16 v[100:103], a[124:127], a[252:255], v[100:103]\n"
"s_cbranch_vccnz .Lfb_slow_1\n"
".Lfb_back_2:\n"
"v_mfma_f32_16x16x32_bf16 v[104:107], a[112:115], a[248:251], v[104:107]\n"
"ds_read_b64_tr_b16 a[216:217], v12 offset:16384\n"
"s_add_u32 m0, s59, 86016\n"
"v_mfma_f32_16x16x32_bf16 v[104:107], a[128:131], a[252:255], v[104:107]\n"
"buffer_load_dwordx4 v55, s[4:7], s64 offen lds\n"
"s_mov_b32 m0, s63\n"
"v_mfma_f32_16x16x32_bf16 v[108:111], a[116:119], a[248:251], v[108:111]\n"
"ds_read_b64_tr_b16 a[218:219], v12 offset:16896\n"
"buffer_load_dwordx4 v2, s[80:83], s77 offen offset:0 sc1 lds\n"
"v_mfma_f32_16x16x32_bf16 v[108:111], a[132:135], a[252:255], v[108:111]\n"
"v_mfma_f32_16x16x32_bf16 v[80:83], a[104:107], a[232:235], v[80:83]\n"
"ds_read_b128 a[248:251], v9 offset:4096\n"
"ds_read_b64_tr_b16 a[204:205], v13 offset:16384\n"
"v_mfma_f32_16x16x32_bf16 v[80:83], a[120:123], a[236:239], v[80:83]\n"
"ds_read_b128 a[252:255], v10 offset:4096\n"
"s_mov_b32 m0, s63\n"
"v_mfma_f32_16x16x32_bf16 v[84:87], a[108:111], a[232:235], v[84:87]\n"
"ds_read_b64_tr_b16 a[206:207], v13 offset:16896\n"
"buffer_load_dwordx4 v2, s[80:83], s77 offen offset:1024 sc1 lds\n"
"v_mfma_f32_16x16x32_bf16 v[84:87], a[124:127], a[236:239], v[84:87]\n"
"v_mfma_f32_16x16x32_bf16 v[88:91], a[112:115], a[232:235], v[88:91]\n"
"ds_read_b64_tr_b16 a[220:221], v14 offset:16384\n"
"v_mfma_f32_16x16x32_bf16 v[88:91], a[128:131], a[236:239], v[88:91]\n"
"buffer_load_dwordx4 v[28:31], v2, s[16:19], s94 offen offset:0 sc1\n"
"v_mfma_f32_16x16x32_bf16 v[92:95], a[116:119], a[232:235], v[92:95]\n"
"ds_read_b64_tr_b16 a[222:223], v14 offset:16896\n"
"v_mfma_f32_16x16x32_bf16 v[92:95], a[132:135], a[236:239], v[92:95]\n"
"ds_read_b64_tr_b16 a[208:209], v11 offset:49152\n"
"s_waitcnt lgkmcnt(10)\n"
"v_mfma_f32_16x16x32_bf16 v[240:243], a[136:139], a[240:243], v[240:243]\n"
"ds_read_b128 a[232:235], v9 offset:8192\n"
"buffer_load_dwordx4 v[32:35], v2, s[16:19], s94 offen offset:1024 sc1\n"
"v_mfma_f32_16x16x32_bf16 v[240:243], a[152:155], a[244:247], v[240:243]\n"
"ds_read_b128 a[236:239], v10 offset:8192\n"
"ds_read_b64_tr_b16 a[210:211], v11 offset:49664\n"
"v_mfma_f32_16x16x32_bf16 v[244:247], a[140:143], a[240:243], v[244:247]\n"
"v_mfma_f32_16x16x32_bf16 v[244:247], a[156:159], a[244:247], v[244:247]\n"
"ds_read_b64_tr_b16 a[224:225], v12 offset:49152\n"
"buffer_load_dwordx4 v[36:39], v2, s[16:19], s94 offen offset:2048 sc1\n"
"v_mfma_f32_16x16x32_bf16 v[248:251], a[144:147], a[240:243], v[248:251]\n"
"v_mfma_f32_16x16x32_bf16 v[248:251], a[160:163], a[244:247], v[248:251]\n"
"ds_read_b64_tr_b16 a[226:227], v12 offset:49664\n"
"v_mfma_f32_16x16x32_bf16 v[252:255], a[148:151], a[240:243], v[252:255]\n"
"buffer_load_dwordx4 v[40:43], v2, s[16:19], s94 offen offset:3072 sc1\n"
"v_mfma_f32_16x16x32_bf16 v[252:255], a[164:167], a[244:247], v[252:255]\n"
"ds_read_b64_tr_b16 a[212:213], v13 offset:49152\n"
"s_waitcnt lgkmcnt(10)\n"
"v_mfma_f32_16x16x32_bf16 v[224:227], a[136:139], a[248:251], v[224:227]\n"
"ds_read_b128 a[240:243], v9 offset:12288\n"
"s_add_u32 s94, s94, 4096\n"
"v_mfma_f32_16x16x32_bf16 v[224:227], a[152:155], a[252:255], v[224:227]\n"
"ds_read_b128 a[244:247], v10 offset:12288\n"
"ds_read_b64_tr_b16 a[214:215], v13 offset:49664\n"
"v_mfma_f32_16x16x32_bf16 v[228:231], a[140:143], a[248:251], v[228:231]\n"
"buffer_load_dwordx4 v[44:47], v2, s[16:19], s94 offen sc1\n"
"v_mfma_f32_16x16x32_bf16 v[228:231], a[156:159], a[252:255], v[228:231]\n"
"ds_read_b64_tr_b16 a[228:229], v14 offset:49152\n"
"s_add_u32 m0, s59, 90112\n"
"v_mfma_f32_16x16x32_bf16 v[232:235], a[144:147], a[248:251], v[232:235]\n"
"v_mfma_f32_16x16x32_bf16 v[232:235], a[160:163], a[252:255], v[232:235]\n"
"ds_read_b64_tr_b16 a[230:231], v14 offset:49664\n"
"buffer_load_dwordx4 v5, s[4:7], s65 offen lds\n"
"v_mfma_f32_16x16x32_bf16 v[236:239], a[148:151], a[248:251], v[236:239]\n"
"s_add_u32 m0, s59, 94208\n"
"v_mfma_f32_16x16x32_bf16 v[236:239], a[164:167], a[252:255], v[236:239]\n"
"s_waitcnt lgkmcnt(9)\n"
"v_mfma_f32_16x16x32_bf16 v[208:211], a[136:139], a[232:235], v[208:211]\n"
"ds_read_b128 a[248:251], v9 offset:16384\n"
"buffer_load_dwordx4 v52, s[4:7], s65 offen lds\n"
"v_mfma_f32_16x16x32_bf16 v[208:211], a[152:155], a[236:239], v[208:211]\n"
"ds_read_b128 a[252:255], v10 offset:16384\n"
"v_mfma_f32_16x16x32_bf16 v[212:215], a[140:143], a[232:235], v[212:215]\n"
"s_cmp_eq_u32 s32, 1\n"
"s_cselect_b32 s85, -9, 1\n"
"v_mfma_f32_16x16x32_bf16 v[212:215], a[156:159], a[236:239], v[212:215]\n"
"s_mul_i32 s33, s85, 221184\n"
"s_add_u32 s75, s75, s33\n"
"v_mfma_f32_16x16x32_bf16 v[216:219], a[144:147], a[232:235], v[216:219]\n"
"s_add_u32 s33, s75, 110592\n"
"s_add_u32 m0, s59, 0\n"
"v_mfma_f32_16x16x32_bf16 v[216:219], a[160:163], a[236:239], v[216:219]\n"
"buffer_load_dwordx4 v6, s[8:11], s75 offen lds\n"
"s_add_u32 m0, s59, 4096\n"
"v_mfma_f32_16x16x32_bf16 v[220:223], a[148:151], a[232:235], v[220:223]\n"
"v_mfma_f32_16x16x32_bf16 v[220:223], a[164:167], a[236:239], v[220:223]\n"
"buffer_load_dwordx4 v6, s[8:11], s33 offen lds\n"
"s_waitcnt lgkmcnt(5)\n"
"v_mfma_f32_16x16x32_bf16 v[192:195], a[136:139], a[240:243], v[192:195]\n"
"ds_read_b128 a[232:235], v9 offset:20480\n"
"s_add_u32 m0, s59, 32768\n"
"v_mfma_f32_16x16x32_bf16 v[192:195], a[152:155], a[244:247], v[192:195]\n"
"ds_read_b128 a[236:239], v10 offset:20480\n"
"v_mfma_f32_16x16x32_bf16 v[196:199], a[140:143], a[240:243], v[196:199]\n"
"buffer_load_dwordx4 v6, s[12:15], s75 offen lds\n"
"v_mfma_f32_16x16x32_bf16 v[196:199], a[156:159], a[244:247], v[196:199]\n"
"s_add_u32 m0, s59, 36864\n"
"v_mfma_f32_16x16x32_bf16 v[200:203], a[144:147], a[240:243], v[200:203]\n"
"v_mfma_f32_16x16x32_bf16 v[200:203], a[160:163], a[244:247], v[200:203]\n"
"buffer_load_dwordx4 v6, s[12:15], s33 offen lds\n"
"v_mfma_f32_16x16x32_bf16 v[204:207], a[148:151], a[240:243], v[204:207]\n"
"v_mfma_f32_16x16x32_bf16 v[204:207], a[164:167], a[244:247], v[204:207]\n"
"s_waitcnt vmcnt(17) lgkmcnt(0)\n"
"s_barrier\n"
"v_mfma_f32_16x16x32_bf16 v[176:179], a[136:139], a[248:251], v[176:179]\n"
"ds_read_b128 a[240:243], v9 offset:24576\n"
"ds_read_b64_tr_b16 a[104:105], v11 offset:24576\n"
"v_mfma_f32_16x16x32_bf16 v[176:179], a[152:155], a[252:255], v[176:179]\n"
"ds_read_b128 a[244:247], v10 offset:24576\n"
"s_add_u32 m0, s59, 98304\n"
"v_mfma_f32_16x16x32_bf16 v[180:183], a[140:143], a[248:251], v[180:183]\n"
"ds_read_b64_tr_b16 a[106:107], v11 offset:25088\n"
"buffer_load_dwordx4 v53, s[4:7], s65 offen lds\n"
"v_mfma_f32_16x16x32_bf16 v[180:183], a[156:159], a[252:255], v[180:183]\n"
"s_add_u32 m0, s59, 102400\n"
"v_mfma_f32_16x16x32_bf16 v[184:187], a[144:147], a[248:251], v[184:187]\n"
"ds_read_b64_tr_b16 a[120:121], v12 offset:24576\n"
"v_mfma_f32_16x16x32_bf16 v[184:187], a[160:163], a[252:255], v[184:187]\n"
"buffer_load_dwordx4 v55, s[4:7], s65 offen lds\n"
"v_mfma_f32_16x16x32_bf16 v[188:191], a[148:151], a[248:251], v[188:191]\n"
"ds_read_b64_tr_b16 a[122:123], v12 offset:25088\n"
"v_mfma_f32_16x16x32_bf16 v[188:191], a[164:167], a[252:255], v[188:191]\n"
"s_add_u32 m0, s59, 106496\n"
"v_mfma_f32_16x16x32_bf16 v[160:163], a[136:139], a[232:235], v[160:163]\n"
"ds_read_b128 a[248:251], v9 offset:28672\n"
"ds_read_b64_tr_b16 a[108:109], v13 offset:24576\n"
"v_mfma_f32_16x16x32_bf16 v[160:163], a[152:155], a[236:239], v[160:163]\n"
"ds_read_b128 a[252:255], v10 offset:28672\n"
"buffer_load_dwordx4 v5, s[4:7], s66 offen lds\n"
"v_mfma_f32_16x16x32_bf16 v[164:167], a[140:143], a[232:235], v[164:167]\n"
"ds_read_b64_tr_b16 a[110:111], v13 offset:25088\n"
"v_mfma_f32_16x16x32_bf16 v[164:167], a[156:159], a[236:239], v[164:167]\n"
"s_add_u32 m0, s59, 110592\n"
"v_mfma_f32_16x16x32_bf16 v[168:171], a[144:147], a[232:235], v[168:171]\n"
"ds_read_b64_tr_b16 a[124:125], v14 offset:24576\n"
"v_mfma_f32_16x16x32_bf16 v[168:171], a[160:163], a[236:239], v[168:171]\n"
"buffer_load_dwordx4 v52, s[4:7], s66 offen lds\n"
"v_mfma_f32_16x16x32_bf16 v[172:175], a[148:151], a[232:235], v[172:175]\n"
"ds_read_b64_tr_b16 a[126:127], v14 offset:25088\n"
"v_mfma_f32_16x16x32_bf16 v[172:175], a[164:167], a[236:239], v[172:175]\n"
"ds_read_b64_tr_b16 a[112:113], v11 offset:57344\n"
"s_waitcnt lgkmcnt(10)\n"
"v_mfma_f32_16x16x32_bf16 v[144:147], a[136:139], a[240:243], v[144:147]\n"
"ds_read_b128 a[232:235], v9 offset:32768\n"
"s_add_u32 m0, s59, 114688\n"
"v_mfma_f32_16x16x32_bf16 v[144:147], a[152:155], a[244:247], v[144:147]\n"
"ds_read_b128 a[236:239], v10 offset:32768\n"
"ds_read_b64_tr_b16 a[114:115], v11 offset:57856\n"
"v_mfma_f32_16x16x32_bf16 v[148:151], a[140:143], a[240:243], v[148:151]\n"
"buffer_load_dwordx4 v53, s[4:7], s66 offen lds\n"
"v_mfma_f32_16x16x32_bf16 v[148:151], a[156:159], a[244:247], v[148:151]\n"
"ds_read_b64_tr_b16 a[128:129], v12 offset:57344\n"
"s_add_u32 m0, s59, 118784\n"
"v_mfma_f32_16x16x32_bf16 v[152:155], a[144:147], a[240:243], v[152:155]\n"
"v_mfma_f32_16x16x32_bf16 v[152:155], a[160:163], a[244:247], v[152:155]\n"
"ds_read_b64_tr_b16 a[130:131], v12 offset:57856\n"
"buffer_load_dwordx4 v55, s[4:7], s66 offen lds\n"
"v_mfma_f32_16x16x32_bf16 v[156:159], a[148:151], a[240:243], v[156:159]\n"
"v_mfma_f32_16x16x32_bf16 v[156:159], a[164:167], a[244:247], v[156:159]\n"
"ds_read_b64_tr_b16 a[116:117], v13 offset:57344\n"
"s_waitcnt lgkmcnt(10)\n"
"v_mfma_f32_16x16x32_bf16 v[128:131], a[136:139], a[248:251], v[128:131]\n"
"ds_read_b128 a[240:243], v9 offset:36864\n"
"s_add_u32 m0, s59, 122880\n"
"v_mfma_f32_16x16x32_bf16 v[128:131], a[152:155], a[252:255], v[128:131]\n"
"ds_read_b128 a[244:247], v10 offset:36864\n"
"ds_read_b64_tr_b16 a[118:119], v13 offset:57856\n"
"v_mfma_f32_16x16x32_bf16 v[132:135], a[140:143], a[248:251], v[132:135]\n"
"buffer_load_dwordx4 v5, s[4:7], s67 offen lds\n"
"v_mfma_f32_16x16x32_bf16 v[132:135], a[156:159], a[252:255], v[132:135]\n"
"ds_read_b64_tr_b16 a[132:133], v14 offset:57344\n"
"s_add_u32 m0, s59, 126976\n"
"v_mfma_f32_16x16x32_bf16 v[136:139], a[144:147], a[248:251], v[136:139]\n"
"v_mfma_f32_16x16x32_bf16 v[136:139], a[160:163], a[252:255], v[136:139]\n"
"ds_read_b64_tr_b16 a[134:135], v14 offset:57856\n"
"buffer_load_dwordx4 v52, s[4:7], s67 offen lds\n"
"v_mfma_f32_16x16x32_bf16 v[140:143], a[148:151], a[248:251], v[140:143]\n"
"v_mfma_f32_16x16x32_bf16 v[140:143], a[164:167], a[252:255], v[140:143]\n"
"s_waitcnt lgkmcnt(9)\n"
"s_add_u32 m0, s59, 131072\n"
"v_mfma_f32_16x16x32_bf16 v[112:115], a[136:139], a[232:235], v[112:115]\n"
"ds_read_b128 a[248:251], v9 offset:40960\n"
"v_mfma_f32_16x16x32_bf16 v[112:115], a[152:155], a[236:239], v[112:115]\n"
"ds_read_b128 a[252:255], v10 offset:40960\n"
"buffer_load_dwordx4 v53, s[4:7], s67 offen lds\n"
"v_mfma_f32_16x16x32_bf16 v[116:119], a[140:143], a[232:235], v[116:119]\n"
"v_mfma_f32_16x16x32_bf16 v[116:119], a[156:159], a[236:239], v[116:119]\n"
"s_add_u32 m0, s59, 135168\n"
"s_waitcnt vmcnt(15)\n"
"v_mfma_f32_16x16x32_bf16 v[120:123], a[144:147], a[232:235], v[120:123]\n"
"v_add_f32 v28, v28, v32\n"
"v_add_f32 v29, v29, v33\n"
"v_mfma_f32_16x16x32_bf16 v[120:123], a[160:163], a[236:239], v[120:123]\n"
"buffer_load_dwordx4 v55, s[4:7], s67 offen lds\n"
"v_add_f32 v30, v30, v34\n"
"v_mfma_f32_16x16x32_bf16 v[124:127], a[148:151], a[232:235], v[124:127]\n"
"v_add_f32 v31, v31, v35\n"
"v_add_f32 v32, v36, v40\n"
"v_mfma_f32_16x16x32_bf16 v[124:127], a[164:167], a[236:239], v[124:127]\n"
"s_waitcnt vmcnt(10) lgkmcnt(0)\n"
"s_barrier\n"
"v_mfma_f32_16x16x32_bf16 v[96:99], a[136:139], a[240:243], v[96:99]\n"
"ds_read_b128 a[232:235], v7 offset:0\n"
"s_add_u32 m0, s59, 139264\n"
"v_mfma_f32_16x16x32_bf16 v[96:99], a[152:155], a[244:247], v[96:99]\n"
"ds_read_b128 a[236:239], v8 offset:0\n"
"buffer_load_dwordx4 v5, s[4:7], s68 offen lds\n"
"v_mfma_f32_16x16x32_bf16 v[100:103], a[140:143], a[240:243], v[100:103]\n"
"v_add_f32 v33, v37, v41\n"
"v_add_f32 v34, v38, v42\n"
"v_mfma_f32_16x16x32_bf16 v[100:103], a[156:159], a[244:247], v[100:103]\n"
"v_add_f32 v35, v39, v43\n"
"s_add_u32 m0, s59, 143360\n"
"v_mfma_f32_16x16x32_bf16 v[104:107], a[144:147], a[240:243], v[104:107]\n"
"v_add_f32 v28, v28, v44\n"
"v_add_f32 v29, v29, v45\n"
"v_mfma_f32_16x16x32_bf16 v[104:107], a[160:163], a[244:247], v[104:107]\n"
"buffer_load_dwordx4 v52, s[4:7], s68 offen lds\n"
"v_add_f32 v30, v30, v46\n"
"v_mfma_f32_16x16x32_bf16 v[108:111], a[148:151], a[240:243], v[108:111]\n"
"ds_read_b128 v[16:19], v57 offset:0\n"
"v_add_f32 v31, v31, v47\n"
"v_mfma_f32_16x16x32_bf16 v[108:111], a[164:167], a[244:247], v[108:111]\n"
"s_add_u32 m0, s59, 147456\n"
"v_mfma_f32_16x16x32_bf16 v[80:83], a[136:139], a[248:251], v[80:83]\n"
"ds_read_b128 a[240:243], v7 offset:4096\n"
"v_add_f32 v28, v28, v32\n"
"v_mfma_f32_16x16x32_bf16 v[80:83], a[152:155], a[252:255], v[80:83]\n"
"ds_read_b128 a[244:247], v8 offset:4096\n"
"buffer_load_dwordx4 v53, s[4:7], s68 offen lds\n"
"v_mfma_f32_16x16x32_bf16 v[84:87], a[140:143], a[248:251], v[84:87]\n"
"v_add_f32 v29, v29, v33\n"
"v_add_f32 v30, v30, v34\n"
"v_mfma_f32_16x16x32_bf16 v[84:87], a[156:159], a[252:255], v[84:87]\n"
"v_add_f32 v31, v31, v35\n"
"s_add_u32 m0, s59, 151552\n"
"v_mfma_f32_16x16x32_bf16 v[88:91], a[144:147], a[248:251], v[88:91]\n"
"v_bfe_u32 v32, v1, 4, 1\n"
"v_lshrrev_b32 v33, 5, v1\n"
"v_mfma_f32_16x16x32_bf16 v[88:91], a[160:163], a[252:255], v[88:91]\n"
"buffer_load_dwordx4 v55, s[4:7], s68 offen lds\n"
"v_permlane32_swap_b32 v28, v29\n"
"v_mfma_f32_16x16x32_bf16 v[92:95], a[148:151], a[248:251], v[92:95]\n"
"v_permlane32_swap_b32 v30, v31\n"
"v_add_f32 v28, v28, v29\n"
"v_mfma_f32_16x16x32_bf16 v[92:95], a[164:167], a[252:255], v[92:95]\n"
"s_waitcnt lgkmcnt(3)\n"
"s_add_u32 m0, s59, 155648\n"
"v_mfma_f32_16x16x32_bf16 v[240:243], a[168:171], a[232:235], v[240:243]\n"
"ds_read_b128 a[248:251], v7 offset:8192\n"
"ds_read_b64_tr_b16 a[136:137], v11 offset:0\n"
"v_mfma_f32_16x16x32_bf16 v[240:243], a[184:187], a[236:239], v[240:243]\n"
"ds_read_b128 a[252:255], v8 offset:8192\n"
"ds_read_b64_tr_b16 a[138:139], v11 offset:512\n"
"v_mfma_f32_16x16x32_bf16 v[244:247], a[172:175], a[232:235], v[244:247]\n"
"ds_read_b64_tr_b16 a[152:153], v12 offset:0\n"
"buffer_load_dwordx4 v5, s[4:7], s69 offen lds\n"
"v_mfma_f32_16x16x32_bf16 v[244:247], a[188:191], a[236:239], v[244:247]\n"
"ds_read_b64_tr_b16 a[154:155], v12 offset:512\n"
"s_add_u32 m0, s59, 159744\n"
"v_mfma_f32_16x16x32_bf16 v[248:251], a[176:179], a[232:235], v[248:251]\n"
"ds_read_b64_tr_b16 a[140:141], v13 offset:0\n"
"v_add_f32 v30, v30, v31\n"
"v_mfma_f32_16x16x32_bf16 v[248:251], a[192:195], a[236:239], v[248:251]\n"
"ds_read_b64_tr_b16 a[142:143], v13 offset:512\n"
"buffer_load_dwordx4 v52, s[4:7], s69 offen lds\n"
"v_mfma_f32_16x16x32_bf16 v[252:255], a[180:183], a[232:235], v[252:255]\n"
"ds_read_b64_tr_b16 a[156:157], v14 offset:0\n"
"v_lshl_add_u32 v32, v32, 1, v33\n"
"v_mfma_f32_16x16x32_bf16 v[252:255], a[196:199], a[236:239], v[252:255]\n"
"ds_read_b64_tr_b16 a[158:159], v14 offset:512\n"
"s_waitcnt lgkmcnt(10)\n"
"v_mfma_f32_16x16x32_bf16 v[240:243], a[200:203], a[240:243], v[240:243]\n"
"ds_read_b128 a[232:235], v7 offset:12288\n"
"ds_read_b64_tr_b16 a[144:145], v11 offset:32768\n"
"v_mfma_f32_16x16x32_bf16 v[240:243], a[216:219], a[244:247], v[240:243]\n"
"ds_read_b128 a[236:239], v8 offset:12288\n"
"ds_read_b64_tr_b16 a[146:147], v11 offset:33280\n"
"v_mfma_f32_16x16x32_bf16 v[244:247], a[204:207], a[240:243], v[244:247]\n"
"ds_read_b64_tr_b16 a[160:161], v12 offset:32768\n"
"s_add_u32 s98, s43, 0\n"
"v_mfma_f32_16x16x32_bf16 v[244:247], a[220:223], a[244:247], v[244:247]\n"
"ds_read_b64_tr_b16 a[162:163], v12 offset:33280\n"
"buffer_load_dwordx4 a[0:3], v2, s[24:27], s98 offen offset:0 sc1\n"
"v_mfma_f32_16x16x32_bf16 v[248:251], a[208:211], a[240:243], v[248:251]\n"
"ds_read_b64_tr_b16 a[148:149], v13 offset:32768\n"
"v_lshl_add_u32 v32, v32, 2, v15\n"
"v_mfma_f32_16x16x32_bf16 v[248:251], a[224:227], a[244:247], v[248:251]\n"
"ds_read_b64_tr_b16 a[150:151], v13 offset:33280\n"
"buffer_load_dwordx4 a[4:7], v2, s[24:27], s98 offen offset:1024 sc1\n"
"v_mfma_f32_16x16x32_bf16 v[252:255], a[212:215], a[240:243], v[252:255]\n"
"ds_read_b64_tr_b16 a[164:165], v14 offset:32768\n"
"v_permlane16_swap_b32 v28, v30\n"
"v_mfma_f32_16x16x32_bf16 v[252:255], a[228:231], a[244:247], v[252:255]\n"
"ds_read_b64_tr_b16 a[166:167], v14 offset:33280\n"
"s_waitcnt lgkmcnt(15)\n"
"v_mfma_f32_16x16x32_bf16 v[240:243], a[104:107], a[248:251], v[240:243]\n"
"ds_read_b128 a[240:243], v7 offset:16384\n"
"s_lshl_b32 s60, s3, 8\n"
"v_mfma_f32_16x16x32_bf16 v[240:243], a[120:123], a[252:255], v[240:243]\n"
"ds_read_b128 a[244:247], v8 offset:16384\n"
"buffer_load_dwordx4 a[8:11], v2, s[24:27], s98 offen offset:2048 sc1\n"
"v_mfma_f32_16x16x32_bf16 v[244:247], a[108:111], a[248:251], v[244:247]\n"
"ds_read_b128 v[20:23], v57 offset:256\n"
"s_add_u32 s60, s60, 71680\n"
"v_mfma_f32_16x16x32_bf16 v[244:247], a[124:127], a[252:255], v[244:247]\n"
"v_add_u32 v32, s60, v32\n"
"v_add_f32 v28, v28, v30\n"
"v_mfma_f32_16x16x32_bf16 v[248:251], a[112:115], a[248:251], v[248:251]\n"
"v_and_b32 v33, 48, v1\n"
"v_add_u32 v33, s60, v33\n"
"v_mfma_f32_16x16x32_bf16 v[248:251], a[128:131], a[252:255], v[248:251]\n"
"buffer_load_dwordx4 a[12:15], v2, s[24:27], s98 offen offset:3072 sc1\n"
"ds_read_b128 v[24:27], v57 offset:512\n"
"v_mfma_f32_16x16x32_bf16 v[252:255], a[116:119], a[248:251], v[252:255]\n"
"ds_write_b32 v32, v28\n"
"v_mfma_f32_16x16x32_bf16 v[252:255], a[132:135], a[252:255], v[252:255]\n"
"s_waitcnt lgkmcnt(12)\n"
"s_add_u32 s98, s43, 4096\n"
"v_mfma_f32_16x16x32_bf16 v[240:243], a[136:139], a[232:235], v[240:243]\n"
"ds_read_b128 a[248:251], v7 offset:20480\n"
"ds_read_b128 v[64:67], v33 offset:0\n"
"v_mfma_f32_16x16x32_bf16 v[240:243], a[152:155], a[236:239], v[240:243]\n"
"ds_read_b128 a[252:255], v8 offset:20480\n"
"buffer_load_dwordx4 a[16:19], v2, s[24:27], s98 offen offset:0 sc1\n"
"v_mfma_f32_16x16x32_bf16 v[244:247], a[140:143], a[232:235], v[244:247]\n"
"ds_read_b128 v[68:71], v33 offset:64\n"
"ds_read_b128 v[72:75], v33 offset:128\n"
"v_mfma_f32_16x16x32_bf16 v[244:247], a[156:159], a[236:239], v[244:247]\n"
"ds_read_b128 v[76:79], v33 offset:192\n"
"s_add_u32 s98, s55, 0\n"
"s_waitcnt lgkmcnt(15)\n"
"v_mfma_f32_16x16x32_bf16 v[248:251], a[144:147], a[232:235], v[248:251]\n"
"v_mfma_f32_16x16x32_bf16 v[248:251], a[160:163], a[236:239], v[248:251]\n"
"buffer_load_dwordx4 a[20:23], v2, s[24:27], s98 offen offset:0 sc1\n"
"s_waitcnt lgkmcnt(13)\n"
"v_mfma_f32_16x16x32_bf16 v[252:255], a[148:151], a[232:235], v[252:255]\n"
"s_waitcnt lgkmcnt(11)\n"
"v_mfma_f32_16x16x32_bf16 v[252:255], a[164:167], a[236:239], v[252:255]\n"
"s_waitcnt vmcnt(17) lgkmcnt(0)\n"
"s_barrier\n"
"v_mfma_f32_16x16x32_bf16 v[224:227], a[168:171], a[240:243], v[224:227]\n"
"ds_read_b128 a[232:235], v7 offset:24576\n"
"s_add_u32 m0, s59, 73728\n"
"v_mfma_f32_16x16x32_bf16 v[224:227], a[184:187], a[244:247], v[224:227]\n"
"ds_read_b128 a[236:239], v8 offset:24576\n"
"buffer_load_dwordx4 v53, s[4:7], s69 offen lds\n"
"v_mfma_f32_16x16x32_bf16 v[228:231], a[172:175], a[240:243], v[228:231]\n"
"ds_read_b128 v[36:39], v57 offset:768\n"
"v_mfma_f32_16x16x32_bf16 v[228:231], a[188:191], a[244:247], v[228:231]\n"
"v_fma_f32 v240, v64, -v16, v240\n"
"s_add_u32 m0, s59, 77824\n"
"v_mfma_f32_16x16x32_bf16 v[232:235], a[176:179], a[240:243], v[232:235]\n"
"v_fma_f32 v241, v65, -v16, v241\n"
"v_fma_f32 v242, v66, -v16, v242\n"
"v_mfma_f32_16x16x32_bf16 v[232:235], a[192:195], a[244:247], v[232:235]\n"
"buffer_load_dwordx4 v55, s[4:7], s69 offen lds\n"
"ds_read_b128 v[40:43], v57 offset:1024\n"
"v_mfma_f32_16x16x32_bf16 v[236:239], a[180:183], a[240:243], v[236:239]\n"
"v_fma_f32 v243, v67, -v16, v243\n"
"v_fma_f32 v248, v72, -v16, v248\n"
"v_mfma_f32_16x16x32_bf16 v[236:239], a[196:199], a[244:247], v[236:239]\n"
"s_add_u32 m0, s59, 81920\n"
"v_mfma_f32_16x16x32_bf16 v[224:227], a[200:203], a[248:251], v[224:227]\n"
"ds_read_b128 a[240:243], v7 offset:28672\n"
"ds_read_b128 v[44:47], v57 offset:1280\n"
"v_mfma_f32_16x16x32_bf16 v[224:227], a[216:219], a[252:255], v[224:227]\n"
"ds_read_b128 a[244:247], v8 offset:28672\n"
"buffer_load_dwordx4 v5, s[4:7], s70 offen lds\n"
"v_mfma_f32_16x16x32_bf16 v[228:231], a[204:207], a[248:251], v[228:231]\n"
"v_fma_f32 v249, v73, -v16, v249\n"
"v_fma_f32 v250, v74, -v16, v250\n"
"v_mfma_f32_16x16x32_bf16 v[228:231], a[220:223], a[252:255], v[228:231]\n"
"ds_read_b128 v[48:51], v57 offset:1536\n"
"s_add_u32 m0, s59, 86016\n"
"v_mfma_f32_16x16x32_bf16 v[232:235], a[208:211], a[248:251], v[232:235]\n"
"v_fma_f32 v251, v75, -v16, v251\n"
"v_fma_f32 v244, v68, -v16, v244\n"
"v_mfma_f32_16x16x32_bf16 v[232:235], a[224:227], a[252:255], v[232:235]\n"
"buffer_load_dwordx4 v52, s[4:7], s70 offen lds\n"
"v_fma_f32 v245, v69, -v16, v245\n"
"v_mfma_f32_16x16x32_bf16 v[236:239], a[212:215], a[248:251], v[236:239]\n"
"ds_read_b128 v[60:63], v57 offset:1792\n"
"v_fma_f32 v246, v70, -v16, v246\n"
"v_mfma_f32_16x16x32_bf16 v[236:239], a[228:231], a[252:255], v[236:239]\n"
"s_waitcnt lgkmcnt(7)\n"
"s_add_u32 m0, s59, 90112\n"
"v_mfma_f32_16x16x32_bf16 v[224:227], a[104:107], a[232:235], v[224:227]\n"
"ds_read_b128 a[248:251], v7 offset:32768\n"
"v_fma_f32 v247, v71, -v16, v247\n"
"v_mfma_f32_16x16x32_bf16 v[224:227], a[120:123], a[236:239], v[224:227]\n"
"ds_read_b128 a[252:255], v8 offset:32768\n"
"buffer_load_dwordx4 v53, s[4:7], s70 offen lds\n"
"v_mfma_f32_16x16x32_bf16 v[228:231], a[108:111], a[232:235], v[228:231]\n"
"ds_read_b128 v[28:31], v57 offset:2048\n"
"v_fma_f32 v252, v76, -v16, v252\n"
"v_mfma_f32_16x16x32_bf16 v[228:231], a[124:127], a[236:239], v[228:231]\n"
"v_fma_f32 v253, v77, -v16, v253\n"
"s_add_u32 m0, s59, 94208\n"
"v_mfma_f32_16x16x32_bf16 v[232:235], a[112:115], a[232:235], v[232:235]\n"
"v_fma_f32 v254, v78, -v16, v254\n"
"v_fma_f32 v255, v79, -v16, v255\n"
"v_mfma_f32_16x16x32_bf16 v[232:235], a[128:131], a[236:239], v[232:235]\n"
"buffer_load_dwordx4 v55, s[4:7], s70 offen lds\n"
"ds_read_b128 v[32:35], v57 offset:2304\n"
"v_mfma_f32_16x16x32_bf16 v[236:239], a[116:119], a[232:235], v[236:239]\n"
"v_mul_f32 v248, v240, v248\n"
"v_mul_f32 v249, v241, v249\n"
"v_mfma_f32_16x16x32_bf16 v[236:239], a[132:135], a[236:239], v[236:239]\n"
"v_mul_f32 v240, v240, v17\n"
"s_waitcnt lgkmcnt(6)\n"
"v_mfma_f32_16x16x32_bf16 v[224:227], a[136:139], a[240:243], v[224:227]\n"
"ds_read_b128 a[232:235], v7 offset:36864\n"
"ds_read_b128 v[56:59], v57 offset:2560\n"
"v_mfma_f32_16x16x32_bf16 v[224:227], a[152:155], a[244:247], v[224:227]\n"
"ds_read_b128 a[236:239], v8 offset:36864\n"
"buffer_load_dwordx4 a[24:27], v2, s[24:27], s98 offen offset:1024 sc1\n"
"v_mfma_f32_16x16x32_bf16 v[228:231], a[140:143], a[240:243], v[228:231]\n"
"v_mul_f32 v241, v241, v17\n"
"v_mul_f32 v250, v242, v250\n"
"v_mfma_f32_16x16x32_bf16 v[228:231], a[156:159], a[244:247], v[228:231]\n"
"v_mul_f32 v251, v243, v251\n"
"v_mul_f32 v242, v242, v17\n"
"v_mfma_f32_16x16x32_bf16 v[232:235], a[144:147], a[240:243], v[232:235]\n"
"v_mul_f32 v243, v243, v17\n"
"v_mul_f32 v252, v244, v252\n"
"v_mfma_f32_16x16x32_bf16 v[232:235], a[160:163], a[244:247], v[232:235]\n"
"buffer_load_dwordx4 a[28:31], v2, s[24:27], s98 offen offset:2048 sc1\n"
"v_mul_f32 v253, v245, v253\n"
"v_mfma_f32_16x16x32_bf16 v[236:239], a[148:151], a[240:243], v[236:239]\n"
"v_mul_f32 v244, v244, v17\n"
"v_mul_f32 v245, v245, v17\n"
"v_mfma_f32_16x16x32_bf16 v[236:239], a[164:167], a[244:247], v[236:239]\n"
"v_mul_f32 v254, v246, v254\n"
"s_waitcnt lgkmcnt(5)\n"
"v_mfma_f32_16x16x32_bf16 v[208:211], a[168:171], a[248:251], v[208:211]\n"
"ds_read_b128 a[240:243], v7 offset:40960\n"
"v_mul_f32 v255, v247, v255\n"
"v_mfma_f32_16x16x32_bf16 v[208:211], a[184:187], a[252:255], v[208:211]\n"
"ds_read_b128 a[244:247], v8 offset:40960\n"
"buffer_load_dwordx4 a[32:35], v2, s[24:27], s98 offen offset:3072 sc1\n"
"v_mfma_f32_16x16x32_bf16 v[212:215], a[172:175], a[248:251], v[212:215]\n"
"v_mul_f32 v246, v246, v17\n"
"v_mul_f32 v247, v247, v17\n"
"v_mfma_f32_16x16x32_bf16 v[212:215], a[188:191], a[252:255], v[212:215]\n"
"v_fma_f32 v224, v64, -v20, v224\n"
"s_add_u32 s98, s55, 4096\n"
"v_mfma_f32_16x16x32_bf16 v[216:219], a[176:179], a[248:251], v[216:219]\n"
"v_exp_f32 v240, v240\n"
"v_mfma_f32_16x16x32_bf16 v[216:219], a[192:195], a[252:255], v[216:219]\n"
"buffer_load_dwordx4 a[36:39], v2, s[24:27], s98 offen offset:0 sc1\n"
"v_fma_f32 v240, v240, v18, v18\n"
"v_mfma_f32_16x16x32_bf16 v[220:223], a[180:183], a[248:251], v[220:223]\n"
"v_exp_f32 v241, v241\n"
"v_mfma_f32_16x16x32_bf16 v[220:223], a[196:199], a[252:255], v[220:223]\n"
"s_waitcnt vmcnt(21) lgkmcnt(0)\n"
"s_barrier\n"
"v_mfma_f32_16x16x32_bf16 v[208:211], a[200:203], a[232:235], v[208:211]\n"
"ds_read_b128 a[248:251], v9 offset:0\n"
"s_add_u32 m0, s59, 98304\n"
"v_mfma_f32_16x16x32_bf16 v[208:211], a[216:219], a[236:239], v[208:211]\n"
"ds_read_b128 a[252:255], v10 offset:0\n"
"buffer_load_dwordx4 v5, s[4:7], s71 offen lds\n"
"v_mfma_f32_16x16x32_bf16 v[212:215], a[204:207], a[232:235], v[212:215]\n"
"v_exp_f32 v242, v242\n"
"v_mfma_f32_16x16x32_bf16 v[212:215], a[220:223], a[236:239], v[212:215]\n"
"v_fma_f32 v241, v241, v18, v18\n"
"s_add_u32 m0, s59, 102400\n"
"v_mfma_f32_16x16x32_bf16 v[216:219], a[208:211], a[232:235], v[216:219]\n"
"v_exp_f32 v243, v243\n"
"v_mfma_f32_16x16x32_bf16 v[216:219], a[224:227], a[236:239], v[216:219]\n"
"buffer_load_dwordx4 v52, s[4:7], s71 offen lds\n"
"v_fma_f32 v242, v242, v18, v18\n"
"v_mfma_f32_16x16x32_bf16 v[220:223], a[212:215], a[232:235], v[220:223]\n"
"v_exp_f32 v244, v244\n"
"v_mfma_f32_16x16x32_bf16 v[220:223], a[228:231], a[236:239], v[220:223]\n"
"s_add_u32 m0, s59, 106496\n"
"v_mfma_f32_16x16x32_bf16 v[208:211], a[104:107], a[240:243], v[208:211]\n"
"ds_read_b128 a[232:235], v9 offset:4096\n"
"v_fma_f32 v243, v243, v18, v18\n"
"v_mfma_f32_16x16x32_bf16 v[208:211], a[120:123], a[244:247], v[208:211]\n"
"ds_read_b128 a[236:239], v10 offset:4096\n"
"buffer_load_dwordx4 v53, s[4:7], s71 offen lds\n"
"v_mfma_f32_16x16x32_bf16 v[212:215], a[108:111], a[240:243], v[212:215]\n"
"v_exp_f32 v245, v245\n"
"v_mfma_f32_16x16x32_bf16 v[212:215], a[124:127], a[244:247], v[212:215]\n"
"v_fma_f32 v244, v244, v18, v18\n"
"s_add_u32 m0, s59, 110592\n"
"v_mfma_f32_16x16x32_bf16 v[216:219], a[112:115], a[240:243], v[216:219]\n"
"v_exp_f32 v246, v246\n"
"v_mfma_f32_16x16x32_bf16 v[216:219], a[128:131], a[244:247], v[216:219]\n"
"buffer_load_dwordx4 v55, s[4:7], s71 offen lds\n"
"v_fma_f32 v245, v245, v18, v18\n"
"v_mfma_f32_16x16x32_bf16 v[220:223], a[116:119], a[240:243], v[220:223]\n"
"v_exp_f32 v247, v247\n"
"v_mfma_f32_16x16x32_bf16 v[220:223], a[132:135], a[244:247], v[220:223]\n"
"s_waitcnt lgkmcnt(2)\n"
"s_add_u32 m0, s59, 114688\n"
"v_mfma_f32_16x16x32_bf16 v[208:211], a[136:139], a[248:251], v[208:211]\n"
"ds_read_b128 a[240:243], v9 offset:8192\n"
"v_fma_f32 v246, v246, v18, v18\n"
"v_mfma_f32_16x16x32_bf16 v[208:211], a[152:155], a[252:255], v[208:211]\n"
"ds_read_b128 a[244:247], v10 offset:8192\n"
"buffer_load_dwordx4 v5, s[4:7], s72 offen lds\n"
"v_mfma_f32_16x16x32_bf16 v[212:215], a[140:143], a[248:251], v[212:215]\n"
"v_fma_f32 v247, v247, v18, v18\n"
"v_fma_f32 v225, v65, -v20, v225\n"
"v_mfma_f32_16x16x32_bf16 v[212:215], a[156:159], a[252:255], v[212:215]\n"
"v_fma_f32 v226, v66, -v20, v226\n"
"s_add_u32 s98, s96, 0\n"
"v_mfma_f32_16x16x32_bf16 v[216:219], a[144:147], a[248:251], v[216:219]\n"
"v_rcp_f32 v240, v240\n"
"v_mfma_f32_16x16x32_bf16 v[216:219], a[160:163], a[252:255], v[216:219]\n"
"buffer_load_dwordx4 a[40:43], v2, s[24:27], s98 offen offset:0 sc1\n"
"v_mul_f32 v240, v240, v248\n"
"v_mfma_f32_16x16x32_bf16 v[220:223], a[148:151], a[248:251], v[220:223]\n"
"v_rcp_f32 v241, v241\n"
"v_mfma_f32_16x16x32_bf16 v[220:223], a[164:167], a[252:255], v[220:223]\n"
"v_mul_f32 v241, v241, v249\n"
"s_waitcnt lgkmcnt(2)\n"
"v_mfma_f32_16x16x32_bf16 v[192:195], a[168:171], a[232:235], v[192:195]\n"
"ds_read_b128 a[248:251], v9 offset:12288\n"
"v_cvt_pk_bf16_f32 v248, v240, v241\n"
"v_mfma_f32_16x16x32_bf16 v[192:195], a[184:187], a[236:239], v[192:195]\n"
"ds_read_b128 a[252:255], v10 offset:12288\n"
"buffer_load_dwordx4 a[44:47], v2, s[24:27], s98 offen offset:1024 sc1\n"
"v_mfma_f32_16x16x32_bf16 v[196:199], a[172:175], a[232:235], v[196:199]\n"
"v_rcp_f32 v242, v242\n"
"v_mfma_f32_16x16x32_bf16 v[196:199], a[188:191], a[236:239], v[196:199]\n"
"v_rcp_f32 v243, v243\n"
"v_mfma_f32_16x16x32_bf16 v[200:203], a[176:179], a[232:235], v[200:203]\n"
"v_rcp_f32 v244, v244\n"
"v_mfma_f32_16x16x32_bf16 v[200:203], a[192:195], a[236:239], v[200:203]\n"
"buffer_load_dwordx4 a[48:51], v2, s[24:27], s98 offen offset:2048 sc1\n"
"v_mul_f32 v242, v242, v250\n"
"v_mfma_f32_16x16x32_bf16 v[204:207], a[180:183], a[232:235], v[204:207]\n"
"v_rcp_f32 v245, v245\n"
"v_mfma_f32_16x16x32_bf16 v[204:207], a[196:199], a[236:239], v[204:207]\n"
"v_mul_f32 v243, v243, v251\n"
"s_waitcnt lgkmcnt(2)\n"
"v_mfma_f32_16x16x32_bf16 v[192:195], a[200:203], a[240:243], v[192:195]\n"
"ds_read_b128 a[232:235], v9 offset:16384\n"
"v_mul_f32 v244, v244, v252\n"
"v_mfma_f32_16x16x32_bf16 v[192:195], a[216:219], a[244:247], v[192:195]\n"
"ds_read_b128 a[236:239], v10 offset:16384\n"
"buffer_load_dwordx4 a[52:55], v2, s[24:27], s98 offen offset:3072 sc1\n"
"v_mfma_f32_16x16x32_bf16 v[196:199], a[204:207], a[240:243], v[196:199]\n"
"v_rcp_f32 v246, v246\n"
"v_mfma_f32_16x16x32_bf16 v[196:199], a[220:223], a[244:247], v[196:199]\n"
"v_mul_f32 v245, v245, v253\n"
"s_add_u32 s98, s96, 4096\n"
"v_mfma_f32_16x16x32_bf16 v[200:203], a[208:211], a[240:243], v[200:203]\n"
"v_rcp_f32 v247, v247\n"
"v_mfma_f32_16x16x32_bf16 v[200:203], a[224:227], a[244:247], v[200:203]\n"
"buffer_load_dwordx4 a[56:59], v2, s[24:27], s98 offen offset:0 sc1\n"
"v_mul_f32 v246, v246, v254\n"
"v_mfma_f32_16x16x32_bf16 v[204:207], a[212:215], a[240:243], v[204:207]\n"
"v_mul_f32 v247, v247, v255\n"
"v_cvt_pk_bf16_f32 v249, v242, v243\n"
"v_mfma_f32_16x16x32_bf16 v[204:207], a[228:231], a[244:247], v[204:207]\n"
"v_cvt_pk_bf16_f32 v250, v244, v245\n"
"s_waitcnt lgkmcnt(2)\n"
"v_mfma_f32_16x16x32_bf16 v[192:195], a[104:107], a[248:251], v[192:195]\n"
"ds_read_b128 a[240:243], v9 offset:20480\n"
"v_cvt_pk_bf16_f32 v251, v246, v247\n"
"v_mfma_f32_16x16x32_bf16 v[192:195], a[120:123], a[252:255], v[192:195]\n"
"ds_read_b128 a[244:247], v10 offset:20480\n"
"buffer_load_dwordx4 a[60:63], v2, s[24:27], s98 offen offset:1024 sc1\n"
"v_mfma_f32_16x16x32_bf16 v[196:199], a[108:111], a[248:251], v[196:199]\n"
"buffer_store_dwordx4 v[248:251], v2, s[20:23], s76 offen sc1\n"
"v_mfma_f32_16x16x32_bf16 v[196:199], a[124:127], a[252:255], v[196:199]\n"
"v_fma_f32 v227, v67, -v20, v227\n"
"s_add_u32 s76, s76, 116736\n"
"v_mfma_f32_16x16x32_bf16 v[200:203], a[112:115], a[248:251], v[200:203]\n"
"v_fma_f32 v232, v72, -v20, v232\n"
"v_fma_f32 v233, v73, -v20, v233\n"
"v_mfma_f32_16x16x32_bf16 v[200:203], a[128:131], a[252:255], v[200:203]\n"
"buffer_load_dwordx4 a[64:67], v2, s[24:27], s98 offen offset:2048 sc1\n"
"v_fma_f32 v234, v74, -v20, v234\n"
"v_mfma_f32_16x16x32_bf16 v[204:207], a[116:119], a[248:251], v[204:207]\n"
"v_fma_f32 v235, v75, -v20, v235\n"
"v_fma_f32 v228, v68, -v20, v228\n"
"v_mfma_f32_16x16x32_bf16 v[204:207], a[132:135], a[252:255], v[204:207]\n"
"s_waitcnt vmcnt(29) lgkmcnt(0)\n"
"s_barrier\n"
"v_mfma_f32_16x16x32_bf16 v[192:195], a[136:139], a[232:235], v[192:195]\n"
"ds_read_b128 a[248:251], v9 offset:24576\n"
"s_add_u32 m0, s59, 118784\n"
"v_mfma_f32_16x16x32_bf16 v[192:195], a[152:155], a[236:239], v[192:195]\n"
"ds_read_b128 a[252:255], v10 offset:24576\n"
"buffer_load_dwordx4 v52, s[4:7], s72 offen lds\n"
"v_mfma_f32_16x16x32_bf16 v[196:199], a[140:143], a[232:235], v[196:199]\n"
"v_fma_f32 v229, v69, -v20, v229\n"
"v_fma_f32 v230, v70, -v20, v230\n"
"v_mfma_f32_16x16x32_bf16 v[196:199], a[156:159], a[236:239], v[196:199]\n"
"v_fma_f32 v231, v71, -v20, v231\n"
"s_add_u32 m0, s59, 122880\n"
"v_mfma_f32_16x16x32_bf16 v[200:203], a[144:147], a[232:235], v[200:203]\n"
"v_fma_f32 v236, v76, -v20, v236\n"
"v_fma_f32 v237, v77, -v20, v237\n"
"v_mfma_f32_16x16x32_bf16 v[200:203], a[160:163], a[236:239], v[200:203]\n"
"buffer_load_dwordx4 v53, s[4:7], s72 offen lds\n"
"v_fma_f32 v238, v78, -v20, v238\n"
"v_mfma_f32_16x16x32_bf16 v[204:207], a[148:151], a[232:235], v[204:207]\n"
"v_fma_f32 v239, v79, -v20, v239\n"
"v_mul_f32 v232, v224, v232\n"
"v_mfma_f32_16x16x32_bf16 v[204:207], a[164:167], a[236:239], v[204:207]\n"
"s_add_u32 m0, s59, 126976\n"
"v_mfma_f32_16x16x32_bf16 v[176:179], a[168:171], a[240:243], v[176:179]\n"
"ds_read_b128 a[232:235], v9 offset:28672\n"
"v_mul_f32 v233, v225, v233\n"
"v_mfma_f32_16x16x32_bf16 v[176:179], a[184:187], a[244:247], v[176:179]\n"
"ds_read_b128 a[236:239], v10 offset:28672\n"
"buffer_load_dwordx4 v55, s[4:7], s72 offen lds\n"
"v_mfma_f32_16x16x32_bf16 v[180:183], a[172:175], a[240:243], v[180:183]\n"
"v_mul_f32 v224, v224, v21\n"
"v_mul_f32 v225, v225, v21\n"
"v_mfma_f32_16x16x32_bf16 v[180:183], a[188:191], a[244:247], v[180:183]\n"
"v_mul_f32 v234, v226, v234\n"
"s_add_u32 m0, s59, 131072\n"
"v_mfma_f32_16x16x32_bf16 v[184:187], a[176:179], a[240:243], v[184:187]\n"
"v_mul_f32 v235, v227, v235\n"
"v_mul_f32 v226, v226, v21\n"
"v_mfma_f32_16x16x32_bf16 v[184:187], a[192:195], a[244:247], v[184:187]\n"
"buffer_load_dwordx4 v5, s[4:7], s73 offen lds\n"
"v_mul_f32 v227, v227, v21\n"
"v_mfma_f32_16x16x32_bf16 v[188:191], a[180:183], a[240:243], v[188:191]\n"
"v_mul_f32 v236, v228, v236\n"
"v_mul_f32 v237, v229, v237\n"
"v_mfma_f32_16x16x32_bf16 v[188:191], a[196:199], a[244:247], v[188:191]\n"
"s_waitcnt lgkmcnt(2)\n"
"s_add_u32 m0, s59, 135168\n"
"v_mfma_f32_16x16x32_bf16 v[176:179], a[200:203], a[248:251], v[176:179]\n"
"ds_read_b128 a[240:243], v9 offset:32768\n"
"v_mul_f32 v228, v228, v21\n"
"v_mfma_f32_16x16x32_bf16 v[176:179], a[216:219], a[252:255], v[176:179]\n"
"ds_read_b128 a[244:247], v10 offset:32768\n"
"buffer_load_dwordx4 v52, s[4:7], s73 offen lds\n"
"v_mfma_f32_16x16x32_bf16 v[180:183], a[204:207], a[248:251], v[180:183]\n"
"v_mul_f32 v229, v229, v21\n"
"v_mul_f32 v238, v230, v238\n"
"v_mfma_f32_16x16x32_bf16 v[180:183], a[220:223], a[252:255], v[180:183]\n"
"v_mul_f32 v239, v231, v239\n"
"s_add_u32 m0, s59, 139264\n"
"v_mfma_f32_16x16x32_bf16 v[184:187], a[208:211], a[248:251], v[184:187]\n"
"v_mul_f32 v230, v230, v21\n"
"v_mul_f32 v231, v231, v21\n"
"v_mfma_f32_16x16x32_bf16 v[184:187], a[224:227], a[252:255], v[184:187]\n"
"buffer_load_dwordx4 v53, s[4:7], s73 offen lds\n"
"v_fma_f32 v208, v64, -v24, v208\n"
"v_mfma_f32_16x16x32_bf16 v[188:191], a[212:215], a[248:251], v[188:191]\n"
"v_exp_f32 v224, v224\n"
"v_mfma_f32_16x16x32_bf16 v[188:191], a[228:231], a[252:255], v[188:191]\n"
"v_fma_f32 v224, v224, v22, v22\n"
"s_waitcnt lgkmcnt(2)\n"
"v_mfma_f32_16x16x32_bf16 v[176:179], a[104:107], a[232:235], v[176:179]\n"
"ds_read_b128 a[248:251], v9 offset:36864\n"
"v_fma_f32 v209, v65, -v24, v209\n"
"v_mfma_f32_16x16x32_bf16 v[176:179], a[120:123], a[236:239], v[176:179]\n"
"ds_read_b128 a[252:255], v10 offset:36864\n"
"buffer_load_dwordx4 a[68:71], v2, s[24:27], s98 offen offset:3072 sc1\n"
"v_mfma_f32_16x16x32_bf16 v[180:183], a[108:111], a[232:235], v[180:183]\n"
"v_exp_f32 v225, v225\n"
"v_mfma_f32_16x16x32_bf16 v[180:183], a[124:127], a[236:239], v[180:183]\n"
"v_fma_f32 v225, v225, v22, v22\n"
"s_add_u32 s98, s96, 8192\n"
"v_mfma_f32_16x16x32_bf16 v[184:187], a[112:115], a[232:235], v[184:187]\n"
"v_exp_f32 v226, v226\n"
"v_mfma_f32_16x16x32_bf16 v[184:187], a[128:131], a[236:239], v[184:187]\n"
"buffer_load_dwordx4 a[72:75], v2, s[24:27], s98 offen offset:0 sc1\n"
"v_fma_f32 v226, v226, v22, v22\n"
"v_mfma_f32_16x16x32_bf16 v[188:191], a[116:119], a[232:235], v[188:191]\n"
"v_exp_f32 v227, v227\n"
"v_mfma_f32_16x16x32_bf16 v[188:191], a[132:135], a[236:239], v[188:191]\n"
"v_fma_f32 v227, v227, v22, v22\n"
"s_waitcnt lgkmcnt(2)\n"
"v_mfma_f32_16x16x32_bf16 v[176:179], a[136:139], a[240:243], v[176:179]\n"
"ds_read_b128 a[232:235], v9 offset:40960\n"
"v_fma_f32 v210, v66, -v24, v210\n"
"v_mfma_f32_16x16x32_bf16 v[176:179], a[152:155], a[244:247], v[176:179]\n"
"ds_read_b128 a[236:239], v10 offset:40960\n"
"buffer_load_dwordx4 a[76:79], v2, s[24:27], s98 offen offset:1024 sc1\n"
"v_mfma_f32_16x16x32_bf16 v[180:183], a[140:143], a[240:243], v[180:183]\n"
"v_exp_f32 v228, v228\n"
"v_mfma_f32_16x16x32_bf16 v[180:183], a[156:159], a[244:247], v[180:183]\n"
"v_exp_f32 v229, v229\n"
"v_mfma_f32_16x16x32_bf16 v[184:187], a[144:147], a[240:243], v[184:187]\n"
"v_exp_f32 v230, v230\n"
"v_mfma_f32_16x16x32_bf16 v[184:187], a[160:163], a[244:247], v[184:187]\n"
"buffer_load_dwordx4 a[80:83], v2, s[24:27], s98 offen offset:2048 sc1\n"
"v_fma_f32 v228, v228, v22, v22\n"
"v_mfma_f32_16x16x32_bf16 v[188:191], a[148:151], a[240:243], v[188:191]\n"
"v_exp_f32 v231, v231\n"
"v_mfma_f32_16x16x32_bf16 v[188:191], a[164:167], a[244:247], v[188:191]\n"
"s_waitcnt vmcnt(27) lgkmcnt(0)\n"
"s_barrier\n"
"v_mfma_f32_16x16x32_bf16 v[160:163], a[168:171], a[248:251], v[160:163]\n"
"ds_read_b128 a[240:243], v7 offset:0\n"
"s_add_u32 m0, s59, 143360\n"
"v_mfma_f32_16x16x32_bf16 v[160:163], a[184:187], a[252:255], v[160:163]\n"
"ds_read_b128 a[244:247], v8 offset:0\n"
"buffer_load_dwordx4 v55, s[4:7], s73 offen lds\n"
"v_mfma_f32_16x16x32_bf16 v[164:167], a[172:175], a[248:251], v[164:167]\n"
"v_fma_f32 v229, v229, v22, v22\n"
"v_fma_f32 v230, v230, v22, v22\n"
"v_mfma_f32_16x16x32_bf16 v[164:167], a[188:191], a[252:255], v[164:167]\n"
"v_fma_f32 v231, v231, v22, v22\n"
"s_add_u32 m0, s59, 147456\n"
"v_mfma_f32_16x16x32_bf16 v[168:171], a[176:179], a[248:251], v[168:171]\n"
"v_rcp_f32 v224, v224\n"
"v_mfma_f32_16x16x32_bf16 v[168:171], a[192:195], a[252:255], v[168:171]\n"
"buffer_load_dwordx4 v5, s[4:7], s74 offen lds\n"
"v_mul_f32 v224, v224, v232\n"
"v_mfma_f32_16x16x32_bf16 v[172:175], a[180:183], a[248:251], v[172:175]\n"
"v_rcp_f32 v225, v225\n"
"v_mfma_f32_16x16x32_bf16 v[172:175], a[196:199], a[252:255], v[172:175]\n"
"s_add_u32 m0, s59, 151552\n"
"v_mfma_f32_16x16x32_bf16 v[160:163], a[200:203], a[232:235], v[160:163]\n"
"ds_read_b128 a[248:251], v7 offset:4096\n"
"v_mul_f32 v225, v225, v233\n"
"v_mfma_f32_16x16x32_bf16 v[160:163], a[216:219], a[236:239], v[160:163]\n"
"ds_read_b128 a[252:255], v8 offset:4096\n"
"buffer_load_dwordx4 v52, s[4:7], s74 offen lds\n"
"v_mfma_f32_16x16x32_bf16 v[164:167], a[204:207], a[232:235], v[164:167]\n"
"v_rcp_f32 v226, v226\n"
"v_mfma_f32_16x16x32_bf16 v[164:167], a[220:223], a[236:239], v[164:167]\n"
"v_mul_f32 v226, v226, v234\n"
"s_add_u32 m0, s59, 155648\n"
"v_mfma_f32_16x16x32_bf16 v[168:171], a[208:211], a[232:235], v[168:171]\n"
"v_rcp_f32 v227, v227\n"
"v_mfma_f32_16x16x32_bf16 v[168:171], a[224:227], a[236:239], v[168:171]\n"
"buffer_load_dwordx4 v53, s[4:7], s74 offen lds\n"
"v_mul_f32 v227, v227, v235\n"
"v_mfma_f32_16x16x32_bf16 v[172:175], a[212:215], a[232:235], v[172:175]\n"
"v_rcp_f32 v228, v228\n"
"v_mfma_f32_16x16x32_bf16 v[172:175], a[228:231], a[236:239], v[172:175]\n"
"s_waitcnt lgkmcnt(2)\n"
"s_add_u32 m0, s59, 159744\n"
"v_mfma_f32_16x16x32_bf16 v[160:163], a[104:107], a[240:243], v[160:163]\n"
"ds_read_b128 a[232:235], v7 offset:8192\n"
"v_mul_f32 v228, v228, v236\n"
"v_mfma_f32_16x16x32_bf16 v[160:163], a[120:123], a[244:247], v[160:163]\n"
"ds_read_b128 a[236:239], v8 offset:8192\n"
"buffer_load_dwordx4 v55, s[4:7], s74 offen lds\n"
"v_mfma_f32_16x16x32_bf16 v[164:167], a[108:111], a[240:243], v[164:167]\n"
"v_rcp_f32 v229, v229\n"
"v_mfma_f32_16x16x32_bf16 v[164:167], a[124:127], a[244:247], v[164:167]\n"
"v_rcp_f32 v230, v230\n"
"v_mfma_f32_16x16x32_bf16 v[168:171], a[112:115], a[240:243], v[168:171]\n"
"v_rcp_f32 v231, v231\n"
"v_mfma_f32_16x16x32_bf16 v[168:171], a[128:131], a[244:247], v[168:171]\n"
"buffer_load_dwordx4 a[84:87], v2, s[24:27], s98 offen offset:3072 sc1\n"
"v_mul_f32 v229, v229, v237\n"
"v_mfma_f32_16x16x32_bf16 v[172:175], a[116:119], a[240:243], v[172:175]\n"
"v_mul_f32 v230, v230, v238\n"
"v_mul_f32 v231, v231, v239\n"
"v_mfma_f32_16x16x32_bf16 v[172:175], a[132:135], a[244:247], v[172:175]\n"
"s_waitcnt lgkmcnt(2)\n"
"s_add_u32 s98, s96, 12288\n"
"v_mfma_f32_16x16x32_bf16 v[160:163], a[136:139], a[248:251], v[160:163]\n"
"ds_read_b128 a[240:243], v7 offset:12288\n"
"v_cvt_pk_bf16_f32 v232, v224, v225\n"
"v_mfma_f32_16x16x32_bf16 v[160:163], a[152:155], a[252:255], v[160:163]\n"
"ds_read_b128 a[244:247], v8 offset:12288\n"
"buffer_load_dwordx4 a[88:91], v2, s[24:27], s98 offen offset:0 sc1\n"
"v_mfma_f32_16x16x32_bf16 v[164:167], a[140:143], a[248:251], v[164:167]\n"
"v_cvt_pk_bf16_f32 v233, v226, v227\n"
"v_cvt_pk_bf16_f32 v234, v228, v229\n"
"v_mfma_f32_16x16x32_bf16 v[164:167], a[156:159], a[252:255], v[164:167]\n"
"v_cvt_pk_bf16_f32 v235, v230, v231\n"
"v_fma_f32 v211, v67, -v24, v211\n"
"v_mfma_f32_16x16x32_bf16 v[168:171], a[144:147], a[248:251], v[168:171]\n"
"buffer_store_dwordx4 v[232:235], v2, s[20:23], s76 offen sc1\n"
"v_mfma_f32_16x16x32_bf16 v[168:171], a[160:163], a[252:255], v[168:171]\n"
"buffer_load_dwordx4 a[92:95], v2, s[24:27], s98 offen offset:1024 sc1\n"
"s_add_u32 s76, s76, 116736\n"
"v_mfma_f32_16x16x32_bf16 v[172:175], a[148:151], a[248:251], v[172:175]\n"
"v_fma_f32 v216, v72, -v24, v216\n"
"v_fma_f32 v217, v73, -v24, v217\n"
"v_mfma_f32_16x16x32_bf16 v[172:175], a[164:167], a[252:255], v[172:175]\n"
"v_fma_f32 v218, v74, -v24, v218\n"
"s_waitcnt lgkmcnt(2)\n"
"v_mfma_f32_16x16x32_bf16 v[144:147], a[168:171], a[232:235], v[144:147]\n"
"ds_read_b128 a[248:251], v7 offset:16384\n"
"v_fma_f32 v219, v75, -v24, v219\n"
"v_mfma_f32_16x16x32_bf16 v[144:147], a[184:187], a[236:239], v[144:147]\n"
"ds_read_b128 a[252:255], v8 offset:16384\n"
"buffer_load_dwordx4 a[96:99], v2, s[24:27], s98 offen offset:2048 sc1\n"
"v_mfma_f32_16x16x32_bf16 v[148:151], a[172:175], a[232:235], v[148:151]\n"
"v_fma_f32 v212, v68, -v24, v212\n"
"v_fma_f32 v213, v69, -v24, v213\n"
"v_mfma_f32_16x16x32_bf16 v[148:151], a[188:191], a[236:239], v[148:151]\n"
"v_fma_f32 v214, v70, -v24, v214\n"
"v_fma_f32 v215, v71, -v24, v215\n"
"v_mfma_f32_16x16x32_bf16 v[152:155], a[176:179], a[232:235], v[152:155]\n"
"v_fma_f32 v220, v76, -v24, v220\n"
"v_fma_f32 v221, v77, -v24, v221\n"
"v_mfma_f32_16x16x32_bf16 v[152:155], a[192:195], a[236:239], v[152:155]\n"
"buffer_load_dwordx4 a[100:103], v2, s[24:27], s98 offen offset:3072 sc1\n"
"v_fma_f32 v222, v78, -v24, v222\n"
"v_mfma_f32_16x16x32_bf16 v[156:159], a[180:183], a[232:235], v[156:159]\n"
"v_fma_f32 v223, v79, -v24, v223\n"
"v_mul_f32 v216, v208, v216\n"
"v_mfma_f32_16x16x32_bf16 v[156:159], a[196:199], a[236:239], v[156:159]\n"
"s_waitcnt lgkmcnt(2)\n"
"s_add_u32 s98, s96, 16384\n"
"v_mfma_f32_16x16x32_bf16 v[144:147], a[200:203], a[240:243], v[144:147]\n"
"ds_read_b128 a[232:235], v7 offset:20480\n"
"v_mul_f32 v217, v209, v217\n"
"v_mfma_f32_16x16x32_bf16 v[144:147], a[216:219], a[244:247], v[144:147]\n"
"ds_read_b128 a[236:239], v8 offset:20480\n"
"buffer_load_dwordx4 v[252:255], v2, s[24:27], s98 offen offset:0 sc1\n"
"v_mfma_f32_16x16x32_bf16 v[148:151], a[204:207], a[240:243], v[148:151]\n"
"v_mul_f32 v208, v208, v25\n"
"v_mul_f32 v209, v209, v25\n"
"v_mfma_f32_16x16x32_bf16 v[148:151], a[220:223], a[244:247], v[148:151]\n"
"v_mul_f32 v218, v210, v218\n"
"v_mul_f32 v219, v211, v219\n"
"v_mfma_f32_16x16x32_bf16 v[152:155], a[208:211], a[240:243], v[152:155]\n"
"v_mul_f32 v210, v210, v25\n"
"v_mul_f32 v211, v211, v25\n"
"v_mfma_f32_16x16x32_bf16 v[152:155], a[224:227], a[244:247], v[152:155]\n"
"buffer_load_dwordx4 v[248:251], v2, s[24:27], s98 offen offset:1024 sc1\n"
"v_mul_f32 v220, v212, v220\n"
"v_mfma_f32_16x16x32_bf16 v[156:159], a[212:215], a[240:243], v[156:159]\n"
"v_mul_f32 v221, v213, v221\n"
"v_mul_f32 v212, v212, v25\n"
"v_mfma_f32_16x16x32_bf16 v[156:159], a[228:231], a[244:247], v[156:159]\n"
"s_waitcnt vmcnt(31) lgkmcnt(0)\n"
"s_barrier\n"
"v_mfma_f32_16x16x32_bf16 v[144:147], a[104:107], a[248:251], v[144:147]\n"
"ds_read_b128 a[240:243], v7 offset:24576\n"
"v_mul_f32 v213, v213, v25\n"
"v_mfma_f32_16x16x32_bf16 v[144:147], a[120:123], a[252:255], v[144:147]\n"
"ds_read_b128 a[244:247], v8 offset:24576\n"
"buffer_load_dwordx4 v[244:247], v2, s[24:27], s98 offen offset:2048 sc1\n"
"v_mfma_f32_16x16x32_bf16 v[148:151], a[108:111], a[248:251], v[148:151]\n"
"v_mul_f32 v222, v214, v222\n"
"v_mul_f32 v223, v215, v223\n"
"v_mfma_f32_16x16x32_bf16 v[148:151], a[124:127], a[252:255], v[148:151]\n"
"v_mul_f32 v214, v214, v25\n"
"v_mul_f32 v215, v215, v25\n"
"v_mfma_f32_16x16x32_bf16 v[152:155], a[112:115], a[248:251], v[152:155]\n"
"v_exp_f32 v208, v208\n"
"v_mfma_f32_16x16x32_bf16 v[152:155], a[128:131], a[252:255], v[152:155]\n"
"buffer_load_dwordx4 v[240:243], v2, s[24:27], s98 offen offset:3072 sc1\n"
"v_fma_f32 v208, v208, v26, v26\n"
"v_mfma_f32_16x16x32_bf16 v[156:159], a[116:119], a[248:251], v[156:159]\n"
"v_exp_f32 v209, v209\n"
"v_mfma_f32_16x16x32_bf16 v[156:159], a[132:135], a[252:255], v[156:159]\n"
"s_add_u32 s98, s96, 20480\n"
"v_mfma_f32_16x16x32_bf16 v[144:147], a[136:139], a[232:235], v[144:147]\n"
"ds_read_b128 a[248:251], v7 offset:28672\n"
"v_fma_f32 v209, v209, v26, v26\n"
"v_mfma_f32_16x16x32_bf16 v[144:147], a[152:155], a[236:239], v[144:147]\n"
"ds_read_b128 a[252:255], v8 offset:28672\n"
"buffer_load_dwordx4 v[236:239], v2, s[24:27], s98 offen offset:0 sc1\n"
"v_mfma_f32_16x16x32_bf16 v[148:151], a[140:143], a[232:235], v[148:151]\n"
"v_exp_f32 v210, v210\n"
"v_mfma_f32_16x16x32_bf16 v[148:151], a[156:159], a[236:239], v[148:151]\n"
"v_exp_f32 v211, v211\n"
"v_mfma_f32_16x16x32_bf16 v[152:155], a[144:147], a[232:235], v[152:155]\n"
"v_exp_f32 v212, v212\n"
"v_mfma_f32_16x16x32_bf16 v[152:155], a[160:163], a[236:239], v[152:155]\n"
"buffer_load_dwordx4 v[232:235], v2, s[24:27], s98 offen offset:1024 sc1\n"
"v_fma_f32 v210, v210, v26, v26\n"
"v_mfma_f32_16x16x32_bf16 v[156:159], a[148:151], a[232:235], v[156:159]\n"
"v_exp_f32 v213, v213\n"
"v_mfma_f32_16x16x32_bf16 v[156:159], a[164:167], a[236:239], v[156:159]\n"
"v_fma_f32 v211, v211, v26, v26\n"
"s_waitcnt lgkmcnt(2)\n"
"v_mfma_f32_16x16x32_bf16 v[128:131], a[168:171], a[240:243], v[128:131]\n"
"ds_read_b128 a[232:235], v7 offset:32768\n"
"v_fma_f32 v212, v212, v26, v26\n"
"v_mfma_f32_16x16x32_bf16 v[128:131], a[184:187], a[244:247], v[128:131]\n"
"ds_read_b128 a[236:239], v8 offset:32768\n"
"buffer_load_dwordx4 v[228:231], v2, s[24:27], s98 offen offset:2048 sc1\n"
"v_mfma_f32_16x16x32_bf16 v[132:135], a[172:175], a[240:243], v[132:135]\n"
"v_exp_f32 v214, v214\n"
"v_mfma_f32_16x16x32_bf16 v[132:135], a[188:191], a[244:247], v[132:135]\n"
"v_exp_f32 v215, v215\n"
"v_mfma_f32_16x16x32_bf16 v[136:139], a[176:179], a[240:243], v[136:139]\n"
"v_fma_f32 v213, v213, v26, v26\n"
"v_fma_f32 v214, v214, v26, v26\n"
"v_mfma_f32_16x16x32_bf16 v[136:139], a[192:195], a[244:247], v[136:139]\n"
"buffer_load_dwordx4 v[224:227], v2, s[24:27], s98 offen offset:3072 sc1\n"
"v_fma_f32 v215, v215, v26, v26\n"
"v_mfma_f32_16x16x32_bf16 v[140:143], a[180:183], a[240:243], v[140:143]\n"
"v_rcp_f32 v208, v208\n"
"v_mfma_f32_16x16x32_bf16 v[140:143], a[196:199], a[244:247], v[140:143]\n"
"v_mul_f32 v208, v208, v216\n"
"s_waitcnt lgkmcnt(2)\n"
"v_mfma_f32_16x16x32_bf16 v[128:131], a[200:203], a[248:251], v[128:131]\n"
"ds_read_b128 a[240:243], v7 offset:36864\n"
"v_fma_f32 v192, v64, -v36, v192\n"
"v_mfma_f32_16x16x32_bf16 v[128:131], a[216:219], a[252:255], v[128:131]\n"
"ds_read_b128 a[244:247], v8 offset:36864\n"
"v_fma_f32 v193, v65, -v36, v193\n"
"v_mfma_f32_16x16x32_bf16 v[132:135], a[204:207], a[248:251], v[132:135]\n"
"v_rcp_f32 v209, v209\n"
"v_mfma_f32_16x16x32_bf16 v[132:135], a[220:223], a[252:255], v[132:135]\n"
"v_rcp_f32 v210, v210\n"
"v_mfma_f32_16x16x32_bf16 v[136:139], a[208:211], a[248:251], v[136:139]\n"
"v_rcp_f32 v211, v211\n"
"v_mfma_f32_16x16x32_bf16 v[136:139], a[224:227], a[252:255], v[136:139]\n"
"v_rcp_f32 v212, v212\n"
"v_mfma_f32_16x16x32_bf16 v[140:143], a[212:215], a[248:251], v[140:143]\n"
"v_rcp_f32 v213, v213\n"
"v_mfma_f32_16x16x32_bf16 v[140:143], a[228:231], a[252:255], v[140:143]\n"
"v_mul_f32 v209, v209, v217\n"
"s_waitcnt lgkmcnt(2)\n"
"v_mfma_f32_16x16x32_bf16 v[128:131], a[104:107], a[232:235], v[128:131]\n"
"ds_read_b128 a[248:251], v7 offset:40960\n"
"v_mul_f32 v210, v210, v218\n"
"v_mfma_f32_16x16x32_bf16 v[128:131], a[120:123], a[236:239], v[128:131]\n"
"ds_read_b128 a[252:255], v8 offset:40960\n"
"v_mul_f32 v211, v211, v219\n"
"v_mfma_f32_16x16x32_bf16 v[132:135], a[108:111], a[232:235], v[132:135]\n"
"v_rcp_f32 v214, v214\n"
"v_mfma_f32_16x16x32_bf16 v[132:135], a[124:127], a[236:239], v[132:135]\n"
"v_rcp_f32 v215, v215\n"
"v_mfma_f32_16x16x32_bf16 v[136:139], a[112:115], a[232:235], v[136:139]\n"
"v_mul_f32 v212, v212, v220\n"
"v_mul_f32 v213, v213, v221\n"
"v_mfma_f32_16x16x32_bf16 v[136:139], a[128:131], a[236:239], v[136:139]\n"
"v_mul_f32 v214, v214, v222\n"
"v_mul_f32 v215, v215, v223\n"
"v_mfma_f32_16x16x32_bf16 v[140:143], a[116:119], a[232:235], v[140:143]\n"
"v_cvt_pk_bf16_f32 v216, v208, v209\n"
"v_cvt_pk_bf16_f32 v217, v210, v211\n"
"v_mfma_f32_16x16x32_bf16 v[140:143], a[132:135], a[236:239], v[140:143]\n"
"s_waitcnt vmcnt(23) lgkmcnt(0)\n"
"s_barrier\n"
"v_mfma_f32_16x16x32_bf16 v[128:131], a[136:139], a[240:243], v[128:131]\n"
"ds_read_b128 a[232:235], v9 offset:0\n"
"v_cvt_pk_bf16_f32 v218, v212, v213\n"
"v_mfma_f32_16x16x32_bf16 v[128:131], a[152:155], a[244:247], v[128:131]\n"
"ds_read_b128 a[236:239], v10 offset:0\n"
"v_cvt_pk_bf16_f32 v219, v214, v215\n"
"v_mfma_f32_16x16x32_bf16 v[132:135], a[140:143], a[240:243], v[132:135]\n"
"buffer_store_dwordx4 v[216:219], v2, s[20:23], s76 offen sc1\n"
"v_mfma_f32_16x16x32_bf16 v[132:135], a[156:159], a[244:247], v[132:135]\n"
"v_fma_f32 v194, v66, -v36, v194\n"
"s_add_u32 s76, s76, 116736\n"
"v_mfma_f32_16x16x32_bf16 v[136:139], a[144:147], a[240:243], v[136:139]\n"
"v_fma_f32 v195, v67, -v36, v195\n"
"v_fma_f32 v200, v72, -v36, v200\n"
"v_mfma_f32_16x16x32_bf16 v[136:139], a[160:163], a[244:247], v[136:139]\n"
"v_fma_f32 v201, v73, -v36, v201\n"
"v_fma_f32 v202, v74, -v36, v202\n"
"v_mfma_f32_16x16x32_bf16 v[140:143], a[148:151], a[240:243], v[140:143]\n"
"v_fma_f32 v203, v75, -v36, v203\n"
"v_fma_f32 v196, v68, -v36, v196\n"
"v_mfma_f32_16x16x32_bf16 v[140:143], a[164:167], a[244:247], v[140:143]\n"
"v_fma_f32 v197, v69, -v36, v197\n"
"v_mfma_f32_16x16x32_bf16 v[112:115], a[168:171], a[248:251], v[112:115]\n"
"ds_read_b128 a[240:243], v9 offset:4096\n"
"v_fma_f32 v198, v70, -v36, v198\n"
"v_mfma_f32_16x16x32_bf16 v[112:115], a[184:187], a[252:255], v[112:115]\n"
"ds_read_b128 a[244:247], v10 offset:4096\n"
"v_fma_f32 v199, v71, -v36, v199\n"
"v_mfma_f32_16x16x32_bf16 v[116:119], a[172:175], a[248:251], v[116:119]\n"
"v_fma_f32 v204, v76, -v36, v204\n"
"v_fma_f32 v205, v77, -v36, v205\n"
"v_mfma_f32_16x16x32_bf16 v[116:119], a[188:191], a[252:255], v[116:119]\n"
"v_fma_f32 v206, v78, -v36, v206\n"
"v_fma_f32 v207, v79, -v36, v207\n"
"v_mfma_f32_16x16x32_bf16 v[120:123], a[176:179], a[248:251], v[120:123]\n"
"v_mul_f32 v200, v192, v200\n"
"v_mul_f32 v201, v193, v201\n"
"v_mfma_f32_16x16x32_bf16 v[120:123], a[192:195], a[252:255], v[120:123]\n"
"v_mul_f32 v192, v192, v37\n"
"v_mul_f32 v193, v193, v37\n"
"v_mfma_f32_16x16x32_bf16 v[124:127], a[180:183], a[248:251], v[124:127]\n"
"v_mul_f32 v202, v194, v202\n"
"v_mul_f32 v203, v195, v203\n"
"v_mfma_f32_16x16x32_bf16 v[124:127], a[196:199], a[252:255], v[124:127]\n"
"v_mul_f32 v194, v194, v37\n"
"s_waitcnt lgkmcnt(2)\n"
"v_mfma_f32_16x16x32_bf16 v[112:115], a[200:203], a[232:235], v[112:115]\n"
"ds_read_b128 a[248:251], v9 offset:8192\n"
"v_mul_f32 v195, v195, v37\n"
"v_mfma_f32_16x16x32_bf16 v[112:115], a[216:219], a[236:239], v[112:115]\n"
"ds_read_b128 a[252:255], v10 offset:8192\n"
"v_mul_f32 v204, v196, v204\n"
"v_mfma_f32_16x16x32_bf16 v[116:119], a[204:207], a[232:235], v[116:119]\n"
"v_mul_f32 v205, v197, v205\n"
"v_mul_f32 v196, v196, v37\n"
"v_mfma_f32_16x16x32_bf16 v[116:119], a[220:223], a[236:239], v[116:119]\n"
"v_mul_f32 v197, v197, v37\n"
"v_mul_f32 v206, v198, v206\n"
"v_mfma_f32_16x16x32_bf16 v[120:123], a[208:211], a[232:235], v[120:123]\n"
"v_mul_f32 v207, v199, v207\n"
"v_mul_f32 v198, v198, v37\n"
"v_mfma_f32_16x16x32_bf16 v[120:123], a[224:227], a[236:239], v[120:123]\n"
"v_mul_f32 v199, v199, v37\n"
"v_fma_f32 v176, v64, -v40, v176\n"
"v_mfma_f32_16x16x32_bf16 v[124:127], a[212:215], a[232:235], v[124:127]\n"
"v_exp_f32 v192, v192\n"
"v_mfma_f32_16x16x32_bf16 v[124:127], a[228:231], a[236:239], v[124:127]\n"
"v_fma_f32 v192, v192, v38, v38\n"
"s_waitcnt lgkmcnt(2)\n"
"v_mfma_f32_16x16x32_bf16 v[112:115], a[104:107], a[240:243], v[112:115]\n"
"ds_read_b128 a[232:235], v9 offset:12288\n"
"v_fma_f32 v177, v65, -v40, v177\n"
"v_mfma_f32_16x16x32_bf16 v[112:115], a[120:123], a[244:247], v[112:115]\n"
"ds_read_b128 a[236:239], v10 offset:12288\n"
"v_fma_f32 v178, v66, -v40, v178\n"
"v_mfma_f32_16x16x32_bf16 v[116:119], a[108:111], a[240:243], v[116:119]\n"
"v_fma_f32 v179, v67, -v40, v179\n"
"s_add_u32 s98, s96, 24576\n"
"v_mfma_f32_16x16x32_bf16 v[116:119], a[124:127], a[244:247], v[116:119]\n"
"buffer_load_dwordx4 v[220:223], v2, s[24:27], s98 offen offset:0 sc1\n"
"v_fma_f32 v184, v72, -v40, v184\n"
"v_mfma_f32_16x16x32_bf16 v[120:123], a[112:115], a[240:243], v[120:123]\n"
"v_exp_f32 v193, v193\n"
"v_mfma_f32_16x16x32_bf16 v[120:123], a[128:131], a[244:247], v[120:123]\n"
"v_exp_f32 v194, v194\n"
"v_mfma_f32_16x16x32_bf16 v[124:127], a[116:119], a[240:243], v[124:127]\n"
"v_exp_f32 v195, v195\n"
"v_mfma_f32_16x16x32_bf16 v[124:127], a[132:135], a[244:247], v[124:127]\n"
"buffer_load_dwordx4 v[216:219], v2, s[24:27], s98 offen offset:1024 sc1\n"
"s_waitcnt lgkmcnt(2)\n"
"v_mfma_f32_16x16x32_bf16 v[112:115], a[136:139], a[248:251], v[112:115]\n"
"ds_read_b128 a[240:243], v9 offset:16384\n"
"v_fma_f32 v193, v193, v38, v38\n"
"v_mfma_f32_16x16x32_bf16 v[112:115], a[152:155], a[252:255], v[112:115]\n"
"ds_read_b128 a[244:247], v10 offset:16384\n"
"v_fma_f32 v194, v194, v38, v38\n"
"v_mfma_f32_16x16x32_bf16 v[116:119], a[140:143], a[248:251], v[116:119]\n"
"v_exp_f32 v196, v196\n"
"v_mfma_f32_16x16x32_bf16 v[116:119], a[156:159], a[252:255], v[116:119]\n"
"buffer_load_dwordx4 v[212:215], v2, s[24:27], s98 offen offset:2048 sc1\n"
"v_fma_f32 v195, v195, v38, v38\n"
"v_mfma_f32_16x16x32_bf16 v[120:123], a[144:147], a[248:251], v[120:123]\n"
"v_exp_f32 v197, v197\n"
"v_mfma_f32_16x16x32_bf16 v[120:123], a[160:163], a[252:255], v[120:123]\n"
"v_exp_f32 v198, v198\n"
"v_mfma_f32_16x16x32_bf16 v[124:127], a[148:151], a[248:251], v[124:127]\n"
"v_exp_f32 v199, v199\n"
"v_mfma_f32_16x16x32_bf16 v[124:127], a[164:167], a[252:255], v[124:127]\n"
"buffer_load_dwordx4 v[208:211], v2, s[24:27], s98 offen offset:3072 sc1\n"
"s_waitcnt lgkmcnt(2)\n"
"v_mfma_f32_16x16x32_bf16 v[96:99], a[168:171], a[232:235], v[96:99]\n"
"ds_read_b128 a[248:251], v9 offset:20480\n"
"v_fma_f32 v196, v196, v38, v38\n"
"v_mfma_f32_16x16x32_bf16 v[96:99], a[184:187], a[236:239], v[96:99]\n"
"ds_read_b128 a[252:255], v10 offset:20480\n"
"v_fma_f32 v197, v197, v38, v38\n"
"v_mfma_f32_16x16x32_bf16 v[100:103], a[172:175], a[232:235], v[100:103]\n"
"v_fma_f32 v198, v198, v38, v38\n"
"v_fma_f32 v199, v199, v38, v38\n"
"v_mfma_f32_16x16x32_bf16 v[100:103], a[188:191], a[236:239], v[100:103]\n"
"v_rcp_f32 v192, v192\n"
"v_mfma_f32_16x16x32_bf16 v[104:107], a[176:179], a[232:235], v[104:107]\n"
"v_rcp_f32 v193, v193\n"
"v_mfma_f32_16x16x32_bf16 v[104:107], a[192:195], a[236:239], v[104:107]\n"
"v_rcp_f32 v194, v194\n"
"v_mfma_f32_16x16x32_bf16 v[108:111], a[180:183], a[232:235], v[108:111]\n"
"v_rcp_f32 v195, v195\n"
"v_mfma_f32_16x16x32_bf16 v[108:111], a[196:199], a[236:239], v[108:111]\n"
"s_waitcnt vmcnt(19) lgkmcnt(0)\n"
"s_barrier\n"
"v_mfma_f32_16x16x32_bf16 v[96:99], a[200:203], a[240:243], v[96:99]\n"
"ds_read_b128 a[232:235], v9 offset:24576\n"
"v_mul_f32 v192, v192, v200\n"
"v_mfma_f32_16x16x32_bf16 v[96:99], a[216:219], a[244:247], v[96:99]\n"
"ds_read_b128 a[236:239], v10 offset:24576\n"
"v_mul_f32 v193, v193, v201\n"
"v_mfma_f32_16x16x32_bf16 v[100:103], a[204:207], a[240:243], v[100:103]\n"
"v_rcp_f32 v196, v196\n"
"v_mfma_f32_16x16x32_bf16 v[100:103], a[220:223], a[244:247], v[100:103]\n"
"v_rcp_f32 v197, v197\n"
"v_mfma_f32_16x16x32_bf16 v[104:107], a[208:211], a[240:243], v[104:107]\n"
"v_rcp_f32 v198, v198\n"
"v_mfma_f32_16x16x32_bf16 v[104:107], a[224:227], a[244:247], v[104:107]\n"
"v_rcp_f32 v199, v199\n"
"v_mfma_f32_16x16x32_bf16 v[108:111], a[212:215], a[240:243], v[108:111]\n"
"v_mul_f32 v194, v194, v202\n"
"v_mul_f32 v195, v195, v203\n"
"v_mfma_f32_16x16x32_bf16 v[108:111], a[228:231], a[244:247], v[108:111]\n"
"v_mul_f32 v196, v196, v204\n"
"v_mfma_f32_16x16x32_bf16 v[96:99], a[104:107], a[248:251], v[96:99]\n"
"ds_read_b128 a[240:243], v9 offset:28672\n"
"v_mul_f32 v197, v197, v205\n"
"v_mfma_f32_16x16x32_bf16 v[96:99], a[120:123], a[252:255], v[96:99]\n"
"ds_read_b128 a[244:247], v10 offset:28672\n"
"v_mul_f32 v198, v198, v206\n"
"v_mfma_f32_16x16x32_bf16 v[100:103], a[108:111], a[248:251], v[100:103]\n"
"v_mul_f32 v199, v199, v207\n"
"v_cvt_pk_bf16_f32 v200, v192, v193\n"
"v_mfma_f32_16x16x32_bf16 v[100:103], a[124:127], a[252:255], v[100:103]\n"
"v_cvt_pk_bf16_f32 v201, v194, v195\n"
"v_cvt_pk_bf16_f32 v202, v196, v197\n"
"v_mfma_f32_16x16x32_bf16 v[104:107], a[112:115], a[248:251], v[104:107]\n"
"v_cvt_pk_bf16_f32 v203, v198, v199\n"
"v_fma_f32 v185, v73, -v40, v185\n"
"v_mfma_f32_16x16x32_bf16 v[104:107], a[128:131], a[252:255], v[104:107]\n"
"buffer_store_dwordx4 v[200:203], v2, s[20:23], s76 offen sc1\n"
"v_mfma_f32_16x16x32_bf16 v[108:111], a[116:119], a[248:251], v[108:111]\n"
"v_fma_f32 v186, v74, -v40, v186\n"
"s_add_u32 s76, s76, 116736\n"
"v_mfma_f32_16x16x32_bf16 v[108:111], a[132:135], a[252:255], v[108:111]\n"
"v_fma_f32 v187, v75, -v40, v187\n"
"s_waitcnt lgkmcnt(2)\n"
"v_mfma_f32_16x16x32_bf16 v[96:99], a[136:139], a[232:235], v[96:99]\n"
"ds_read_b128 a[248:251], v9 offset:32768\n"
"v_fma_f32 v180, v68, -v40, v180\n"
"v_mfma_f32_16x16x32_bf16 v[96:99], a[152:155], a[236:239], v[96:99]\n"
"ds_read_b128 a[252:255], v10 offset:32768\n"
"v_fma_f32 v181, v69, -v40, v181\n"
"v_mfma_f32_16x16x32_bf16 v[100:103], a[140:143], a[232:235], v[100:103]\n"
"v_fma_f32 v182, v70, -v40, v182\n"
"v_fma_f32 v183, v71, -v40, v183\n"
"v_mfma_f32_16x16x32_bf16 v[100:103], a[156:159], a[236:239], v[100:103]\n"
"v_fma_f32 v188, v76, -v40, v188\n"
"v_fma_f32 v189, v77, -v40, v189\n"
"v_mfma_f32_16x16x32_bf16 v[104:107], a[144:147], a[232:235], v[104:107]\n"
"v_fma_f32 v190, v78, -v40, v190\n"
"v_fma_f32 v191, v79, -v40, v191\n"
"v_mfma_f32_16x16x32_bf16 v[104:107], a[160:163], a[236:239], v[104:107]\n"
"v_mul_f32 v184, v176, v184\n"
"v_mul_f32 v185, v177, v185\n"
"v_mfma_f32_16x16x32_bf16 v[108:111], a[148:151], a[232:235], v[108:111]\n"
"v_mul_f32 v176, v176, v41\n"
"v_mul_f32 v177, v177, v41\n"
"v_mfma_f32_16x16x32_bf16 v[108:111], a[164:167], a[236:239], v[108:111]\n"
"v_mul_f32 v186, v178, v186\n"
"s_waitcnt lgkmcnt(2)\n"
"v_mfma_f32_16x16x32_bf16 v[80:83], a[168:171], a[240:243], v[80:83]\n"
"ds_read_b128 a[232:235], v9 offset:36864\n"
"v_mul_f32 v187, v179, v187\n"
"v_mfma_f32_16x16x32_bf16 v[80:83], a[184:187], a[244:247], v[80:83]\n"
"ds_read_b128 a[236:239], v10 offset:36864\n"
"v_mul_f32 v178, v178, v41\n"
"v_mfma_f32_16x16x32_bf16 v[84:87], a[172:175], a[240:243], v[84:87]\n"
"v_mul_f32 v179, v179, v41\n"
"v_mul_f32 v188, v180, v188\n"
"v_mfma_f32_16x16x32_bf16 v[84:87], a[188:191], a[244:247], v[84:87]\n"
"v_mul_f32 v189, v181, v189\n"
"v_mul_f32 v180, v180, v41\n"
"v_mfma_f32_16x16x32_bf16 v[88:91], a[176:179], a[240:243], v[88:91]\n"
"v_mul_f32 v181, v181, v41\n"
"v_mul_f32 v190, v182, v190\n"
"v_mfma_f32_16x16x32_bf16 v[88:91], a[192:195], a[244:247], v[88:91]\n"
"v_mul_f32 v191, v183, v191\n"
"v_mul_f32 v182, v182, v41\n"
"v_mfma_f32_16x16x32_bf16 v[92:95], a[180:183], a[240:243], v[92:95]\n"
"v_mul_f32 v183, v183, v41\n"
"v_fma_f32 v160, v64, -v44, v160\n"
"v_mfma_f32_16x16x32_bf16 v[92:95], a[196:199], a[244:247], v[92:95]\n"
"v_fma_f32 v161, v65, -v44, v161\n"
"s_waitcnt lgkmcnt(2)\n"
"v_mfma_f32_16x16x32_bf16 v[80:83], a[200:203], a[248:251], v[80:83]\n"
"ds_read_b128 a[240:243], v9 offset:40960\n"
"v_fma_f32 v162, v66, -v44, v162\n"
"v_mfma_f32_16x16x32_bf16 v[80:83], a[216:219], a[252:255], v[80:83]\n"
"ds_read_b128 a[244:247], v10 offset:40960\n"
"v_fma_f32 v163, v67, -v44, v163\n"
"v_mfma_f32_16x16x32_bf16 v[84:87], a[204:207], a[248:251], v[84:87]\n"
"v_exp_f32 v176, v176\n"
"v_mfma_f32_16x16x32_bf16 v[84:87], a[220:223], a[252:255], v[84:87]\n"
"v_exp_f32 v177, v177\n"
"v_mfma_f32_16x16x32_bf16 v[88:91], a[208:211], a[248:251], v[88:91]\n"
"v_exp_f32 v178, v178\n"
"v_mfma_f32_16x16x32_bf16 v[88:91], a[224:227], a[252:255], v[88:91]\n"
"v_exp_f32 v179, v179\n"
"v_mfma_f32_16x16x32_bf16 v[92:95], a[212:215], a[248:251], v[92:95]\n"
"v_fma_f32 v176, v176, v42, v42\n"
"s_add_u32 s98, s96, 28672\n"
"v_mfma_f32_16x16x32_bf16 v[92:95], a[228:231], a[252:255], v[92:95]\n"
"buffer_load_dwordx4 v[204:207], v2, s[24:27], s98 offen offset:0 sc1\n"
"s_waitcnt lgkmcnt(2)\n"
"v_mfma_f32_16x16x32_bf16 v[80:83], a[104:107], a[232:235], v[80:83]\n"
"v_exp_f32 v180, v180\n"
"v_mfma_f32_16x16x32_bf16 v[80:83], a[120:123], a[236:239], v[80:83]\n"
"v_exp_f32 v181, v181\n"
"v_mfma_f32_16x16x32_bf16 v[84:87], a[108:111], a[232:235], v[84:87]\n"
"v_exp_f32 v182, v182\n"
"v_mfma_f32_16x16x32_bf16 v[84:87], a[124:127], a[236:239], v[84:87]\n"
"buffer_load_dwordx4 v[200:203], v2, s[24:27], s98 offen offset:1024 sc1\n"
"v_fma_f32 v177, v177, v42, v42\n"
"v_mfma_f32_16x16x32_bf16 v[88:91], a[112:115], a[232:235], v[88:91]\n"
"v_exp_f32 v183, v183\n"
"v_mfma_f32_16x16x32_bf16 v[88:91], a[128:131], a[236:239], v[88:91]\n"
"v_fma_f32 v178, v178, v42, v42\n"
"s_add_u32 s98, s97, 0\n"
"v_mfma_f32_16x16x32_bf16 v[92:95], a[116:119], a[232:235], v[92:95]\n"
"v_fma_f32 v179, v179, v42, v42\n"
"v_fma_f32 v180, v180, v42, v42\n"
"v_mfma_f32_16x16x32_bf16 v[92:95], a[132:135], a[236:239], v[92:95]\n"
"buffer_load_dwordx4 v[196:199], v2, s[24:27], s98 offen offset:0 sc1\n"
"s_waitcnt lgkmcnt(0)\n"
"v_mfma_f32_16x16x32_bf16 v[80:83], a[136:139], a[240:243], v[80:83]\n"
"v_fma_f32 v181, v181, v42, v42\n"
"v_fma_f32 v182, v182, v42, v42\n"
"v_mfma_f32_16x16x32_bf16 v[80:83], a[152:155], a[244:247], v[80:83]\n"
"v_fma_f32 v183, v183, v42, v42\n"
"v_fma_f32 v168, v72, -v44, v168\n"
"v_mfma_f32_16x16x32_bf16 v[84:87], a[140:143], a[240:243], v[84:87]\n"
"v_rcp_f32 v176, v176\n"
"v_mfma_f32_16x16x32_bf16 v[84:87], a[156:159], a[244:247], v[84:87]\n"
"buffer_load_dwordx4 v[192:195], v2, s[24:27], s98 offen offset:1024 sc1\n"
"v_fma_f32 v169, v73, -v44, v169\n"
"v_mfma_f32_16x16x32_bf16 v[88:91], a[144:147], a[240:243], v[88:91]\n"
"v_rcp_f32 v177, v177\n"
"v_mfma_f32_16x16x32_bf16 v[88:91], a[160:163], a[244:247], v[88:91]\n"
"v_rcp_f32 v178, v178\n"
"v_mfma_f32_16x16x32_bf16 v[92:95], a[148:151], a[240:243], v[92:95]\n"
"v_rcp_f32 v179, v179\n"
"v_mfma_f32_16x16x32_bf16 v[92:95], a[164:167], a[244:247], v[92:95]\n"
"v_rcp_f32 v180, v180\n"
".Lfb_ffne:\n"
"v_rcp_f32 v181, v181\n"
"v_rcp_f32 v182, v182\n"
"v_rcp_f32 v183, v183\n"
"v_pk_mul_f32 v[176:177], v[176:177], v[184:185] op_sel:[0,0] op_sel_hi:[1,1]\n"
"v_pk_mul_f32 v[178:179], v[178:179], v[186:187] op_sel:[0,0] op_sel_hi:[1,1]\n"
"v_pk_mul_f32 v[180:181], v[180:181], v[188:189] op_sel:[0,0] op_sel_hi:[1,1]\n"
"v_pk_mul_f32 v[182:183], v[182:183], v[190:191] op_sel:[0,0] op_sel_hi:[1,1]\n"
"v_cvt_pk_bf16_f32 v184, v176, v177\n"
"v_cvt_pk_bf16_f32 v185, v178, v179\n"
"v_cvt_pk_bf16_f32 v186, v180, v181\n"
"v_cvt_pk_bf16_f32 v187, v182, v183\n"
"buffer_store_dwordx4 v[184:187], v2, s[20:23], s76 offen sc1\n"
"s_add_u32 s76, s76, 116736\n"
"v_pk_fma_f32 v[170:171], v[74:75], v[44:45], v[170:171] op_sel:[0,0,0] op_sel_hi:[1,0,1] neg_lo:[0,1,0] neg_hi:[0,1,0]\n"
"v_pk_fma_f32 v[164:165], v[68:69], v[44:45], v[164:165] op_sel:[0,0,0] op_sel_hi:[1,0,1] neg_lo:[0,1,0] neg_hi:[0,1,0]\n"
"v_pk_fma_f32 v[166:167], v[70:71], v[44:45], v[166:167] op_sel:[0,0,0] op_sel_hi:[1,0,1] neg_lo:[0,1,0] neg_hi:[0,1,0]\n"
"v_pk_fma_f32 v[172:173], v[76:77], v[44:45], v[172:173] op_sel:[0,0,0] op_sel_hi:[1,0,1] neg_lo:[0,1,0] neg_hi:[0,1,0]\n"
"v_pk_fma_f32 v[174:175], v[78:79], v[44:45], v[174:175] op_sel:[0,0,0] op_sel_hi:[1,0,1] neg_lo:[0,1,0] neg_hi:[0,1,0]\n"
"v_pk_mul_f32 v[168:169], v[160:161], v[168:169] op_sel:[0,0] op_sel_hi:[1,1]\n"
"v_pk_mul_f32 v[160:161], v[160:161], v[44:45] op_sel:[0,1] op_sel_hi:[1,1]\n"
"v_pk_mul_f32 v[170:171], v[162:163], v[170:171] op_sel:[0,0] op_sel_hi:[1,1]\n"
"v_pk_mul_f32 v[162:163], v[162:163], v[44:45] op_sel:[0,1] op_sel_hi:[1,1]\n"
"v_pk_mul_f32 v[172:173], v[164:165], v[172:173] op_sel:[0,0] op_sel_hi:[1,1]\n"
"v_pk_mul_f32 v[164:165], v[164:165], v[44:45] op_sel:[0,1] op_sel_hi:[1,1]\n"
"v_pk_mul_f32 v[174:175], v[166:167], v[174:175] op_sel:[0,0] op_sel_hi:[1,1]\n"
"v_pk_mul_f32 v[166:167], v[166:167], v[44:45] op_sel:[0,1] op_sel_hi:[1,1]\n"
"v_exp_f32 v160, v160\n"
"s_add_u32 s98, s97, 0\n"
"buffer_load_dwordx4 a[104:107], v2, s[24:27], s98 offen offset:2048 sc1\n"
"v_exp_f32 v161, v161\n"
"v_exp_f32 v162, v162\n"
"v_exp_f32 v163, v163\n"
"v_exp_f32 v164, v164\n"
"v_exp_f32 v165, v165\n"
"v_exp_f32 v166, v166\n"
"v_exp_f32 v167, v167\n"
"v_pk_fma_f32 v[160:161], v[160:161], v[46:47], v[46:47] op_sel:[0,0,0] op_sel_hi:[1,0,0]\n"
"buffer_load_dwordx4 a[108:111], v2, s[24:27], s98 offen offset:3072 sc1\n"
"v_pk_fma_f32 v[162:163], v[162:163], v[46:47], v[46:47] op_sel:[0,0,0] op_sel_hi:[1,0,0]\n"
"v_pk_fma_f32 v[164:165], v[164:165], v[46:47], v[46:47] op_sel:[0,0,0] op_sel_hi:[1,0,0]\n"
"v_pk_fma_f32 v[166:167], v[166:167], v[46:47], v[46:47] op_sel:[0,0,0] op_sel_hi:[1,0,0]\n"
"v_rcp_f32 v160, v160\n"
"v_rcp_f32 v161, v161\n"
"v_rcp_f32 v162, v162\n"
"v_rcp_f32 v163, v163\n"
"v_rcp_f32 v164, v164\n"
"v_rcp_f32 v165, v165\n"
"s_add_u32 s98, s97, 4096\n"
"buffer_load_dwordx4 a[112:115], v2, s[24:27], s98 offen offset:0 sc1\n"
"v_rcp_f32 v166, v166\n"
"v_rcp_f32 v167, v167\n"
"v_pk_mul_f32 v[160:161], v[160:161], v[168:169] op_sel:[0,0] op_sel_hi:[1,1]\n"
"v_pk_mul_f32 v[162:163], v[162:163], v[170:171] op_sel:[0,0] op_sel_hi:[1,1]\n"
"v_pk_mul_f32 v[164:165], v[164:165], v[172:173] op_sel:[0,0] op_sel_hi:[1,1]\n"
"v_pk_mul_f32 v[166:167], v[166:167], v[174:175] op_sel:[0,0] op_sel_hi:[1,1]\n"
"v_cvt_pk_bf16_f32 v168, v160, v161\n"
"v_cvt_pk_bf16_f32 v169, v162, v163\n"
"v_cvt_pk_bf16_f32 v170, v164, v165\n"
"v_cvt_pk_bf16_f32 v171, v166, v167\n"
"buffer_store_dwordx4 v[168:171], v2, s[20:23], s76 offen sc1\n"
"s_add_u32 s76, s76, 116736\n"
"v_pk_fma_f32 v[144:145], v[64:65], v[48:49], v[144:145] op_sel:[0,0,0] op_sel_hi:[1,0,1] neg_lo:[0,1,0] neg_hi:[0,1,0]\n"
"v_pk_fma_f32 v[146:147], v[66:67], v[48:49], v[146:147] op_sel:[0,0,0] op_sel_hi:[1,0,1] neg_lo:[0,1,0] neg_hi:[0,1,0]\n"
"v_pk_fma_f32 v[152:153], v[72:73], v[48:49], v[152:153] op_sel:[0,0,0] op_sel_hi:[1,0,1] neg_lo:[0,1,0] neg_hi:[0,1,0]\n"
"v_pk_fma_f32 v[154:155], v[74:75], v[48:49], v[154:155] op_sel:[0,0,0] op_sel_hi:[1,0,1] neg_lo:[0,1,0] neg_hi:[0,1,0]\n"
"v_pk_fma_f32 v[148:149], v[68:69], v[48:49], v[148:149] op_sel:[0,0,0] op_sel_hi:[1,0,1] neg_lo:[0,1,0] neg_hi:[0,1,0]\n"
"v_pk_fma_f32 v[150:151], v[70:71], v[48:49], v[150:151] op_sel:[0,0,0] op_sel_hi:[1,0,1] neg_lo:[0,1,0] neg_hi:[0,1,0]\n"
"v_pk_fma_f32 v[156:157], v[76:77], v[48:49], v[156:157] op_sel:[0,0,0] op_sel_hi:[1,0,1] neg_lo:[0,1,0] neg_hi:[0,1,0]\n"
"v_pk_fma_f32 v[158:159], v[78:79], v[48:49], v[158:159] op_sel:[0,0,0] op_sel_hi:[1,0,1] neg_lo:[0,1,0] neg_hi:[0,1,0]\n"
"v_pk_mul_f32 v[152:153], v[144:145], v[152:153] op_sel:[0,0] op_sel_hi:[1,1]\n"
"v_pk_mul_f32 v[144:145], v[144:145], v[48:49] op_sel:[0,1] op_sel_hi:[1,1]\n"
"v_pk_mul_f32 v[154:155], v[146:147], v[154:155] op_sel:[0,0] op_sel_hi:[1,1]\n"
"v_pk_mul_f32 v[146:147], v[146:147], v[48:49] op_sel:[0,1] op_sel_hi:[1,1]\n"
"v_pk_mul_f32 v[156:157], v[148:149], v[156:157] op_sel:[0,0] op_sel_hi:[1,1]\n"
"v_pk_mul_f32 v[148:149], v[148:149], v[48:49] op_sel:[0,1] op_sel_hi:[1,1]\n"
"buffer_load_dwordx4 a[116:119], v2, s[24:27], s98 offen offset:1024 sc1\n"
"v_pk_mul_f32 v[158:159], v[150:151], v[158:159] op_sel:[0,0] op_sel_hi:[1,1]\n"
"v_pk_mul_f32 v[150:151], v[150:151], v[48:49] op_sel:[0,1] op_sel_hi:[1,1]\n"
"v_exp_f32 v144, v144\n"
"v_exp_f32 v145, v145\n"
"v_exp_f32 v146, v146\n"
"v_exp_f32 v147, v147\n"
"v_exp_f32 v148, v148\n"
"v_exp_f32 v149, v149\n"
"v_exp_f32 v150, v150\n"
"buffer_load_dwordx4 a[120:123], v2, s[24:27], s98 offen offset:2048 sc1\n"
"v_exp_f32 v151, v151\n"
"v_pk_fma_f32 v[144:145], v[144:145], v[50:51], v[50:51] op_sel:[0,0,0] op_sel_hi:[1,0,0]\n"
"v_pk_fma_f32 v[146:147], v[146:147], v[50:51], v[50:51] op_sel:[0,0,0] op_sel_hi:[1,0,0]\n"
"v_pk_fma_f32 v[148:149], v[148:149], v[50:51], v[50:51] op_sel:[0,0,0] op_sel_hi:[1,0,0]\n"
"v_pk_fma_f32 v[150:151], v[150:151], v[50:51], v[50:51] op_sel:[0,0,0] op_sel_hi:[1,0,0]\n"
"v_rcp_f32 v144, v144\n"
"v_rcp_f32 v145, v145\n"
"v_rcp_f32 v146, v146\n"
"v_rcp_f32 v147, v147\n"
"v_rcp_f32 v148, v148\n"
"buffer_load_dwordx4 a[124:127], v2, s[24:27], s98 offen offset:3072 sc1\n"
"v_rcp_f32 v149, v149\n"
"v_rcp_f32 v150, v150\n"
"v_rcp_f32 v151, v151\n"
"v_pk_mul_f32 v[144:145], v[144:145], v[152:153] op_sel:[0,0] op_sel_hi:[1,1]\n"
"v_pk_mul_f32 v[146:147], v[146:147], v[154:155] op_sel:[0,0] op_sel_hi:[1,1]\n"
"v_pk_mul_f32 v[148:149], v[148:149], v[156:157] op_sel:[0,0] op_sel_hi:[1,1]\n"
"v_pk_mul_f32 v[150:151], v[150:151], v[158:159] op_sel:[0,0] op_sel_hi:[1,1]\n"
"v_cvt_pk_bf16_f32 v152, v144, v145\n"
"v_cvt_pk_bf16_f32 v153, v146, v147\n"
"v_cvt_pk_bf16_f32 v154, v148, v149\n"
"v_cvt_pk_bf16_f32 v155, v150, v151\n"
"buffer_store_dwordx4 v[152:155], v2, s[20:23], s76 offen sc1\n"
"s_add_u32 s76, s76, 116736\n"
"v_pk_fma_f32 v[128:129], v[64:65], v[60:61], v[128:129] op_sel:[0,0,0] op_sel_hi:[1,0,1] neg_lo:[0,1,0] neg_hi:[0,1,0]\n"
"v_pk_fma_f32 v[130:131], v[66:67], v[60:61], v[130:131] op_sel:[0,0,0] op_sel_hi:[1,0,1] neg_lo:[0,1,0] neg_hi:[0,1,0]\n"
"v_pk_fma_f32 v[136:137], v[72:73], v[60:61], v[136:137] op_sel:[0,0,0] op_sel_hi:[1,0,1] neg_lo:[0,1,0] neg_hi:[0,1,0]\n"
"v_pk_fma_f32 v[138:139], v[74:75], v[60:61], v[138:139] op_sel:[0,0,0] op_sel_hi:[1,0,1] neg_lo:[0,1,0] neg_hi:[0,1,0]\n"
"v_pk_fma_f32 v[132:133], v[68:69], v[60:61], v[132:133] op_sel:[0,0,0] op_sel_hi:[1,0,1] neg_lo:[0,1,0] neg_hi:[0,1,0]\n"
"v_pk_fma_f32 v[134:135], v[70:71], v[60:61], v[134:135] op_sel:[0,0,0] op_sel_hi:[1,0,1] neg_lo:[0,1,0] neg_hi:[0,1,0]\n"
"v_pk_fma_f32 v[140:141], v[76:77], v[60:61], v[140:141] op_sel:[0,0,0] op_sel_hi:[1,0,1] neg_lo:[0,1,0] neg_hi:[0,1,0]\n"
"v_pk_fma_f32 v[142:143], v[78:79], v[60:61], v[142:143] op_sel:[0,0,0] op_sel_hi:[1,0,1] neg_lo:[0,1,0] neg_hi:[0,1,0]\n"
"v_pk_mul_f32 v[136:137], v[128:129], v[136:137] op_sel:[0,0] op_sel_hi:[1,1]\n"
"v_pk_mul_f32 v[128:129], v[128:129], v[60:61] op_sel:[0,1] op_sel_hi:[1,1]\n"
"v_pk_mul_f32 v[138:139], v[130:131], v[138:139] op_sel:[0,0] op_sel_hi:[1,1]\n"
"v_pk_mul_f32 v[130:131], v[130:131], v[60:61] op_sel:[0,1] op_sel_hi:[1,1]\n"
"v_pk_mul_f32 v[140:141], v[132:133], v[140:141] op_sel:[0,0] op_sel_hi:[1,1]\n"
"v_pk_mul_f32 v[132:133], v[132:133], v[60:61] op_sel:[0,1] op_sel_hi:[1,1]\n"
"s_add_u32 s98, s97, 8192\n"
"buffer_load_dwordx4 a[240:243], v2, s[24:27], s98 offen offset:0 sc1\n"
"v_pk_mul_f32 v[142:143], v[134:135], v[142:143] op_sel:[0,0] op_sel_hi:[1,1]\n"
"v_pk_mul_f32 v[134:135], v[134:135], v[60:61] op_sel:[0,1] op_sel_hi:[1,1]\n"
"v_exp_f32 v128, v128\n"
"v_exp_f32 v129, v129\n"
"v_exp_f32 v130, v130\n"
"v_exp_f32 v131, v131\n"
"v_exp_f32 v132, v132\n"
"v_exp_f32 v133, v133\n"
"v_exp_f32 v134, v134\n"
"buffer_load_dwordx4 a[244:247], v2, s[24:27], s98 offen offset:1024 sc1\n"
"v_exp_f32 v135, v135\n"
"v_pk_fma_f32 v[128:129], v[128:129], v[62:63], v[62:63] op_sel:[0,0,0] op_sel_hi:[1,0,0]\n"
"v_pk_fma_f32 v[130:131], v[130:131], v[62:63], v[62:63] op_sel:[0,0,0] op_sel_hi:[1,0,0]\n"
"v_pk_fma_f32 v[132:133], v[132:133], v[62:63], v[62:63] op_sel:[0,0,0] op_sel_hi:[1,0,0]\n"
"v_pk_fma_f32 v[134:135], v[134:135], v[62:63], v[62:63] op_sel:[0,0,0] op_sel_hi:[1,0,0]\n"
"v_rcp_f32 v128, v128\n"
"v_rcp_f32 v129, v129\n"
"v_rcp_f32 v130, v130\n"
"v_rcp_f32 v131, v131\n"
"v_rcp_f32 v132, v132\n"
"buffer_load_dwordx4 a[248:251], v2, s[24:27], s98 offen offset:2048 sc1\n"
"v_rcp_f32 v133, v133\n"
"v_rcp_f32 v134, v134\n"
"v_rcp_f32 v135, v135\n"
"v_pk_mul_f32 v[128:129], v[128:129], v[136:137] op_sel:[0,0] op_sel_hi:[1,1]\n"
"v_pk_mul_f32 v[130:131], v[130:131], v[138:139] op_sel:[0,0] op_sel_hi:[1,1]\n"
"v_pk_mul_f32 v[132:133], v[132:133], v[140:141] op_sel:[0,0] op_sel_hi:[1,1]\n"
"v_pk_mul_f32 v[134:135], v[134:135], v[142:143] op_sel:[0,0] op_sel_hi:[1,1]\n"
"v_cvt_pk_bf16_f32 v136, v128, v129\n"
"v_cvt_pk_bf16_f32 v137, v130, v131\n"
"v_cvt_pk_bf16_f32 v138, v132, v133\n"
"v_cvt_pk_bf16_f32 v139, v134, v135\n"
"buffer_store_dwordx4 v[136:139], v2, s[20:23], s76 offen sc1\n"
"s_add_u32 s76, s76, 116736\n"
"v_pk_fma_f32 v[112:113], v[64:65], v[28:29], v[112:113] op_sel:[0,0,0] op_sel_hi:[1,0,1] neg_lo:[0,1,0] neg_hi:[0,1,0]\n"
"v_pk_fma_f32 v[114:115], v[66:67], v[28:29], v[114:115] op_sel:[0,0,0] op_sel_hi:[1,0,1] neg_lo:[0,1,0] neg_hi:[0,1,0]\n"
"v_pk_fma_f32 v[120:121], v[72:73], v[28:29], v[120:121] op_sel:[0,0,0] op_sel_hi:[1,0,1] neg_lo:[0,1,0] neg_hi:[0,1,0]\n"
"v_pk_fma_f32 v[122:123], v[74:75], v[28:29], v[122:123] op_sel:[0,0,0] op_sel_hi:[1,0,1] neg_lo:[0,1,0] neg_hi:[0,1,0]\n"
"v_pk_fma_f32 v[116:117], v[68:69], v[28:29], v[116:117] op_sel:[0,0,0] op_sel_hi:[1,0,1] neg_lo:[0,1,0] neg_hi:[0,1,0]\n"
"v_pk_fma_f32 v[118:119], v[70:71], v[28:29], v[118:119] op_sel:[0,0,0] op_sel_hi:[1,0,1] neg_lo:[0,1,0] neg_hi:[0,1,0]\n"
"v_pk_fma_f32 v[124:125], v[76:77], v[28:29], v[124:125] op_sel:[0,0,0] op_sel_hi:[1,0,1] neg_lo:[0,1,0] neg_hi:[0,1,0]\n"
"v_pk_fma_f32 v[126:127], v[78:79], v[28:29], v[126:127] op_sel:[0,0,0] op_sel_hi:[1,0,1] neg_lo:[0,1,0] neg_hi:[0,1,0]\n"
"v_pk_mul_f32 v[120:121], v[112:113], v[120:121] op_sel:[0,0] op_sel_hi:[1,1]\n"
"v_pk_mul_f32 v[112:113], v[112:113], v[28:29] op_sel:[0,1] op_sel_hi:[1,1]\n"
"v_pk_mul_f32 v[122:123], v[114:115], v[122:123] op_sel:[0,0] op_sel_hi:[1,1]\n"
"v_pk_mul_f32 v[114:115], v[114:115], v[28:29] op_sel:[0,1] op_sel_hi:[1,1]\n"
"v_pk_mul_f32 v[124:125], v[116:117], v[124:125] op_sel:[0,0] op_sel_hi:[1,1]\n"
"v_pk_mul_f32 v[116:117], v[116:117], v[28:29] op_sel:[0,1] op_sel_hi:[1,1]\n"
"buffer_load_dwordx4 a[252:255], v2, s[24:27], s98 offen offset:3072 sc1\n"
"v_pk_mul_f32 v[126:127], v[118:119], v[126:127] op_sel:[0,0] op_sel_hi:[1,1]\n"
"v_pk_mul_f32 v[118:119], v[118:119], v[28:29] op_sel:[0,1] op_sel_hi:[1,1]\n"
"v_exp_f32 v112, v112\n"
"v_exp_f32 v113, v113\n"
"v_exp_f32 v114, v114\n"
"v_exp_f32 v115, v115\n"
"v_exp_f32 v116, v116\n"
"v_exp_f32 v117, v117\n"
"v_exp_f32 v118, v118\n"
"s_add_u32 s98, s97, 12288\n"
"buffer_load_dwordx4 v[188:191], v2, s[24:27], s98 offen offset:0 sc1\n"
"v_exp_f32 v119, v119\n"
"v_pk_fma_f32 v[112:113], v[112:113], v[30:31], v[30:31] op_sel:[0,0,0] op_sel_hi:[1,0,0]\n"
"v_pk_fma_f32 v[114:115], v[114:115], v[30:31], v[30:31] op_sel:[0,0,0] op_sel_hi:[1,0,0]\n"
"v_pk_fma_f32 v[116:117], v[116:117], v[30:31], v[30:31] op_sel:[0,0,0] op_sel_hi:[1,0,0]\n"
"v_pk_fma_f32 v[118:119], v[118:119], v[30:31], v[30:31] op_sel:[0,0,0] op_sel_hi:[1,0,0]\n"
"v_rcp_f32 v112, v112\n"
"v_rcp_f32 v113, v113\n"
"v_rcp_f32 v114, v114\n"
"v_rcp_f32 v115, v115\n"
"v_rcp_f32 v116, v116\n"
"buffer_load_dwordx4 v[184:187], v2, s[24:27], s98 offen offset:1024 sc1\n"
"v_rcp_f32 v117, v117\n"
"v_rcp_f32 v118, v118\n"
"v_rcp_f32 v119, v119\n"
"v_pk_mul_f32 v[112:113], v[112:113], v[120:121] op_sel:[0,0] op_sel_hi:[1,1]\n"
"v_pk_mul_f32 v[114:115], v[114:115], v[122:123] op_sel:[0,0] op_sel_hi:[1,1]\n"
"v_pk_mul_f32 v[116:117], v[116:117], v[124:125] op_sel:[0,0] op_sel_hi:[1,1]\n"
"v_pk_mul_f32 v[118:119], v[118:119], v[126:127] op_sel:[0,0] op_sel_hi:[1,1]\n"
"v_cvt_pk_bf16_f32 v120, v112, v113\n"
"v_cvt_pk_bf16_f32 v121, v114, v115\n"
"v_cvt_pk_bf16_f32 v122, v116, v117\n"
"v_cvt_pk_bf16_f32 v123, v118, v119\n"
"buffer_store_dwordx4 v[120:123], v2, s[20:23], s76 offen sc1\n"
"s_add_u32 s76, s76, 116736\n"
"v_pk_fma_f32 v[96:97], v[64:65], v[32:33], v[96:97] op_sel:[0,0,0] op_sel_hi:[1,0,1] neg_lo:[0,1,0] neg_hi:[0,1,0]\n"
"v_pk_fma_f32 v[98:99], v[66:67], v[32:33], v[98:99] op_sel:[0,0,0] op_sel_hi:[1,0,1] neg_lo:[0,1,0] neg_hi:[0,1,0]\n"
"v_pk_fma_f32 v[104:105], v[72:73], v[32:33], v[104:105] op_sel:[0,0,0] op_sel_hi:[1,0,1] neg_lo:[0,1,0] neg_hi:[0,1,0]\n"
"v_pk_fma_f32 v[106:107], v[74:75], v[32:33], v[106:107] op_sel:[0,0,0] op_sel_hi:[1,0,1] neg_lo:[0,1,0] neg_hi:[0,1,0]\n"
"v_pk_fma_f32 v[100:101], v[68:69], v[32:33], v[100:101] op_sel:[0,0,0] op_sel_hi:[1,0,1] neg_lo:[0,1,0] neg_hi:[0,1,0]\n"
"v_pk_fma_f32 v[102:103], v[70:71], v[32:33], v[102:103] op_sel:[0,0,0] op_sel_hi:[1,0,1] neg_lo:[0,1,0] neg_hi:[0,1,0]\n"
"v_pk_fma_f32 v[108:109], v[76:77], v[32:33], v[108:109] op_sel:[0,0,0] op_sel_hi:[1,0,1] neg_lo:[0,1,0] neg_hi:[0,1,0]\n"
"v_pk_fma_f32 v[110:111], v[78:79], v[32:33], v[110:111] op_sel:[0,0,0] op_sel_hi:[1,0,1] neg_lo:[0,1,0] neg_hi:[0,1,0]\n"
"v_pk_mul_f32 v[104:105], v[96:97], v[104:105] op_sel:[0,0] op_sel_hi:[1,1]\n"
"v_pk_mul_f32 v[96:97], v[96:97], v[32:33] op_sel:[0,1] op_sel_hi:[1,1]\n"
"v_pk_mul_f32 v[106:107], v[98:99], v[106:107] op_sel:[0,0] op_sel_hi:[1,1]\n"
"v_pk_mul_f32 v[98:99], v[98:99], v[32:33] op_sel:[0,1] op_sel_hi:[1,1]\n"
"v_pk_mul_f32 v[108:109], v[100:101], v[108:109] op_sel:[0,0] op_sel_hi:[1,1]\n"
"v_pk_mul_f32 v[100:101], v[100:101], v[32:33] op_sel:[0,1] op_sel_hi:[1,1]\n"
"buffer_load_dwordx4 v[180:183], v2, s[24:27], s98 offen offset:2048 sc1\n"
"v_pk_mul_f32 v[110:111], v[102:103], v[110:111] op_sel:[0,0] op_sel_hi:[1,1]\n"
"v_pk_mul_f32 v[102:103], v[102:103], v[32:33] op_sel:[0,1] op_sel_hi:[1,1]\n"
"v_exp_f32 v96, v96\n"
"v_exp_f32 v97, v97\n"
"v_exp_f32 v98, v98\n"
"v_exp_f32 v99, v99\n"
"v_exp_f32 v100, v100\n"
"v_exp_f32 v101, v101\n"
"v_exp_f32 v102, v102\n"
"buffer_load_dwordx4 v[176:179], v2, s[24:27], s98 offen offset:3072 sc1\n"
"v_exp_f32 v103, v103\n"
"v_pk_fma_f32 v[96:97], v[96:97], v[34:35], v[34:35] op_sel:[0,0,0] op_sel_hi:[1,0,0]\n"
"v_pk_fma_f32 v[98:99], v[98:99], v[34:35], v[34:35] op_sel:[0,0,0] op_sel_hi:[1,0,0]\n"
"v_pk_fma_f32 v[100:101], v[100:101], v[34:35], v[34:35] op_sel:[0,0,0] op_sel_hi:[1,0,0]\n"
"v_pk_fma_f32 v[102:103], v[102:103], v[34:35], v[34:35] op_sel:[0,0,0] op_sel_hi:[1,0,0]\n"
"v_rcp_f32 v96, v96\n"
"v_rcp_f32 v97, v97\n"
"v_rcp_f32 v98, v98\n"
"v_rcp_f32 v99, v99\n"
"v_rcp_f32 v100, v100\n"
"s_add_u32 s98, s97, 16384\n"
"buffer_load_dwordx4 v[172:175], v2, s[24:27], s98 offen offset:0 sc1\n"
"v_rcp_f32 v101, v101\n"
"v_rcp_f32 v102, v102\n"
"v_rcp_f32 v103, v103\n"
"v_pk_mul_f32 v[96:97], v[96:97], v[104:105] op_sel:[0,0] op_sel_hi:[1,1]\n"
"v_pk_mul_f32 v[98:99], v[98:99], v[106:107] op_sel:[0,0] op_sel_hi:[1,1]\n"
"v_pk_mul_f32 v[100:101], v[100:101], v[108:109] op_sel:[0,0] op_sel_hi:[1,1]\n"
"v_pk_mul_f32 v[102:103], v[102:103], v[110:111] op_sel:[0,0] op_sel_hi:[1,1]\n"
"v_cvt_pk_bf16_f32 v104, v96, v97\n"
"v_cvt_pk_bf16_f32 v105, v98, v99\n"
"v_cvt_pk_bf16_f32 v106, v100, v101\n"
"v_cvt_pk_bf16_f32 v107, v102, v103\n"
"buffer_store_dwordx4 v[104:107], v2, s[20:23], s76 offen sc1\n"
"s_add_u32 s76, s76, 116736\n"
"v_pk_fma_f32 v[80:81], v[64:65], v[56:57], v[80:81] op_sel:[0,0,0] op_sel_hi:[1,0,1] neg_lo:[0,1,0] neg_hi:[0,1,0]\n"
"v_pk_fma_f32 v[82:83], v[66:67], v[56:57], v[82:83] op_sel:[0,0,0] op_sel_hi:[1,0,1] neg_lo:[0,1,0] neg_hi:[0,1,0]\n"
"v_pk_fma_f32 v[88:89], v[72:73], v[56:57], v[88:89] op_sel:[0,0,0] op_sel_hi:[1,0,1] neg_lo:[0,1,0] neg_hi:[0,1,0]\n"
"v_pk_fma_f32 v[90:91], v[74:75], v[56:57], v[90:91] op_sel:[0,0,0] op_sel_hi:[1,0,1] neg_lo:[0,1,0] neg_hi:[0,1,0]\n"
"v_pk_fma_f32 v[84:85], v[68:69], v[56:57], v[84:85] op_sel:[0,0,0] op_sel_hi:[1,0,1] neg_lo:[0,1,0] neg_hi:[0,1,0]\n"
"v_pk_fma_f32 v[86:87], v[70:71], v[56:57], v[86:87] op_sel:[0,0,0] op_sel_hi:[1,0,1] neg_lo:[0,1,0] neg_hi:[0,1,0]\n"
"v_pk_fma_f32 v[92:93], v[76:77], v[56:57], v[92:93] op_sel:[0,0,0] op_sel_hi:[1,0,1] neg_lo:[0,1,0] neg_hi:[0,1,0]\n"
"v_pk_fma_f32 v[94:95], v[78:79], v[56:57], v[94:95] op_sel:[0,0,0] op_sel_hi:[1,0,1] neg_lo:[0,1,0] neg_hi:[0,1,0]\n"
"v_pk_mul_f32 v[88:89], v[80:81], v[88:89] op_sel:[0,0] op_sel_hi:[1,1]\n"
"v_pk_mul_f32 v[80:81], v[80:81], v[56:57] op_sel:[0,1] op_sel_hi:[1,1]\n"
"v_pk_mul_f32 v[90:91], v[82:83], v[90:91] op_sel:[0,0] op_sel_hi:[1,1]\n"
"v_pk_mul_f32 v[82:83], v[82:83], v[56:57] op_sel:[0,1] op_sel_hi:[1,1]\n"
"v_pk_mul_f32 v[92:93], v[84:85], v[92:93] op_sel:[0,0] op_sel_hi:[1,1]\n"
"v_pk_mul_f32 v[84:85], v[84:85], v[56:57] op_sel:[0,1] op_sel_hi:[1,1]\n"
"buffer_load_dwordx4 v[168:171], v2, s[24:27], s98 offen offset:1024 sc1\n"
"v_pk_mul_f32 v[94:95], v[86:87], v[94:95] op_sel:[0,0] op_sel_hi:[1,1]\n"
"v_pk_mul_f32 v[86:87], v[86:87], v[56:57] op_sel:[0,1] op_sel_hi:[1,1]\n"
"v_exp_f32 v80, v80\n"
"v_exp_f32 v81, v81\n"
"v_exp_f32 v82, v82\n"
"v_exp_f32 v83, v83\n"
"v_exp_f32 v84, v84\n"
"v_exp_f32 v85, v85\n"
"v_exp_f32 v86, v86\n"
"buffer_load_dwordx4 v[164:167], v2, s[24:27], s98 offen offset:2048 sc1\n"
"v_exp_f32 v87, v87\n"
"v_pk_fma_f32 v[80:81], v[80:81], v[58:59], v[58:59] op_sel:[0,0,0] op_sel_hi:[1,0,0]\n"
"v_pk_fma_f32 v[82:83], v[82:83], v[58:59], v[58:59] op_sel:[0,0,0] op_sel_hi:[1,0,0]\n"
"v_pk_fma_f32 v[84:85], v[84:85], v[58:59], v[58:59] op_sel:[0,0,0] op_sel_hi:[1,0,0]\n"
"v_pk_fma_f32 v[86:87], v[86:87], v[58:59], v[58:59] op_sel:[0,0,0] op_sel_hi:[1,0,0]\n"
"v_rcp_f32 v80, v80\n"
"v_rcp_f32 v81, v81\n"
"v_rcp_f32 v82, v82\n"
"v_rcp_f32 v83, v83\n"
"v_rcp_f32 v84, v84\n"
"buffer_load_dwordx4 v[160:163], v2, s[24:27], s98 offen offset:3072 sc1\n"
"v_rcp_f32 v85, v85\n"
"v_rcp_f32 v86, v86\n"
"v_rcp_f32 v87, v87\n"
"v_pk_mul_f32 v[80:81], v[80:81], v[88:89] op_sel:[0,0] op_sel_hi:[1,1]\n"
"v_pk_mul_f32 v[82:83], v[82:83], v[90:91] op_sel:[0,0] op_sel_hi:[1,1]\n"
"v_pk_mul_f32 v[84:85], v[84:85], v[92:93] op_sel:[0,0] op_sel_hi:[1,1]\n"
"v_pk_mul_f32 v[86:87], v[86:87], v[94:95] op_sel:[0,0] op_sel_hi:[1,1]\n"
"v_cvt_pk_bf16_f32 v88, v80, v81\n"
"v_cvt_pk_bf16_f32 v89, v82, v83\n"
"v_cvt_pk_bf16_f32 v90, v84, v85\n"
"v_cvt_pk_bf16_f32 v91, v86, v87\n"
"buffer_store_dwordx4 v[88:91], v2, s[20:23], s76 offen sc1\n"
"s_add_u32 s76, s76, 116736\n"
"s_barrier\n"
"s_add_u32 s98, s97, 20480\n"
"buffer_load_dwordx4 a[228:231], v2, s[24:27], s98 offen offset:0 sc1\n"
"v_and_b32 v10, 31, v1\n"
"v_min_u32 v10, 7, v10\n"
"v_lshrrev_b32 v9, 5, v1\n"
"v_mul_u32_u24 v9, s46, v9\n"
"v_add3_u32 v10, v10, v9, s41\n"
"v_lshlrev_b32 v10, 7, v10\n"
"v_add_u32 v9, 0, v1\n"
"v_mul_u32_u24 v9, 6554, v9\n"
"v_lshrrev_b32 v9, 16, v9\n"
"v_mul_u32_u24 v9, 1120, v9\n"
"buffer_load_dwordx4 a[232:235], v2, s[24:27], s98 offen offset:1024 sc1\n"
"v_add_u32 v5, v9, v2\n"
"v_add_u32 v9, 64, v1\n"
"v_mul_u32_u24 v9, 6554, v9\n"
"v_lshrrev_b32 v9, 16, v9\n"
"v_mul_u32_u24 v9, 1120, v9\n"
"v_add_u32 v6, v9, v2\n"
"v_add_u32 v9, 128, v1\n"
"v_mul_u32_u24 v9, 6554, v9\n"
"v_lshrrev_b32 v9, 16, v9\n"
"v_mul_u32_u24 v9, 1120, v9\n"
"buffer_load_dwordx4 a[236:239], v2, s[24:27], s98 offen offset:2048 sc1\n"
"v_add_u32 v7, v9, v2\n"
"v_lshrrev_b32 v9, 5, v1\n"
"v_lshl_add_u32 v7, v9, 30, v7\n"
"v_mov_b32 v13, s52\n"
"s_mul_i32 s58, s3, 3072\n"
"s_add_u32 m0, s58, 126976\n"
"s_nop 0\n"
"buffer_load_dwordx4 v5, s[4:7], s79 offen offset:0 lds\n"
"buffer_load_dwordx4 v6, s[4:7], s79 offen offset:1024 lds\n"
"buffer_load_dwordx4 v7, s[4:7], s79 offen offset:2048 lds\n"
"buffer_load_dwordx4 v[36:39], v2, s[24:27], s98 offen offset:3072 sc1\n"
"s_add_u32 s98, s97, 24576\n"
"buffer_load_dwordx4 v[40:43], v2, s[24:27], s98 offen offset:0 sc1\n"
"s_waitcnt vmcnt(8)\n"
"buffer_store_dword v13, v3, s[16:19], s57 offen sc1\n"
"s_sleep 8\n"
"buffer_load_dword v12, v10, s[16:19], 0 offen sc1\n"
"s_waitcnt vmcnt(0)\n"
"v_cmp_ne_u32 vcc, s52, v12\n"
"s_nop 0\n"
"s_cbranch_vccnz .Lfb_slow_5\n"
".Lfb_back_6:\n"
"s_xor_b32 s85, s3, 3\n"
"s_mul_i32 s85, s85, 116736\n"
"s_add_u32 s85, s85, s45\n"
"buffer_load_dwordx4 a[128:131], v2, s[20:23], s85 offen offset:0 sc1\n"
"buffer_load_dwordx4 a[132:135], v2, s[20:23], s85 offen offset:1024 sc1\n"
"buffer_load_dwordx4 a[136:139], v2, s[20:23], s85 offen offset:2048 sc1\n"
"buffer_load_dwordx4 a[140:143], v2, s[20:23], s85 offen offset:3072 sc1\n"
"s_add_u32 s85, s85, 4096\n"
"buffer_load_dwordx4 a[144:147], v2, s[20:23], s85 offen offset:0 sc1\n"
"buffer_load_dwordx4 a[148:151], v2, s[20:23], s85 offen offset:1024 sc1\n"
"buffer_load_dwordx4 a[152:155], v2, s[20:23], s85 offen offset:2048 sc1\n"
"buffer_load_dwordx4 a[156:159], v2, s[20:23], s85 offen offset:3072 sc1\n"
"s_xor_b32 s85, s3, 2\n"
"s_mul_i32 s85, s85, 116736\n"
"s_add_u32 s85, s85, s45\n"
"buffer_load_dwordx4 a[160:163], v2, s[20:23], s85 offen offset:0 sc1\n"
"buffer_load_dwordx4 a[164:167], v2, s[20:23], s85 offen offset:1024 sc1\n"
"s_waitcnt vmcnt(8)\n"
"v_mfma_f32_16x16x32_bf16 a[188:191], a[0:3], a[128:131], 0\n"
"buffer_load_dwordx4 a[168:171], v2, s[20:23], s85 offen offset:2048 sc1\n"
"s_mul_i32 s62, s3, 3072\n"
"v_mfma_f32_16x16x32_bf16 a[188:191], a[20:23], a[132:135], a[188:191]\n"
"s_add_u32 s62, s62, 126976\n"
"v_mfma_f32_16x16x32_bf16 a[192:195], a[4:7], a[128:131], 0\n"
"buffer_load_dwordx4 a[172:175], v2, s[20:23], s85 offen offset:3072 sc1\n"
"s_add_u32 s85, s85, 4096\n"
"v_mfma_f32_16x16x32_bf16 a[192:195], a[24:27], a[132:135], a[192:195]\n"
"v_and_b32 v11, 15, v1\n"
"v_mul_u32_u24 v10, 160, v11\n"
"v_mfma_f32_16x16x32_bf16 a[196:199], a[8:11], a[128:131], 0\n"
"buffer_load_dwordx4 a[176:179], v2, s[20:23], s85 offen offset:0 sc1\n"
"v_lshrrev_b32 v14, 4, v1\n"
"v_mfma_f32_16x16x32_bf16 a[196:199], a[28:31], a[132:135], a[196:199]\n"
"v_lshl_add_u32 v10, v14, 3, v10\n"
"v_add_u32 v10, s62, v10\n"
"v_mfma_f32_16x16x32_bf16 a[200:203], a[12:15], a[128:131], 0\n"
"v_mfma_f32_16x16x32_bf16 a[200:203], a[32:35], a[132:135], a[200:203]\n"
"ds_read_b64 v[44:45], v10 offset:0\n"
"ds_read_b64 v[46:47], v10 offset:32\n"
"v_mfma_f32_16x16x32_bf16 a[204:207], a[16:19], a[128:131], 0\n"
"buffer_load_dwordx4 a[180:183], v2, s[20:23], s85 offen offset:1024 sc1\n"
"ds_read_b64 v[48:49], v10 offset:64\n"
"v_mfma_f32_16x16x32_bf16 a[204:207], a[36:39], a[132:135], a[204:207]\n"
"ds_read_b64 v[50:51], v10 offset:96\n"
"s_waitcnt vmcnt(9)\n"
"v_mfma_f32_16x16x32_bf16 a[188:191], a[40:43], a[136:139], a[188:191]\n"
"ds_read_b64 v[52:53], v10 offset:128\n"
"v_lshl_add_u32 v10, v11, 4, v10\n"
"v_mfma_f32_16x16x32_bf16 a[188:191], a[60:63], a[140:143], a[188:191]\n"
"v_add_u32 v10, 16384, v10\n"
"s_add_u32 s62, s62, 16384\n"
"v_mfma_f32_16x16x32_bf16 a[188:191], a[80:83], a[144:147], a[188:191]\n"
"buffer_load_dwordx4 a[184:187], v2, s[20:23], s85 offen offset:2048 sc1\n"
"v_bfe_u32 v11, v1, 4, 1\n"
"v_mfma_f32_16x16x32_bf16 a[192:195], a[44:47], a[136:139], a[192:195]\n"
"v_mul_u32_u24 v11, 44, v11\n"
"v_lshrrev_b32 v14, 5, v1\n"
"v_mfma_f32_16x16x32_bf16 a[192:195], a[64:67], a[140:143], a[192:195]\n"
"v_add3_u32 v11, v11, v14, s47\n"
"v_lshlrev_b32 v11, 7, v11\n"
"v_mfma_f32_16x16x32_bf16 a[192:195], a[84:87], a[144:147], a[192:195]\n"
"s_lshl_b32 s34, s40, 7\n"
"s_add_u32 s34, s34, 1325056\n"
"v_mfma_f32_16x16x32_bf16 a[196:199], a[48:51], a[136:139], a[196:199]\n"
"buffer_load_dwordx4 v[20:23], v2, s[20:23], s85 offen offset:3072 sc1\n"
"s_xor_b32 s60, s40, 32\n"
"v_mfma_f32_16x16x32_bf16 a[196:199], a[68:71], a[140:143], a[196:199]\n"
"s_lshl_b32 s61, s60, 7\n"
"s_add_u32 s61, s61, 1325056\n"
"v_mfma_f32_16x16x32_bf16 a[196:199], a[88:91], a[144:147], a[196:199]\n"
"s_xor_b32 s85, s3, 1\n"
"s_mul_i32 s85, s85, 116736\n"
"v_mfma_f32_16x16x32_bf16 a[200:203], a[52:55], a[136:139], a[200:203]\n"
"v_mov_b32 v8, s61\n"
"s_add_u32 s85, s85, s45\n"
"v_mfma_f32_16x16x32_bf16 a[200:203], a[72:75], a[140:143], a[200:203]\n"
"v_lshlrev_b32 v15, 4, v0\n"
"v_add_u32 v15, 81920, v15\n"
"v_mfma_f32_16x16x32_bf16 a[200:203], a[92:95], a[144:147], a[200:203]\n"
"v_xor_b32 v156, 3072, v15\n"
"v_xor_b32 v157, 2048, v15\n"
"v_mfma_f32_16x16x32_bf16 a[204:207], a[56:59], a[136:139], a[204:207]\n"
"buffer_load_dwordx4 v[24:27], v2, s[20:23], s85 offen offset:0 sc1\n"
"v_xor_b32 v158, 1024, v15\n"
"v_mfma_f32_16x16x32_bf16 a[204:207], a[76:79], a[140:143], a[204:207]\n"
"s_mul_i32 s63, s56, 5120\n"
"s_add_u32 s63, s63, 75776\n"
"v_mfma_f32_16x16x32_bf16 a[204:207], a[96:99], a[144:147], a[204:207]\n"
"s_waitcnt vmcnt(9)\n"
"s_lshr_b32 s60, s60, 3\n"
"v_mfma_f32_16x16x32_bf16 a[188:191], a[100:103], a[148:151], a[188:191]\n"
"buffer_load_dwordx4 v[28:31], v2, s[20:23], s85 offen offset:1024 sc1\n"
"s_mul_i32 s60, s60, 116736\n"
"v_mfma_f32_16x16x32_bf16 a[188:191], v[236:239], a[152:155], a[188:191]\n"
"s_add_u32 s60, s60, s63\n"
"s_add_u32 s61, s60, 4096\n"
"v_mfma_f32_16x16x32_bf16 a[188:191], v[216:219], a[156:159], a[188:191]\n"
"s_lshr_b32 s40, s40, 3\n"
"s_mul_i32 s40, s40, 116736\n"
"v_mfma_f32_16x16x32_bf16 a[192:195], v[252:255], a[148:151], a[192:195]\n"
"s_add_u32 s40, s40, s63\n"
"s_add_u32 s63, s40, 4096\n"
"v_mfma_f32_16x16x32_bf16 a[192:195], v[232:235], a[152:155], a[192:195]\n"
"buffer_load_dwordx4 v[32:35], v2, s[20:23], s85 offen offset:2048 sc1\n"
"s_and_b32 s35, s52, 0x1fff\n"
"v_mfma_f32_16x16x32_bf16 a[192:195], v[212:215], a[156:159], a[192:195]\n"
"s_or_b32 s35, s35, 0x4000\n"
"s_lshl_b32 s35, s35, 16\n"
"v_mfma_f32_16x16x32_bf16 a[196:199], v[248:251], a[148:151], a[196:199]\n"
"v_mov_b32 v16, s35\n"
"v_lshlrev_b32 v9, 3, v1\n"
"v_mfma_f32_16x16x32_bf16 a[196:199], v[228:231], a[152:155], a[196:199]\n"
"v_mfma_f32_16x16x32_bf16 a[196:199], v[208:211], a[156:159], a[196:199]\n"
"v_mfma_f32_16x16x32_bf16 a[200:203], v[244:247], a[148:151], a[200:203]\n"
"ds_write_b128 v15, a[188:191] offset:0\n"
"v_mfma_f32_16x16x32_bf16 a[200:203], v[224:227], a[152:155], a[200:203]\n"
"buffer_load_dwordx4 a[128:131], v2, s[20:23], s85 offen offset:3072 sc1\n"
"v_mfma_f32_16x16x32_bf16 a[200:203], v[204:207], a[156:159], a[200:203]\n"
"s_add_u32 s85, s85, 4096\n"
"v_mfma_f32_16x16x32_bf16 a[204:207], v[240:243], a[148:151], a[204:207]\n"
"ds_write_b128 v15, a[192:195] offset:4096\n"
"v_mfma_f32_16x16x32_bf16 a[204:207], v[220:223], a[152:155], a[204:207]\n"
"buffer_load_dwordx4 a[132:135], v2, s[20:23], s85 offen offset:0 sc1\n"
"v_mfma_f32_16x16x32_bf16 a[204:207], v[200:203], a[156:159], a[204:207]\n"
"s_waitcnt vmcnt(11)\n"
"v_mfma_f32_16x16x32_bf16 a[208:211], a[0:3], a[160:163], 0\n"
"ds_write_b128 v15, a[196:199] offset:8192\n"
"v_mfma_f32_16x16x32_bf16 a[208:211], a[20:23], a[164:167], a[208:211]\n"
"v_mfma_f32_16x16x32_bf16 a[212:215], a[4:7], a[160:163], 0\n"
"buffer_load_dwordx4 a[136:139], v2, s[20:23], s85 offen offset:1024 sc1\n"
"v_mfma_f32_16x16x32_bf16 a[212:215], a[24:27], a[164:167], a[212:215]\n"
"ds_write_b128 v15, a[200:203] offset:12288\n"
"v_mfma_f32_16x16x32_bf16 a[216:219], a[8:11], a[160:163], 0\n"
"v_mfma_f32_16x16x32_bf16 a[216:219], a[28:31], a[164:167], a[216:219]\n"
"v_mfma_f32_16x16x32_bf16 a[220:223], a[12:15], a[160:163], 0\n"
"ds_write_b128 v15, a[204:207] offset:16384\n"
"v_mfma_f32_16x16x32_bf16 a[220:223], a[32:35], a[164:167], a[220:223]\n"
"buffer_load_dwordx4 a[140:143], v2, s[20:23], s85 offen offset:2048 sc1\n"
"v_mfma_f32_16x16x32_bf16 a[224:227], a[16:19], a[160:163], 0\n"
"v_mfma_f32_16x16x32_bf16 a[224:227], a[36:39], a[164:167], a[224:227]\n"
"s_waitcnt vmcnt(10)\n"
"v_mfma_f32_16x16x32_bf16 a[208:211], a[40:43], a[168:171], a[208:211]\n"
"v_mfma_f32_16x16x32_bf16 a[208:211], a[60:63], a[172:175], a[208:211]\n"
"buffer_load_dwordx4 a[144:147], v2, s[20:23], s85 offen offset:3072 sc1\n"
"v_mfma_f32_16x16x32_bf16 a[208:211], a[80:83], a[176:179], a[208:211]\n"
"s_xor_b32 s85, s3, 0\n"
"s_mul_i32 s85, s85, 116736\n"
"v_mfma_f32_16x16x32_bf16 a[212:215], a[44:47], a[168:171], a[212:215]\n"
"s_add_u32 s85, s85, s45\n"
"v_mfma_f32_16x16x32_bf16 a[212:215], a[64:67], a[172:175], a[212:215]\n"
"v_mfma_f32_16x16x32_bf16 a[212:215], a[84:87], a[176:179], a[212:215]\n"
"buffer_load_dwordx4 a[148:151], v2, s[20:23], s85 offen offset:0 sc1\n"
"v_mfma_f32_16x16x32_bf16 a[216:219], a[48:51], a[168:171], a[216:219]\n"
"s_waitcnt lgkmcnt(0)\n"
"v_mfma_f32_16x16x32_bf16 a[216:219], a[68:71], a[172:175], a[216:219]\n"
"v_mfma_f32_16x16x32_bf16 a[216:219], a[88:91], a[176:179], a[216:219]\n"
"v_mfma_f32_16x16x32_bf16 a[220:223], a[52:55], a[168:171], a[220:223]\n"
"buffer_load_dwordx4 a[152:155], v2, s[20:23], s85 offen offset:1024 sc1\n"
"v_mfma_f32_16x16x32_bf16 a[220:223], a[72:75], a[172:175], a[220:223]\n"
"v_mfma_f32_16x16x32_bf16 a[220:223], a[92:95], a[176:179], a[220:223]\n"
"v_mfma_f32_16x16x32_bf16 a[224:227], a[56:59], a[168:171], a[224:227]\n"
"v_mfma_f32_16x16x32_bf16 a[224:227], a[76:79], a[172:175], a[224:227]\n"
"v_mfma_f32_16x16x32_bf16 a[224:227], a[96:99], a[176:179], a[224:227]\n"
"buffer_load_dwordx4 a[156:159], v2, s[20:23], s85 offen offset:2048 sc1\n"
"s_waitcnt vmcnt(11)\n"
"v_mfma_f32_16x16x32_bf16 a[208:211], a[100:103], a[180:183], a[208:211]\n"
"v_mfma_f32_16x16x32_bf16 a[208:211], v[236:239], a[184:187], a[208:211]\n"
"v_mfma_f32_16x16x32_bf16 a[208:211], v[216:219], v[20:23], a[208:211]\n"
"v_mfma_f32_16x16x32_bf16 a[212:215], v[252:255], a[180:183], a[212:215]\n"
"buffer_load_dwordx4 a[160:163], v2, s[20:23], s85 offen offset:3072 sc1\n"
"v_mfma_f32_16x16x32_bf16 a[212:215], v[232:235], a[184:187], a[212:215]\n"
"v_mfma_f32_16x16x32_bf16 a[212:215], v[212:215], v[20:23], a[212:215]\n"
"s_add_u32 s85, s85, 4096\n"
"v_mfma_f32_16x16x32_bf16 a[216:219], v[248:251], a[180:183], a[216:219]\n"
"v_mfma_f32_16x16x32_bf16 a[216:219], v[228:231], a[184:187], a[216:219]\n"
"buffer_load_dwordx4 a[164:167], v2, s[20:23], s85 offen offset:0 sc1\n"
"v_mfma_f32_16x16x32_bf16 a[216:219], v[208:211], v[20:23], a[216:219]\n"
"v_mfma_f32_16x16x32_bf16 a[220:223], v[244:247], a[180:183], a[220:223]\n"
"ds_write_b128 v15, a[208:211] offset:20480\n"
"v_mfma_f32_16x16x32_bf16 a[220:223], v[224:227], a[184:187], a[220:223]\n"
"v_mfma_f32_16x16x32_bf16 a[220:223], v[204:207], v[20:23], a[220:223]\n"
"v_mfma_f32_16x16x32_bf16 a[224:227], v[240:243], a[180:183], a[224:227]\n"
"ds_write_b128 v15, a[212:215] offset:24576\n"
"v_mfma_f32_16x16x32_bf16 a[224:227], v[220:223], a[184:187], a[224:227]\n"
"buffer_load_dwordx4 a[168:171], v2, s[20:23], s85 offen offset:1024 sc1\n"
"v_mfma_f32_16x16x32_bf16 a[224:227], v[200:203], v[20:23], a[224:227]\n"
"s_waitcnt vmcnt(12)\n"
"v_mfma_f32_16x16x32_bf16 a[188:191], a[0:3], v[24:27], 0\n"
"ds_write_b128 v15, a[216:219] offset:28672\n"
"v_mfma_f32_16x16x32_bf16 a[188:191], a[20:23], v[28:31], a[188:191]\n"
"buffer_load_dwordx4 a[172:175], v2, s[20:23], s85 offen offset:2048 sc1\n"
"v_mfma_f32_16x16x32_bf16 a[192:195], a[4:7], v[24:27], 0\n"
"v_mfma_f32_16x16x32_bf16 a[192:195], a[24:27], v[28:31], a[192:195]\n"
"ds_write_b128 v15, a[220:223] offset:32768\n"
"v_mfma_f32_16x16x32_bf16 a[196:199], a[8:11], v[24:27], 0\n"
"v_mfma_f32_16x16x32_bf16 a[196:199], a[28:31], v[28:31], a[196:199]\n"
"buffer_load_dwordx4 a[176:179], v2, s[20:23], s85 offen offset:3072 sc1\n"
"v_mfma_f32_16x16x32_bf16 a[200:203], a[12:15], v[24:27], 0\n"
"ds_write_b128 v15, a[224:227] offset:36864\n"
"v_mfma_f32_16x16x32_bf16 a[200:203], a[32:35], v[28:31], a[200:203]\n"
"s_xor_b32 s85, s3, 3\n"
"s_mul_i32 s85, s85, 116736\n"
"v_mfma_f32_16x16x32_bf16 a[204:207], a[16:19], v[24:27], 0\n"
"s_add_u32 s85, s85, s44\n"
"v_mfma_f32_16x16x32_bf16 a[204:207], a[36:39], v[28:31], a[204:207]\n"
"buffer_load_dwordx4 a[180:183], v2, s[20:23], s85 offen offset:0 sc1\n"
"s_waitcnt vmcnt(12)\n"
"v_mfma_f32_16x16x32_bf16 a[188:191], a[40:43], v[32:35], a[188:191]\n"
"v_mfma_f32_16x16x32_bf16 a[188:191], a[60:63], a[128:131], a[188:191]\n"
"v_mfma_f32_16x16x32_bf16 a[188:191], a[80:83], a[132:135], a[188:191]\n"
"v_mfma_f32_16x16x32_bf16 a[192:195], a[44:47], v[32:35], a[192:195]\n"
"v_mfma_f32_16x16x32_bf16 a[192:195], a[64:67], a[128:131], a[192:195]\n"
"buffer_load_dwordx4 a[184:187], v2, s[20:23], s85 offen offset:1024 sc1\n"
"v_mfma_f32_16x16x32_bf16 a[192:195], a[84:87], a[132:135], a[192:195]\n"
"v_mfma_f32_16x16x32_bf16 a[196:199], a[48:51], v[32:35], a[196:199]\n"
"s_waitcnt lgkmcnt(0)\n"
"s_barrier\n"
"v_mfma_f32_16x16x32_bf16 a[196:199], a[68:71], a[128:131], a[196:199]\n"
"ds_read_b128 v[116:119], v156 offset:0\n"
"ds_read_b128 v[120:123], v156 offset:4096\n"
"v_mfma_f32_16x16x32_bf16 a[196:199], a[88:91], a[132:135], a[196:199]\n"
"ds_read_b128 v[124:127], v156 offset:8192\n"
"ds_read_b128 v[128:131], v156 offset:12288\n"
"v_mfma_f32_16x16x32_bf16 a[200:203], a[52:55], v[32:35], a[200:203]\n"
"ds_read_b128 v[132:135], v156 offset:16384\n"
"buffer_load_dwordx4 v[20:23], v2, s[20:23], s85 offen offset:2048 sc1\n"
"v_mfma_f32_16x16x32_bf16 a[200:203], a[72:75], a[128:131], a[200:203]\n"
"ds_read_b128 v[136:139], v157 offset:20480\n"
"ds_read_b128 v[140:143], v157 offset:24576\n"
"v_mfma_f32_16x16x32_bf16 a[200:203], a[92:95], a[132:135], a[200:203]\n"
"ds_read_b128 v[144:147], v157 offset:28672\n"
"ds_read_b128 v[148:151], v157 offset:32768\n"
"v_mfma_f32_16x16x32_bf16 a[204:207], a[56:59], v[32:35], a[204:207]\n"
"ds_read_b128 v[152:155], v157 offset:36864\n"
"buffer_load_dwordx4 v[24:27], v2, s[20:23], s85 offen offset:3072 sc1\n"
"v_mfma_f32_16x16x32_bf16 a[204:207], a[76:79], a[128:131], a[204:207]\n"
"v_mfma_f32_16x16x32_bf16 a[204:207], a[96:99], a[132:135], a[204:207]\n"
"s_waitcnt vmcnt(12)\n"
"v_mfma_f32_16x16x32_bf16 a[188:191], a[100:103], a[136:139], a[188:191]\n"
"s_add_u32 s85, s85, 4096\n"
"v_mfma_f32_16x16x32_bf16 a[188:191], v[236:239], a[140:143], a[188:191]\n"
"v_mfma_f32_16x16x32_bf16 a[188:191], v[216:219], a[144:147], a[188:191]\n"
"buffer_load_dwordx4 v[28:31], v2, s[20:23], s85 offen offset:0 sc1\n"
"v_mfma_f32_16x16x32_bf16 a[192:195], v[252:255], a[136:139], a[192:195]\n"
"v_mfma_f32_16x16x32_bf16 a[192:195], v[232:235], a[140:143], a[192:195]\n"
"s_waitcnt lgkmcnt(4)\n"
"v_add_f32 v116, v116, v136\n"
"v_add_f32 v117, v117, v137\n"
"v_mfma_f32_16x16x32_bf16 a[192:195], v[212:215], a[144:147], a[192:195]\n"
"v_add_f32 v118, v118, v138\n"
"v_add_f32 v119, v119, v139\n"
"v_mfma_f32_16x16x32_bf16 a[196:199], v[248:251], a[136:139], a[196:199]\n"
"buffer_load_dwordx4 v[32:35], v2, s[20:23], s85 offen offset:1024 sc1\n"
"s_waitcnt lgkmcnt(3)\n"
"v_add_f32 v120, v120, v140\n"
"v_mfma_f32_16x16x32_bf16 a[196:199], v[228:231], a[140:143], a[196:199]\n"
"v_add_f32 v121, v121, v141\n"
"v_add_f32 v122, v122, v142\n"
"v_mfma_f32_16x16x32_bf16 a[196:199], v[208:211], a[144:147], a[196:199]\n"
"v_add_f32 v123, v123, v143\n"
"s_waitcnt lgkmcnt(2)\n"
"v_add_f32 v124, v124, v144\n"
"v_mfma_f32_16x16x32_bf16 a[200:203], v[244:247], a[136:139], a[200:203]\n"
"ds_write_b128 v15, a[188:191] offset:40960\n"
"v_mfma_f32_16x16x32_bf16 a[200:203], v[224:227], a[140:143], a[200:203]\n"
"buffer_load_dwordx4 a[208:211], v2, s[20:23], s85 offen offset:2048 sc1\n"
"v_add_f32 v125, v125, v145\n"
"v_mfma_f32_16x16x32_bf16 a[200:203], v[204:207], a[144:147], a[200:203]\n"
"v_add_f32 v126, v126, v146\n"
"v_add_f32 v127, v127, v147\n"
"v_mfma_f32_16x16x32_bf16 a[204:207], v[240:243], a[136:139], a[204:207]\n"
"ds_write_b128 v15, a[192:195] offset:45056\n"
"v_mfma_f32_16x16x32_bf16 a[204:207], v[220:223], a[140:143], a[204:207]\n"
"s_waitcnt lgkmcnt(3)\n"
"v_add_f32 v128, v128, v148\n"
"v_add_f32 v129, v129, v149\n"
"v_mfma_f32_16x16x32_bf16 a[204:207], v[200:203], a[144:147], a[204:207]\n"
"v_add_f32 v130, v130, v150\n"
"s_waitcnt vmcnt(13)\n"
"v_mfma_f32_16x16x32_bf16 v[76:79], a[0:3], a[148:151], 0\n"
"ds_write_b128 v15, a[196:199] offset:49152\n"
"v_mfma_f32_16x16x32_bf16 v[76:79], a[20:23], a[152:155], v[76:79]\n"
"buffer_load_dwordx4 a[212:215], v2, s[20:23], s85 offen offset:3072 sc1\n"
"s_xor_b32 s85, s3, 2\n"
"v_mfma_f32_16x16x32_bf16 v[80:83], a[4:7], a[148:151], 0\n"
"s_mul_i32 s85, s85, 116736\n"
"s_add_u32 s85, s85, s44\n"
"v_mfma_f32_16x16x32_bf16 v[80:83], a[24:27], a[152:155], v[80:83]\n"
"ds_write_b128 v15, a[200:203] offset:53248\n"
"v_mfma_f32_16x16x32_bf16 v[84:87], a[8:11], a[148:151], 0\n"
"buffer_load_dwordx4 a[216:219], v2, s[20:23], s85 offen offset:0 sc1\n"
"v_add_f32 v131, v131, v151\n"
"v_mfma_f32_16x16x32_bf16 v[84:87], a[28:31], a[152:155], v[84:87]\n"
"s_waitcnt lgkmcnt(4)\n"
"v_add_f32 v132, v132, v152\n"
"v_add_f32 v133, v133, v153\n"
"v_mfma_f32_16x16x32_bf16 v[88:91], a[12:15], a[148:151], 0\n"
"ds_write_b128 v15, a[204:207] offset:57344\n"
"v_mfma_f32_16x16x32_bf16 v[88:91], a[32:35], a[152:155], v[88:91]\n"
"v_add_f32 v134, v134, v154\n"
"v_add_f32 v135, v135, v155\n"
"v_mfma_f32_16x16x32_bf16 v[92:95], a[16:19], a[148:151], 0\n"
"buffer_load_dwordx4 a[220:223], v2, s[20:23], s85 offen offset:1024 sc1\n"
"v_mfma_f32_16x16x32_bf16 v[92:95], a[36:39], a[152:155], v[92:95]\n"
"s_waitcnt vmcnt(13)\n"
"v_mfma_f32_16x16x32_bf16 v[76:79], a[40:43], a[156:159], v[76:79]\n"
"v_mfma_f32_16x16x32_bf16 v[76:79], a[60:63], a[160:163], v[76:79]\n"
"v_mfma_f32_16x16x32_bf16 v[76:79], a[80:83], a[164:167], v[76:79]\n"
"buffer_load_dwordx4 a[224:227], v2, s[20:23], s85 offen offset:2048 sc1\n"
"v_mfma_f32_16x16x32_bf16 v[80:83], a[44:47], a[156:159], v[80:83]\n"
"v_mfma_f32_16x16x32_bf16 v[80:83], a[64:67], a[160:163], v[80:83]\n"
"s_waitcnt lgkmcnt(0)\n"
"s_barrier\n"
"v_mfma_f32_16x16x32_bf16 v[80:83], a[84:87], a[164:167], v[80:83]\n"
"ds_read_b128 v[56:59], v158 offset:40960\n"
"ds_read_b128 v[60:63], v158 offset:45056\n"
"v_mfma_f32_16x16x32_bf16 v[84:87], a[48:51], a[156:159], v[84:87]\n"
"ds_read_b128 v[64:67], v158 offset:49152\n"
"ds_read_b128 v[68:71], v158 offset:53248\n"
"v_mfma_f32_16x16x32_bf16 v[84:87], a[68:71], a[160:163], v[84:87]\n"
"ds_read_b128 v[72:75], v158 offset:57344\n"
"v_mfma_f32_16x16x32_bf16 v[84:87], a[88:91], a[164:167], v[84:87]\n"
"v_mfma_f32_16x16x32_bf16 v[88:91], a[52:55], a[156:159], v[88:91]\n"
"v_mfma_f32_16x16x32_bf16 v[88:91], a[72:75], a[160:163], v[88:91]\n"
"v_mfma_f32_16x16x32_bf16 v[88:91], a[92:95], a[164:167], v[88:91]\n"
"v_mfma_f32_16x16x32_bf16 v[92:95], a[56:59], a[156:159], v[92:95]\n"
"v_mfma_f32_16x16x32_bf16 v[92:95], a[76:79], a[160:163], v[92:95]\n"
"v_mfma_f32_16x16x32_bf16 v[92:95], a[96:99], a[164:167], v[92:95]\n"
"s_waitcnt vmcnt(11)\n"
"v_mfma_f32_16x16x32_bf16 v[76:79], a[100:103], a[168:171], v[76:79]\n"
"buffer_load_dwordx4 a[188:191], v2, s[20:23], s85 offen offset:3072 sc1\n"
"v_mfma_f32_16x16x32_bf16 v[76:79], v[236:239], a[172:175], v[76:79]\n"
"v_mfma_f32_16x16x32_bf16 v[76:79], v[216:219], a[176:179], v[76:79]\n"
"s_add_u32 s85, s85, 4096\n"
"v_mfma_f32_16x16x32_bf16 v[80:83], v[252:255], a[168:171], v[80:83]\n"
"v_mfma_f32_16x16x32_bf16 v[80:83], v[232:235], a[172:175], v[80:83]\n"
"buffer_load_dwordx4 a[192:195], v2, s[20:23], s85 offen offset:0 sc1\n"
"v_mfma_f32_16x16x32_bf16 v[80:83], v[212:215], a[176:179], v[80:83]\n"
"v_mfma_f32_16x16x32_bf16 v[84:87], v[248:251], a[168:171], v[84:87]\n"
"v_mfma_f32_16x16x32_bf16 v[84:87], v[228:231], a[172:175], v[84:87]\n"
"s_nop 0\n"
"v_add_f32 v116, v116, v76\n"
"v_add_f32 v117, v117, v77\n"
"v_mfma_f32_16x16x32_bf16 v[84:87], v[208:211], a[176:179], v[84:87]\n"
"buffer_load_dwordx4 a[196:199], v2, s[20:23], s85 offen offset:1024 sc1\n"
"v_add_f32 v118, v118, v78\n"
"v_mfma_f32_16x16x32_bf16 v[88:91], v[244:247], a[168:171], v[88:91]\n"
"v_add_f32 v119, v119, v79\n"
"s_waitcnt lgkmcnt(4)\n"
"v_add_f32 v116, v116, v56\n"
"v_mfma_f32_16x16x32_bf16 v[88:91], v[224:227], a[172:175], v[88:91]\n"
"v_add_f32 v117, v117, v57\n"
"v_add_f32 v118, v118, v58\n"
"v_mfma_f32_16x16x32_bf16 v[88:91], v[204:207], a[176:179], v[88:91]\n"
"v_add_f32 v119, v119, v59\n"
"v_cvt_pk_bf16_f32 v96, v116, v117\n"
"v_mfma_f32_16x16x32_bf16 v[92:95], v[240:243], a[168:171], v[92:95]\n"
"v_cvt_pk_bf16_f32 v97, v118, v16\n"
"v_add_f32 v120, v120, v80\n"
"v_mfma_f32_16x16x32_bf16 v[92:95], v[220:223], a[172:175], v[92:95]\n"
"v_add_f32 v121, v121, v81\n"
"v_add_f32 v122, v122, v82\n"
"v_mfma_f32_16x16x32_bf16 v[92:95], v[200:203], a[176:179], v[92:95]\n"
"v_add_f32 v123, v123, v83\n"
"s_waitcnt vmcnt(12)\n"
"v_mfma_f32_16x16x32_bf16 a[128:131], a[0:3], a[180:183], 0\n"
"s_waitcnt lgkmcnt(3)\n"
"v_add_f32 v120, v120, v60\n"
"v_add_f32 v121, v121, v61\n"
"v_mfma_f32_16x16x32_bf16 a[128:131], a[20:23], a[184:187], a[128:131]\n"
"v_add_f32 v122, v122, v62\n"
"v_add_f32 v123, v123, v63\n"
"v_mfma_f32_16x16x32_bf16 a[132:135], a[4:7], a[180:183], 0\n"
"v_cvt_pk_bf16_f32 v98, v119, v120\n"
"v_cvt_pk_bf16_f32 v99, v121, v16\n"
"v_mfma_f32_16x16x32_bf16 a[132:135], a[24:27], a[184:187], a[132:135]\n"
"buffer_store_dwordx4 v[96:99], v2, s[20:23], s60 offen offset:0 sc1\n"
"v_mfma_f32_16x16x32_bf16 a[136:139], a[8:11], a[180:183], 0\n"
"v_add_f32 v124, v124, v84\n"
"v_add_f32 v125, v125, v85\n"
"v_mfma_f32_16x16x32_bf16 a[136:139], a[28:31], a[184:187], a[136:139]\n"
"v_add_f32 v126, v126, v86\n"
"v_add_f32 v127, v127, v87\n"
"v_mfma_f32_16x16x32_bf16 a[140:143], a[12:15], a[180:183], 0\n"
"s_waitcnt lgkmcnt(2)\n"
"v_add_f32 v124, v124, v64\n"
"v_add_f32 v125, v125, v65\n"
"v_mfma_f32_16x16x32_bf16 a[140:143], a[32:35], a[184:187], a[140:143]\n"
"v_add_f32 v126, v126, v66\n"
"v_add_f32 v127, v127, v67\n"
"v_mfma_f32_16x16x32_bf16 a[144:147], a[16:19], a[180:183], 0\n"
"v_cvt_pk_bf16_f32 v100, v122, v123\n"
"v_cvt_pk_bf16_f32 v101, v124, v16\n"
"v_mfma_f32_16x16x32_bf16 a[144:147], a[36:39], a[184:187], a[144:147]\n"
"v_cvt_pk_bf16_f32 v102, v125, v126\n"
"s_waitcnt vmcnt(10)\n"
"v_mfma_f32_16x16x32_bf16 a[128:131], a[40:43], v[20:23], a[128:131]\n"
"buffer_load_dwordx4 a[200:203], v2, s[20:23], s85 offen offset:2048 sc1\n"
"v_cvt_pk_bf16_f32 v103, v127, v16\n"
"v_mfma_f32_16x16x32_bf16 a[128:131], a[60:63], v[24:27], a[128:131]\n"
"buffer_store_dwordx4 v[100:103], v2, s[20:23], s60 offen offset:1024 sc1\n"
"v_mfma_f32_16x16x32_bf16 a[128:131], a[80:83], v[28:31], a[128:131]\n"
"v_add_f32 v128, v128, v88\n"
"v_add_f32 v129, v129, v89\n"
"v_mfma_f32_16x16x32_bf16 a[132:135], a[44:47], v[20:23], a[132:135]\n"
"v_add_f32 v130, v130, v90\n"
"v_add_f32 v131, v131, v91\n"
"v_mfma_f32_16x16x32_bf16 a[132:135], a[64:67], v[24:27], a[132:135]\n"
"s_waitcnt lgkmcnt(1)\n"
"v_add_f32 v128, v128, v68\n"
"v_add_f32 v129, v129, v69\n"
"v_mfma_f32_16x16x32_bf16 a[132:135], a[84:87], v[28:31], a[132:135]\n"
"v_add_f32 v130, v130, v70\n"
"v_add_f32 v131, v131, v71\n"
"v_mfma_f32_16x16x32_bf16 a[136:139], a[48:51], v[20:23], a[136:139]\n"
"v_cvt_pk_bf16_f32 v104, v128, v129\n"
"v_cvt_pk_bf16_f32 v105, v130, v16\n"
"v_mfma_f32_16x16x32_bf16 a[136:139], a[68:71], v[24:27], a[136:139]\n"
"v_add_f32 v132, v132, v92\n"
"v_add_f32 v133, v133, v93\n"
"v_mfma_f32_16x16x32_bf16 a[136:139], a[88:91], v[28:31], a[136:139]\n"
"v_add_f32 v134, v134, v94\n"
"v_add_f32 v135, v135, v95\n"
"v_mfma_f32_16x16x32_bf16 a[140:143], a[52:55], v[20:23], a[140:143]\n"
"buffer_load_dwordx4 a[204:207], v2, s[20:23], s85 offen offset:3072 sc1\n"
"s_waitcnt lgkmcnt(0)\n"
"v_add_f32 v132, v132, v72\n"
"v_mfma_f32_16x16x32_bf16 a[140:143], a[72:75], v[24:27], a[140:143]\n"
"v_add_f32 v133, v133, v73\n"
"v_add_f32 v134, v134, v74\n"
"v_mfma_f32_16x16x32_bf16 a[140:143], a[92:95], v[28:31], a[140:143]\n"
"v_add_f32 v135, v135, v75\n"
"v_cvt_pk_bf16_f32 v106, v131, v132\n"
"v_mfma_f32_16x16x32_bf16 a[144:147], a[56:59], v[20:23], a[144:147]\n"
"v_cvt_pk_bf16_f32 v107, v133, v16\n"
"v_cvt_pk_bf16_f32 v108, v134, v135\n"
"v_mfma_f32_16x16x32_bf16 a[144:147], a[76:79], v[24:27], a[144:147]\n"
"buffer_store_dwordx4 v[104:107], v2, s[20:23], s60 offen offset:2048 sc1\n"
"v_mfma_f32_16x16x32_bf16 a[144:147], a[96:99], v[28:31], a[144:147]\n"
"s_waitcnt vmcnt(11)\n"
"s_xor_b32 s85, s3, 1\n"
"v_mfma_f32_16x16x32_bf16 a[128:131], a[100:103], v[32:35], a[128:131]\n"
"s_mul_i32 s85, s85, 116736\n"
"s_add_u32 s85, s85, s44\n"
"v_mfma_f32_16x16x32_bf16 a[128:131], v[236:239], a[208:211], a[128:131]\n"
"buffer_load_dwordx4 v[20:23], v2, s[20:23], s85 offen offset:0 sc1\n"
"v_cvt_pk_bf16_f32 v109, v135, v16\n"
"v_mfma_f32_16x16x32_bf16 a[128:131], v[216:219], a[212:215], a[128:131]\n"
"buffer_store_dwordx2 v[108:109], v9, s[20:23], s60 offen offset:3072 sc1\n"
"v_mfma_f32_16x16x32_bf16 a[132:135], v[252:255], v[32:35], a[132:135]\n"
"v_lshlrev_b32 v76, 16, v44\n"
"v_lshlrev_b32 v78, 16, v45\n"
"v_mfma_f32_16x16x32_bf16 a[132:135], v[232:235], a[208:211], a[132:135]\n"
"buffer_load_dwordx4 v[24:27], v2, s[20:23], s85 offen offset:1024 sc1\n"
"v_lshlrev_b32 v80, 16, v46\n"
"v_mfma_f32_16x16x32_bf16 a[132:135], v[212:215], a[212:215], a[132:135]\n"
"v_lshlrev_b32 v82, 16, v47\n"
"v_lshlrev_b32 v84, 16, v48\n"
"v_mfma_f32_16x16x32_bf16 a[136:139], v[248:251], v[32:35], a[136:139]\n"
"v_lshlrev_b32 v86, 16, v49\n"
"v_lshlrev_b32 v88, 16, v50\n"
"v_mfma_f32_16x16x32_bf16 a[136:139], v[228:231], a[208:211], a[136:139]\n"
"v_lshlrev_b32 v90, 16, v51\n"
"v_lshlrev_b32 v92, 16, v52\n"
"v_mfma_f32_16x16x32_bf16 a[136:139], v[208:211], a[212:215], a[136:139]\n"
"buffer_load_dwordx4 v[28:31], v2, s[20:23], s85 offen offset:2048 sc1\n"
"v_lshlrev_b32 v94, 16, v53\n"
"v_mfma_f32_16x16x32_bf16 a[140:143], v[244:247], v[32:35], a[140:143]\n"
"v_and_b32 v77, 0xffff0000, v44\n"
"v_and_b32 v79, 0xffff0000, v45\n"
"v_mfma_f32_16x16x32_bf16 a[140:143], v[224:227], a[208:211], a[140:143]\n"
"v_and_b32 v81, 0xffff0000, v46\n"
"v_and_b32 v83, 0xffff0000, v47\n"
"v_mfma_f32_16x16x32_bf16 a[140:143], v[204:207], a[212:215], a[140:143]\n"
"v_and_b32 v85, 0xffff0000, v48\n"
"v_and_b32 v87, 0xffff0000, v49\n"
"v_mfma_f32_16x16x32_bf16 a[144:147], v[240:243], v[32:35], a[144:147]\n"
"v_and_b32 v89, 0xffff0000, v50\n"
"v_and_b32 v91, 0xffff0000, v51\n"
"v_mfma_f32_16x16x32_bf16 a[144:147], v[220:223], a[208:211], a[144:147]\n"
"v_and_b32 v93, 0xffff0000, v52\n"
"v_and_b32 v95, 0xffff0000, v53\n"
"v_mfma_f32_16x16x32_bf16 a[144:147], v[200:203], a[212:215], a[144:147]\n"
"v_add_u32 v14, 0, v1\n"
"s_waitcnt vmcnt(13)\n"
"v_mfma_f32_16x16x32_bf16 a[148:151], a[0:3], a[216:219], 0\n"
"buffer_load_dwordx4 v[32:35], v2, s[20:23], s85 offen offset:3072 sc1\n"
"s_add_u32 s85, s85, 4096\n"
"v_mfma_f32_16x16x32_bf16 a[148:151], a[20:23], a[220:223], a[148:151]\n"
"v_mul_u32_u24 v17, 6554, v14\n"
"v_lshrrev_b32 v17, 16, v17\n"
"v_mfma_f32_16x16x32_bf16 a[152:155], a[4:7], a[216:219], 0\n"
"buffer_load_dwordx4 a[208:211], v2, s[20:23], s85 offen offset:0 sc1\n"
"v_add_u32 v14, v14, v17\n"
"v_mfma_f32_16x16x32_bf16 a[152:155], a[24:27], a[220:223], a[152:155]\n"
"v_lshl_add_u32 v17, v14, 4, s62\n"
"v_add_u32 v14, 64, v1\n"
"v_mfma_f32_16x16x32_bf16 a[156:159], a[8:11], a[216:219], 0\n"
"v_mul_u32_u24 v18, 6554, v14\n"
"v_lshrrev_b32 v18, 16, v18\n"
"v_mfma_f32_16x16x32_bf16 a[156:159], a[28:31], a[220:223], a[156:159]\n"
"v_add_u32 v14, v14, v18\n"
"v_lshl_add_u32 v18, v14, 4, s62\n"
"v_mfma_f32_16x16x32_bf16 a[160:163], a[12:15], a[216:219], 0\n"
"buffer_load_dwordx4 a[212:215], v2, s[20:23], s85 offen offset:1024 sc1\n"
"v_add_u32 v14, 128, v1\n"
"v_mfma_f32_16x16x32_bf16 a[160:163], a[32:35], a[220:223], a[160:163]\n"
"v_mul_u32_u24 v19, 6554, v14\n"
"v_lshrrev_b32 v19, 16, v19\n"
"v_mfma_f32_16x16x32_bf16 a[164:167], a[16:19], a[216:219], 0\n"
"v_add_u32 v14, v14, v19\n"
"v_lshl_add_u32 v19, v14, 4, s62\n"
"v_mfma_f32_16x16x32_bf16 a[164:167], a[36:39], a[220:223], a[164:167]\n"
"s_waitcnt vmcnt(13)\n"
"v_mfma_f32_16x16x32_bf16 a[148:151], a[40:43], a[224:227], a[148:151]\n"
"buffer_load_dwordx4 v[136:139], v2, s[20:23], s85 offen offset:2048 sc1\n"
"v_mfma_f32_16x16x32_bf16 a[148:151], a[60:63], a[188:191], a[148:151]\n"
"v_mfma_f32_16x16x32_bf16 a[148:151], a[80:83], a[192:195], a[148:151]\n"
"v_mfma_f32_16x16x32_bf16 a[152:155], a[44:47], a[224:227], a[152:155]\n"
"v_mfma_f32_16x16x32_bf16 a[152:155], a[64:67], a[188:191], a[152:155]\n"
"buffer_load_dwordx4 v[140:143], v2, s[20:23], s85 offen offset:3072 sc1\n"
"v_mfma_f32_16x16x32_bf16 a[152:155], a[84:87], a[192:195], a[152:155]\n"
"v_mfma_f32_16x16x32_bf16 a[156:159], a[48:51], a[224:227], a[156:159]\n"
"v_mfma_f32_16x16x32_bf16 a[156:159], a[68:71], a[188:191], a[156:159]\n"
"v_mfma_f32_16x16x32_bf16 a[156:159], a[88:91], a[192:195], a[156:159]\n"
"v_mfma_f32_16x16x32_bf16 a[160:163], a[52:55], a[224:227], a[160:163]\n"
"buffer_load_dword v12, v11, s[16:19], 0 offen sc1\n"
"s_xor_b32 s85, s3, 0\n"
"v_mfma_f32_16x16x32_bf16 a[160:163], a[72:75], a[188:191], a[160:163]\n"
"s_mul_i32 s85, s85, 116736\n"
"s_add_u32 s85, s85, s44\n"
"v_mfma_f32_16x16x32_bf16 a[160:163], a[92:95], a[192:195], a[160:163]\n"
"buffer_load_dwordx4 v[144:147], v2, s[20:23], s85 offen offset:0 sc1\n"
"v_mfma_f32_16x16x32_bf16 a[164:167], a[56:59], a[224:227], a[164:167]\n"
"v_mfma_f32_16x16x32_bf16 a[164:167], a[76:79], a[188:191], a[164:167]\n"
"v_mfma_f32_16x16x32_bf16 a[164:167], a[96:99], a[192:195], a[164:167]\n"
"s_waitcnt vmcnt(12)\n"
"v_mfma_f32_16x16x32_bf16 a[148:151], a[100:103], a[196:199], a[148:151]\n"
"v_mfma_f32_16x16x32_bf16 a[148:151], v[236:239], a[200:203], a[148:151]\n"
"buffer_load_dwordx4 v[148:151], v2, s[20:23], s85 offen offset:1024 sc1\n"
"v_mfma_f32_16x16x32_bf16 a[148:151], v[216:219], a[204:207], a[148:151]\n"
"v_mfma_f32_16x16x32_bf16 a[152:155], v[252:255], a[196:199], a[152:155]\n"
"v_mfma_f32_16x16x32_bf16 a[152:155], v[232:235], a[200:203], a[152:155]\n"
"v_mfma_f32_16x16x32_bf16 a[152:155], v[212:215], a[204:207], a[152:155]\n"
"buffer_load_dwordx4 v[152:155], v2, s[20:23], s85 offen offset:2048 sc1\n"
"v_mfma_f32_16x16x32_bf16 a[156:159], v[248:251], a[196:199], a[156:159]\n"
"v_mfma_f32_16x16x32_bf16 a[156:159], v[228:231], a[200:203], a[156:159]\n"
"v_mfma_f32_16x16x32_bf16 a[156:159], v[208:211], a[204:207], a[156:159]\n"
"v_mfma_f32_16x16x32_bf16 a[160:163], v[244:247], a[196:199], a[160:163]\n"
"buffer_load_dwordx4 a[216:219], v2, s[20:23], s85 offen offset:3072 sc1\n"
"v_mfma_f32_16x16x32_bf16 a[160:163], v[224:227], a[200:203], a[160:163]\n"
"v_mfma_f32_16x16x32_bf16 a[160:163], v[204:207], a[204:207], a[160:163]\n"
"s_add_u32 s85, s85, 4096\n"
"v_mfma_f32_16x16x32_bf16 a[164:167], v[240:243], a[196:199], a[164:167]\n"
"v_mfma_f32_16x16x32_bf16 a[164:167], v[220:223], a[200:203], a[164:167]\n"
"buffer_load_dwordx4 a[220:223], v2, s[20:23], s85 offen offset:0 sc1\n"
"v_mfma_f32_16x16x32_bf16 a[164:167], v[200:203], a[204:207], a[164:167]\n"
"s_waitcnt vmcnt(12)\n"
"v_mfma_f32_16x16x32_bf16 a[168:171], a[0:3], v[20:23], 0\n"
"v_mfma_f32_16x16x32_bf16 a[168:171], a[20:23], v[24:27], a[168:171]\n"
"v_mfma_f32_16x16x32_bf16 a[172:175], a[4:7], v[20:23], 0\n"
"v_mfma_f32_16x16x32_bf16 a[172:175], a[24:27], v[24:27], a[172:175]\n"
"buffer_load_dwordx4 a[188:191], v2, s[20:23], s85 offen offset:1024 sc1\n"
"v_mfma_f32_16x16x32_bf16 a[176:179], a[8:11], v[20:23], 0\n"
"v_mfma_f32_16x16x32_bf16 a[176:179], a[28:31], v[24:27], a[176:179]\n"
"v_mfma_f32_16x16x32_bf16 a[180:183], a[12:15], v[20:23], 0\n"
"v_mfma_f32_16x16x32_bf16 a[180:183], a[32:35], v[24:27], a[180:183]\n"
"buffer_load_dwordx4 a[192:195], v2, s[20:23], s85 offen offset:2048 sc1\n"
"v_mfma_f32_16x16x32_bf16 a[184:187], a[16:19], v[20:23], 0\n"
"v_mfma_f32_16x16x32_bf16 a[184:187], a[36:39], v[24:27], a[184:187]\n"
"s_waitcnt vmcnt(11)\n"
"v_mfma_f32_16x16x32_bf16 a[168:171], a[40:43], v[28:31], a[168:171]\n"
"v_mfma_f32_16x16x32_bf16 a[168:171], a[60:63], v[32:35], a[168:171]\n"
"buffer_load_dwordx4 a[224:227], v2, s[20:23], s85 offen offset:3072 sc1\n"
"v_mfma_f32_16x16x32_bf16 a[168:171], a[80:83], a[208:211], a[168:171]\n"
"v_mfma_f32_16x16x32_bf16 a[172:175], a[44:47], v[28:31], a[172:175]\n"
"v_mfma_f32_16x16x32_bf16 a[172:175], a[64:67], v[32:35], a[172:175]\n"
"v_mfma_f32_16x16x32_bf16 a[172:175], a[84:87], a[208:211], a[172:175]\n"
"v_mfma_f32_16x16x32_bf16 a[176:179], a[48:51], v[28:31], a[176:179]\n"
"v_mfma_f32_16x16x32_bf16 a[176:179], a[68:71], v[32:35], a[176:179]\n"
"v_mfma_f32_16x16x32_bf16 a[176:179], a[88:91], a[208:211], a[176:179]\n"
"v_mfma_f32_16x16x32_bf16 a[180:183], a[52:55], v[28:31], a[180:183]\n"
"v_mfma_f32_16x16x32_bf16 a[180:183], a[72:75], v[32:35], a[180:183]\n"
"v_mfma_f32_16x16x32_bf16 a[180:183], a[92:95], a[208:211], a[180:183]\n"
"v_mfma_f32_16x16x32_bf16 a[184:187], a[56:59], v[28:31], a[184:187]\n"
"v_mfma_f32_16x16x32_bf16 a[184:187], a[76:79], v[32:35], a[184:187]\n"
"v_mfma_f32_16x16x32_bf16 a[184:187], a[96:99], a[208:211], a[184:187]\n"
"s_waitcnt vmcnt(9)\n"
"v_mfma_f32_16x16x32_bf16 a[168:171], a[100:103], a[212:215], a[168:171]\n"
"v_mfma_f32_16x16x32_bf16 a[168:171], v[236:239], v[136:139], a[168:171]\n"
"v_mfma_f32_16x16x32_bf16 a[168:171], v[216:219], v[140:143], a[168:171]\n"
"v_mfma_f32_16x16x32_bf16 a[172:175], v[252:255], a[212:215], a[172:175]\n"
"s_waitcnt vmcnt(8)\n"
"v_mfma_f32_16x16x32_bf16 a[172:175], v[232:235], v[136:139], a[172:175]\n"
"v_cmp_ne_u32 vcc, s52, v12\n"
"v_mfma_f32_16x16x32_bf16 a[172:175], v[212:215], v[140:143], a[172:175]\n"
"s_cbranch_vccnz .Lfb_slow_8\n"
".Lfb_back_9:\n"
"v_mfma_f32_16x16x32_bf16 a[176:179], v[248:251], a[212:215], a[176:179]\n"
"s_xor_b32 s85, s3, 3\n"
"s_mul_i32 s85, s85, 116736\n"
"v_mfma_f32_16x16x32_bf16 a[176:179], v[228:231], v[136:139], a[176:179]\n"
"s_add_u32 s85, s85, s84\n"
"v_mfma_f32_16x16x32_bf16 a[176:179], v[208:211], v[140:143], a[176:179]\n"
"buffer_load_dwordx4 a[196:199], v2, s[20:23], s85 offen offset:0 sc1\n"
"v_mfma_f32_16x16x32_bf16 a[180:183], v[244:247], a[212:215], a[180:183]\n"
"v_mfma_f32_16x16x32_bf16 a[180:183], v[224:227], v[136:139], a[180:183]\n"
"buffer_load_dwordx4 a[200:203], v2, s[20:23], s85 offen offset:1024 sc1\n"
"v_mfma_f32_16x16x32_bf16 a[180:183], v[204:207], v[140:143], a[180:183]\n"
"v_mfma_f32_16x16x32_bf16 a[184:187], v[240:243], a[212:215], a[184:187]\n"
"v_mfma_f32_16x16x32_bf16 a[184:187], v[220:223], v[136:139], a[184:187]\n"
"v_mfma_f32_16x16x32_bf16 a[184:187], v[200:203], v[140:143], a[184:187]\n"
"buffer_load_dwordx4 a[204:207], v2, s[20:23], s85 offen offset:2048 sc1\n"
"s_waitcnt vmcnt(9)\n"
"v_mfma_f32_16x16x32_bf16 v[96:99], a[0:3], v[144:147], 0\n"
"v_mfma_f32_16x16x32_bf16 v[96:99], a[20:23], v[148:151], v[96:99]\n"
"buffer_load_dwordx4 v[20:23], v2, s[20:23], s85 offen offset:3072 sc1\n"
"s_add_u32 s85, s85, 4096\n"
"v_mfma_f32_16x16x32_bf16 v[100:103], a[4:7], v[144:147], 0\n"
"v_mfma_f32_16x16x32_bf16 v[100:103], a[24:27], v[148:151], v[100:103]\n"
"buffer_load_dwordx4 v[24:27], v2, s[20:23], s85 offen offset:0 sc1\n"
"v_mfma_f32_16x16x32_bf16 v[104:107], a[8:11], v[144:147], 0\n"
"s_xor_b32 s85, s3, 2\n"
"s_mul_i32 s85, s85, 116736\n"
"v_mfma_f32_16x16x32_bf16 v[104:107], a[28:31], v[148:151], v[104:107]\n"
"s_add_u32 s85, s85, s84\n"
"v_mfma_f32_16x16x32_bf16 v[108:111], a[12:15], v[144:147], 0\n"
"v_mfma_f32_16x16x32_bf16 v[108:111], a[32:35], v[148:151], v[108:111]\n"
"buffer_load_dwordx4 v[28:31], v2, s[20:23], s85 offen offset:0 sc1\n"
"v_mfma_f32_16x16x32_bf16 v[112:115], a[16:19], v[144:147], 0\n"
"v_mfma_f32_16x16x32_bf16 v[112:115], a[36:39], v[148:151], v[112:115]\n"
"s_waitcnt vmcnt(9)\n"
"v_mfma_f32_16x16x32_bf16 v[96:99], a[40:43], v[152:155], v[96:99]\n"
"v_mfma_f32_16x16x32_bf16 v[96:99], a[60:63], a[216:219], v[96:99]\n"
"buffer_load_dwordx4 v[32:35], v2, s[20:23], s85 offen offset:1024 sc1\n"
"v_mfma_f32_16x16x32_bf16 v[96:99], a[80:83], a[220:223], v[96:99]\n"
"v_mfma_f32_16x16x32_bf16 v[100:103], a[44:47], v[152:155], v[100:103]\n"
"v_mfma_f32_16x16x32_bf16 v[100:103], a[64:67], a[216:219], v[100:103]\n"
"v_mfma_f32_16x16x32_bf16 v[100:103], a[84:87], a[220:223], v[100:103]\n"
"buffer_load_dwordx4 a[208:211], v2, s[20:23], s85 offen offset:2048 sc1\n"
"v_mfma_f32_16x16x32_bf16 v[104:107], a[48:51], v[152:155], v[104:107]\n"
"v_mfma_f32_16x16x32_bf16 v[104:107], a[68:71], a[216:219], v[104:107]\n"
"v_mfma_f32_16x16x32_bf16 v[104:107], a[88:91], a[220:223], v[104:107]\n"
"v_mfma_f32_16x16x32_bf16 v[108:111], a[52:55], v[152:155], v[108:111]\n"
"v_mfma_f32_16x16x32_bf16 v[108:111], a[72:75], a[216:219], v[108:111]\n"
"buffer_load_dwordx4 a[212:215], v2, s[20:23], s85 offen offset:3072 sc1\n"
"v_mfma_f32_16x16x32_bf16 v[108:111], a[92:95], a[220:223], v[108:111]\n"
"v_mfma_f32_16x16x32_bf16 v[112:115], a[56:59], v[152:155], v[112:115]\n"
"v_mfma_f32_16x16x32_bf16 v[112:115], a[76:79], a[216:219], v[112:115]\n"
"v_mfma_f32_16x16x32_bf16 v[112:115], a[96:99], a[220:223], v[112:115]\n"
"s_waitcnt vmcnt(9)\n"
"s_add_u32 s85, s85, 4096\n"
"v_mfma_f32_16x16x32_bf16 v[96:99], a[100:103], a[188:191], v[96:99]\n"
"buffer_load_dwordx4 a[216:219], v2, s[20:23], s85 offen offset:0 sc1\n"
"v_mfma_f32_16x16x32_bf16 v[96:99], v[236:239], a[192:195], v[96:99]\n"
"s_xor_b32 s85, s3, 1\n"
"s_mul_i32 s85, s85, 116736\n"
"v_mfma_f32_16x16x32_bf16 v[96:99], v[216:219], a[224:227], v[96:99]\n"
"s_add_u32 s85, s85, s84\n"
"v_mfma_f32_16x16x32_bf16 v[100:103], v[252:255], a[188:191], v[100:103]\n"
"v_mfma_f32_16x16x32_bf16 v[100:103], v[232:235], a[192:195], v[100:103]\n"
"buffer_load_dwordx4 a[220:223], v2, s[20:23], s85 offen offset:0 sc1\n"
"v_mfma_f32_16x16x32_bf16 v[100:103], v[212:215], a[224:227], v[100:103]\n"
"v_mfma_f32_16x16x32_bf16 v[104:107], v[248:251], a[188:191], v[104:107]\n"
"v_mfma_f32_16x16x32_bf16 v[104:107], v[228:231], a[192:195], v[104:107]\n"
"v_mfma_f32_16x16x32_bf16 v[104:107], v[208:211], a[224:227], v[104:107]\n"
"v_mfma_f32_16x16x32_bf16 v[108:111], v[244:247], a[188:191], v[108:111]\n"
"v_mfma_f32_16x16x32_bf16 v[108:111], v[224:227], a[192:195], v[108:111]\n"
"v_mfma_f32_16x16x32_bf16 v[108:111], v[204:207], a[224:227], v[108:111]\n"
"v_mfma_f32_16x16x32_bf16 v[112:115], v[240:243], a[188:191], v[112:115]\n"
"v_mfma_f32_16x16x32_bf16 v[112:115], v[220:223], a[192:195], v[112:115]\n"
"v_mfma_f32_16x16x32_bf16 v[112:115], v[200:203], a[224:227], v[112:115]\n"
"s_waitcnt vmcnt(9)\n"
"v_mfma_f32_16x16x32_bf16 a[128:131], v[196:199], a[196:199], a[128:131]\n"
"buffer_load_dwordx4 a[188:191], v2, s[20:23], s85 offen offset:1024 sc1\n"
"v_mfma_f32_16x16x32_bf16 a[128:131], a[116:119], a[200:203], a[128:131]\n"
"v_mfma_f32_16x16x32_bf16 a[132:135], v[192:195], a[196:199], a[132:135]\n"
"v_mfma_f32_16x16x32_bf16 a[132:135], a[120:123], a[200:203], a[132:135]\n"
"v_mfma_f32_16x16x32_bf16 a[136:139], a[104:107], a[196:199], a[136:139]\n"
"buffer_load_dwordx4 a[192:195], v2, s[20:23], s85 offen offset:2048 sc1\n"
"v_mfma_f32_16x16x32_bf16 a[136:139], a[124:127], a[200:203], a[136:139]\n"
"v_mfma_f32_16x16x32_bf16 a[140:143], a[108:111], a[196:199], a[140:143]\n"
"v_mfma_f32_16x16x32_bf16 a[140:143], a[240:243], a[200:203], a[140:143]\n"
"v_mfma_f32_16x16x32_bf16 a[144:147], a[112:115], a[196:199], a[144:147]\n"
"buffer_load_dwordx4 a[224:227], v2, s[20:23], s85 offen offset:3072 sc1\n"
"v_mfma_f32_16x16x32_bf16 a[144:147], a[244:247], a[200:203], a[144:147]\n"
"s_waitcnt vmcnt(9)\n"
"s_add_u32 s85, s85, 4096\n"
"v_mfma_f32_16x16x32_bf16 a[128:131], a[248:251], a[204:207], a[128:131]\n"
"buffer_load_dwordx4 a[196:199], v2, s[20:23], s85 offen offset:0 sc1\n"
"v_mfma_f32_16x16x32_bf16 a[128:131], v[176:179], v[20:23], a[128:131]\n"
"s_xor_b32 s85, s3, 0\n"
"s_mul_i32 s85, s85, 116736\n"
"v_mfma_f32_16x16x32_bf16 a[128:131], a[228:231], v[24:27], a[128:131]\n"
"s_add_u32 s85, s85, s84\n"
"v_mfma_f32_16x16x32_bf16 a[132:135], a[252:255], a[204:207], a[132:135]\n"
"v_mfma_f32_16x16x32_bf16 a[132:135], v[172:175], v[20:23], a[132:135]\n"
"buffer_load_dwordx4 a[200:203], v2, s[20:23], s85 offen offset:0 sc1\n"
"v_mfma_f32_16x16x32_bf16 a[132:135], a[232:235], v[24:27], a[132:135]\n"
"v_mfma_f32_16x16x32_bf16 a[136:139], v[188:191], a[204:207], a[136:139]\n"
"v_mfma_f32_16x16x32_bf16 a[136:139], v[168:171], v[20:23], a[136:139]\n"
"v_mfma_f32_16x16x32_bf16 a[136:139], a[236:239], v[24:27], a[136:139]\n"
"buffer_load_dwordx4 v[116:119], v2, s[20:23], s40 offen offset:0 sc1\n"
"v_mfma_f32_16x16x32_bf16 a[140:143], v[184:187], a[204:207], a[140:143]\n"
"ds_write_b128 v15, a[128:131] offset:0\n"
"v_mfma_f32_16x16x32_bf16 a[140:143], v[164:167], v[20:23], a[140:143]\n"
"v_mfma_f32_16x16x32_bf16 a[140:143], v[36:39], v[24:27], a[140:143]\n"
"v_mfma_f32_16x16x32_bf16 a[144:147], v[180:183], a[204:207], a[144:147]\n"
"ds_write_b128 v15, a[132:135] offset:4096\n"
"v_mfma_f32_16x16x32_bf16 a[144:147], v[160:163], v[20:23], a[144:147]\n"
"buffer_load_dwordx4 v[120:123], v2, s[20:23], s40 offen offset:1024 sc1\n"
"v_mfma_f32_16x16x32_bf16 a[144:147], v[40:43], v[24:27], a[144:147]\n"
"s_waitcnt vmcnt(11)\n"
"v_mfma_f32_16x16x32_bf16 a[148:151], v[196:199], v[28:31], a[148:151]\n"
"ds_write_b128 v15, a[136:139] offset:8192\n"
"v_mfma_f32_16x16x32_bf16 a[148:151], a[116:119], v[32:35], a[148:151]\n"
"buffer_load_dwordx4 v[20:23], v2, s[20:23], s85 offen offset:1024 sc1\n"
"v_mfma_f32_16x16x32_bf16 a[152:155], v[192:195], v[28:31], a[152:155]\n"
"v_mfma_f32_16x16x32_bf16 a[152:155], a[120:123], v[32:35], a[152:155]\n"
"ds_write_b128 v15, a[140:143] offset:12288\n"
"v_mfma_f32_16x16x32_bf16 a[156:159], a[104:107], v[28:31], a[156:159]\n"
"buffer_load_dwordx4 v[124:127], v2, s[20:23], s40 offen offset:2048 sc1\n"
"v_mfma_f32_16x16x32_bf16 a[156:159], a[124:127], v[32:35], a[156:159]\n"
"v_mfma_f32_16x16x32_bf16 a[160:163], a[108:111], v[28:31], a[160:163]\n"
"ds_write_b128 v15, a[144:147] offset:16384\n"
"v_mfma_f32_16x16x32_bf16 a[160:163], a[240:243], v[32:35], a[160:163]\n"
"v_mfma_f32_16x16x32_bf16 a[164:167], a[112:115], v[28:31], a[164:167]\n"
"buffer_load_dwordx2 v[128:129], v9, s[20:23], s40 offen offset:3072 sc1\n"
"v_mfma_f32_16x16x32_bf16 a[164:167], a[244:247], v[32:35], a[164:167]\n"
"s_waitcnt vmcnt(11)\n"
"v_mfma_f32_16x16x32_bf16 a[148:151], a[248:251], a[208:211], a[148:151]\n"
"buffer_load_dwordx4 v[24:27], v2, s[20:23], s85 offen offset:2048 sc1\n"
"v_mfma_f32_16x16x32_bf16 a[148:151], v[176:179], a[212:215], a[148:151]\n"
"s_waitcnt lgkmcnt(0)\n"
"v_mfma_f32_16x16x32_bf16 a[148:151], a[228:231], a[216:219], a[148:151]\n"
"buffer_load_dwordx4 a[204:207], v2, s[20:23], s85 offen offset:3072 sc1\n"
"s_add_u32 s85, s85, 4096\n"
"v_mfma_f32_16x16x32_bf16 a[152:155], a[252:255], a[208:211], a[152:155]\n"
"v_mfma_f32_16x16x32_bf16 a[152:155], v[172:175], a[212:215], a[152:155]\n"
"buffer_load_dwordx4 v[28:31], v2, s[20:23], s85 offen offset:0 sc1\n"
"v_mfma_f32_16x16x32_bf16 a[152:155], a[232:235], a[216:219], a[152:155]\n"
"v_mfma_f32_16x16x32_bf16 a[156:159], v[188:191], a[208:211], a[156:159]\n"
"v_mfma_f32_16x16x32_bf16 a[156:159], v[168:171], a[212:215], a[156:159]\n"
"v_mfma_f32_16x16x32_bf16 a[156:159], a[236:239], a[216:219], a[156:159]\n"
"v_mfma_f32_16x16x32_bf16 a[160:163], v[184:187], a[208:211], a[160:163]\n"
"ds_write_b128 v15, a[148:151] offset:20480\n"
"v_mfma_f32_16x16x32_bf16 a[160:163], v[164:167], a[212:215], a[160:163]\n"
"v_mfma_f32_16x16x32_bf16 a[160:163], v[36:39], a[216:219], a[160:163]\n"
"v_mfma_f32_16x16x32_bf16 a[164:167], v[180:183], a[208:211], a[164:167]\n"
"ds_write_b128 v15, a[152:155] offset:24576\n"
"v_mfma_f32_16x16x32_bf16 a[164:167], v[160:163], a[212:215], a[164:167]\n"
"v_mfma_f32_16x16x32_bf16 a[164:167], v[40:43], a[216:219], a[164:167]\n"
"s_waitcnt vmcnt(12)\n"
"v_mfma_f32_16x16x32_bf16 a[168:171], v[196:199], a[220:223], a[168:171]\n"
"ds_write_b128 v15, a[156:159] offset:28672\n"
"v_mfma_f32_16x16x32_bf16 a[168:171], a[116:119], a[188:191], a[168:171]\n"
"v_mfma_f32_16x16x32_bf16 a[172:175], v[192:195], a[220:223], a[172:175]\n"
"v_mfma_f32_16x16x32_bf16 a[172:175], a[120:123], a[188:191], a[172:175]\n"
"ds_write_b128 v15, a[160:163] offset:32768\n"
"v_mfma_f32_16x16x32_bf16 a[176:179], a[104:107], a[220:223], a[176:179]\n"
"v_mfma_f32_16x16x32_bf16 a[176:179], a[124:127], a[188:191], a[176:179]\n"
"v_mfma_f32_16x16x32_bf16 a[180:183], a[108:111], a[220:223], a[180:183]\n"
"ds_write_b128 v15, a[164:167] offset:36864\n"
"v_mfma_f32_16x16x32_bf16 a[180:183], a[240:243], a[188:191], a[180:183]\n"
"v_mfma_f32_16x16x32_bf16 a[184:187], a[112:115], a[220:223], a[184:187]\n"
"v_mfma_f32_16x16x32_bf16 a[184:187], a[244:247], a[188:191], a[184:187]\n"
"s_waitcnt vmcnt(9)\n"
"v_mfma_f32_16x16x32_bf16 a[168:171], a[248:251], a[192:195], a[168:171]\n"
"v_mfma_f32_16x16x32_bf16 a[168:171], v[176:179], a[224:227], a[168:171]\n"
"s_waitcnt lgkmcnt(0)\n"
"s_barrier\n"
"v_mfma_f32_16x16x32_bf16 a[168:171], a[228:231], a[196:199], a[168:171]\n"
"ds_read_b128 v[136:139], v156 offset:0\n"
"ds_read_b128 v[140:143], v156 offset:4096\n"
"v_mfma_f32_16x16x32_bf16 a[172:175], a[252:255], a[192:195], a[172:175]\n"
"ds_read_b128 v[144:147], v156 offset:8192\n"
"ds_read_b128 v[148:151], v156 offset:12288\n"
"v_mfma_f32_16x16x32_bf16 a[172:175], v[172:175], a[224:227], a[172:175]\n"
"ds_read_b128 v[152:155], v156 offset:16384\n"
"v_mfma_f32_16x16x32_bf16 a[172:175], a[232:235], a[196:199], a[172:175]\n"
"ds_read_b128 v[220:223], v157 offset:20480\n"
"ds_read_b128 v[224:227], v157 offset:24576\n"
"v_mfma_f32_16x16x32_bf16 a[176:179], v[188:191], a[192:195], a[176:179]\n"
"ds_read_b128 v[228:231], v157 offset:28672\n"
"ds_read_b128 v[232:235], v157 offset:32768\n"
"v_mfma_f32_16x16x32_bf16 a[176:179], v[168:171], a[224:227], a[176:179]\n"
"ds_read_b128 v[236:239], v157 offset:36864\n"
"v_mfma_f32_16x16x32_bf16 a[176:179], a[236:239], a[196:199], a[176:179]\n"
"v_mfma_f32_16x16x32_bf16 a[180:183], v[184:187], a[192:195], a[180:183]\n"
"ds_write_b128 v15, a[168:171] offset:40960\n"
"v_mfma_f32_16x16x32_bf16 a[180:183], v[164:167], a[224:227], a[180:183]\n"
"v_mfma_f32_16x16x32_bf16 a[180:183], v[36:39], a[196:199], a[180:183]\n"
"s_waitcnt lgkmcnt(10)\n"
"v_add_f32 v76, v76, v136\n"
"v_add_f32 v77, v77, v137\n"
"v_mfma_f32_16x16x32_bf16 a[184:187], v[180:183], a[192:195], a[184:187]\n"
"ds_write_b128 v15, a[172:175] offset:45056\n"
"v_mfma_f32_16x16x32_bf16 a[184:187], v[160:163], a[224:227], a[184:187]\n"
"v_add_f32 v78, v78, v138\n"
"v_add_f32 v79, v79, v139\n"
"v_mfma_f32_16x16x32_bf16 a[184:187], v[40:43], a[196:199], a[184:187]\n"
"s_waitcnt lgkmcnt(10)\n"
"v_add_f32 v80, v80, v140\n"
"s_waitcnt vmcnt(5)\n"
"v_mfma_f32_16x16x32_bf16 v[96:99], v[196:199], a[200:203], v[96:99]\n"
"ds_write_b128 v15, a[176:179] offset:49152\n"
"v_mfma_f32_16x16x32_bf16 v[96:99], a[116:119], v[20:23], v[96:99]\n"
"v_add_f32 v81, v81, v141\n"
"v_add_f32 v82, v82, v142\n"
"v_mfma_f32_16x16x32_bf16 v[100:103], v[192:195], a[200:203], v[100:103]\n"
"v_add_f32 v83, v83, v143\n"
"v_mfma_f32_16x16x32_bf16 v[100:103], a[120:123], v[20:23], v[100:103]\n"
"ds_write_b128 v15, a[180:183] offset:53248\n"
"v_mfma_f32_16x16x32_bf16 v[104:107], a[104:107], a[200:203], v[104:107]\n"
"s_waitcnt vmcnt(3)\n"
"v_mfma_f32_16x16x32_bf16 v[104:107], a[124:127], v[20:23], v[104:107]\n"
"v_xor_b32 v14, s35, v117\n"
"v_bitop3_b32 v14, v119, s35, v14 bitop3:0xbe\n"
"v_mfma_f32_16x16x32_bf16 v[108:111], a[108:111], a[200:203], v[108:111]\n"
"ds_write_b128 v15, a[184:187] offset:57344\n"
"v_mfma_f32_16x16x32_bf16 v[108:111], a[240:243], v[20:23], v[108:111]\n"
"v_bitop3_b32 v14, v121, s35, v14 bitop3:0xbe\n"
"v_bitop3_b32 v14, v123, s35, v14 bitop3:0xbe\n"
"v_mfma_f32_16x16x32_bf16 v[112:115], a[112:115], a[200:203], v[112:115]\n"
"v_bitop3_b32 v14, v125, s35, v14 bitop3:0xbe\n"
"v_bitop3_b32 v14, v127, s35, v14 bitop3:0xbe\n"
"v_mfma_f32_16x16x32_bf16 v[112:115], a[244:247], v[20:23], v[112:115]\n"
"v_bitop3_b32 v14, v129, s35, v14 bitop3:0xbe\n"
"s_waitcnt vmcnt(0)\n"
"v_mfma_f32_16x16x32_bf16 v[96:99], a[248:251], v[24:27], v[96:99]\n"
"v_cmp_lt_u32 vcc, 0xffff, v14\n"
"v_mfma_f32_16x16x32_bf16 v[96:99], v[176:179], a[204:207], v[96:99]\n"
"s_waitcnt lgkmcnt(0)\n"
"s_barrier\n"
"v_mfma_f32_16x16x32_bf16 v[96:99], a[228:231], v[28:31], v[96:99]\n"
"ds_read_b128 v[56:59], v158 offset:40960\n"
"ds_read_b128 v[60:63], v158 offset:45056\n"
"v_mfma_f32_16x16x32_bf16 v[100:103], a[252:255], v[24:27], v[100:103]\n"
"ds_read_b128 v[64:67], v158 offset:49152\n"
"ds_read_b128 v[68:71], v158 offset:53248\n"
"v_mfma_f32_16x16x32_bf16 v[100:103], v[172:175], a[204:207], v[100:103]\n"
"ds_read_b128 v[72:75], v158 offset:57344\n"
"s_cbranch_vccnz .Lfb_slow_11\n"
".Lfb_back_12:\n"
"v_mfma_f32_16x16x32_bf16 v[100:103], a[232:235], v[28:31], v[100:103]\n"
"v_lshlrev_b32 v240, 16, v116\n"
"v_and_b32 v241, 0xffff0000, v116\n"
"v_mfma_f32_16x16x32_bf16 v[104:107], v[188:191], v[24:27], v[104:107]\n"
"v_lshlrev_b32 v242, 16, v117\n"
"v_lshlrev_b32 v243, 16, v118\n"
"v_mfma_f32_16x16x32_bf16 v[104:107], v[168:171], a[204:207], v[104:107]\n"
"v_and_b32 v244, 0xffff0000, v118\n"
"v_lshlrev_b32 v245, 16, v119\n"
"v_mfma_f32_16x16x32_bf16 v[104:107], a[236:239], v[28:31], v[104:107]\n"
"v_lshlrev_b32 v246, 16, v120\n"
"v_and_b32 v247, 0xffff0000, v120\n"
"v_mfma_f32_16x16x32_bf16 v[108:111], v[184:187], v[24:27], v[108:111]\n"
"v_lshlrev_b32 v248, 16, v121\n"
"v_lshlrev_b32 v249, 16, v122\n"
"v_mfma_f32_16x16x32_bf16 v[108:111], v[164:167], a[204:207], v[108:111]\n"
"v_and_b32 v250, 0xffff0000, v122\n"
"v_lshlrev_b32 v251, 16, v123\n"
"v_mfma_f32_16x16x32_bf16 v[108:111], v[36:39], v[28:31], v[108:111]\n"
"v_lshlrev_b32 v252, 16, v124\n"
"v_and_b32 v253, 0xffff0000, v124\n"
"v_mfma_f32_16x16x32_bf16 v[112:115], v[180:183], v[24:27], v[112:115]\n"
"v_lshlrev_b32 v254, 16, v125\n"
"v_lshlrev_b32 v255, 16, v126\n"
"v_mfma_f32_16x16x32_bf16 v[112:115], v[160:163], a[204:207], v[112:115]\n"
"v_and_b32 v44, 0xffff0000, v126\n"
"v_lshlrev_b32 v45, 16, v127\n"
"v_mfma_f32_16x16x32_bf16 v[112:115], v[40:43], v[28:31], v[112:115]\n"
"v_lshlrev_b32 v46, 16, v128\n"
"v_and_b32 v47, 0xffff0000, v128\n"
"v_pk_add_f32 v[76:77], v[76:77], v[220:221] op_sel:[0,0] op_sel_hi:[1,1]\n"
"v_pk_add_f32 v[78:79], v[78:79], v[222:223] op_sel:[0,0] op_sel_hi:[1,1]\n"
"v_pk_add_f32 v[76:77], v[76:77], v[240:241] op_sel:[0,0] op_sel_hi:[1,1]\n"
"v_pk_add_f32 v[78:79], v[78:79], v[242:243] op_sel:[0,0] op_sel_hi:[1,1]\n"
"v_pk_add_f32 v[76:77], v[76:77], v[96:97] op_sel:[0,0] op_sel_hi:[1,1]\n"
"v_pk_add_f32 v[78:79], v[78:79], v[98:99] op_sel:[0,0] op_sel_hi:[1,1]\n"
"s_waitcnt lgkmcnt(4)\n"
"v_pk_add_f32 v[76:77], v[76:77], v[56:57] op_sel:[0,0] op_sel_hi:[1,1]\n"
"v_pk_add_f32 v[78:79], v[78:79], v[58:59] op_sel:[0,0] op_sel_hi:[1,1]\n"
"v_cvt_pk_bf16_f32 v76, v76, v77\n"
"v_cvt_pk_bf16_f32 v77, v78, v79\n"
"ds_write_b64 v10, v[76:77] offset:0\n"
"v_pk_add_f32 v[80:81], v[80:81], v[224:225] op_sel:[0,0] op_sel_hi:[1,1]\n"
"v_pk_add_f32 v[82:83], v[82:83], v[226:227] op_sel:[0,0] op_sel_hi:[1,1]\n"
"v_pk_add_f32 v[80:81], v[80:81], v[244:245] op_sel:[0,0] op_sel_hi:[1,1]\n"
"v_pk_add_f32 v[82:83], v[82:83], v[246:247] op_sel:[0,0] op_sel_hi:[1,1]\n"
"v_pk_add_f32 v[80:81], v[80:81], v[100:101] op_sel:[0,0] op_sel_hi:[1,1]\n"
"v_pk_add_f32 v[82:83], v[82:83], v[102:103] op_sel:[0,0] op_sel_hi:[1,1]\n"
"s_waitcnt lgkmcnt(4)\n"
"v_pk_add_f32 v[80:81], v[80:81], v[60:61] op_sel:[0,0] op_sel_hi:[1,1]\n"
"v_pk_add_f32 v[82:83], v[82:83], v[62:63] op_sel:[0,0] op_sel_hi:[1,1]\n"
"v_cvt_pk_bf16_f32 v80, v80, v81\n"
"v_cvt_pk_bf16_f32 v81, v82, v83\n"
"ds_write_b64 v10, v[80:81] offset:32\n"
"v_pk_add_f32 v[84:85], v[84:85], v[144:145] op_sel:[0,0] op_sel_hi:[1,1]\n"
"v_pk_add_f32 v[86:87], v[86:87], v[146:147] op_sel:[0,0] op_sel_hi:[1,1]\n"
"v_pk_add_f32 v[84:85], v[84:85], v[228:229] op_sel:[0,0] op_sel_hi:[1,1]\n"
"v_pk_add_f32 v[86:87], v[86:87], v[230:231] op_sel:[0,0] op_sel_hi:[1,1]\n"
"v_pk_add_f32 v[84:85], v[84:85], v[248:249] op_sel:[0,0] op_sel_hi:[1,1]\n"
"v_pk_add_f32 v[86:87], v[86:87], v[250:251] op_sel:[0,0] op_sel_hi:[1,1]\n"
"s_waitcnt lgkmcnt(4)\n"
"v_pk_add_f32 v[84:85], v[84:85], v[64:65] op_sel:[0,0] op_sel_hi:[1,1]\n"
"v_pk_add_f32 v[86:87], v[86:87], v[66:67] op_sel:[0,0] op_sel_hi:[1,1]\n"
"v_pk_add_f32 v[84:85], v[84:85], v[104:105] op_sel:[0,0] op_sel_hi:[1,1]\n"
"v_pk_add_f32 v[86:87], v[86:87], v[106:107] op_sel:[0,0] op_sel_hi:[1,1]\n"
"v_cvt_pk_bf16_f32 v84, v84, v85\n"
"v_cvt_pk_bf16_f32 v85, v86, v87\n"
"ds_write_b64 v10, v[84:85] offset:64\n"
"v_pk_add_f32 v[88:89], v[88:89], v[148:149] op_sel:[0,0] op_sel_hi:[1,1]\n"
"v_pk_add_f32 v[90:91], v[90:91], v[150:151] op_sel:[0,0] op_sel_hi:[1,1]\n"
"v_pk_add_f32 v[88:89], v[88:89], v[232:233] op_sel:[0,0] op_sel_hi:[1,1]\n"
"v_pk_add_f32 v[90:91], v[90:91], v[234:235] op_sel:[0,0] op_sel_hi:[1,1]\n"
"v_pk_add_f32 v[88:89], v[88:89], v[252:253] op_sel:[0,0] op_sel_hi:[1,1]\n"
"v_pk_add_f32 v[90:91], v[90:91], v[254:255] op_sel:[0,0] op_sel_hi:[1,1]\n"
"s_waitcnt lgkmcnt(4)\n"
"v_pk_add_f32 v[88:89], v[88:89], v[68:69] op_sel:[0,0] op_sel_hi:[1,1]\n"
"v_pk_add_f32 v[90:91], v[90:91], v[70:71] op_sel:[0,0] op_sel_hi:[1,1]\n"
"v_pk_add_f32 v[88:89], v[88:89], v[108:109] op_sel:[0,0] op_sel_hi:[1,1]\n"
"v_pk_add_f32 v[90:91], v[90:91], v[110:111] op_sel:[0,0] op_sel_hi:[1,1]\n"
"v_cvt_pk_bf16_f32 v88, v88, v89\n"
"v_cvt_pk_bf16_f32 v89, v90, v91\n"
"ds_write_b64 v10, v[88:89] offset:96\n"
"v_pk_add_f32 v[92:93], v[92:93], v[152:153] op_sel:[0,0] op_sel_hi:[1,1]\n"
"v_pk_add_f32 v[94:95], v[94:95], v[154:155] op_sel:[0,0] op_sel_hi:[1,1]\n"
"v_pk_add_f32 v[92:93], v[92:93], v[236:237] op_sel:[0,0] op_sel_hi:[1,1]\n"
"v_pk_add_f32 v[94:95], v[94:95], v[238:239] op_sel:[0,0] op_sel_hi:[1,1]\n"
"v_pk_add_f32 v[92:93], v[92:93], v[44:45] op_sel:[0,0] op_sel_hi:[1,1]\n"
"v_pk_add_f32 v[94:95], v[94:95], v[46:47] op_sel:[0,0] op_sel_hi:[1,1]\n"
"s_waitcnt lgkmcnt(4)\n"
"v_pk_add_f32 v[92:93], v[92:93], v[72:73] op_sel:[0,0] op_sel_hi:[1,1]\n"
"v_pk_add_f32 v[94:95], v[94:95], v[74:75] op_sel:[0,0] op_sel_hi:[1,1]\n"
"v_pk_add_f32 v[92:93], v[92:93], v[112:113] op_sel:[0,0] op_sel_hi:[1,1]\n"
"v_pk_add_f32 v[94:95], v[94:95], v[114:115] op_sel:[0,0] op_sel_hi:[1,1]\n"
"v_cvt_pk_bf16_f32 v92, v92, v93\n"
"v_cvt_pk_bf16_f32 v93, v94, v95\n"
"ds_write_b64 v10, v[92:93] offset:128\n"
"ds_read_b128 v[20:23], v17\n"
"ds_read_b128 v[24:27], v18\n"
"ds_read_b128 v[28:31], v19\n"
"s_waitcnt lgkmcnt(2)\n"
"buffer_store_dwordx4 v[20:23], v5, s[48:51], s79 offen offset:0 \n"
"s_waitcnt lgkmcnt(1)\n"
"buffer_store_dwordx4 v[24:27], v6, s[48:51], s79 offen offset:1024 \n"
"s_waitcnt lgkmcnt(0)\n"
"buffer_store_dwordx4 v[28:31], v7, s[48:51], s79 offen offset:2048 \n"
"s_endpgm\n"
".Lfb_idle:\n"
"v_lshrrev_b32 v20, 3, v1\n"
"v_mul_u32_u24 v20, 3840, v20\n"
"v_and_b32 v32, 7, v1\n"
"v_lshl_add_u32 v20, v32, 4, v20\n"
"s_mov_b32 s10, 2457600\n"
"s_mov_b32 s11, 0x20000\n"
"s_waitcnt lgkmcnt(0)\n"
"s_bfe_u32 s53, s34, 0x50016\n"
"s_lshr_b32 s32, s34, 27\n"
"s_lshr_b32 s54, s35, 22\n"
"s_lshl_b32 s60, s54, 2\n"
"s_add_u32 s60, s60, s3\n"
"s_add_u32 s60, s60, 10\n"
"s_lshl_b32 s64, s60, 7\n"
"s_lshl_b32 s60, s54, 1\n"
"s_add_u32 s60, s60, s3\n"
"s_lshl_b32 s65, s60, 7\n"
"s_cmp_lt_u32 s3, 2\n"
"s_cselect_b32 s65, s65, 536870912\n"
"s_cmp_lt_u32 s54, 5\n"
"s_cselect_b32 s64, s64, 536870912\n"
"s_mov_b32 s8, s38\n"
"s_and_b32 s9, s39, 0xffff\n"
"s_add_u32 s34, s64, 0\n"
"buffer_load_dwordx4 v[64:67], v20, s[8:11], s34 offen\n"
"s_add_u32 s34, s64, 30720\n"
"buffer_load_dwordx4 v[68:71], v20, s[8:11], s34 offen\n"
"s_add_u32 s34, s64, 61440\n"
"buffer_load_dwordx4 v[72:75], v20, s[8:11], s34 offen\n"
"s_add_u32 s34, s64, 92160\n"
"buffer_load_dwordx4 v[76:79], v20, s[8:11], s34 offen\n"
"s_add_u32 s34, s64, 122880\n"
"buffer_load_dwordx4 v[80:83], v20, s[8:11], s34 offen\n"
"s_branch .Lfb_idlem\n"
".Lfb_idlem:\n"
"s_add_u32 s34, s64, 153600\n"
"buffer_load_dwordx4 v[84:87], v20, s[8:11], s34 offen\n"
"s_mov_b32 s4, s36\n"
"s_and_b32 s5, s37, 0xffff\n"
"s_load_dword s52, s[50:51], 0\n"
"v_lshlrev_b32 v2, 4, v1\n"
"v_mov_b32 v32, 1073741824\n"
"v_cmp_eq_u32 vcc, 0, v1\n"
"s_mov_b32 s16, s50\n"
"s_and_b32 s17, s51, 0xffff\n"
"s_mov_b32 s18, 1456128\n"
"s_mov_b32 s19, 0x20000\n"
"v_cndmask_b32 v3, v32, v1, vcc\n"
"s_add_u32 s24, s50, 20971520\n"
"s_addc_u32 s25, s51, 0\n"
"s_and_b32 s25, s25, 0xffff\n"
"s_add_u32 s34, s64, 184320\n"
"buffer_load_dwordx4 v[88:91], v20, s[8:11], s34 offen\n"
"s_mov_b32 s26, 3031040\n"
"s_mov_b32 s27, 0x20000\n"
"s_add_u32 s20, s50, 4194304\n"
"s_addc_u32 s21, s51, 0\n"
"s_and_b32 s21, s21, 0xffff\n"
"s_mov_b32 s22, 14942208\n"
"s_mov_b32 s23, 0x20000\n"
"s_add_u32 s80, s50, 262144\n"
"s_addc_u32 s81, s51, 0\n"
"s_and_b32 s81, s81, 0xffff\n"
"s_mov_b32 s82, 32768\n"
"s_mov_b32 s83, 0x20000\n"
"s_mul_i32 s42, s53, 6\n"
"s_add_u32 s42, s42, s54\n"
"s_add_u32 s34, s64, 215040\n"
"buffer_load_dwordx4 v[92:95], v20, s[8:11], s34 offen\n"
"s_lshl_b32 s42, s42, 7\n"
"s_add_u32 s42, s42, 212992\n"
"s_cmp_eq_u32 s3, 0\n"
"s_cselect_b32 s42, s42, 1073741824\n"
"s_lshr_b32 s60, s2, 3\n"
"s_sub_u32 s60, s60, 11\n"
"s_add_u32 s62, s60, 11\n"
"s_and_b32 s61, s62, 1\n"
"s_lshr_b32 s62, s62, 1\n"
"s_mul_i32 s63, s61, 454761243\n"
"s_add_u32 s63, s63, 2475509504\n"
"s_cmp_lt_u32 s60, 11\n"
"s_cselect_b32 s60, s60, s62\n"
"s_cselect_b32 s61, 0, s61\n"
"s_cselect_b32 s63, 403703808, s63\n"
"s_add_u32 s34, s64, 245760\n"
"buffer_load_dwordx4 v[96:99], v20, s[8:11], s34 offen\n"
"s_lshr_b32 s56, s60, 1\n"
"s_and_b32 s60, s60, 1\n"
"s_lshl_b32 s60, s60, 1\n"
"s_add_u32 s60, s60, s61\n"
"s_and_b32 s62, s2, 7\n"
"s_lshl_b32 s62, s62, 2\n"
"s_add_u32 s55, s62, s60\n"
"s_lshl_b32 s62, s3, 3\n"
"s_lshr_b32 s63, s63, s62\n"
"s_and_b32 s78, s63, 63\n"
"s_bfe_u32 s58, s63, 0x20006\n"
"s_and_b32 s49, s49, 0xffff\n"
"s_mov_b32 s50, 2621440\n"
"s_mov_b32 s51, 0x20000\n"
"s_add_u32 s34, s64, 276480\n"
"buffer_load_dwordx4 v[100:103], v20, s[8:11], s34 offen\n"
"s_mul_i32 s60, s55, 456\n"
"s_add_u32 s44, s60, s78\n"
"s_lshl_b32 s44, s44, 10\n"
"s_mul_i32 s62, s3, 5\n"
"s_add_u32 s62, s62, 54\n"
"s_add_u32 s84, s60, s62\n"
"s_lshl_b32 s84, s84, 10\n"
"s_xor_b32 s60, s55, 1\n"
"s_mul_i32 s60, s60, 456\n"
"s_add_u32 s45, s60, s78\n"
"s_lshl_b32 s45, s45, 10\n"
"s_mul_i32 s61, s56, 74\n"
"s_add_u32 s62, s62, s61\n"
"s_mul_i32 s97, s62, 5120\n"
"s_add_u32 s34, s64, 307200\n"
"buffer_load_dwordx4 v[104:107], v20, s[8:11], s34 offen\n"
"s_add_u32 s61, s61, s78\n"
"s_mul_i32 s43, s61, 5120\n"
"s_add_u32 s96, s43, 10240\n"
"s_cmp_ge_u32 s58, 1\n"
"s_cselect_b32 s43, 1073741824, s43\n"
"s_lshl_b32 s60, s55, 2\n"
"s_add_u32 s60, s60, s3\n"
"s_lshl_b32 s60, s60, 3\n"
"s_add_u32 s40, s60, s56\n"
"s_lshr_b32 s61, s55, 1\n"
"s_lshl_b32 s61, s61, 3\n"
"s_mul_i32 s62, s61, 373\n"
"s_lshr_b32 s62, s62, 12\n"
"s_mul_i32 s62, s62, 54\n"
"s_add_u32 s34, s64, 337920\n"
"buffer_load_dwordx4 v[108:111], v20, s[8:11], s34 offen\n"
"s_add_u32 s63, s61, 7\n"
"s_mul_i32 s63, s63, 373\n"
"s_lshr_b32 s63, s63, 12\n"
"s_mul_i32 s63, s63, 54\n"
"s_sub_u32 s46, s63, s62\n"
"s_add_u32 s41, s62, s78\n"
"s_add_u32 s41, s41, 64\n"
"s_lshl_b32 s61, s55, 2\n"
"s_mul_i32 s62, s61, 1366\n"
"s_lshr_b32 s62, s62, 12\n"
"s_mul_i32 s63, s3, 88\n"
"s_add_u32 s62, s62, s63\n"
"s_add_u32 s47, s62, 768\n"
"s_add_u32 s61, s61, s3\n"
"s_add_u32 s34, s64, 368640\n"
"buffer_load_dwordx4 v[112:115], v20, s[8:11], s34 offen\n"
"s_mul_i32 s60, s61, 20480\n"
"s_mul_i32 s61, s56, 160\n"
"s_add_u32 s79, s60, s61\n"
"s_sub_u32 s55, s96, 5120\n"
"s_cmp_ge_u32 s58, 2\n"
"s_cselect_b32 s55, 1073741824, s55\n"
"s_waitcnt lgkmcnt(0)\n"
"s_add_u32 s52, s52, 1\n"
"v_and_b32 v10, 31, v1\n"
"v_min_u32 v10, 7, v10\n"
"v_lshrrev_b32 v9, 5, v1\n"
"v_mul_u32_u24 v9, s46, v9\n"
"v_add3_u32 v10, v10, v9, s41\n"
"v_lshlrev_b32 v10, 7, v10\n"
"v_add_u32 v9, 0, v1\n"
"s_add_u32 s34, s64, 399360\n"
"buffer_load_dwordx4 v[116:119], v20, s[8:11], s34 offen\n"
"v_mul_u32_u24 v9, 6554, v9\n"
"v_lshrrev_b32 v9, 16, v9\n"
"v_mul_u32_u24 v9, 1120, v9\n"
"v_add_u32 v5, v9, v2\n"
"v_add_u32 v9, 64, v1\n"
"v_mul_u32_u24 v9, 6554, v9\n"
"v_lshrrev_b32 v9, 16, v9\n"
"v_mul_u32_u24 v9, 1120, v9\n"
"v_add_u32 v6, v9, v2\n"
"v_add_u32 v9, 128, v1\n"
"v_mul_u32_u24 v9, 6554, v9\n"
"v_lshrrev_b32 v9, 16, v9\n"
"v_mul_u32_u24 v9, 1120, v9\n"
"v_add_u32 v7, v9, v2\n"
"v_lshrrev_b32 v9, 5, v1\n"
"v_lshl_add_u32 v7, v9, 30, v7\n"
"v_mov_b32 v19, s52\n"
"s_mov_b32 s66, 0x3f80\n"
"s_mov_b32 s67, 0x3f800000\n"
"v_and_b32 v21, 7, v1\n"
"v_lshrrev_b32 v22, 4, v1\n"
"v_lshlrev_b32 v22, 3, v22\n"
"v_lshl_add_u32 v21, v21, 5, v22\n"
"v_bfe_u32 v22, v1, 3, 1\n"
"v_lshl_add_u32 v21, v22, 29, v21\n"
"s_lshl_b32 s68, s64, 1\n"
"s_add_u32 s68, s68, 245760\n"
"s_add_u32 s70, s64, 253952\n"
"s_lshl_b32 s69, s65, 1\n"
"s_add_u32 s69, s69, 245760\n"
"s_add_u32 s71, s65, 253952\n"
"s_barrier\n"
"buffer_store_dword v19, v3, s[16:19], s42 offen sc1\n"
"s_cmp_eq_u32 s54, 5\n"
"s_cbranch_scc1 .Lfb_agg\n"
"s_waitcnt vmcnt(14)\n"
"v_dot2_f32_bf16 v24, v64, s66, 0\n"
"v_dot2_f32_bf16 v25, v64, s67, 0\n"
"v_dot2_f32_bf16 v26, v65, s66, 0\n"
"v_dot2_f32_bf16 v27, v65, s67, 0\n"
"v_dot2_f32_bf16 v28, v66, s66, 0\n"
"v_dot2_f32_bf16 v29, v66, s67, 0\n"
"v_dot2_f32_bf16 v30, v67, s66, 0\n"
"v_dot2_f32_bf16 v31, v67, s67, 0\n"
"s_add_u32 s34, s64, 430080\n"
"buffer_load_dwordx4 v[64:67], v20, s[8:11], s34 offen\n"
"s_waitcnt vmcnt(14)\n"
"v_dot2_f32_bf16 v24, v68, s66, v24\n"
"v_dot2_f32_bf16 v25, v68, s67, v25\n"
"v_dot2_f32_bf16 v26, v69, s66, v26\n"
"v_dot2_f32_bf16 v27, v69, s67, v27\n"
"v_dot2_f32_bf16 v28, v70, s66, v28\n"
"v_dot2_f32_bf16 v29, v70, s67, v29\n"
"v_dot2_f32_bf16 v30, v71, s66, v30\n"
"v_dot2_f32_bf16 v31, v71, s67, v31\n"
"s_add_u32 s34, s64, 460800\n"
"buffer_load_dwordx4 v[68:71], v20, s[8:11], s34 offen\n"
"s_waitcnt vmcnt(14)\n"
"v_dot2_f32_bf16 v24, v72, s66, v24\n"
"v_dot2_f32_bf16 v25, v72, s67, v25\n"
"v_dot2_f32_bf16 v26, v73, s66, v26\n"
"v_dot2_f32_bf16 v27, v73, s67, v27\n"
"v_dot2_f32_bf16 v28, v74, s66, v28\n"
"v_dot2_f32_bf16 v29, v74, s67, v29\n"
"v_dot2_f32_bf16 v30, v75, s66, v30\n"
"v_dot2_f32_bf16 v31, v75, s67, v31\n"
"s_add_u32 s34, s64, 491520\n"
"buffer_load_dwordx4 v[72:75], v20, s[8:11], s34 offen\n"
"s_waitcnt vmcnt(14)\n"
"v_dot2_f32_bf16 v24, v76, s66, v24\n"
"v_dot2_f32_bf16 v25, v76, s67, v25\n"
"v_dot2_f32_bf16 v26, v77, s66, v26\n"
"v_dot2_f32_bf16 v27, v77, s67, v27\n"
"v_dot2_f32_bf16 v28, v78, s66, v28\n"
"v_dot2_f32_bf16 v29, v78, s67, v29\n"
"v_dot2_f32_bf16 v30, v79, s66, v30\n"
"v_dot2_f32_bf16 v31, v79, s67, v31\n"
"s_add_u32 s34, s64, 522240\n"
"buffer_load_dwordx4 v[76:79], v20, s[8:11], s34 offen\n"
"s_waitcnt vmcnt(14)\n"
"v_dot2_f32_bf16 v24, v80, s66, v24\n"
"v_dot2_f32_bf16 v25, v80, s67, v25\n"
"v_dot2_f32_bf16 v26, v81, s66, v26\n"
"v_dot2_f32_bf16 v27, v81, s67, v27\n"
"v_dot2_f32_bf16 v28, v82, s66, v28\n"
"v_dot2_f32_bf16 v29, v82, s67, v29\n"
"v_dot2_f32_bf16 v30, v83, s66, v30\n"
"v_dot2_f32_bf16 v31, v83, s67, v31\n"
"s_add_u32 s34, s64, 552960\n"
"buffer_load_dwordx4 v[80:83], v20, s[8:11], s34 offen\n"
"s_waitcnt vmcnt(14)\n"
"v_dot2_f32_bf16 v24, v84, s66, v24\n"
"v_dot2_f32_bf16 v25, v84, s67, v25\n"
"v_dot2_f32_bf16 v26, v85, s66, v26\n"
"v_dot2_f32_bf16 v27, v85, s67, v27\n"
"v_dot2_f32_bf16 v28, v86, s66, v28\n"
"v_dot2_f32_bf16 v29, v86, s67, v29\n"
"v_dot2_f32_bf16 v30, v87, s66, v30\n"
"v_dot2_f32_bf16 v31, v87, s67, v31\n"
"s_add_u32 s34, s64, 583680\n"
"buffer_load_dwordx4 v[84:87], v20, s[8:11], s34 offen\n"
"s_waitcnt vmcnt(14)\n"
"v_dot2_f32_bf16 v24, v88, s66, v24\n"
"v_dot2_f32_bf16 v25, v88, s67, v25\n"
"v_dot2_f32_bf16 v26, v89, s66, v26\n"
"v_dot2_f32_bf16 v27, v89, s67, v27\n"
"v_dot2_f32_bf16 v28, v90, s66, v28\n"
"v_dot2_f32_bf16 v29, v90, s67, v29\n"
"v_dot2_f32_bf16 v30, v91, s66, v30\n"
"v_dot2_f32_bf16 v31, v91, s67, v31\n"
"s_add_u32 s34, s64, 614400\n"
"buffer_load_dwordx4 v[88:91], v20, s[8:11], s34 offen\n"
"s_waitcnt vmcnt(14)\n"
"v_dot2_f32_bf16 v24, v92, s66, v24\n"
"v_dot2_f32_bf16 v25, v92, s67, v25\n"
"v_dot2_f32_bf16 v26, v93, s66, v26\n"
"v_dot2_f32_bf16 v27, v93, s67, v27\n"
"v_dot2_f32_bf16 v28, v94, s66, v28\n"
"v_dot2_f32_bf16 v29, v94, s67, v29\n"
"v_dot2_f32_bf16 v30, v95, s66, v30\n"
"v_dot2_f32_bf16 v31, v95, s67, v31\n"
"s_add_u32 s34, s64, 645120\n"
"buffer_load_dwordx4 v[92:95], v20, s[8:11], s34 offen\n"
"s_waitcnt vmcnt(14)\n"
"v_dot2_f32_bf16 v24, v96, s66, v24\n"
"v_dot2_f32_bf16 v25, v96, s67, v25\n"
"v_dot2_f32_bf16 v26, v97, s66, v26\n"
"v_dot2_f32_bf16 v27, v97, s67, v27\n"
"v_dot2_f32_bf16 v28, v98, s66, v28\n"
"v_dot2_f32_bf16 v29, v98, s67, v29\n"
"v_dot2_f32_bf16 v30, v99, s66, v30\n"
"v_dot2_f32_bf16 v31, v99, s67, v31\n"
"s_add_u32 s34, s64, 675840\n"
"buffer_load_dwordx4 v[96:99], v20, s[8:11], s34 offen\n"
"s_waitcnt vmcnt(14)\n"
"v_dot2_f32_bf16 v24, v100, s66, v24\n"
"v_dot2_f32_bf16 v25, v100, s67, v25\n"
"v_dot2_f32_bf16 v26, v101, s66, v26\n"
"v_dot2_f32_bf16 v27, v101, s67, v27\n"
"v_dot2_f32_bf16 v28, v102, s66, v28\n"
"v_dot2_f32_bf16 v29, v102, s67, v29\n"
"v_dot2_f32_bf16 v30, v103, s66, v30\n"
"v_dot2_f32_bf16 v31, v103, s67, v31\n"
"s_add_u32 s34, s64, 706560\n"
"buffer_load_dwordx4 v[100:103], v20, s[8:11], s34 offen\n"
"s_waitcnt vmcnt(14)\n"
"v_dot2_f32_bf16 v24, v104, s66, v24\n"
"v_dot2_f32_bf16 v25, v104, s67, v25\n"
"v_dot2_f32_bf16 v26, v105, s66, v26\n"
"v_dot2_f32_bf16 v27, v105, s67, v27\n"
"v_dot2_f32_bf16 v28, v106, s66, v28\n"
"v_dot2_f32_bf16 v29, v106, s67, v29\n"
"v_dot2_f32_bf16 v30, v107, s66, v30\n"
"v_dot2_f32_bf16 v31, v107, s67, v31\n"
"s_add_u32 s34, s64, 737280\n"
"buffer_load_dwordx4 v[104:107], v20, s[8:11], s34 offen\n"
"s_waitcnt vmcnt(14)\n"
"v_dot2_f32_bf16 v24, v108, s66, v24\n"
"v_dot2_f32_bf16 v25, v108, s67, v25\n"
"v_dot2_f32_bf16 v26, v109, s66, v26\n"
"v_dot2_f32_bf16 v27, v109, s67, v27\n"
"v_dot2_f32_bf16 v28, v110, s66, v28\n"
"v_dot2_f32_bf16 v29, v110, s67, v29\n"
"v_dot2_f32_bf16 v30, v111, s66, v30\n"
"v_dot2_f32_bf16 v31, v111, s67, v31\n"
"s_add_u32 s34, s64, 768000\n"
"buffer_load_dwordx4 v[108:111], v20, s[8:11], s34 offen\n"
"s_waitcnt vmcnt(14)\n"
"v_dot2_f32_bf16 v24, v112, s66, v24\n"
"v_dot2_f32_bf16 v25, v112, s67, v25\n"
"v_dot2_f32_bf16 v26, v113, s66, v26\n"
"v_dot2_f32_bf16 v27, v113, s67, v27\n"
"v_dot2_f32_bf16 v28, v114, s66, v28\n"
"v_dot2_f32_bf16 v29, v114, s67, v29\n"
"v_dot2_f32_bf16 v30, v115, s66, v30\n"
"v_dot2_f32_bf16 v31, v115, s67, v31\n"
"s_add_u32 s34, s64, 798720\n"
"buffer_load_dwordx4 v[112:115], v20, s[8:11], s34 offen\n"
"s_waitcnt vmcnt(14)\n"
"v_dot2_f32_bf16 v24, v116, s66, v24\n"
"v_dot2_f32_bf16 v25, v116, s67, v25\n"
"v_dot2_f32_bf16 v26, v117, s66, v26\n"
"v_dot2_f32_bf16 v27, v117, s67, v27\n"
"v_dot2_f32_bf16 v28, v118, s66, v28\n"
"v_dot2_f32_bf16 v29, v118, s67, v29\n"
"v_dot2_f32_bf16 v30, v119, s66, v30\n"
"v_dot2_f32_bf16 v31, v119, s67, v31\n"
"s_add_u32 s34, s64, 829440\n"
"buffer_load_dwordx4 v[116:119], v20, s[8:11], s34 offen\n"
"s_waitcnt vmcnt(13)\n"
"v_dot2_f32_bf16 v24, v64, s66, v24\n"
"v_dot2_f32_bf16 v25, v64, s67, v25\n"
"v_dot2_f32_bf16 v26, v65, s66, v26\n"
"v_dot2_f32_bf16 v27, v65, s67, v27\n"
"v_dot2_f32_bf16 v28, v66, s66, v28\n"
"v_dot2_f32_bf16 v29, v66, s67, v29\n"
"v_dot2_f32_bf16 v30, v67, s66, v30\n"
"v_dot2_f32_bf16 v31, v67, s67, v31\n"
"s_add_u32 s34, s64, 860160\n"
"buffer_load_dwordx4 v[64:67], v20, s[8:11], s34 offen\n"
"s_waitcnt vmcnt(13)\n"
"v_dot2_f32_bf16 v24, v68, s66, v24\n"
"v_dot2_f32_bf16 v25, v68, s67, v25\n"
"v_dot2_f32_bf16 v26, v69, s66, v26\n"
"v_dot2_f32_bf16 v27, v69, s67, v27\n"
"v_dot2_f32_bf16 v28, v70, s66, v28\n"
"v_dot2_f32_bf16 v29, v70, s67, v29\n"
"v_dot2_f32_bf16 v30, v71, s66, v30\n"
"v_dot2_f32_bf16 v31, v71, s67, v31\n"
"s_add_u32 s34, s64, 890880\n"
"buffer_load_dwordx4 v[68:71], v20, s[8:11], s34 offen\n"
"s_waitcnt vmcnt(13)\n"
"v_dot2_f32_bf16 v24, v72, s66, v24\n"
"v_dot2_f32_bf16 v25, v72, s67, v25\n"
"v_dot2_f32_bf16 v26, v73, s66, v26\n"
"v_dot2_f32_bf16 v27, v73, s67, v27\n"
"v_dot2_f32_bf16 v28, v74, s66, v28\n"
"v_dot2_f32_bf16 v29, v74, s67, v29\n"
"v_dot2_f32_bf16 v30, v75, s66, v30\n"
"v_dot2_f32_bf16 v31, v75, s67, v31\n"
"s_add_u32 s34, s64, 921600\n"
"buffer_load_dwordx4 v[72:75], v20, s[8:11], s34 offen\n"
"s_waitcnt vmcnt(13)\n"
"v_dot2_f32_bf16 v24, v76, s66, v24\n"
"v_dot2_f32_bf16 v25, v76, s67, v25\n"
"v_dot2_f32_bf16 v26, v77, s66, v26\n"
"v_dot2_f32_bf16 v27, v77, s67, v27\n"
"v_dot2_f32_bf16 v28, v78, s66, v28\n"
"v_dot2_f32_bf16 v29, v78, s67, v29\n"
"v_dot2_f32_bf16 v30, v79, s66, v30\n"
"v_dot2_f32_bf16 v31, v79, s67, v31\n"
"s_add_u32 s34, s64, 952320\n"
"buffer_load_dwordx4 v[76:79], v20, s[8:11], s34 offen\n"
"s_waitcnt vmcnt(13)\n"
"v_dot2_f32_bf16 v24, v80, s66, v24\n"
"v_dot2_f32_bf16 v25, v80, s67, v25\n"
"v_dot2_f32_bf16 v26, v81, s66, v26\n"
"v_dot2_f32_bf16 v27, v81, s67, v27\n"
"v_dot2_f32_bf16 v28, v82, s66, v28\n"
"v_dot2_f32_bf16 v29, v82, s67, v29\n"
"v_dot2_f32_bf16 v30, v83, s66, v30\n"
"v_dot2_f32_bf16 v31, v83, s67, v31\n"
"s_add_u32 s34, s64, 983040\n"
"buffer_load_dwordx4 v[80:83], v20, s[8:11], s34 offen\n"
"s_waitcnt vmcnt(13)\n"
"v_dot2_f32_bf16 v24, v84, s66, v24\n"
"v_dot2_f32_bf16 v25, v84, s67, v25\n"
"v_dot2_f32_bf16 v26, v85, s66, v26\n"
"v_dot2_f32_bf16 v27, v85, s67, v27\n"
"v_dot2_f32_bf16 v28, v86, s66, v28\n"
"v_dot2_f32_bf16 v29, v86, s67, v29\n"
"v_dot2_f32_bf16 v30, v87, s66, v30\n"
"v_dot2_f32_bf16 v31, v87, s67, v31\n"
"s_add_u32 s34, s64, 1013760\n"
"buffer_load_dwordx4 v[84:87], v20, s[8:11], s34 offen\n"
"s_waitcnt vmcnt(13)\n"
"v_dot2_f32_bf16 v24, v88, s66, v24\n"
"v_dot2_f32_bf16 v25, v88, s67, v25\n"
"v_dot2_f32_bf16 v26, v89, s66, v26\n"
"v_dot2_f32_bf16 v27, v89, s67, v27\n"
"v_dot2_f32_bf16 v28, v90, s66, v28\n"
"v_dot2_f32_bf16 v29, v90, s67, v29\n"
"v_dot2_f32_bf16 v30, v91, s66, v30\n"
"v_dot2_f32_bf16 v31, v91, s67, v31\n"
"s_add_u32 s34, s64, 1044480\n"
"buffer_load_dwordx4 v[88:91], v20, s[8:11], s34 offen\n"
"s_waitcnt vmcnt(13)\n"
"v_dot2_f32_bf16 v24, v92, s66, v24\n"
"v_dot2_f32_bf16 v25, v92, s67, v25\n"
"v_dot2_f32_bf16 v26, v93, s66, v26\n"
"v_dot2_f32_bf16 v27, v93, s67, v27\n"
"v_dot2_f32_bf16 v28, v94, s66, v28\n"
"v_dot2_f32_bf16 v29, v94, s67, v29\n"
"v_dot2_f32_bf16 v30, v95, s66, v30\n"
"v_dot2_f32_bf16 v31, v95, s67, v31\n"
"s_add_u32 s34, s64, 1075200\n"
"buffer_load_dwordx4 v[92:95], v20, s[8:11], s34 offen\n"
"s_waitcnt vmcnt(13)\n"
"v_dot2_f32_bf16 v24, v96, s66, v24\n"
"v_dot2_f32_bf16 v25, v96, s67, v25\n"
"v_dot2_f32_bf16 v26, v97, s66, v26\n"
"v_dot2_f32_bf16 v27, v97, s67, v27\n"
"v_dot2_f32_bf16 v28, v98, s66, v28\n"
"v_dot2_f32_bf16 v29, v98, s67, v29\n"
"v_dot2_f32_bf16 v30, v99, s66, v30\n"
"v_dot2_f32_bf16 v31, v99, s67, v31\n"
"s_add_u32 s34, s64, 1105920\n"
"buffer_load_dwordx4 v[96:99], v20, s[8:11], s34 offen\n"
"s_waitcnt vmcnt(13)\n"
"v_dot2_f32_bf16 v24, v100, s66, v24\n"
"v_dot2_f32_bf16 v25, v100, s67, v25\n"
"v_dot2_f32_bf16 v26, v101, s66, v26\n"
"v_dot2_f32_bf16 v27, v101, s67, v27\n"
"v_dot2_f32_bf16 v28, v102, s66, v28\n"
"v_dot2_f32_bf16 v29, v102, s67, v29\n"
"v_dot2_f32_bf16 v30, v103, s66, v30\n"
"v_dot2_f32_bf16 v31, v103, s67, v31\n"
"s_add_u32 s34, s64, 1136640\n"
"buffer_load_dwordx4 v[100:103], v20, s[8:11], s34 offen\n"
"s_waitcnt vmcnt(13)\n"
"v_dot2_f32_bf16 v24, v104, s66, v24\n"
"v_dot2_f32_bf16 v25, v104, s67, v25\n"
"v_dot2_f32_bf16 v26, v105, s66, v26\n"
"v_dot2_f32_bf16 v27, v105, s67, v27\n"
"v_dot2_f32_bf16 v28, v106, s66, v28\n"
"v_dot2_f32_bf16 v29, v106, s67, v29\n"
"v_dot2_f32_bf16 v30, v107, s66, v30\n"
"v_dot2_f32_bf16 v31, v107, s67, v31\n"
"s_add_u32 s34, s64, 1167360\n"
"buffer_load_dwordx4 v[104:107], v20, s[8:11], s34 offen\n"
"s_waitcnt vmcnt(13)\n"
"v_dot2_f32_bf16 v24, v108, s66, v24\n"
"v_dot2_f32_bf16 v25, v108, s67, v25\n"
"v_dot2_f32_bf16 v26, v109, s66, v26\n"
"v_dot2_f32_bf16 v27, v109, s67, v27\n"
"v_dot2_f32_bf16 v28, v110, s66, v28\n"
"v_dot2_f32_bf16 v29, v110, s67, v29\n"
"v_dot2_f32_bf16 v30, v111, s66, v30\n"
"v_dot2_f32_bf16 v31, v111, s67, v31\n"
"s_add_u32 s34, s64, 1198080\n"
"buffer_load_dwordx4 v[108:111], v20, s[8:11], s34 offen\n"
"s_waitcnt vmcnt(13)\n"
"v_dot2_f32_bf16 v24, v112, s66, v24\n"
"v_dot2_f32_bf16 v25, v112, s67, v25\n"
"v_dot2_f32_bf16 v26, v113, s66, v26\n"
"v_dot2_f32_bf16 v27, v113, s67, v27\n"
"v_dot2_f32_bf16 v28, v114, s66, v28\n"
"v_dot2_f32_bf16 v29, v114, s67, v29\n"
"v_dot2_f32_bf16 v30, v115, s66, v30\n"
"v_dot2_f32_bf16 v31, v115, s67, v31\n"
"s_add_u32 s34, s64, 1228800\n"
"buffer_load_dwordx4 v[112:115], v20, s[8:11], s34 offen\n"
"s_waitcnt vmcnt(13)\n"
"v_dot2_f32_bf16 v24, v116, s66, v24\n"
"v_dot2_f32_bf16 v25, v116, s67, v25\n"
"v_dot2_f32_bf16 v26, v117, s66, v26\n"
"v_dot2_f32_bf16 v27, v117, s67, v27\n"
"v_dot2_f32_bf16 v28, v118, s66, v28\n"
"v_dot2_f32_bf16 v29, v118, s67, v29\n"
"v_dot2_f32_bf16 v30, v119, s66, v30\n"
"v_dot2_f32_bf16 v31, v119, s67, v31\n"
"s_add_u32 s34, s64, 1259520\n"
"buffer_load_dwordx4 v[116:119], v20, s[8:11], s34 offen\n"
"s_waitcnt vmcnt(13)\n"
"v_dot2_f32_bf16 v24, v64, s66, v24\n"
"v_dot2_f32_bf16 v25, v64, s67, v25\n"
"v_dot2_f32_bf16 v26, v65, s66, v26\n"
"v_dot2_f32_bf16 v27, v65, s67, v27\n"
"v_dot2_f32_bf16 v28, v66, s66, v28\n"
"v_dot2_f32_bf16 v29, v66, s67, v29\n"
"v_dot2_f32_bf16 v30, v67, s66, v30\n"
"v_dot2_f32_bf16 v31, v67, s67, v31\n"
"s_add_u32 s34, s64, 1290240\n"
"buffer_load_dwordx4 v[64:67], v20, s[8:11], s34 offen\n"
"s_waitcnt vmcnt(13)\n"
"v_dot2_f32_bf16 v24, v68, s66, v24\n"
"v_dot2_f32_bf16 v25, v68, s67, v25\n"
"v_dot2_f32_bf16 v26, v69, s66, v26\n"
"v_dot2_f32_bf16 v27, v69, s67, v27\n"
"v_dot2_f32_bf16 v28, v70, s66, v28\n"
"v_dot2_f32_bf16 v29, v70, s67, v29\n"
"v_dot2_f32_bf16 v30, v71, s66, v30\n"
"v_dot2_f32_bf16 v31, v71, s67, v31\n"
"s_add_u32 s34, s64, 1320960\n"
"buffer_load_dwordx4 v[68:71], v20, s[8:11], s34 offen\n"
"s_waitcnt vmcnt(13)\n"
"v_dot2_f32_bf16 v24, v72, s66, v24\n"
"v_dot2_f32_bf16 v25, v72, s67, v25\n"
"v_dot2_f32_bf16 v26, v73, s66, v26\n"
"v_dot2_f32_bf16 v27, v73, s67, v27\n"
"v_dot2_f32_bf16 v28, v74, s66, v28\n"
"v_dot2_f32_bf16 v29, v74, s67, v29\n"
"v_dot2_f32_bf16 v30, v75, s66, v30\n"
"v_dot2_f32_bf16 v31, v75, s67, v31\n"
"s_add_u32 s34, s64, 1351680\n"
"buffer_load_dwordx4 v[72:75], v20, s[8:11], s34 offen\n"
"s_waitcnt vmcnt(13)\n"
"v_dot2_f32_bf16 v24, v76, s66, v24\n"
"v_dot2_f32_bf16 v25, v76, s67, v25\n"
"v_dot2_f32_bf16 v26, v77, s66, v26\n"
"v_dot2_f32_bf16 v27, v77, s67, v27\n"
"v_dot2_f32_bf16 v28, v78, s66, v28\n"
"v_dot2_f32_bf16 v29, v78, s67, v29\n"
"v_dot2_f32_bf16 v30, v79, s66, v30\n"
"v_dot2_f32_bf16 v31, v79, s67, v31\n"
"s_add_u32 s34, s64, 1382400\n"
"buffer_load_dwordx4 v[76:79], v20, s[8:11], s34 offen\n"
"s_waitcnt vmcnt(13)\n"
"v_dot2_f32_bf16 v24, v80, s66, v24\n"
"v_dot2_f32_bf16 v25, v80, s67, v25\n"
"v_dot2_f32_bf16 v26, v81, s66, v26\n"
"v_dot2_f32_bf16 v27, v81, s67, v27\n"
"v_dot2_f32_bf16 v28, v82, s66, v28\n"
"v_dot2_f32_bf16 v29, v82, s67, v29\n"
"v_dot2_f32_bf16 v30, v83, s66, v30\n"
"v_dot2_f32_bf16 v31, v83, s67, v31\n"
"s_add_u32 s34, s64, 1413120\n"
"buffer_load_dwordx4 v[80:83], v20, s[8:11], s34 offen\n"
"s_waitcnt vmcnt(13)\n"
"v_dot2_f32_bf16 v24, v84, s66, v24\n"
"v_dot2_f32_bf16 v25, v84, s67, v25\n"
"v_dot2_f32_bf16 v26, v85, s66, v26\n"
"v_dot2_f32_bf16 v27, v85, s67, v27\n"
"v_dot2_f32_bf16 v28, v86, s66, v28\n"
"v_dot2_f32_bf16 v29, v86, s67, v29\n"
"v_dot2_f32_bf16 v30, v87, s66, v30\n"
"v_dot2_f32_bf16 v31, v87, s67, v31\n"
"s_add_u32 s34, s64, 1443840\n"
"buffer_load_dwordx4 v[84:87], v20, s[8:11], s34 offen\n"
"s_waitcnt vmcnt(13)\n"
"v_dot2_f32_bf16 v24, v88, s66, v24\n"
"v_dot2_f32_bf16 v25, v88, s67, v25\n"
"v_dot2_f32_bf16 v26, v89, s66, v26\n"
"v_dot2_f32_bf16 v27, v89, s67, v27\n"
"v_dot2_f32_bf16 v28, v90, s66, v28\n"
"v_dot2_f32_bf16 v29, v90, s67, v29\n"
"v_dot2_f32_bf16 v30, v91, s66, v30\n"
"v_dot2_f32_bf16 v31, v91, s67, v31\n"
"s_add_u32 s34, s64, 1474560\n"
"buffer_load_dwordx4 v[88:91], v20, s[8:11], s34 offen\n"
"s_waitcnt vmcnt(13)\n"
"v_dot2_f32_bf16 v24, v92, s66, v24\n"
"v_dot2_f32_bf16 v25, v92, s67, v25\n"
"v_dot2_f32_bf16 v26, v93, s66, v26\n"
"v_dot2_f32_bf16 v27, v93, s67, v27\n"
"v_dot2_f32_bf16 v28, v94, s66, v28\n"
"v_dot2_f32_bf16 v29, v94, s67, v29\n"
"v_dot2_f32_bf16 v30, v95, s66, v30\n"
"v_dot2_f32_bf16 v31, v95, s67, v31\n"
"s_add_u32 s34, s64, 1505280\n"
"buffer_load_dwordx4 v[92:95], v20, s[8:11], s34 offen\n"
"s_waitcnt vmcnt(13)\n"
"v_dot2_f32_bf16 v24, v96, s66, v24\n"
"v_dot2_f32_bf16 v25, v96, s67, v25\n"
"v_dot2_f32_bf16 v26, v97, s66, v26\n"
"v_dot2_f32_bf16 v27, v97, s67, v27\n"
"v_dot2_f32_bf16 v28, v98, s66, v28\n"
"v_dot2_f32_bf16 v29, v98, s67, v29\n"
"v_dot2_f32_bf16 v30, v99, s66, v30\n"
"v_dot2_f32_bf16 v31, v99, s67, v31\n"
"s_add_u32 s34, s64, 1536000\n"
"buffer_load_dwordx4 v[96:99], v20, s[8:11], s34 offen\n"
"s_waitcnt vmcnt(13)\n"
"v_dot2_f32_bf16 v24, v100, s66, v24\n"
"v_dot2_f32_bf16 v25, v100, s67, v25\n"
"v_dot2_f32_bf16 v26, v101, s66, v26\n"
"v_dot2_f32_bf16 v27, v101, s67, v27\n"
"v_dot2_f32_bf16 v28, v102, s66, v28\n"
"v_dot2_f32_bf16 v29, v102, s67, v29\n"
"v_dot2_f32_bf16 v30, v103, s66, v30\n"
"v_dot2_f32_bf16 v31, v103, s67, v31\n"
"s_add_u32 s34, s64, 1566720\n"
"buffer_load_dwordx4 v[100:103], v20, s[8:11], s34 offen\n"
"s_waitcnt vmcnt(13)\n"
"v_dot2_f32_bf16 v24, v104, s66, v24\n"
"v_dot2_f32_bf16 v25, v104, s67, v25\n"
"v_dot2_f32_bf16 v26, v105, s66, v26\n"
"v_dot2_f32_bf16 v27, v105, s67, v27\n"
"v_dot2_f32_bf16 v28, v106, s66, v28\n"
"v_dot2_f32_bf16 v29, v106, s67, v29\n"
"v_dot2_f32_bf16 v30, v107, s66, v30\n"
"v_dot2_f32_bf16 v31, v107, s67, v31\n"
"s_add_u32 s34, s64, 1597440\n"
"buffer_load_dwordx4 v[104:107], v20, s[8:11], s34 offen\n"
"s_waitcnt vmcnt(13)\n"
"v_dot2_f32_bf16 v24, v108, s66, v24\n"
"v_dot2_f32_bf16 v25, v108, s67, v25\n"
"v_dot2_f32_bf16 v26, v109, s66, v26\n"
"v_dot2_f32_bf16 v27, v109, s67, v27\n"
"v_dot2_f32_bf16 v28, v110, s66, v28\n"
"v_dot2_f32_bf16 v29, v110, s67, v29\n"
"v_dot2_f32_bf16 v30, v111, s66, v30\n"
"v_dot2_f32_bf16 v31, v111, s67, v31\n"
"s_add_u32 s34, s64, 1628160\n"
"buffer_load_dwordx4 v[108:111], v20, s[8:11], s34 offen\n"
"s_waitcnt vmcnt(13)\n"
"v_dot2_f32_bf16 v24, v112, s66, v24\n"
"v_dot2_f32_bf16 v25, v112, s67, v25\n"
"v_dot2_f32_bf16 v26, v113, s66, v26\n"
"v_dot2_f32_bf16 v27, v113, s67, v27\n"
"v_dot2_f32_bf16 v28, v114, s66, v28\n"
"v_dot2_f32_bf16 v29, v114, s67, v29\n"
"v_dot2_f32_bf16 v30, v115, s66, v30\n"
"v_dot2_f32_bf16 v31, v115, s67, v31\n"
"s_add_u32 s34, s64, 1658880\n"
"buffer_load_dwordx4 v[112:115], v20, s[8:11], s34 offen\n"
"s_waitcnt vmcnt(13)\n"
"v_dot2_f32_bf16 v24, v116, s66, v24\n"
"v_dot2_f32_bf16 v25, v116, s67, v25\n"
"v_dot2_f32_bf16 v26, v117, s66, v26\n"
"v_dot2_f32_bf16 v27, v117, s67, v27\n"
"v_dot2_f32_bf16 v28, v118, s66, v28\n"
"v_dot2_f32_bf16 v29, v118, s67, v29\n"
"v_dot2_f32_bf16 v30, v119, s66, v30\n"
"v_dot2_f32_bf16 v31, v119, s67, v31\n"
"s_add_u32 s34, s64, 1689600\n"
"buffer_load_dwordx4 v[116:119], v20, s[8:11], s34 offen\n"
"s_waitcnt vmcnt(13)\n"
"v_dot2_f32_bf16 v24, v64, s66, v24\n"
"v_dot2_f32_bf16 v25, v64, s67, v25\n"
"v_dot2_f32_bf16 v26, v65, s66, v26\n"
"v_dot2_f32_bf16 v27, v65, s67, v27\n"
"v_dot2_f32_bf16 v28, v66, s66, v28\n"
"v_dot2_f32_bf16 v29, v66, s67, v29\n"
"v_dot2_f32_bf16 v30, v67, s66, v30\n"
"v_dot2_f32_bf16 v31, v67, s67, v31\n"
"s_add_u32 s34, s64, 1720320\n"
"buffer_load_dwordx4 v[64:67], v20, s[8:11], s34 offen\n"
"s_waitcnt vmcnt(13)\n"
"v_dot2_f32_bf16 v24, v68, s66, v24\n"
"v_dot2_f32_bf16 v25, v68, s67, v25\n"
"v_dot2_f32_bf16 v26, v69, s66, v26\n"
"v_dot2_f32_bf16 v27, v69, s67, v27\n"
"v_dot2_f32_bf16 v28, v70, s66, v28\n"
"v_dot2_f32_bf16 v29, v70, s67, v29\n"
"v_dot2_f32_bf16 v30, v71, s66, v30\n"
"v_dot2_f32_bf16 v31, v71, s67, v31\n"
"s_add_u32 s34, s64, 1751040\n"
"buffer_load_dwordx4 v[68:71], v20, s[8:11], s34 offen\n"
"s_waitcnt vmcnt(13)\n"
"v_dot2_f32_bf16 v24, v72, s66, v24\n"
"v_dot2_f32_bf16 v25, v72, s67, v25\n"
"v_dot2_f32_bf16 v26, v73, s66, v26\n"
"v_dot2_f32_bf16 v27, v73, s67, v27\n"
"v_dot2_f32_bf16 v28, v74, s66, v28\n"
"v_dot2_f32_bf16 v29, v74, s67, v29\n"
"v_dot2_f32_bf16 v30, v75, s66, v30\n"
"v_dot2_f32_bf16 v31, v75, s67, v31\n"
"s_add_u32 s34, s64, 1781760\n"
"buffer_load_dwordx4 v[72:75], v20, s[8:11], s34 offen\n"
"s_waitcnt vmcnt(13)\n"
"v_dot2_f32_bf16 v24, v76, s66, v24\n"
"v_dot2_f32_bf16 v25, v76, s67, v25\n"
"v_dot2_f32_bf16 v26, v77, s66, v26\n"
"v_dot2_f32_bf16 v27, v77, s67, v27\n"
"v_dot2_f32_bf16 v28, v78, s66, v28\n"
"v_dot2_f32_bf16 v29, v78, s67, v29\n"
"v_dot2_f32_bf16 v30, v79, s66, v30\n"
"v_dot2_f32_bf16 v31, v79, s67, v31\n"
"s_add_u32 s34, s64, 1812480\n"
"buffer_load_dwordx4 v[76:79], v20, s[8:11], s34 offen\n"
"s_waitcnt vmcnt(13)\n"
"v_dot2_f32_bf16 v24, v80, s66, v24\n"
"v_dot2_f32_bf16 v25, v80, s67, v25\n"
"v_dot2_f32_bf16 v26, v81, s66, v26\n"
"v_dot2_f32_bf16 v27, v81, s67, v27\n"
"v_dot2_f32_bf16 v28, v82, s66, v28\n"
"v_dot2_f32_bf16 v29, v82, s67, v29\n"
"v_dot2_f32_bf16 v30, v83, s66, v30\n"
"v_dot2_f32_bf16 v31, v83, s67, v31\n"
"s_add_u32 s34, s64, 1843200\n"
"buffer_load_dwordx4 v[80:83], v20, s[8:11], s34 offen\n"
"s_waitcnt vmcnt(13)\n"
"v_dot2_f32_bf16 v24, v84, s66, v24\n"
"v_dot2_f32_bf16 v25, v84, s67, v25\n"
"v_dot2_f32_bf16 v26, v85, s66, v26\n"
"v_dot2_f32_bf16 v27, v85, s67, v27\n"
"v_dot2_f32_bf16 v28, v86, s66, v28\n"
"v_dot2_f32_bf16 v29, v86, s67, v29\n"
"v_dot2_f32_bf16 v30, v87, s66, v30\n"
"v_dot2_f32_bf16 v31, v87, s67, v31\n"
"s_add_u32 s34, s64, 1873920\n"
"buffer_load_dwordx4 v[84:87], v20, s[8:11], s34 offen\n"
"s_waitcnt vmcnt(13)\n"
"v_dot2_f32_bf16 v24, v88, s66, v24\n"
"v_dot2_f32_bf16 v25, v88, s67, v25\n"
"v_dot2_f32_bf16 v26, v89, s66, v26\n"
"v_dot2_f32_bf16 v27, v89, s67, v27\n"
"v_dot2_f32_bf16 v28, v90, s66, v28\n"
"v_dot2_f32_bf16 v29, v90, s67, v29\n"
"v_dot2_f32_bf16 v30, v91, s66, v30\n"
"v_dot2_f32_bf16 v31, v91, s67, v31\n"
"s_add_u32 s34, s64, 1904640\n"
"buffer_load_dwordx4 v[88:91], v20, s[8:11], s34 offen\n"
"s_waitcnt vmcnt(13)\n"
"v_dot2_f32_bf16 v24, v92, s66, v24\n"
"v_dot2_f32_bf16 v25, v92, s67, v25\n"
"v_dot2_f32_bf16 v26, v93, s66, v26\n"
"v_dot2_f32_bf16 v27, v93, s67, v27\n"
"v_dot2_f32_bf16 v28, v94, s66, v28\n"
"v_dot2_f32_bf16 v29, v94, s67, v29\n"
"v_dot2_f32_bf16 v30, v95, s66, v30\n"
"v_dot2_f32_bf16 v31, v95, s67, v31\n"
"s_add_u32 s34, s64, 1935360\n"
"buffer_load_dwordx4 v[92:95], v20, s[8:11], s34 offen\n"
"s_waitcnt vmcnt(13)\n"
"v_dot2_f32_bf16 v24, v96, s66, v24\n"
"v_dot2_f32_bf16 v25, v96, s67, v25\n"
"v_dot2_f32_bf16 v26, v97, s66, v26\n"
"v_dot2_f32_bf16 v27, v97, s67, v27\n"
"v_dot2_f32_bf16 v28, v98, s66, v28\n"
"v_dot2_f32_bf16 v29, v98, s67, v29\n"
"v_dot2_f32_bf16 v30, v99, s66, v30\n"
"v_dot2_f32_bf16 v31, v99, s67, v31\n"
"s_add_u32 s34, s64, 1966080\n"
"buffer_load_dwordx4 v[96:99], v20, s[8:11], s34 offen\n"
"s_waitcnt vmcnt(13)\n"
"v_dot2_f32_bf16 v24, v100, s66, v24\n"
"v_dot2_f32_bf16 v25, v100, s67, v25\n"
"v_dot2_f32_bf16 v26, v101, s66, v26\n"
"v_dot2_f32_bf16 v27, v101, s67, v27\n"
"v_dot2_f32_bf16 v28, v102, s66, v28\n"
"v_dot2_f32_bf16 v29, v102, s67, v29\n"
"v_dot2_f32_bf16 v30, v103, s66, v30\n"
"v_dot2_f32_bf16 v31, v103, s67, v31\n"
"s_add_u32 s34, s64, 1996800\n"
"buffer_load_dwordx4 v[100:103], v20, s[8:11], s34 offen\n"
"s_waitcnt vmcnt(13)\n"
"v_dot2_f32_bf16 v24, v104, s66, v24\n"
"v_dot2_f32_bf16 v25, v104, s67, v25\n"
"v_dot2_f32_bf16 v26, v105, s66, v26\n"
"v_dot2_f32_bf16 v27, v105, s67, v27\n"
"v_dot2_f32_bf16 v28, v106, s66, v28\n"
"v_dot2_f32_bf16 v29, v106, s67, v29\n"
"v_dot2_f32_bf16 v30, v107, s66, v30\n"
"v_dot2_f32_bf16 v31, v107, s67, v31\n"
"s_add_u32 s34, s64, 2027520\n"
"buffer_load_dwordx4 v[104:107], v20, s[8:11], s34 offen\n"
"s_waitcnt vmcnt(13)\n"
"v_dot2_f32_bf16 v24, v108, s66, v24\n"
"v_dot2_f32_bf16 v25, v108, s67, v25\n"
"v_dot2_f32_bf16 v26, v109, s66, v26\n"
"v_dot2_f32_bf16 v27, v109, s67, v27\n"
"v_dot2_f32_bf16 v28, v110, s66, v28\n"
"v_dot2_f32_bf16 v29, v110, s67, v29\n"
"v_dot2_f32_bf16 v30, v111, s66, v30\n"
"v_dot2_f32_bf16 v31, v111, s67, v31\n"
"s_add_u32 s34, s64, 2058240\n"
"buffer_load_dwordx4 v[108:111], v20, s[8:11], s34 offen\n"
"s_waitcnt vmcnt(13)\n"
"v_dot2_f32_bf16 v24, v112, s66, v24\n"
"v_dot2_f32_bf16 v25, v112, s67, v25\n"
"v_dot2_f32_bf16 v26, v113, s66, v26\n"
"v_dot2_f32_bf16 v27, v113, s67, v27\n"
"v_dot2_f32_bf16 v28, v114, s66, v28\n"
"v_dot2_f32_bf16 v29, v114, s67, v29\n"
"v_dot2_f32_bf16 v30, v115, s66, v30\n"
"v_dot2_f32_bf16 v31, v115, s67, v31\n"
"s_add_u32 s34, s64, 2088960\n"
"buffer_load_dwordx4 v[112:115], v20, s[8:11], s34 offen\n"
"s_waitcnt vmcnt(13)\n"
"v_dot2_f32_bf16 v24, v116, s66, v24\n"
"v_dot2_f32_bf16 v25, v116, s67, v25\n"
"v_dot2_f32_bf16 v26, v117, s66, v26\n"
"v_dot2_f32_bf16 v27, v117, s67, v27\n"
"v_dot2_f32_bf16 v28, v118, s66, v28\n"
"v_dot2_f32_bf16 v29, v118, s67, v29\n"
"v_dot2_f32_bf16 v30, v119, s66, v30\n"
"v_dot2_f32_bf16 v31, v119, s67, v31\n"
"s_add_u32 s34, s64, 2119680\n"
"buffer_load_dwordx4 v[116:119], v20, s[8:11], s34 offen\n"
"s_waitcnt vmcnt(13)\n"
"v_dot2_f32_bf16 v24, v64, s66, v24\n"
"v_dot2_f32_bf16 v25, v64, s67, v25\n"
"v_dot2_f32_bf16 v26, v65, s66, v26\n"
"v_dot2_f32_bf16 v27, v65, s67, v27\n"
"v_dot2_f32_bf16 v28, v66, s66, v28\n"
"v_dot2_f32_bf16 v29, v66, s67, v29\n"
"v_dot2_f32_bf16 v30, v67, s66, v30\n"
"v_dot2_f32_bf16 v31, v67, s67, v31\n"
"s_add_u32 s34, s64, 2150400\n"
"buffer_load_dwordx4 v[64:67], v20, s[8:11], s34 offen\n"
"s_waitcnt vmcnt(13)\n"
"v_dot2_f32_bf16 v24, v68, s66, v24\n"
"v_dot2_f32_bf16 v25, v68, s67, v25\n"
"v_dot2_f32_bf16 v26, v69, s66, v26\n"
"v_dot2_f32_bf16 v27, v69, s67, v27\n"
"v_dot2_f32_bf16 v28, v70, s66, v28\n"
"v_dot2_f32_bf16 v29, v70, s67, v29\n"
"v_dot2_f32_bf16 v30, v71, s66, v30\n"
"v_dot2_f32_bf16 v31, v71, s67, v31\n"
"s_add_u32 s34, s64, 2181120\n"
"buffer_load_dwordx4 v[68:71], v20, s[8:11], s34 offen\n"
"s_waitcnt vmcnt(13)\n"
"v_dot2_f32_bf16 v24, v72, s66, v24\n"
"v_dot2_f32_bf16 v25, v72, s67, v25\n"
"v_dot2_f32_bf16 v26, v73, s66, v26\n"
"v_dot2_f32_bf16 v27, v73, s67, v27\n"
"v_dot2_f32_bf16 v28, v74, s66, v28\n"
"v_dot2_f32_bf16 v29, v74, s67, v29\n"
"v_dot2_f32_bf16 v30, v75, s66, v30\n"
"v_dot2_f32_bf16 v31, v75, s67, v31\n"
"s_add_u32 s34, s64, 2211840\n"
"buffer_load_dwordx4 v[72:75], v20, s[8:11], s34 offen\n"
"s_waitcnt vmcnt(13)\n"
"v_dot2_f32_bf16 v24, v76, s66, v24\n"
"v_dot2_f32_bf16 v25, v76, s67, v25\n"
"v_dot2_f32_bf16 v26, v77, s66, v26\n"
"v_dot2_f32_bf16 v27, v77, s67, v27\n"
"v_dot2_f32_bf16 v28, v78, s66, v28\n"
"v_dot2_f32_bf16 v29, v78, s67, v29\n"
"v_dot2_f32_bf16 v30, v79, s66, v30\n"
"v_dot2_f32_bf16 v31, v79, s67, v31\n"
"s_add_u32 s34, s64, 2242560\n"
"buffer_load_dwordx4 v[76:79], v20, s[8:11], s34 offen\n"
"s_waitcnt vmcnt(13)\n"
"v_dot2_f32_bf16 v24, v80, s66, v24\n"
"v_dot2_f32_bf16 v25, v80, s67, v25\n"
"v_dot2_f32_bf16 v26, v81, s66, v26\n"
"v_dot2_f32_bf16 v27, v81, s67, v27\n"
"v_dot2_f32_bf16 v28, v82, s66, v28\n"
"v_dot2_f32_bf16 v29, v82, s67, v29\n"
"v_dot2_f32_bf16 v30, v83, s66, v30\n"
"v_dot2_f32_bf16 v31, v83, s67, v31\n"
"s_add_u32 s34, s64, 2273280\n"
"buffer_load_dwordx4 v[80:83], v20, s[8:11], s34 offen\n"
"s_waitcnt vmcnt(13)\n"
"v_dot2_f32_bf16 v24, v84, s66, v24\n"
"v_dot2_f32_bf16 v25, v84, s67, v25\n"
"v_dot2_f32_bf16 v26, v85, s66, v26\n"
"v_dot2_f32_bf16 v27, v85, s67, v27\n"
"v_dot2_f32_bf16 v28, v86, s66, v28\n"
"v_dot2_f32_bf16 v29, v86, s67, v29\n"
"v_dot2_f32_bf16 v30, v87, s66, v30\n"
"v_dot2_f32_bf16 v31, v87, s67, v31\n"
"s_add_u32 s34, s64, 2304000\n"
"buffer_load_dwordx4 v[84:87], v20, s[8:11], s34 offen\n"
"s_waitcnt vmcnt(13)\n"
"v_dot2_f32_bf16 v24, v88, s66, v24\n"
"v_dot2_f32_bf16 v25, v88, s67, v25\n"
"v_dot2_f32_bf16 v26, v89, s66, v26\n"
"v_dot2_f32_bf16 v27, v89, s67, v27\n"
"v_dot2_f32_bf16 v28, v90, s66, v28\n"
"v_dot2_f32_bf16 v29, v90, s67, v29\n"
"v_dot2_f32_bf16 v30, v91, s66, v30\n"
"v_dot2_f32_bf16 v31, v91, s67, v31\n"
"s_add_u32 s34, s64, 2334720\n"
"buffer_load_dwordx4 v[88:91], v20, s[8:11], s34 offen\n"
"s_waitcnt vmcnt(13)\n"
"v_dot2_f32_bf16 v24, v92, s66, v24\n"
"v_dot2_f32_bf16 v25, v92, s67, v25\n"
"v_dot2_f32_bf16 v26, v93, s66, v26\n"
"v_dot2_f32_bf16 v27, v93, s67, v27\n"
"v_dot2_f32_bf16 v28, v94, s66, v28\n"
"v_dot2_f32_bf16 v29, v94, s67, v29\n"
"v_dot2_f32_bf16 v30, v95, s66, v30\n"
"v_dot2_f32_bf16 v31, v95, s67, v31\n"
"s_add_u32 s34, s64, 2365440\n"
"buffer_load_dwordx4 v[92:95], v20, s[8:11], s34 offen\n"
"s_waitcnt vmcnt(13)\n"
"v_dot2_f32_bf16 v24, v96, s66, v24\n"
"v_dot2_f32_bf16 v25, v96, s67, v25\n"
"v_dot2_f32_bf16 v26, v97, s66, v26\n"
"v_dot2_f32_bf16 v27, v97, s67, v27\n"
"v_dot2_f32_bf16 v28, v98, s66, v28\n"
"v_dot2_f32_bf16 v29, v98, s67, v29\n"
"v_dot2_f32_bf16 v30, v99, s66, v30\n"
"v_dot2_f32_bf16 v31, v99, s67, v31\n"
"s_add_u32 s34, s64, 2396160\n"
"buffer_load_dwordx4 v[96:99], v20, s[8:11], s34 offen\n"
"s_waitcnt vmcnt(13)\n"
"v_dot2_f32_bf16 v24, v100, s66, v24\n"
"v_dot2_f32_bf16 v25, v100, s67, v25\n"
"v_dot2_f32_bf16 v26, v101, s66, v26\n"
"v_dot2_f32_bf16 v27, v101, s67, v27\n"
"v_dot2_f32_bf16 v28, v102, s66, v28\n"
"v_dot2_f32_bf16 v29, v102, s67, v29\n"
"v_dot2_f32_bf16 v30, v103, s66, v30\n"
"v_dot2_f32_bf16 v31, v103, s67, v31\n"
"s_add_u32 s34, s64, 2426880\n"
"buffer_load_dwordx4 v[100:103], v20, s[8:11], s34 offen\n"
"s_waitcnt vmcnt(13)\n"
"v_dot2_f32_bf16 v24, v104, s66, v24\n"
"v_dot2_f32_bf16 v25, v104, s67, v25\n"
"v_dot2_f32_bf16 v26, v105, s66, v26\n"
"v_dot2_f32_bf16 v27, v105, s67, v27\n"
"v_dot2_f32_bf16 v28, v106, s66, v28\n"
"v_dot2_f32_bf16 v29, v106, s67, v29\n"
"v_dot2_f32_bf16 v30, v107, s66, v30\n"
"v_dot2_f32_bf16 v31, v107, s67, v31\n"
"s_waitcnt vmcnt(12)\n"
"v_dot2_f32_bf16 v24, v108, s66, v24\n"
"v_dot2_f32_bf16 v25, v108, s67, v25\n"
"v_dot2_f32_bf16 v26, v109, s66, v26\n"
"v_dot2_f32_bf16 v27, v109, s67, v27\n"
"v_dot2_f32_bf16 v28, v110, s66, v28\n"
"v_dot2_f32_bf16 v29, v110, s67, v29\n"
"v_dot2_f32_bf16 v30, v111, s66, v30\n"
"v_dot2_f32_bf16 v31, v111, s67, v31\n"
"s_waitcnt vmcnt(11)\n"
"v_dot2_f32_bf16 v24, v112, s66, v24\n"
"v_dot2_f32_bf16 v25, v112, s67, v25\n"
"v_dot2_f32_bf16 v26, v113, s66, v26\n"
"v_dot2_f32_bf16 v27, v113, s67, v27\n"
"v_dot2_f32_bf16 v28, v114, s66, v28\n"
"v_dot2_f32_bf16 v29, v114, s67, v29\n"
"v_dot2_f32_bf16 v30, v115, s66, v30\n"
"v_dot2_f32_bf16 v31, v115, s67, v31\n"
"s_waitcnt vmcnt(10)\n"
"v_dot2_f32_bf16 v24, v116, s66, v24\n"
"v_dot2_f32_bf16 v25, v116, s67, v25\n"
"v_dot2_f32_bf16 v26, v117, s66, v26\n"
"v_dot2_f32_bf16 v27, v117, s67, v27\n"
"v_dot2_f32_bf16 v28, v118, s66, v28\n"
"v_dot2_f32_bf16 v29, v118, s67, v29\n"
"v_dot2_f32_bf16 v30, v119, s66, v30\n"
"v_dot2_f32_bf16 v31, v119, s67, v31\n"
"s_waitcnt vmcnt(9)\n"
"v_dot2_f32_bf16 v24, v64, s66, v24\n"
"v_dot2_f32_bf16 v25, v64, s67, v25\n"
"v_dot2_f32_bf16 v26, v65, s66, v26\n"
"v_dot2_f32_bf16 v27, v65, s67, v27\n"
"v_dot2_f32_bf16 v28, v66, s66, v28\n"
"v_dot2_f32_bf16 v29, v66, s67, v29\n"
"v_dot2_f32_bf16 v30, v67, s66, v30\n"
"v_dot2_f32_bf16 v31, v67, s67, v31\n"
"s_waitcnt vmcnt(8)\n"
"v_dot2_f32_bf16 v24, v68, s66, v24\n"
"v_dot2_f32_bf16 v25, v68, s67, v25\n"
"v_dot2_f32_bf16 v26, v69, s66, v26\n"
"v_dot2_f32_bf16 v27, v69, s67, v27\n"
"v_dot2_f32_bf16 v28, v70, s66, v28\n"
"v_dot2_f32_bf16 v29, v70, s67, v29\n"
"v_dot2_f32_bf16 v30, v71, s66, v30\n"
"v_dot2_f32_bf16 v31, v71, s67, v31\n"
"s_waitcnt vmcnt(7)\n"
"v_dot2_f32_bf16 v24, v72, s66, v24\n"
"v_dot2_f32_bf16 v25, v72, s67, v25\n"
"v_dot2_f32_bf16 v26, v73, s66, v26\n"
"v_dot2_f32_bf16 v27, v73, s67, v27\n"
"v_dot2_f32_bf16 v28, v74, s66, v28\n"
"v_dot2_f32_bf16 v29, v74, s67, v29\n"
"v_dot2_f32_bf16 v30, v75, s66, v30\n"
"v_dot2_f32_bf16 v31, v75, s67, v31\n"
"s_waitcnt vmcnt(6)\n"
"v_dot2_f32_bf16 v24, v76, s66, v24\n"
"v_dot2_f32_bf16 v25, v76, s67, v25\n"
"v_dot2_f32_bf16 v26, v77, s66, v26\n"
"v_dot2_f32_bf16 v27, v77, s67, v27\n"
"v_dot2_f32_bf16 v28, v78, s66, v28\n"
"v_dot2_f32_bf16 v29, v78, s67, v29\n"
"v_dot2_f32_bf16 v30, v79, s66, v30\n"
"v_dot2_f32_bf16 v31, v79, s67, v31\n"
"s_waitcnt vmcnt(5)\n"
"v_dot2_f32_bf16 v24, v80, s66, v24\n"
"v_dot2_f32_bf16 v25, v80, s67, v25\n"
"v_dot2_f32_bf16 v26, v81, s66, v26\n"
"v_dot2_f32_bf16 v27, v81, s67, v27\n"
"v_dot2_f32_bf16 v28, v82, s66, v28\n"
"v_dot2_f32_bf16 v29, v82, s67, v29\n"
"v_dot2_f32_bf16 v30, v83, s66, v30\n"
"v_dot2_f32_bf16 v31, v83, s67, v31\n"
"s_waitcnt vmcnt(4)\n"
"v_dot2_f32_bf16 v24, v84, s66, v24\n"
"v_dot2_f32_bf16 v25, v84, s67, v25\n"
"v_dot2_f32_bf16 v26, v85, s66, v26\n"
"v_dot2_f32_bf16 v27, v85, s67, v27\n"
"v_dot2_f32_bf16 v28, v86, s66, v28\n"
"v_dot2_f32_bf16 v29, v86, s67, v29\n"
"v_dot2_f32_bf16 v30, v87, s66, v30\n"
"v_dot2_f32_bf16 v31, v87, s67, v31\n"
"s_waitcnt vmcnt(3)\n"
"v_dot2_f32_bf16 v24, v88, s66, v24\n"
"v_dot2_f32_bf16 v25, v88, s67, v25\n"
"v_dot2_f32_bf16 v26, v89, s66, v26\n"
"v_dot2_f32_bf16 v27, v89, s67, v27\n"
"v_dot2_f32_bf16 v28, v90, s66, v28\n"
"v_dot2_f32_bf16 v29, v90, s67, v29\n"
"v_dot2_f32_bf16 v30, v91, s66, v30\n"
"v_dot2_f32_bf16 v31, v91, s67, v31\n"
"s_waitcnt vmcnt(2)\n"
"v_dot2_f32_bf16 v24, v92, s66, v24\n"
"v_dot2_f32_bf16 v25, v92, s67, v25\n"
"v_dot2_f32_bf16 v26, v93, s66, v26\n"
"v_dot2_f32_bf16 v27, v93, s67, v27\n"
"v_dot2_f32_bf16 v28, v94, s66, v28\n"
"v_dot2_f32_bf16 v29, v94, s67, v29\n"
"v_dot2_f32_bf16 v30, v95, s66, v30\n"
"v_dot2_f32_bf16 v31, v95, s67, v31\n"
"s_waitcnt vmcnt(1)\n"
"v_dot2_f32_bf16 v24, v96, s66, v24\n"
"v_dot2_f32_bf16 v25, v96, s67, v25\n"
"v_dot2_f32_bf16 v26, v97, s66, v26\n"
"v_dot2_f32_bf16 v27, v97, s67, v27\n"
"v_dot2_f32_bf16 v28, v98, s66, v28\n"
"v_dot2_f32_bf16 v29, v98, s67, v29\n"
"v_dot2_f32_bf16 v30, v99, s66, v30\n"
"v_dot2_f32_bf16 v31, v99, s67, v31\n"
"s_waitcnt vmcnt(0)\n"
"v_dot2_f32_bf16 v24, v100, s66, v24\n"
"v_dot2_f32_bf16 v25, v100, s67, v25\n"
"v_dot2_f32_bf16 v26, v101, s66, v26\n"
"v_dot2_f32_bf16 v27, v101, s67, v27\n"
"v_dot2_f32_bf16 v28, v102, s66, v28\n"
"v_dot2_f32_bf16 v29, v102, s67, v29\n"
"v_dot2_f32_bf16 v30, v103, s66, v30\n"
"v_dot2_f32_bf16 v31, v103, s67, v31\n"
"v_permlane32_swap_b32 v24, v28\n"
"v_permlane32_swap_b32 v25, v29\n"
"v_permlane32_swap_b32 v26, v30\n"
"v_permlane32_swap_b32 v27, v31\n"
"v_add_f32 v24, v24, v28\n"
"v_add_f32 v25, v25, v29\n"
"v_add_f32 v26, v26, v30\n"
"v_add_f32 v27, v27, v31\n"
"s_nop 0\n"
"v_permlane16_swap_b32 v24, v26\n"
"v_permlane16_swap_b32 v25, v27\n"
"v_add_f32 v24, v24, v26\n"
"v_add_f32 v25, v25, v27\n"
"s_nop 0\n"
"v_add_f32_dpp v24, v24, v24 row_ror:8 row_mask:0xf bank_mask:0xf\n"
"v_add_f32_dpp v25, v25, v25 row_ror:8 row_mask:0xf bank_mask:0xf\n"
"buffer_store_dwordx2 v[24:25], v21, s[16:19], s68 offen sc1\n"
"s_add_u32 s34, s65, 0\n"
"buffer_load_dwordx4 v[104:107], v20, s[8:11], s34 offen\n"
"s_add_u32 s34, s65, 30720\n"
"buffer_load_dwordx4 v[108:111], v20, s[8:11], s34 offen\n"
"s_add_u32 s34, s65, 61440\n"
"buffer_load_dwordx4 v[112:115], v20, s[8:11], s34 offen\n"
"s_add_u32 s34, s65, 92160\n"
"buffer_load_dwordx4 v[116:119], v20, s[8:11], s34 offen\n"
"s_add_u32 s34, s65, 122880\n"
"buffer_load_dwordx4 v[64:67], v20, s[8:11], s34 offen\n"
"s_add_u32 s34, s65, 153600\n"
"buffer_load_dwordx4 v[68:71], v20, s[8:11], s34 offen\n"
"s_add_u32 s34, s65, 184320\n"
"buffer_load_dwordx4 v[72:75], v20, s[8:11], s34 offen\n"
"s_add_u32 s34, s65, 215040\n"
"buffer_load_dwordx4 v[76:79], v20, s[8:11], s34 offen\n"
"s_add_u32 s34, s65, 245760\n"
"buffer_load_dwordx4 v[80:83], v20, s[8:11], s34 offen\n"
"s_add_u32 s34, s65, 276480\n"
"buffer_load_dwordx4 v[84:87], v20, s[8:11], s34 offen\n"
"s_add_u32 s34, s65, 307200\n"
"buffer_load_dwordx4 v[88:91], v20, s[8:11], s34 offen\n"
"s_add_u32 s34, s65, 337920\n"
"buffer_load_dwordx4 v[92:95], v20, s[8:11], s34 offen\n"
"s_add_u32 s34, s65, 368640\n"
"buffer_load_dwordx4 v[96:99], v20, s[8:11], s34 offen\n"
"s_add_u32 s34, s65, 399360\n"
"buffer_load_dwordx4 v[100:103], v20, s[8:11], s34 offen\n"
"s_waitcnt vmcnt(14)\n"
"buffer_store_dword v19, v3, s[16:19], s70 offen sc1\n"
"s_waitcnt vmcnt(14)\n"
"v_dot2_f32_bf16 v32, v104, s66, 0\n"
"v_dot2_f32_bf16 v33, v104, s67, 0\n"
"v_dot2_f32_bf16 v34, v105, s66, 0\n"
"v_dot2_f32_bf16 v35, v105, s67, 0\n"
"v_dot2_f32_bf16 v36, v106, s66, 0\n"
"v_dot2_f32_bf16 v37, v106, s67, 0\n"
"v_dot2_f32_bf16 v38, v107, s66, 0\n"
"v_dot2_f32_bf16 v39, v107, s67, 0\n"
"s_add_u32 s34, s65, 430080\n"
"buffer_load_dwordx4 v[104:107], v20, s[8:11], s34 offen\n"
"s_waitcnt vmcnt(14)\n"
"v_dot2_f32_bf16 v32, v108, s66, v32\n"
"v_dot2_f32_bf16 v33, v108, s67, v33\n"
"v_dot2_f32_bf16 v34, v109, s66, v34\n"
"v_dot2_f32_bf16 v35, v109, s67, v35\n"
"v_dot2_f32_bf16 v36, v110, s66, v36\n"
"v_dot2_f32_bf16 v37, v110, s67, v37\n"
"v_dot2_f32_bf16 v38, v111, s66, v38\n"
"v_dot2_f32_bf16 v39, v111, s67, v39\n"
"s_add_u32 s34, s65, 460800\n"
"buffer_load_dwordx4 v[108:111], v20, s[8:11], s34 offen\n"
"s_waitcnt vmcnt(14)\n"
"v_dot2_f32_bf16 v32, v112, s66, v32\n"
"v_dot2_f32_bf16 v33, v112, s67, v33\n"
"v_dot2_f32_bf16 v34, v113, s66, v34\n"
"v_dot2_f32_bf16 v35, v113, s67, v35\n"
"v_dot2_f32_bf16 v36, v114, s66, v36\n"
"v_dot2_f32_bf16 v37, v114, s67, v37\n"
"v_dot2_f32_bf16 v38, v115, s66, v38\n"
"v_dot2_f32_bf16 v39, v115, s67, v39\n"
"s_add_u32 s34, s65, 491520\n"
"buffer_load_dwordx4 v[112:115], v20, s[8:11], s34 offen\n"
"s_waitcnt vmcnt(14)\n"
"v_dot2_f32_bf16 v32, v116, s66, v32\n"
"v_dot2_f32_bf16 v33, v116, s67, v33\n"
"v_dot2_f32_bf16 v34, v117, s66, v34\n"
"v_dot2_f32_bf16 v35, v117, s67, v35\n"
"v_dot2_f32_bf16 v36, v118, s66, v36\n"
"v_dot2_f32_bf16 v37, v118, s67, v37\n"
"v_dot2_f32_bf16 v38, v119, s66, v38\n"
"v_dot2_f32_bf16 v39, v119, s67, v39\n"
"s_add_u32 s34, s65, 522240\n"
"buffer_load_dwordx4 v[116:119], v20, s[8:11], s34 offen\n"
"s_waitcnt vmcnt(14)\n"
"v_dot2_f32_bf16 v32, v64, s66, v32\n"
"v_dot2_f32_bf16 v33, v64, s67, v33\n"
"v_dot2_f32_bf16 v34, v65, s66, v34\n"
"v_dot2_f32_bf16 v35, v65, s67, v35\n"
"v_dot2_f32_bf16 v36, v66, s66, v36\n"
"v_dot2_f32_bf16 v37, v66, s67, v37\n"
"v_dot2_f32_bf16 v38, v67, s66, v38\n"
"v_dot2_f32_bf16 v39, v67, s67, v39\n"
"s_add_u32 s34, s65, 552960\n"
"buffer_load_dwordx4 v[64:67], v20, s[8:11], s34 offen\n"
"s_waitcnt vmcnt(14)\n"
"v_dot2_f32_bf16 v32, v68, s66, v32\n"
"v_dot2_f32_bf16 v33, v68, s67, v33\n"
"v_dot2_f32_bf16 v34, v69, s66, v34\n"
"v_dot2_f32_bf16 v35, v69, s67, v35\n"
"v_dot2_f32_bf16 v36, v70, s66, v36\n"
"v_dot2_f32_bf16 v37, v70, s67, v37\n"
"v_dot2_f32_bf16 v38, v71, s66, v38\n"
"v_dot2_f32_bf16 v39, v71, s67, v39\n"
"s_add_u32 s34, s65, 583680\n"
"buffer_load_dwordx4 v[68:71], v20, s[8:11], s34 offen\n"
"s_waitcnt vmcnt(14)\n"
"v_dot2_f32_bf16 v32, v72, s66, v32\n"
"v_dot2_f32_bf16 v33, v72, s67, v33\n"
"v_dot2_f32_bf16 v34, v73, s66, v34\n"
"v_dot2_f32_bf16 v35, v73, s67, v35\n"
"v_dot2_f32_bf16 v36, v74, s66, v36\n"
"v_dot2_f32_bf16 v37, v74, s67, v37\n"
"v_dot2_f32_bf16 v38, v75, s66, v38\n"
"v_dot2_f32_bf16 v39, v75, s67, v39\n"
"s_add_u32 s34, s65, 614400\n"
"buffer_load_dwordx4 v[72:75], v20, s[8:11], s34 offen\n"
"s_waitcnt vmcnt(14)\n"
"v_dot2_f32_bf16 v32, v76, s66, v32\n"
"v_dot2_f32_bf16 v33, v76, s67, v33\n"
"v_dot2_f32_bf16 v34, v77, s66, v34\n"
"v_dot2_f32_bf16 v35, v77, s67, v35\n"
"v_dot2_f32_bf16 v36, v78, s66, v36\n"
"v_dot2_f32_bf16 v37, v78, s67, v37\n"
"v_dot2_f32_bf16 v38, v79, s66, v38\n"
"v_dot2_f32_bf16 v39, v79, s67, v39\n"
"s_add_u32 s34, s65, 645120\n"
"buffer_load_dwordx4 v[76:79], v20, s[8:11], s34 offen\n"
"s_waitcnt vmcnt(14)\n"
"v_dot2_f32_bf16 v32, v80, s66, v32\n"
"v_dot2_f32_bf16 v33, v80, s67, v33\n"
"v_dot2_f32_bf16 v34, v81, s66, v34\n"
"v_dot2_f32_bf16 v35, v81, s67, v35\n"
"v_dot2_f32_bf16 v36, v82, s66, v36\n"
"v_dot2_f32_bf16 v37, v82, s67, v37\n"
"v_dot2_f32_bf16 v38, v83, s66, v38\n"
"v_dot2_f32_bf16 v39, v83, s67, v39\n"
"s_add_u32 s34, s65, 675840\n"
"buffer_load_dwordx4 v[80:83], v20, s[8:11], s34 offen\n"
"s_waitcnt vmcnt(14)\n"
"v_dot2_f32_bf16 v32, v84, s66, v32\n"
"v_dot2_f32_bf16 v33, v84, s67, v33\n"
"v_dot2_f32_bf16 v34, v85, s66, v34\n"
"v_dot2_f32_bf16 v35, v85, s67, v35\n"
"v_dot2_f32_bf16 v36, v86, s66, v36\n"
"v_dot2_f32_bf16 v37, v86, s67, v37\n"
"v_dot2_f32_bf16 v38, v87, s66, v38\n"
"v_dot2_f32_bf16 v39, v87, s67, v39\n"
"s_add_u32 s34, s65, 706560\n"
"buffer_load_dwordx4 v[84:87], v20, s[8:11], s34 offen\n"
"s_waitcnt vmcnt(14)\n"
"v_dot2_f32_bf16 v32, v88, s66, v32\n"
"v_dot2_f32_bf16 v33, v88, s67, v33\n"
"v_dot2_f32_bf16 v34, v89, s66, v34\n"
"v_dot2_f32_bf16 v35, v89, s67, v35\n"
"v_dot2_f32_bf16 v36, v90, s66, v36\n"
"v_dot2_f32_bf16 v37, v90, s67, v37\n"
"v_dot2_f32_bf16 v38, v91, s66, v38\n"
"v_dot2_f32_bf16 v39, v91, s67, v39\n"
"s_add_u32 s34, s65, 737280\n"
"buffer_load_dwordx4 v[88:91], v20, s[8:11], s34 offen\n"
"s_waitcnt vmcnt(14)\n"
"v_dot2_f32_bf16 v32, v92, s66, v32\n"
"v_dot2_f32_bf16 v33, v92, s67, v33\n"
"v_dot2_f32_bf16 v34, v93, s66, v34\n"
"v_dot2_f32_bf16 v35, v93, s67, v35\n"
"v_dot2_f32_bf16 v36, v94, s66, v36\n"
"v_dot2_f32_bf16 v37, v94, s67, v37\n"
"v_dot2_f32_bf16 v38, v95, s66, v38\n"
"v_dot2_f32_bf16 v39, v95, s67, v39\n"
"s_add_u32 s34, s65, 768000\n"
"buffer_load_dwordx4 v[92:95], v20, s[8:11], s34 offen\n"
"s_waitcnt vmcnt(14)\n"
"v_dot2_f32_bf16 v32, v96, s66, v32\n"
"v_dot2_f32_bf16 v33, v96, s67, v33\n"
"v_dot2_f32_bf16 v34, v97, s66, v34\n"
"v_dot2_f32_bf16 v35, v97, s67, v35\n"
"v_dot2_f32_bf16 v36, v98, s66, v36\n"
"v_dot2_f32_bf16 v37, v98, s67, v37\n"
"v_dot2_f32_bf16 v38, v99, s66, v38\n"
"v_dot2_f32_bf16 v39, v99, s67, v39\n"
"s_add_u32 s34, s65, 798720\n"
"buffer_load_dwordx4 v[96:99], v20, s[8:11], s34 offen\n"
"s_waitcnt vmcnt(14)\n"
"v_dot2_f32_bf16 v32, v100, s66, v32\n"
"v_dot2_f32_bf16 v33, v100, s67, v33\n"
"v_dot2_f32_bf16 v34, v101, s66, v34\n"
"v_dot2_f32_bf16 v35, v101, s67, v35\n"
"v_dot2_f32_bf16 v36, v102, s66, v36\n"
"v_dot2_f32_bf16 v37, v102, s67, v37\n"
"v_dot2_f32_bf16 v38, v103, s66, v38\n"
"v_dot2_f32_bf16 v39, v103, s67, v39\n"
"s_add_u32 s34, s65, 829440\n"
"buffer_load_dwordx4 v[100:103], v20, s[8:11], s34 offen\n"
"s_waitcnt vmcnt(13)\n"
"v_dot2_f32_bf16 v32, v104, s66, v32\n"
"v_dot2_f32_bf16 v33, v104, s67, v33\n"
"v_dot2_f32_bf16 v34, v105, s66, v34\n"
"v_dot2_f32_bf16 v35, v105, s67, v35\n"
"v_dot2_f32_bf16 v36, v106, s66, v36\n"
"v_dot2_f32_bf16 v37, v106, s67, v37\n"
"v_dot2_f32_bf16 v38, v107, s66, v38\n"
"v_dot2_f32_bf16 v39, v107, s67, v39\n"
"s_add_u32 s34, s65, 860160\n"
"buffer_load_dwordx4 v[104:107], v20, s[8:11], s34 offen\n"
"s_waitcnt vmcnt(13)\n"
"v_dot2_f32_bf16 v32, v108, s66, v32\n"
"v_dot2_f32_bf16 v33, v108, s67, v33\n"
"v_dot2_f32_bf16 v34, v109, s66, v34\n"
"v_dot2_f32_bf16 v35, v109, s67, v35\n"
"v_dot2_f32_bf16 v36, v110, s66, v36\n"
"v_dot2_f32_bf16 v37, v110, s67, v37\n"
"v_dot2_f32_bf16 v38, v111, s66, v38\n"
"v_dot2_f32_bf16 v39, v111, s67, v39\n"
"s_add_u32 s34, s65, 890880\n"
"buffer_load_dwordx4 v[108:111], v20, s[8:11], s34 offen\n"
"s_waitcnt vmcnt(13)\n"
"v_dot2_f32_bf16 v32, v112, s66, v32\n"
"v_dot2_f32_bf16 v33, v112, s67, v33\n"
"v_dot2_f32_bf16 v34, v113, s66, v34\n"
"v_dot2_f32_bf16 v35, v113, s67, v35\n"
"v_dot2_f32_bf16 v36, v114, s66, v36\n"
"v_dot2_f32_bf16 v37, v114, s67, v37\n"
"v_dot2_f32_bf16 v38, v115, s66, v38\n"
"v_dot2_f32_bf16 v39, v115, s67, v39\n"
"s_add_u32 s34, s65, 921600\n"
"buffer_load_dwordx4 v[112:115], v20, s[8:11], s34 offen\n"
"s_waitcnt vmcnt(13)\n"
"v_dot2_f32_bf16 v32, v116, s66, v32\n"
"v_dot2_f32_bf16 v33, v116, s67, v33\n"
"v_dot2_f32_bf16 v34, v117, s66, v34\n"
"v_dot2_f32_bf16 v35, v117, s67, v35\n"
"v_dot2_f32_bf16 v36, v118, s66, v36\n"
"v_dot2_f32_bf16 v37, v118, s67, v37\n"
"v_dot2_f32_bf16 v38, v119, s66, v38\n"
"v_dot2_f32_bf16 v39, v119, s67, v39\n"
"s_add_u32 s34, s65, 952320\n"
"buffer_load_dwordx4 v[116:119], v20, s[8:11], s34 offen\n"
"s_waitcnt vmcnt(13)\n"
"v_dot2_f32_bf16 v32, v64, s66, v32\n"
"v_dot2_f32_bf16 v33, v64, s67, v33\n"
"v_dot2_f32_bf16 v34, v65, s66, v34\n"
"v_dot2_f32_bf16 v35, v65, s67, v35\n"
"v_dot2_f32_bf16 v36, v66, s66, v36\n"
"v_dot2_f32_bf16 v37, v66, s67, v37\n"
"v_dot2_f32_bf16 v38, v67, s66, v38\n"
"v_dot2_f32_bf16 v39, v67, s67, v39\n"
"s_add_u32 s34, s65, 983040\n"
"buffer_load_dwordx4 v[64:67], v20, s[8:11], s34 offen\n"
"s_waitcnt vmcnt(13)\n"
"v_dot2_f32_bf16 v32, v68, s66, v32\n"
"v_dot2_f32_bf16 v33, v68, s67, v33\n"
"v_dot2_f32_bf16 v34, v69, s66, v34\n"
"v_dot2_f32_bf16 v35, v69, s67, v35\n"
"v_dot2_f32_bf16 v36, v70, s66, v36\n"
"v_dot2_f32_bf16 v37, v70, s67, v37\n"
"v_dot2_f32_bf16 v38, v71, s66, v38\n"
"v_dot2_f32_bf16 v39, v71, s67, v39\n"
"s_add_u32 s34, s65, 1013760\n"
"buffer_load_dwordx4 v[68:71], v20, s[8:11], s34 offen\n"
"s_waitcnt vmcnt(13)\n"
"v_dot2_f32_bf16 v32, v72, s66, v32\n"
"v_dot2_f32_bf16 v33, v72, s67, v33\n"
"v_dot2_f32_bf16 v34, v73, s66, v34\n"
"v_dot2_f32_bf16 v35, v73, s67, v35\n"
"v_dot2_f32_bf16 v36, v74, s66, v36\n"
"v_dot2_f32_bf16 v37, v74, s67, v37\n"
"v_dot2_f32_bf16 v38, v75, s66, v38\n"
"v_dot2_f32_bf16 v39, v75, s67, v39\n"
"s_add_u32 s34, s65, 1044480\n"
"buffer_load_dwordx4 v[72:75], v20, s[8:11], s34 offen\n"
"s_waitcnt vmcnt(13)\n"
"v_dot2_f32_bf16 v32, v76, s66, v32\n"
"v_dot2_f32_bf16 v33, v76, s67, v33\n"
"v_dot2_f32_bf16 v34, v77, s66, v34\n"
"v_dot2_f32_bf16 v35, v77, s67, v35\n"
"v_dot2_f32_bf16 v36, v78, s66, v36\n"
"v_dot2_f32_bf16 v37, v78, s67, v37\n"
"v_dot2_f32_bf16 v38, v79, s66, v38\n"
"v_dot2_f32_bf16 v39, v79, s67, v39\n"
"s_add_u32 s34, s65, 1075200\n"
"buffer_load_dwordx4 v[76:79], v20, s[8:11], s34 offen\n"
"s_waitcnt vmcnt(13)\n"
"v_dot2_f32_bf16 v32, v80, s66, v32\n"
"v_dot2_f32_bf16 v33, v80, s67, v33\n"
"v_dot2_f32_bf16 v34, v81, s66, v34\n"
"v_dot2_f32_bf16 v35, v81, s67, v35\n"
"v_dot2_f32_bf16 v36, v82, s66, v36\n"
"v_dot2_f32_bf16 v37, v82, s67, v37\n"
"v_dot2_f32_bf16 v38, v83, s66, v38\n"
"v_dot2_f32_bf16 v39, v83, s67, v39\n"
"s_add_u32 s34, s65, 1105920\n"
"buffer_load_dwordx4 v[80:83], v20, s[8:11], s34 offen\n"
"s_waitcnt vmcnt(13)\n"
"v_dot2_f32_bf16 v32, v84, s66, v32\n"
"v_dot2_f32_bf16 v33, v84, s67, v33\n"
"v_dot2_f32_bf16 v34, v85, s66, v34\n"
"v_dot2_f32_bf16 v35, v85, s67, v35\n"
"v_dot2_f32_bf16 v36, v86, s66, v36\n"
"v_dot2_f32_bf16 v37, v86, s67, v37\n"
"v_dot2_f32_bf16 v38, v87, s66, v38\n"
"v_dot2_f32_bf16 v39, v87, s67, v39\n"
"s_add_u32 s34, s65, 1136640\n"
"buffer_load_dwordx4 v[84:87], v20, s[8:11], s34 offen\n"
"s_waitcnt vmcnt(13)\n"
"v_dot2_f32_bf16 v32, v88, s66, v32\n"
"v_dot2_f32_bf16 v33, v88, s67, v33\n"
"v_dot2_f32_bf16 v34, v89, s66, v34\n"
"v_dot2_f32_bf16 v35, v89, s67, v35\n"
"v_dot2_f32_bf16 v36, v90, s66, v36\n"
"v_dot2_f32_bf16 v37, v90, s67, v37\n"
"v_dot2_f32_bf16 v38, v91, s66, v38\n"
"v_dot2_f32_bf16 v39, v91, s67, v39\n"
"s_add_u32 s34, s65, 1167360\n"
"buffer_load_dwordx4 v[88:91], v20, s[8:11], s34 offen\n"
"s_waitcnt vmcnt(13)\n"
"v_dot2_f32_bf16 v32, v92, s66, v32\n"
"v_dot2_f32_bf16 v33, v92, s67, v33\n"
"v_dot2_f32_bf16 v34, v93, s66, v34\n"
"v_dot2_f32_bf16 v35, v93, s67, v35\n"
"v_dot2_f32_bf16 v36, v94, s66, v36\n"
"v_dot2_f32_bf16 v37, v94, s67, v37\n"
"v_dot2_f32_bf16 v38, v95, s66, v38\n"
"v_dot2_f32_bf16 v39, v95, s67, v39\n"
"s_add_u32 s34, s65, 1198080\n"
"buffer_load_dwordx4 v[92:95], v20, s[8:11], s34 offen\n"
"s_waitcnt vmcnt(13)\n"
"v_dot2_f32_bf16 v32, v96, s66, v32\n"
"v_dot2_f32_bf16 v33, v96, s67, v33\n"
"v_dot2_f32_bf16 v34, v97, s66, v34\n"
"v_dot2_f32_bf16 v35, v97, s67, v35\n"
"v_dot2_f32_bf16 v36, v98, s66, v36\n"
"v_dot2_f32_bf16 v37, v98, s67, v37\n"
"v_dot2_f32_bf16 v38, v99, s66, v38\n"
"v_dot2_f32_bf16 v39, v99, s67, v39\n"
"s_add_u32 s34, s65, 1228800\n"
"buffer_load_dwordx4 v[96:99], v20, s[8:11], s34 offen\n"
"s_waitcnt vmcnt(13)\n"
"v_dot2_f32_bf16 v32, v100, s66, v32\n"
"v_dot2_f32_bf16 v33, v100, s67, v33\n"
"v_dot2_f32_bf16 v34, v101, s66, v34\n"
"v_dot2_f32_bf16 v35, v101, s67, v35\n"
"v_dot2_f32_bf16 v36, v102, s66, v36\n"
"v_dot2_f32_bf16 v37, v102, s67, v37\n"
"v_dot2_f32_bf16 v38, v103, s66, v38\n"
"v_dot2_f32_bf16 v39, v103, s67, v39\n"
"s_add_u32 s34, s65, 1259520\n"
"buffer_load_dwordx4 v[100:103], v20, s[8:11], s34 offen\n"
"s_waitcnt vmcnt(13)\n"
"v_dot2_f32_bf16 v32, v104, s66, v32\n"
"v_dot2_f32_bf16 v33, v104, s67, v33\n"
"v_dot2_f32_bf16 v34, v105, s66, v34\n"
"v_dot2_f32_bf16 v35, v105, s67, v35\n"
"v_dot2_f32_bf16 v36, v106, s66, v36\n"
"v_dot2_f32_bf16 v37, v106, s67, v37\n"
"v_dot2_f32_bf16 v38, v107, s66, v38\n"
"v_dot2_f32_bf16 v39, v107, s67, v39\n"
"s_add_u32 s34, s65, 1290240\n"
"buffer_load_dwordx4 v[104:107], v20, s[8:11], s34 offen\n"
"s_waitcnt vmcnt(13)\n"
"v_dot2_f32_bf16 v32, v108, s66, v32\n"
"v_dot2_f32_bf16 v33, v108, s67, v33\n"
"v_dot2_f32_bf16 v34, v109, s66, v34\n"
"v_dot2_f32_bf16 v35, v109, s67, v35\n"
"v_dot2_f32_bf16 v36, v110, s66, v36\n"
"v_dot2_f32_bf16 v37, v110, s67, v37\n"
"v_dot2_f32_bf16 v38, v111, s66, v38\n"
"v_dot2_f32_bf16 v39, v111, s67, v39\n"
"s_add_u32 s34, s65, 1320960\n"
"buffer_load_dwordx4 v[108:111], v20, s[8:11], s34 offen\n"
"s_waitcnt vmcnt(13)\n"
"v_dot2_f32_bf16 v32, v112, s66, v32\n"
"v_dot2_f32_bf16 v33, v112, s67, v33\n"
"v_dot2_f32_bf16 v34, v113, s66, v34\n"
"v_dot2_f32_bf16 v35, v113, s67, v35\n"
"v_dot2_f32_bf16 v36, v114, s66, v36\n"
"v_dot2_f32_bf16 v37, v114, s67, v37\n"
"v_dot2_f32_bf16 v38, v115, s66, v38\n"
"v_dot2_f32_bf16 v39, v115, s67, v39\n"
"s_add_u32 s34, s65, 1351680\n"
"buffer_load_dwordx4 v[112:115], v20, s[8:11], s34 offen\n"
"s_waitcnt vmcnt(13)\n"
"v_dot2_f32_bf16 v32, v116, s66, v32\n"
"v_dot2_f32_bf16 v33, v116, s67, v33\n"
"v_dot2_f32_bf16 v34, v117, s66, v34\n"
"v_dot2_f32_bf16 v35, v117, s67, v35\n"
"v_dot2_f32_bf16 v36, v118, s66, v36\n"
"v_dot2_f32_bf16 v37, v118, s67, v37\n"
"v_dot2_f32_bf16 v38, v119, s66, v38\n"
"v_dot2_f32_bf16 v39, v119, s67, v39\n"
"s_add_u32 s34, s65, 1382400\n"
"buffer_load_dwordx4 v[116:119], v20, s[8:11], s34 offen\n"
"s_waitcnt vmcnt(13)\n"
"v_dot2_f32_bf16 v32, v64, s66, v32\n"
"v_dot2_f32_bf16 v33, v64, s67, v33\n"
"v_dot2_f32_bf16 v34, v65, s66, v34\n"
"v_dot2_f32_bf16 v35, v65, s67, v35\n"
"v_dot2_f32_bf16 v36, v66, s66, v36\n"
"v_dot2_f32_bf16 v37, v66, s67, v37\n"
"v_dot2_f32_bf16 v38, v67, s66, v38\n"
"v_dot2_f32_bf16 v39, v67, s67, v39\n"
"s_add_u32 s34, s65, 1413120\n"
"buffer_load_dwordx4 v[64:67], v20, s[8:11], s34 offen\n"
"s_waitcnt vmcnt(13)\n"
"v_dot2_f32_bf16 v32, v68, s66, v32\n"
"v_dot2_f32_bf16 v33, v68, s67, v33\n"
"v_dot2_f32_bf16 v34, v69, s66, v34\n"
"v_dot2_f32_bf16 v35, v69, s67, v35\n"
"v_dot2_f32_bf16 v36, v70, s66, v36\n"
"v_dot2_f32_bf16 v37, v70, s67, v37\n"
"v_dot2_f32_bf16 v38, v71, s66, v38\n"
"v_dot2_f32_bf16 v39, v71, s67, v39\n"
"s_add_u32 s34, s65, 1443840\n"
"buffer_load_dwordx4 v[68:71], v20, s[8:11], s34 offen\n"
"s_waitcnt vmcnt(13)\n"
"v_dot2_f32_bf16 v32, v72, s66, v32\n"
"v_dot2_f32_bf16 v33, v72, s67, v33\n"
"v_dot2_f32_bf16 v34, v73, s66, v34\n"
"v_dot2_f32_bf16 v35, v73, s67, v35\n"
"v_dot2_f32_bf16 v36, v74, s66, v36\n"
"v_dot2_f32_bf16 v37, v74, s67, v37\n"
"v_dot2_f32_bf16 v38, v75, s66, v38\n"
"v_dot2_f32_bf16 v39, v75, s67, v39\n"
"s_add_u32 s34, s65, 1474560\n"
"buffer_load_dwordx4 v[72:75], v20, s[8:11], s34 offen\n"
"s_waitcnt vmcnt(13)\n"
"v_dot2_f32_bf16 v32, v76, s66, v32\n"
"v_dot2_f32_bf16 v33, v76, s67, v33\n"
"v_dot2_f32_bf16 v34, v77, s66, v34\n"
"v_dot2_f32_bf16 v35, v77, s67, v35\n"
"v_dot2_f32_bf16 v36, v78, s66, v36\n"
"v_dot2_f32_bf16 v37, v78, s67, v37\n"
"v_dot2_f32_bf16 v38, v79, s66, v38\n"
"v_dot2_f32_bf16 v39, v79, s67, v39\n"
"s_add_u32 s34, s65, 1505280\n"
"buffer_load_dwordx4 v[76:79], v20, s[8:11], s34 offen\n"
"s_waitcnt vmcnt(13)\n"
"v_dot2_f32_bf16 v32, v80, s66, v32\n"
"v_dot2_f32_bf16 v33, v80, s67, v33\n"
"v_dot2_f32_bf16 v34, v81, s66, v34\n"
"v_dot2_f32_bf16 v35, v81, s67, v35\n"
"v_dot2_f32_bf16 v36, v82, s66, v36\n"
"v_dot2_f32_bf16 v37, v82, s67, v37\n"
"v_dot2_f32_bf16 v38, v83, s66, v38\n"
"v_dot2_f32_bf16 v39, v83, s67, v39\n"
"s_add_u32 s34, s65, 1536000\n"
"buffer_load_dwordx4 v[80:83], v20, s[8:11], s34 offen\n"
"s_waitcnt vmcnt(13)\n"
"v_dot2_f32_bf16 v32, v84, s66, v32\n"
"v_dot2_f32_bf16 v33, v84, s67, v33\n"
"v_dot2_f32_bf16 v34, v85, s66, v34\n"
"v_dot2_f32_bf16 v35, v85, s67, v35\n"
"v_dot2_f32_bf16 v36, v86, s66, v36\n"
"v_dot2_f32_bf16 v37, v86, s67, v37\n"
"v_dot2_f32_bf16 v38, v87, s66, v38\n"
"v_dot2_f32_bf16 v39, v87, s67, v39\n"
"s_add_u32 s34, s65, 1566720\n"
"buffer_load_dwordx4 v[84:87], v20, s[8:11], s34 offen\n"
"s_waitcnt vmcnt(13)\n"
"v_dot2_f32_bf16 v32, v88, s66, v32\n"
"v_dot2_f32_bf16 v33, v88, s67, v33\n"
"v_dot2_f32_bf16 v34, v89, s66, v34\n"
"v_dot2_f32_bf16 v35, v89, s67, v35\n"
"v_dot2_f32_bf16 v36, v90, s66, v36\n"
"v_dot2_f32_bf16 v37, v90, s67, v37\n"
"v_dot2_f32_bf16 v38, v91, s66, v38\n"
"v_dot2_f32_bf16 v39, v91, s67, v39\n"
"s_add_u32 s34, s65, 1597440\n"
"buffer_load_dwordx4 v[88:91], v20, s[8:11], s34 offen\n"
"s_waitcnt vmcnt(13)\n"
"v_dot2_f32_bf16 v32, v92, s66, v32\n"
"v_dot2_f32_bf16 v33, v92, s67, v33\n"
"v_dot2_f32_bf16 v34, v93, s66, v34\n"
"v_dot2_f32_bf16 v35, v93, s67, v35\n"
"v_dot2_f32_bf16 v36, v94, s66, v36\n"
"v_dot2_f32_bf16 v37, v94, s67, v37\n"
"v_dot2_f32_bf16 v38, v95, s66, v38\n"
"v_dot2_f32_bf16 v39, v95, s67, v39\n"
"s_add_u32 s34, s65, 1628160\n"
"buffer_load_dwordx4 v[92:95], v20, s[8:11], s34 offen\n"
"s_waitcnt vmcnt(13)\n"
"v_dot2_f32_bf16 v32, v96, s66, v32\n"
"v_dot2_f32_bf16 v33, v96, s67, v33\n"
"v_dot2_f32_bf16 v34, v97, s66, v34\n"
"v_dot2_f32_bf16 v35, v97, s67, v35\n"
"v_dot2_f32_bf16 v36, v98, s66, v36\n"
"v_dot2_f32_bf16 v37, v98, s67, v37\n"
"v_dot2_f32_bf16 v38, v99, s66, v38\n"
"v_dot2_f32_bf16 v39, v99, s67, v39\n"
"s_add_u32 s34, s65, 1658880\n"
"buffer_load_dwordx4 v[96:99], v20, s[8:11], s34 offen\n"
"s_waitcnt vmcnt(13)\n"
"v_dot2_f32_bf16 v32, v100, s66, v32\n"
"v_dot2_f32_bf16 v33, v100, s67, v33\n"
"v_dot2_f32_bf16 v34, v101, s66, v34\n"
"v_dot2_f32_bf16 v35, v101, s67, v35\n"
"v_dot2_f32_bf16 v36, v102, s66, v36\n"
"v_dot2_f32_bf16 v37, v102, s67, v37\n"
"v_dot2_f32_bf16 v38, v103, s66, v38\n"
"v_dot2_f32_bf16 v39, v103, s67, v39\n"
"s_add_u32 s34, s65, 1689600\n"
"buffer_load_dwordx4 v[100:103], v20, s[8:11], s34 offen\n"
"s_waitcnt vmcnt(13)\n"
"v_dot2_f32_bf16 v32, v104, s66, v32\n"
"v_dot2_f32_bf16 v33, v104, s67, v33\n"
"v_dot2_f32_bf16 v34, v105, s66, v34\n"
"v_dot2_f32_bf16 v35, v105, s67, v35\n"
"v_dot2_f32_bf16 v36, v106, s66, v36\n"
"v_dot2_f32_bf16 v37, v106, s67, v37\n"
"v_dot2_f32_bf16 v38, v107, s66, v38\n"
"v_dot2_f32_bf16 v39, v107, s67, v39\n"
"s_add_u32 s34, s65, 1720320\n"
"buffer_load_dwordx4 v[104:107], v20, s[8:11], s34 offen\n"
"s_waitcnt vmcnt(13)\n"
"v_dot2_f32_bf16 v32, v108, s66, v32\n"
"v_dot2_f32_bf16 v33, v108, s67, v33\n"
"v_dot2_f32_bf16 v34, v109, s66, v34\n"
"v_dot2_f32_bf16 v35, v109, s67, v35\n"
"v_dot2_f32_bf16 v36, v110, s66, v36\n"
"v_dot2_f32_bf16 v37, v110, s67, v37\n"
"v_dot2_f32_bf16 v38, v111, s66, v38\n"
"v_dot2_f32_bf16 v39, v111, s67, v39\n"
"s_add_u32 s34, s65, 1751040\n"
"buffer_load_dwordx4 v[108:111], v20, s[8:11], s34 offen\n"
"s_waitcnt vmcnt(13)\n"
"v_dot2_f32_bf16 v32, v112, s66, v32\n"
"v_dot2_f32_bf16 v33, v112, s67, v33\n"
"v_dot2_f32_bf16 v34, v113, s66, v34\n"
"v_dot2_f32_bf16 v35, v113, s67, v35\n"
"v_dot2_f32_bf16 v36, v114, s66, v36\n"
"v_dot2_f32_bf16 v37, v114, s67, v37\n"
"v_dot2_f32_bf16 v38, v115, s66, v38\n"
"v_dot2_f32_bf16 v39, v115, s67, v39\n"
"s_add_u32 s34, s65, 1781760\n"
"buffer_load_dwordx4 v[112:115], v20, s[8:11], s34 offen\n"
"s_waitcnt vmcnt(13)\n"
"v_dot2_f32_bf16 v32, v116, s66, v32\n"
"v_dot2_f32_bf16 v33, v116, s67, v33\n"
"v_dot2_f32_bf16 v34, v117, s66, v34\n"
"v_dot2_f32_bf16 v35, v117, s67, v35\n"
"v_dot2_f32_bf16 v36, v118, s66, v36\n"
"v_dot2_f32_bf16 v37, v118, s67, v37\n"
"v_dot2_f32_bf16 v38, v119, s66, v38\n"
"v_dot2_f32_bf16 v39, v119, s67, v39\n"
"s_add_u32 s34, s65, 1812480\n"
"buffer_load_dwordx4 v[116:119], v20, s[8:11], s34 offen\n"
"s_waitcnt vmcnt(13)\n"
"v_dot2_f32_bf16 v32, v64, s66, v32\n"
"v_dot2_f32_bf16 v33, v64, s67, v33\n"
"v_dot2_f32_bf16 v34, v65, s66, v34\n"
"v_dot2_f32_bf16 v35, v65, s67, v35\n"
"v_dot2_f32_bf16 v36, v66, s66, v36\n"
"v_dot2_f32_bf16 v37, v66, s67, v37\n"
"v_dot2_f32_bf16 v38, v67, s66, v38\n"
"v_dot2_f32_bf16 v39, v67, s67, v39\n"
"s_add_u32 s34, s65, 1843200\n"
"buffer_load_dwordx4 v[64:67], v20, s[8:11], s34 offen\n"
"s_waitcnt vmcnt(13)\n"
"v_dot2_f32_bf16 v32, v68, s66, v32\n"
"v_dot2_f32_bf16 v33, v68, s67, v33\n"
"v_dot2_f32_bf16 v34, v69, s66, v34\n"
"v_dot2_f32_bf16 v35, v69, s67, v35\n"
"v_dot2_f32_bf16 v36, v70, s66, v36\n"
"v_dot2_f32_bf16 v37, v70, s67, v37\n"
"v_dot2_f32_bf16 v38, v71, s66, v38\n"
"v_dot2_f32_bf16 v39, v71, s67, v39\n"
"s_add_u32 s34, s65, 1873920\n"
"buffer_load_dwordx4 v[68:71], v20, s[8:11], s34 offen\n"
"s_waitcnt vmcnt(13)\n"
"v_dot2_f32_bf16 v32, v72, s66, v32\n"
"v_dot2_f32_bf16 v33, v72, s67, v33\n"
"v_dot2_f32_bf16 v34, v73, s66, v34\n"
"v_dot2_f32_bf16 v35, v73, s67, v35\n"
"v_dot2_f32_bf16 v36, v74, s66, v36\n"
"v_dot2_f32_bf16 v37, v74, s67, v37\n"
"v_dot2_f32_bf16 v38, v75, s66, v38\n"
"v_dot2_f32_bf16 v39, v75, s67, v39\n"
"s_add_u32 s34, s65, 1904640\n"
"buffer_load_dwordx4 v[72:75], v20, s[8:11], s34 offen\n"
"s_waitcnt vmcnt(13)\n"
"v_dot2_f32_bf16 v32, v76, s66, v32\n"
"v_dot2_f32_bf16 v33, v76, s67, v33\n"
"v_dot2_f32_bf16 v34, v77, s66, v34\n"
"v_dot2_f32_bf16 v35, v77, s67, v35\n"
"v_dot2_f32_bf16 v36, v78, s66, v36\n"
"v_dot2_f32_bf16 v37, v78, s67, v37\n"
"v_dot2_f32_bf16 v38, v79, s66, v38\n"
"v_dot2_f32_bf16 v39, v79, s67, v39\n"
"s_add_u32 s34, s65, 1935360\n"
"buffer_load_dwordx4 v[76:79], v20, s[8:11], s34 offen\n"
"s_waitcnt vmcnt(13)\n"
"v_dot2_f32_bf16 v32, v80, s66, v32\n"
"v_dot2_f32_bf16 v33, v80, s67, v33\n"
"v_dot2_f32_bf16 v34, v81, s66, v34\n"
"v_dot2_f32_bf16 v35, v81, s67, v35\n"
"v_dot2_f32_bf16 v36, v82, s66, v36\n"
"v_dot2_f32_bf16 v37, v82, s67, v37\n"
"v_dot2_f32_bf16 v38, v83, s66, v38\n"
"v_dot2_f32_bf16 v39, v83, s67, v39\n"
"s_add_u32 s34, s65, 1966080\n"
"buffer_load_dwordx4 v[80:83], v20, s[8:11], s34 offen\n"
"s_waitcnt vmcnt(13)\n"
"v_dot2_f32_bf16 v32, v84, s66, v32\n"
"v_dot2_f32_bf16 v33, v84, s67, v33\n"
"v_dot2_f32_bf16 v34, v85, s66, v34\n"
"v_dot2_f32_bf16 v35, v85, s67, v35\n"
"v_dot2_f32_bf16 v36, v86, s66, v36\n"
"v_dot2_f32_bf16 v37, v86, s67, v37\n"
"v_dot2_f32_bf16 v38, v87, s66, v38\n"
"v_dot2_f32_bf16 v39, v87, s67, v39\n"
"s_add_u32 s34, s65, 1996800\n"
"buffer_load_dwordx4 v[84:87], v20, s[8:11], s34 offen\n"
"s_waitcnt vmcnt(13)\n"
"v_dot2_f32_bf16 v32, v88, s66, v32\n"
"v_dot2_f32_bf16 v33, v88, s67, v33\n"
"v_dot2_f32_bf16 v34, v89, s66, v34\n"
"v_dot2_f32_bf16 v35, v89, s67, v35\n"
"v_dot2_f32_bf16 v36, v90, s66, v36\n"
"v_dot2_f32_bf16 v37, v90, s67, v37\n"
"v_dot2_f32_bf16 v38, v91, s66, v38\n"
"v_dot2_f32_bf16 v39, v91, s67, v39\n"
"s_add_u32 s34, s65, 2027520\n"
"buffer_load_dwordx4 v[88:91], v20, s[8:11], s34 offen\n"
"s_waitcnt vmcnt(13)\n"
"v_dot2_f32_bf16 v32, v92, s66, v32\n"
"v_dot2_f32_bf16 v33, v92, s67, v33\n"
"v_dot2_f32_bf16 v34, v93, s66, v34\n"
"v_dot2_f32_bf16 v35, v93, s67, v35\n"
"v_dot2_f32_bf16 v36, v94, s66, v36\n"
"v_dot2_f32_bf16 v37, v94, s67, v37\n"
"v_dot2_f32_bf16 v38, v95, s66, v38\n"
"v_dot2_f32_bf16 v39, v95, s67, v39\n"
"s_add_u32 s34, s65, 2058240\n"
"buffer_load_dwordx4 v[92:95], v20, s[8:11], s34 offen\n"
"s_waitcnt vmcnt(13)\n"
"v_dot2_f32_bf16 v32, v96, s66, v32\n"
"v_dot2_f32_bf16 v33, v96, s67, v33\n"
"v_dot2_f32_bf16 v34, v97, s66, v34\n"
"v_dot2_f32_bf16 v35, v97, s67, v35\n"
"v_dot2_f32_bf16 v36, v98, s66, v36\n"
"v_dot2_f32_bf16 v37, v98, s67, v37\n"
"v_dot2_f32_bf16 v38, v99, s66, v38\n"
"v_dot2_f32_bf16 v39, v99, s67, v39\n"
"s_add_u32 s34, s65, 2088960\n"
"buffer_load_dwordx4 v[96:99], v20, s[8:11], s34 offen\n"
"s_waitcnt vmcnt(13)\n"
"v_dot2_f32_bf16 v32, v100, s66, v32\n"
"v_dot2_f32_bf16 v33, v100, s67, v33\n"
"v_dot2_f32_bf16 v34, v101, s66, v34\n"
"v_dot2_f32_bf16 v35, v101, s67, v35\n"
"v_dot2_f32_bf16 v36, v102, s66, v36\n"
"v_dot2_f32_bf16 v37, v102, s67, v37\n"
"v_dot2_f32_bf16 v38, v103, s66, v38\n"
"v_dot2_f32_bf16 v39, v103, s67, v39\n"
"s_add_u32 s34, s65, 2119680\n"
"buffer_load_dwordx4 v[100:103], v20, s[8:11], s34 offen\n"
"s_waitcnt vmcnt(13)\n"
"v_dot2_f32_bf16 v32, v104, s66, v32\n"
"v_dot2_f32_bf16 v33, v104, s67, v33\n"
"v_dot2_f32_bf16 v34, v105, s66, v34\n"
"v_dot2_f32_bf16 v35, v105, s67, v35\n"
"v_dot2_f32_bf16 v36, v106, s66, v36\n"
"v_dot2_f32_bf16 v37, v106, s67, v37\n"
"v_dot2_f32_bf16 v38, v107, s66, v38\n"
"v_dot2_f32_bf16 v39, v107, s67, v39\n"
"s_add_u32 s34, s65, 2150400\n"
"buffer_load_dwordx4 v[104:107], v20, s[8:11], s34 offen\n"
"s_waitcnt vmcnt(13)\n"
"v_dot2_f32_bf16 v32, v108, s66, v32\n"
"v_dot2_f32_bf16 v33, v108, s67, v33\n"
"v_dot2_f32_bf16 v34, v109, s66, v34\n"
"v_dot2_f32_bf16 v35, v109, s67, v35\n"
"v_dot2_f32_bf16 v36, v110, s66, v36\n"
"v_dot2_f32_bf16 v37, v110, s67, v37\n"
"v_dot2_f32_bf16 v38, v111, s66, v38\n"
"v_dot2_f32_bf16 v39, v111, s67, v39\n"
"s_add_u32 s34, s65, 2181120\n"
"buffer_load_dwordx4 v[108:111], v20, s[8:11], s34 offen\n"
"s_waitcnt vmcnt(13)\n"
"v_dot2_f32_bf16 v32, v112, s66, v32\n"
"v_dot2_f32_bf16 v33, v112, s67, v33\n"
"v_dot2_f32_bf16 v34, v113, s66, v34\n"
"v_dot2_f32_bf16 v35, v113, s67, v35\n"
"v_dot2_f32_bf16 v36, v114, s66, v36\n"
"v_dot2_f32_bf16 v37, v114, s67, v37\n"
"v_dot2_f32_bf16 v38, v115, s66, v38\n"
"v_dot2_f32_bf16 v39, v115, s67, v39\n"
"s_add_u32 s34, s65, 2211840\n"
"buffer_load_dwordx4 v[112:115], v20, s[8:11], s34 offen\n"
"s_waitcnt vmcnt(13)\n"
"v_dot2_f32_bf16 v32, v116, s66, v32\n"
"v_dot2_f32_bf16 v33, v116, s67, v33\n"
"v_dot2_f32_bf16 v34, v117, s66, v34\n"
"v_dot2_f32_bf16 v35, v117, s67, v35\n"
"v_dot2_f32_bf16 v36, v118, s66, v36\n"
"v_dot2_f32_bf16 v37, v118, s67, v37\n"
"v_dot2_f32_bf16 v38, v119, s66, v38\n"
"v_dot2_f32_bf16 v39, v119, s67, v39\n"
"s_add_u32 s34, s65, 2242560\n"
"buffer_load_dwordx4 v[116:119], v20, s[8:11], s34 offen\n"
"s_waitcnt vmcnt(13)\n"
"v_dot2_f32_bf16 v32, v64, s66, v32\n"
"v_dot2_f32_bf16 v33, v64, s67, v33\n"
"v_dot2_f32_bf16 v34, v65, s66, v34\n"
"v_dot2_f32_bf16 v35, v65, s67, v35\n"
"v_dot2_f32_bf16 v36, v66, s66, v36\n"
"v_dot2_f32_bf16 v37, v66, s67, v37\n"
"v_dot2_f32_bf16 v38, v67, s66, v38\n"
"v_dot2_f32_bf16 v39, v67, s67, v39\n"
"s_add_u32 s34, s65, 2273280\n"
"buffer_load_dwordx4 v[64:67], v20, s[8:11], s34 offen\n"
"s_waitcnt vmcnt(13)\n"
"v_dot2_f32_bf16 v32, v68, s66, v32\n"
"v_dot2_f32_bf16 v33, v68, s67, v33\n"
"v_dot2_f32_bf16 v34, v69, s66, v34\n"
"v_dot2_f32_bf16 v35, v69, s67, v35\n"
"v_dot2_f32_bf16 v36, v70, s66, v36\n"
"v_dot2_f32_bf16 v37, v70, s67, v37\n"
"v_dot2_f32_bf16 v38, v71, s66, v38\n"
"v_dot2_f32_bf16 v39, v71, s67, v39\n"
"s_add_u32 s34, s65, 2304000\n"
"buffer_load_dwordx4 v[68:71], v20, s[8:11], s34 offen\n"
"s_waitcnt vmcnt(13)\n"
"v_dot2_f32_bf16 v32, v72, s66, v32\n"
"v_dot2_f32_bf16 v33, v72, s67, v33\n"
"v_dot2_f32_bf16 v34, v73, s66, v34\n"
"v_dot2_f32_bf16 v35, v73, s67, v35\n"
"v_dot2_f32_bf16 v36, v74, s66, v36\n"
"v_dot2_f32_bf16 v37, v74, s67, v37\n"
"v_dot2_f32_bf16 v38, v75, s66, v38\n"
"v_dot2_f32_bf16 v39, v75, s67, v39\n"
"s_add_u32 s34, s65, 2334720\n"
"buffer_load_dwordx4 v[72:75], v20, s[8:11], s34 offen\n"
"s_waitcnt vmcnt(13)\n"
"v_dot2_f32_bf16 v32, v76, s66, v32\n"
"v_dot2_f32_bf16 v33, v76, s67, v33\n"
"v_dot2_f32_bf16 v34, v77, s66, v34\n"
"v_dot2_f32_bf16 v35, v77, s67, v35\n"
"v_dot2_f32_bf16 v36, v78, s66, v36\n"
"v_dot2_f32_bf16 v37, v78, s67, v37\n"
"v_dot2_f32_bf16 v38, v79, s66, v38\n"
"v_dot2_f32_bf16 v39, v79, s67, v39\n"
"s_add_u32 s34, s65, 2365440\n"
"buffer_load_dwordx4 v[76:79], v20, s[8:11], s34 offen\n"
"s_waitcnt vmcnt(13)\n"
"v_dot2_f32_bf16 v32, v80, s66, v32\n"
"v_dot2_f32_bf16 v33, v80, s67, v33\n"
"v_dot2_f32_bf16 v34, v81, s66, v34\n"
"v_dot2_f32_bf16 v35, v81, s67, v35\n"
"v_dot2_f32_bf16 v36, v82, s66, v36\n"
"v_dot2_f32_bf16 v37, v82, s67, v37\n"
"v_dot2_f32_bf16 v38, v83, s66, v38\n"
"v_dot2_f32_bf16 v39, v83, s67, v39\n"
"s_add_u32 s34, s65, 2396160\n"
"buffer_load_dwordx4 v[80:83], v20, s[8:11], s34 offen\n"
"s_waitcnt vmcnt(13)\n"
"v_dot2_f32_bf16 v32, v84, s66, v32\n"
"v_dot2_f32_bf16 v33, v84, s67, v33\n"
"v_dot2_f32_bf16 v34, v85, s66, v34\n"
"v_dot2_f32_bf16 v35, v85, s67, v35\n"
"v_dot2_f32_bf16 v36, v86, s66, v36\n"
"v_dot2_f32_bf16 v37, v86, s67, v37\n"
"v_dot2_f32_bf16 v38, v87, s66, v38\n"
"v_dot2_f32_bf16 v39, v87, s67, v39\n"
"s_add_u32 s34, s65, 2426880\n"
"buffer_load_dwordx4 v[84:87], v20, s[8:11], s34 offen\n"
"s_waitcnt vmcnt(13)\n"
"v_dot2_f32_bf16 v32, v88, s66, v32\n"
"v_dot2_f32_bf16 v33, v88, s67, v33\n"
"v_dot2_f32_bf16 v34, v89, s66, v34\n"
"v_dot2_f32_bf16 v35, v89, s67, v35\n"
"v_dot2_f32_bf16 v36, v90, s66, v36\n"
"v_dot2_f32_bf16 v37, v90, s67, v37\n"
"v_dot2_f32_bf16 v38, v91, s66, v38\n"
"v_dot2_f32_bf16 v39, v91, s67, v39\n"
"s_waitcnt vmcnt(12)\n"
"v_dot2_f32_bf16 v32, v92, s66, v32\n"
"v_dot2_f32_bf16 v33, v92, s67, v33\n"
"v_dot2_f32_bf16 v34, v93, s66, v34\n"
"v_dot2_f32_bf16 v35, v93, s67, v35\n"
"v_dot2_f32_bf16 v36, v94, s66, v36\n"
"v_dot2_f32_bf16 v37, v94, s67, v37\n"
"v_dot2_f32_bf16 v38, v95, s66, v38\n"
"v_dot2_f32_bf16 v39, v95, s67, v39\n"
"s_waitcnt vmcnt(11)\n"
"v_dot2_f32_bf16 v32, v96, s66, v32\n"
"v_dot2_f32_bf16 v33, v96, s67, v33\n"
"v_dot2_f32_bf16 v34, v97, s66, v34\n"
"v_dot2_f32_bf16 v35, v97, s67, v35\n"
"v_dot2_f32_bf16 v36, v98, s66, v36\n"
"v_dot2_f32_bf16 v37, v98, s67, v37\n"
"v_dot2_f32_bf16 v38, v99, s66, v38\n"
"v_dot2_f32_bf16 v39, v99, s67, v39\n"
"s_waitcnt vmcnt(10)\n"
"v_dot2_f32_bf16 v32, v100, s66, v32\n"
"v_dot2_f32_bf16 v33, v100, s67, v33\n"
"v_dot2_f32_bf16 v34, v101, s66, v34\n"
"v_dot2_f32_bf16 v35, v101, s67, v35\n"
"v_dot2_f32_bf16 v36, v102, s66, v36\n"
"v_dot2_f32_bf16 v37, v102, s67, v37\n"
"v_dot2_f32_bf16 v38, v103, s66, v38\n"
"v_dot2_f32_bf16 v39, v103, s67, v39\n"
"s_waitcnt vmcnt(9)\n"
"v_dot2_f32_bf16 v32, v104, s66, v32\n"
"v_dot2_f32_bf16 v33, v104, s67, v33\n"
"v_dot2_f32_bf16 v34, v105, s66, v34\n"
"v_dot2_f32_bf16 v35, v105, s67, v35\n"
"v_dot2_f32_bf16 v36, v106, s66, v36\n"
"v_dot2_f32_bf16 v37, v106, s67, v37\n"
"v_dot2_f32_bf16 v38, v107, s66, v38\n"
"v_dot2_f32_bf16 v39, v107, s67, v39\n"
"s_waitcnt vmcnt(8)\n"
"v_dot2_f32_bf16 v32, v108, s66, v32\n"
"v_dot2_f32_bf16 v33, v108, s67, v33\n"
"v_dot2_f32_bf16 v34, v109, s66, v34\n"
"v_dot2_f32_bf16 v35, v109, s67, v35\n"
"v_dot2_f32_bf16 v36, v110, s66, v36\n"
"v_dot2_f32_bf16 v37, v110, s67, v37\n"
"v_dot2_f32_bf16 v38, v111, s66, v38\n"
"v_dot2_f32_bf16 v39, v111, s67, v39\n"
"s_waitcnt vmcnt(7)\n"
"v_dot2_f32_bf16 v32, v112, s66, v32\n"
"v_dot2_f32_bf16 v33, v112, s67, v33\n"
"v_dot2_f32_bf16 v34, v113, s66, v34\n"
"v_dot2_f32_bf16 v35, v113, s67, v35\n"
"v_dot2_f32_bf16 v36, v114, s66, v36\n"
"v_dot2_f32_bf16 v37, v114, s67, v37\n"
"v_dot2_f32_bf16 v38, v115, s66, v38\n"
"v_dot2_f32_bf16 v39, v115, s67, v39\n"
"s_waitcnt vmcnt(6)\n"
"v_dot2_f32_bf16 v32, v116, s66, v32\n"
"v_dot2_f32_bf16 v33, v116, s67, v33\n"
"v_dot2_f32_bf16 v34, v117, s66, v34\n"
"v_dot2_f32_bf16 v35, v117, s67, v35\n"
"v_dot2_f32_bf16 v36, v118, s66, v36\n"
"v_dot2_f32_bf16 v37, v118, s67, v37\n"
"v_dot2_f32_bf16 v38, v119, s66, v38\n"
"v_dot2_f32_bf16 v39, v119, s67, v39\n"
"s_waitcnt vmcnt(5)\n"
"v_dot2_f32_bf16 v32, v64, s66, v32\n"
"v_dot2_f32_bf16 v33, v64, s67, v33\n"
"v_dot2_f32_bf16 v34, v65, s66, v34\n"
"v_dot2_f32_bf16 v35, v65, s67, v35\n"
"v_dot2_f32_bf16 v36, v66, s66, v36\n"
"v_dot2_f32_bf16 v37, v66, s67, v37\n"
"v_dot2_f32_bf16 v38, v67, s66, v38\n"
"v_dot2_f32_bf16 v39, v67, s67, v39\n"
"s_waitcnt vmcnt(4)\n"
"v_dot2_f32_bf16 v32, v68, s66, v32\n"
"v_dot2_f32_bf16 v33, v68, s67, v33\n"
"v_dot2_f32_bf16 v34, v69, s66, v34\n"
"v_dot2_f32_bf16 v35, v69, s67, v35\n"
"v_dot2_f32_bf16 v36, v70, s66, v36\n"
"v_dot2_f32_bf16 v37, v70, s67, v37\n"
"v_dot2_f32_bf16 v38, v71, s66, v38\n"
"v_dot2_f32_bf16 v39, v71, s67, v39\n"
"s_waitcnt vmcnt(3)\n"
"v_dot2_f32_bf16 v32, v72, s66, v32\n"
"v_dot2_f32_bf16 v33, v72, s67, v33\n"
"v_dot2_f32_bf16 v34, v73, s66, v34\n"
"v_dot2_f32_bf16 v35, v73, s67, v35\n"
"v_dot2_f32_bf16 v36, v74, s66, v36\n"
"v_dot2_f32_bf16 v37, v74, s67, v37\n"
"v_dot2_f32_bf16 v38, v75, s66, v38\n"
"v_dot2_f32_bf16 v39, v75, s67, v39\n"
"s_waitcnt vmcnt(2)\n"
"v_dot2_f32_bf16 v32, v76, s66, v32\n"
"v_dot2_f32_bf16 v33, v76, s67, v33\n"
"v_dot2_f32_bf16 v34, v77, s66, v34\n"
"v_dot2_f32_bf16 v35, v77, s67, v35\n"
"v_dot2_f32_bf16 v36, v78, s66, v36\n"
"v_dot2_f32_bf16 v37, v78, s67, v37\n"
"v_dot2_f32_bf16 v38, v79, s66, v38\n"
"v_dot2_f32_bf16 v39, v79, s67, v39\n"
"s_waitcnt vmcnt(1)\n"
"v_dot2_f32_bf16 v32, v80, s66, v32\n"
"v_dot2_f32_bf16 v33, v80, s67, v33\n"
"v_dot2_f32_bf16 v34, v81, s66, v34\n"
"v_dot2_f32_bf16 v35, v81, s67, v35\n"
"v_dot2_f32_bf16 v36, v82, s66, v36\n"
"v_dot2_f32_bf16 v37, v82, s67, v37\n"
"v_dot2_f32_bf16 v38, v83, s66, v38\n"
"v_dot2_f32_bf16 v39, v83, s67, v39\n"
"s_waitcnt vmcnt(0)\n"
"v_dot2_f32_bf16 v32, v84, s66, v32\n"
"v_dot2_f32_bf16 v33, v84, s67, v33\n"
"v_dot2_f32_bf16 v34, v85, s66, v34\n"
"v_dot2_f32_bf16 v35, v85, s67, v35\n"
"v_dot2_f32_bf16 v36, v86, s66, v36\n"
"v_dot2_f32_bf16 v37, v86, s67, v37\n"
"v_dot2_f32_bf16 v38, v87, s66, v38\n"
"v_dot2_f32_bf16 v39, v87, s67, v39\n"
"v_permlane32_swap_b32 v32, v36\n"
"v_permlane32_swap_b32 v33, v37\n"
"v_permlane32_swap_b32 v34, v38\n"
"v_permlane32_swap_b32 v35, v39\n"
"v_add_f32 v32, v32, v36\n"
"v_add_f32 v33, v33, v37\n"
"v_add_f32 v34, v34, v38\n"
"v_add_f32 v35, v35, v39\n"
"s_nop 0\n"
"v_permlane16_swap_b32 v32, v34\n"
"v_permlane16_swap_b32 v33, v35\n"
"v_add_f32 v32, v32, v34\n"
"v_add_f32 v33, v33, v35\n"
"s_nop 0\n"
"v_add_f32_dpp v32, v32, v32 row_ror:8 row_mask:0xf bank_mask:0xf\n"
"v_add_f32_dpp v33, v33, v33 row_ror:8 row_mask:0xf bank_mask:0xf\n"
"buffer_store_dwordx2 v[32:33], v21, s[16:19], s69 offen sc1\n"
"s_waitcnt vmcnt(0)\n"
"buffer_store_dword v19, v3, s[16:19], s71 offen sc1\n"
"s_waitcnt vmcnt(0)\n"
".Lfb_aggb:\n"
"s_mul_i32 s61, s3, 5\n"
"s_add_u32 s61, s61, 46\n"
"v_min_u32 v56, 12, v1\n"
"v_cmp_gt_u32 vcc, 8, v56\n"
"v_add_u32 v57, s61, v56\n"
"v_add_u32 v56, s78, v56\n"
"s_lshr_b32 s60, s56, 2\n"
"v_cndmask_b32 v56, v57, v56, vcc\n"
"v_lshl_add_u32 v56, v56, 1, s60\n"
"v_lshlrev_b32 v56, 7, v56\n"
"v_add_u32 v56, 212992, v56\n"
"buffer_load_dword v60, v56, s[16:19], 0 offen sc1\n"
"s_waitcnt vmcnt(0)\n"
"v_cmp_ne_u32 vcc, s52, v60\n"
"s_nop 0\n"
"s_cbranch_vccnz .Lfb_slow_16\n"
".Lfb_back_17:\n"
"s_add_u32 s98, s43, 0\n"
"buffer_load_dwordx4 a[0:3], v2, s[24:27], s98 offen offset:0 sc1\n"
"buffer_load_dwordx4 a[4:7], v2, s[24:27], s98 offen offset:1024 sc1\n"
"buffer_load_dwordx4 a[8:11], v2, s[24:27], s98 offen offset:2048 sc1\n"
"buffer_load_dwordx4 a[12:15], v2, s[24:27], s98 offen offset:3072 sc1\n"
"s_add_u32 s98, s43, 4096\n"
"buffer_load_dwordx4 a[16:19], v2, s[24:27], s98 offen offset:0 sc1\n"
"s_add_u32 s98, s55, 0\n"
"buffer_load_dwordx4 a[20:23], v2, s[24:27], s98 offen offset:0 sc1\n"
"buffer_load_dwordx4 a[24:27], v2, s[24:27], s98 offen offset:1024 sc1\n"
"buffer_load_dwordx4 a[28:31], v2, s[24:27], s98 offen offset:2048 sc1\n"
"buffer_load_dwordx4 a[32:35], v2, s[24:27], s98 offen offset:3072 sc1\n"
"s_add_u32 s98, s55, 4096\n"
"buffer_load_dwordx4 a[36:39], v2, s[24:27], s98 offen offset:0 sc1\n"
"s_add_u32 s98, s96, 0\n"
"buffer_load_dwordx4 a[40:43], v2, s[24:27], s98 offen offset:0 sc1\n"
"buffer_load_dwordx4 a[44:47], v2, s[24:27], s98 offen offset:1024 sc1\n"
"buffer_load_dwordx4 a[48:51], v2, s[24:27], s98 offen offset:2048 sc1\n"
"buffer_load_dwordx4 a[52:55], v2, s[24:27], s98 offen offset:3072 sc1\n"
"s_add_u32 s98, s96, 4096\n"
"buffer_load_dwordx4 a[56:59], v2, s[24:27], s98 offen offset:0 sc1\n"
"buffer_load_dwordx4 a[60:63], v2, s[24:27], s98 offen offset:1024 sc1\n"
"buffer_load_dwordx4 a[64:67], v2, s[24:27], s98 offen offset:2048 sc1\n"
"buffer_load_dwordx4 a[68:71], v2, s[24:27], s98 offen offset:3072 sc1\n"
"s_add_u32 s98, s96, 8192\n"
"buffer_load_dwordx4 a[72:75], v2, s[24:27], s98 offen offset:0 sc1\n"
"buffer_load_dwordx4 a[76:79], v2, s[24:27], s98 offen offset:1024 sc1\n"
"buffer_load_dwordx4 a[80:83], v2, s[24:27], s98 offen offset:2048 sc1\n"
"buffer_load_dwordx4 a[84:87], v2, s[24:27], s98 offen offset:3072 sc1\n"
"s_add_u32 s98, s96, 12288\n"
"buffer_load_dwordx4 a[88:91], v2, s[24:27], s98 offen offset:0 sc1\n"
"buffer_load_dwordx4 a[92:95], v2, s[24:27], s98 offen offset:1024 sc1\n"
"buffer_load_dwordx4 a[96:99], v2, s[24:27], s98 offen offset:2048 sc1\n"
"buffer_load_dwordx4 a[100:103], v2, s[24:27], s98 offen offset:3072 sc1\n"
"s_add_u32 s98, s96, 16384\n"
"buffer_load_dwordx4 v[252:255], v2, s[24:27], s98 offen offset:0 sc1\n"
"buffer_load_dwordx4 v[248:251], v2, s[24:27], s98 offen offset:1024 sc1\n"
"buffer_load_dwordx4 v[244:247], v2, s[24:27], s98 offen offset:2048 sc1\n"
"buffer_load_dwordx4 v[240:243], v2, s[24:27], s98 offen offset:3072 sc1\n"
"s_add_u32 s98, s96, 20480\n"
"buffer_load_dwordx4 v[236:239], v2, s[24:27], s98 offen offset:0 sc1\n"
"buffer_load_dwordx4 v[232:235], v2, s[24:27], s98 offen offset:1024 sc1\n"
"buffer_load_dwordx4 v[228:231], v2, s[24:27], s98 offen offset:2048 sc1\n"
"buffer_load_dwordx4 v[224:227], v2, s[24:27], s98 offen offset:3072 sc1\n"
"s_add_u32 s98, s96, 24576\n"
"buffer_load_dwordx4 v[220:223], v2, s[24:27], s98 offen offset:0 sc1\n"
"buffer_load_dwordx4 v[216:219], v2, s[24:27], s98 offen offset:1024 sc1\n"
"buffer_load_dwordx4 v[212:215], v2, s[24:27], s98 offen offset:2048 sc1\n"
"buffer_load_dwordx4 v[208:211], v2, s[24:27], s98 offen offset:3072 sc1\n"
"s_add_u32 s98, s96, 28672\n"
"buffer_load_dwordx4 v[204:207], v2, s[24:27], s98 offen offset:0 sc1\n"
"buffer_load_dwordx4 v[200:203], v2, s[24:27], s98 offen offset:1024 sc1\n"
"s_add_u32 s98, s97, 0\n"
"buffer_load_dwordx4 v[196:199], v2, s[24:27], s98 offen offset:0 sc1\n"
"buffer_load_dwordx4 v[192:195], v2, s[24:27], s98 offen offset:1024 sc1\n"
"buffer_load_dwordx4 a[104:107], v2, s[24:27], s98 offen offset:2048 sc1\n"
"buffer_load_dwordx4 a[108:111], v2, s[24:27], s98 offen offset:3072 sc1\n"
"s_add_u32 s98, s97, 4096\n"
"buffer_load_dwordx4 a[112:115], v2, s[24:27], s98 offen offset:0 sc1\n"
"buffer_load_dwordx4 a[116:119], v2, s[24:27], s98 offen offset:1024 sc1\n"
"buffer_load_dwordx4 a[120:123], v2, s[24:27], s98 offen offset:2048 sc1\n"
"buffer_load_dwordx4 a[124:127], v2, s[24:27], s98 offen offset:3072 sc1\n"
"s_add_u32 s98, s97, 8192\n"
"buffer_load_dwordx4 a[240:243], v2, s[24:27], s98 offen offset:0 sc1\n"
"buffer_load_dwordx4 a[244:247], v2, s[24:27], s98 offen offset:1024 sc1\n"
"buffer_load_dwordx4 a[248:251], v2, s[24:27], s98 offen offset:2048 sc1\n"
"buffer_load_dwordx4 a[252:255], v2, s[24:27], s98 offen offset:3072 sc1\n"
"s_add_u32 s98, s97, 12288\n"
"buffer_load_dwordx4 v[188:191], v2, s[24:27], s98 offen offset:0 sc1\n"
"buffer_load_dwordx4 v[184:187], v2, s[24:27], s98 offen offset:1024 sc1\n"
"buffer_load_dwordx4 v[180:183], v2, s[24:27], s98 offen offset:2048 sc1\n"
"buffer_load_dwordx4 v[176:179], v2, s[24:27], s98 offen offset:3072 sc1\n"
"s_add_u32 s98, s97, 16384\n"
"buffer_load_dwordx4 v[172:175], v2, s[24:27], s98 offen offset:0 sc1\n"
"buffer_load_dwordx4 v[168:171], v2, s[24:27], s98 offen offset:1024 sc1\n"
"buffer_load_dwordx4 v[164:167], v2, s[24:27], s98 offen offset:2048 sc1\n"
"buffer_load_dwordx4 v[160:163], v2, s[24:27], s98 offen offset:3072 sc1\n"
"s_add_u32 s98, s97, 20480\n"
"buffer_load_dwordx4 a[228:231], v2, s[24:27], s98 offen offset:0 sc1\n"
"buffer_load_dwordx4 a[232:235], v2, s[24:27], s98 offen offset:1024 sc1\n"
"buffer_load_dwordx4 a[236:239], v2, s[24:27], s98 offen offset:2048 sc1\n"
"s_waitcnt vmcnt(61)\n"
"buffer_load_dwordx4 v[36:39], v2, s[24:27], s98 offen offset:3072 sc1\n"
"s_add_u32 s98, s97, 24576\n"
"buffer_load_dwordx4 v[40:43], v2, s[24:27], s98 offen offset:0 sc1\n"
"s_lshr_b32 s34, s2, 3\n"
"s_sub_u32 s34, s34, 29\n"
"s_lshl_b32 s34, s34, 6\n"
"v_lshrrev_b32 v56, 2, v0\n"
"v_add_lshl_u32 v56, v56, s34, 7\n"
"v_add_u32 v56, .Lfb_idlep-.Lfb_pc, v56\n"
"v_min_u32 v56, .Lfb_end-.Lfb_pc-4, v56\n"
"s_waitcnt vmcnt(61)\n"
"global_load_dword v57, v56, s[86:87]\n"
".Lfb_idlep:\n"
"s_sleep 50\n"
"s_mul_i32 s58, s3, 3072\n"
"s_add_u32 m0, s58, 126976\n"
"s_nop 0\n"
"buffer_load_dwordx4 v5, s[4:7], s79 offen offset:0 lds\n"
"s_waitcnt vmcnt(61)\n"
"buffer_load_dwordx4 v6, s[4:7], s79 offen offset:1024 lds\n"
"buffer_load_dwordx4 v7, s[4:7], s79 offen offset:2048 lds\n"
"s_waitcnt vmcnt(61)\n"
"buffer_load_dword v12, v10, s[16:19], 0 offen sc1\n"
"s_waitcnt vmcnt(0)\n"
"v_cmp_ne_u32 vcc, s52, v12\n"
"s_nop 0\n"
"s_cbranch_vccnz .Lfb_slow_19\n"
".Lfb_back_20:\n"
"s_xor_b32 s85, s3, 3\n"
"s_mul_i32 s85, s85, 116736\n"
"s_add_u32 s85, s85, s45\n"
"buffer_load_dwordx4 a[128:131], v2, s[20:23], s85 offen offset:0 sc1\n"
"buffer_load_dwordx4 a[132:135], v2, s[20:23], s85 offen offset:1024 sc1\n"
"buffer_load_dwordx4 a[136:139], v2, s[20:23], s85 offen offset:2048 sc1\n"
"buffer_load_dwordx4 a[140:143], v2, s[20:23], s85 offen offset:3072 sc1\n"
"s_add_u32 s85, s85, 4096\n"
"buffer_load_dwordx4 a[144:147], v2, s[20:23], s85 offen offset:0 sc1\n"
"buffer_load_dwordx4 a[148:151], v2, s[20:23], s85 offen offset:1024 sc1\n"
"buffer_load_dwordx4 a[152:155], v2, s[20:23], s85 offen offset:2048 sc1\n"
"buffer_load_dwordx4 a[156:159], v2, s[20:23], s85 offen offset:3072 sc1\n"
"s_xor_b32 s85, s3, 2\n"
"s_mul_i32 s85, s85, 116736\n"
"s_add_u32 s85, s85, s45\n"
"buffer_load_dwordx4 a[160:163], v2, s[20:23], s85 offen offset:0 sc1\n"
"buffer_load_dwordx4 a[164:167], v2, s[20:23], s85 offen offset:1024 sc1\n"
"s_waitcnt vmcnt(8)\n"
"v_mfma_f32_16x16x32_bf16 a[188:191], a[0:3], a[128:131], 0\n"
"buffer_load_dwordx4 a[168:171], v2, s[20:23], s85 offen offset:2048 sc1\n"
"s_mul_i32 s62, s3, 3072\n"
"v_mfma_f32_16x16x32_bf16 a[188:191], a[20:23], a[132:135], a[188:191]\n"
"s_add_u32 s62, s62, 126976\n"
"v_mfma_f32_16x16x32_bf16 a[192:195], a[4:7], a[128:131], 0\n"
"buffer_load_dwordx4 a[172:175], v2, s[20:23], s85 offen offset:3072 sc1\n"
"s_add_u32 s85, s85, 4096\n"
"v_mfma_f32_16x16x32_bf16 a[192:195], a[24:27], a[132:135], a[192:195]\n"
"v_and_b32 v11, 15, v1\n"
"v_mul_u32_u24 v10, 160, v11\n"
"v_mfma_f32_16x16x32_bf16 a[196:199], a[8:11], a[128:131], 0\n"
"buffer_load_dwordx4 a[176:179], v2, s[20:23], s85 offen offset:0 sc1\n"
"v_lshrrev_b32 v14, 4, v1\n"
"v_mfma_f32_16x16x32_bf16 a[196:199], a[28:31], a[132:135], a[196:199]\n"
"v_lshl_add_u32 v10, v14, 3, v10\n"
"v_add_u32 v10, s62, v10\n"
"v_mfma_f32_16x16x32_bf16 a[200:203], a[12:15], a[128:131], 0\n"
"v_mfma_f32_16x16x32_bf16 a[200:203], a[32:35], a[132:135], a[200:203]\n"
"ds_read_b64 v[44:45], v10 offset:0\n"
"ds_read_b64 v[46:47], v10 offset:32\n"
"v_mfma_f32_16x16x32_bf16 a[204:207], a[16:19], a[128:131], 0\n"
"buffer_load_dwordx4 a[180:183], v2, s[20:23], s85 offen offset:1024 sc1\n"
"ds_read_b64 v[48:49], v10 offset:64\n"
"v_mfma_f32_16x16x32_bf16 a[204:207], a[36:39], a[132:135], a[204:207]\n"
"ds_read_b64 v[50:51], v10 offset:96\n"
"s_waitcnt vmcnt(9)\n"
"v_mfma_f32_16x16x32_bf16 a[188:191], a[40:43], a[136:139], a[188:191]\n"
"ds_read_b64 v[52:53], v10 offset:128\n"
"v_lshl_add_u32 v10, v11, 4, v10\n"
"v_mfma_f32_16x16x32_bf16 a[188:191], a[60:63], a[140:143], a[188:191]\n"
"v_add_u32 v10, 16384, v10\n"
"s_add_u32 s62, s62, 16384\n"
"v_mfma_f32_16x16x32_bf16 a[188:191], a[80:83], a[144:147], a[188:191]\n"
"buffer_load_dwordx4 a[184:187], v2, s[20:23], s85 offen offset:2048 sc1\n"
"v_bfe_u32 v11, v1, 4, 1\n"
"v_mfma_f32_16x16x32_bf16 a[192:195], a[44:47], a[136:139], a[192:195]\n"
"v_mul_u32_u24 v11, 44, v11\n"
"v_lshrrev_b32 v14, 5, v1\n"
"v_mfma_f32_16x16x32_bf16 a[192:195], a[64:67], a[140:143], a[192:195]\n"
"v_add3_u32 v11, v11, v14, s47\n"
"v_lshlrev_b32 v11, 7, v11\n"
"v_mfma_f32_16x16x32_bf16 a[192:195], a[84:87], a[144:147], a[192:195]\n"
"s_lshl_b32 s34, s40, 7\n"
"s_add_u32 s34, s34, 1325056\n"
"v_mfma_f32_16x16x32_bf16 a[196:199], a[48:51], a[136:139], a[196:199]\n"
"buffer_load_dwordx4 v[20:23], v2, s[20:23], s85 offen offset:3072 sc1\n"
"s_xor_b32 s60, s40, 32\n"
"v_mfma_f32_16x16x32_bf16 a[196:199], a[68:71], a[140:143], a[196:199]\n"
"s_lshl_b32 s61, s60, 7\n"
"s_add_u32 s61, s61, 1325056\n"
"v_mfma_f32_16x16x32_bf16 a[196:199], a[88:91], a[144:147], a[196:199]\n"
"s_xor_b32 s85, s3, 1\n"
"s_mul_i32 s85, s85, 116736\n"
"v_mfma_f32_16x16x32_bf16 a[200:203], a[52:55], a[136:139], a[200:203]\n"
"v_mov_b32 v8, s61\n"
"s_add_u32 s85, s85, s45\n"
"v_mfma_f32_16x16x32_bf16 a[200:203], a[72:75], a[140:143], a[200:203]\n"
"v_lshlrev_b32 v15, 4, v0\n"
"v_add_u32 v15, 81920, v15\n"
"v_mfma_f32_16x16x32_bf16 a[200:203], a[92:95], a[144:147], a[200:203]\n"
"v_xor_b32 v156, 3072, v15\n"
"v_xor_b32 v157, 2048, v15\n"
"v_mfma_f32_16x16x32_bf16 a[204:207], a[56:59], a[136:139], a[204:207]\n"
"buffer_load_dwordx4 v[24:27], v2, s[20:23], s85 offen offset:0 sc1\n"
"v_xor_b32 v158, 1024, v15\n"
"v_mfma_f32_16x16x32_bf16 a[204:207], a[76:79], a[140:143], a[204:207]\n"
"s_mul_i32 s63, s56, 5120\n"
"s_add_u32 s63, s63, 75776\n"
"v_mfma_f32_16x16x32_bf16 a[204:207], a[96:99], a[144:147], a[204:207]\n"
"s_waitcnt vmcnt(9)\n"
"s_lshr_b32 s60, s60, 3\n"
"v_mfma_f32_16x16x32_bf16 a[188:191], a[100:103], a[148:151], a[188:191]\n"
"buffer_load_dwordx4 v[28:31], v2, s[20:23], s85 offen offset:1024 sc1\n"
"s_mul_i32 s60, s60, 116736\n"
"v_mfma_f32_16x16x32_bf16 a[188:191], v[236:239], a[152:155], a[188:191]\n"
"s_add_u32 s60, s60, s63\n"
"s_add_u32 s61, s60, 4096\n"
"v_mfma_f32_16x16x32_bf16 a[188:191], v[216:219], a[156:159], a[188:191]\n"
"s_lshr_b32 s40, s40, 3\n"
"s_mul_i32 s40, s40, 116736\n"
"v_mfma_f32_16x16x32_bf16 a[192:195], v[252:255], a[148:151], a[192:195]\n"
"s_add_u32 s40, s40, s63\n"
"s_add_u32 s63, s40, 4096\n"
"v_mfma_f32_16x16x32_bf16 a[192:195], v[232:235], a[152:155], a[192:195]\n"
"buffer_load_dwordx4 v[32:35], v2, s[20:23], s85 offen offset:2048 sc1\n"
"s_and_b32 s35, s52, 0x1fff\n"
"v_mfma_f32_16x16x32_bf16 a[192:195], v[212:215], a[156:159], a[192:195]\n"
"s_or_b32 s35, s35, 0x4000\n"
"s_lshl_b32 s35, s35, 16\n"
"v_mfma_f32_16x16x32_bf16 a[196:199], v[248:251], a[148:151], a[196:199]\n"
"v_mov_b32 v16, s35\n"
"v_lshlrev_b32 v9, 3, v1\n"
"v_mfma_f32_16x16x32_bf16 a[196:199], v[228:231], a[152:155], a[196:199]\n"
"v_mfma_f32_16x16x32_bf16 a[196:199], v[208:211], a[156:159], a[196:199]\n"
"v_mfma_f32_16x16x32_bf16 a[200:203], v[244:247], a[148:151], a[200:203]\n"
"ds_write_b128 v15, a[188:191] offset:0\n"
"v_mfma_f32_16x16x32_bf16 a[200:203], v[224:227], a[152:155], a[200:203]\n"
"buffer_load_dwordx4 a[128:131], v2, s[20:23], s85 offen offset:3072 sc1\n"
"v_mfma_f32_16x16x32_bf16 a[200:203], v[204:207], a[156:159], a[200:203]\n"
"s_add_u32 s85, s85, 4096\n"
"v_mfma_f32_16x16x32_bf16 a[204:207], v[240:243], a[148:151], a[204:207]\n"
"ds_write_b128 v15, a[192:195] offset:4096\n"
"v_mfma_f32_16x16x32_bf16 a[204:207], v[220:223], a[152:155], a[204:207]\n"
"buffer_load_dwordx4 a[132:135], v2, s[20:23], s85 offen offset:0 sc1\n"
"v_mfma_f32_16x16x32_bf16 a[204:207], v[200:203], a[156:159], a[204:207]\n"
"s_waitcnt vmcnt(11)\n"
"v_mfma_f32_16x16x32_bf16 a[208:211], a[0:3], a[160:163], 0\n"
"ds_write_b128 v15, a[196:199] offset:8192\n"
"v_mfma_f32_16x16x32_bf16 a[208:211], a[20:23], a[164:167], a[208:211]\n"
"v_mfma_f32_16x16x32_bf16 a[212:215], a[4:7], a[160:163], 0\n"
"buffer_load_dwordx4 a[136:139], v2, s[20:23], s85 offen offset:1024 sc1\n"
"v_mfma_f32_16x16x32_bf16 a[212:215], a[24:27], a[164:167], a[212:215]\n"
"ds_write_b128 v15, a[200:203] offset:12288\n"
"v_mfma_f32_16x16x32_bf16 a[216:219], a[8:11], a[160:163], 0\n"
"v_mfma_f32_16x16x32_bf16 a[216:219], a[28:31], a[164:167], a[216:219]\n"
"v_mfma_f32_16x16x32_bf16 a[220:223], a[12:15], a[160:163], 0\n"
"ds_write_b128 v15, a[204:207] offset:16384\n"
"v_mfma_f32_16x16x32_bf16 a[220:223], a[32:35], a[164:167], a[220:223]\n"
"buffer_load_dwordx4 a[140:143], v2, s[20:23], s85 offen offset:2048 sc1\n"
"v_mfma_f32_16x16x32_bf16 a[224:227], a[16:19], a[160:163], 0\n"
"v_mfma_f32_16x16x32_bf16 a[224:227], a[36:39], a[164:167], a[224:227]\n"
"s_waitcnt vmcnt(10)\n"
"v_mfma_f32_16x16x32_bf16 a[208:211], a[40:43], a[168:171], a[208:211]\n"
"v_mfma_f32_16x16x32_bf16 a[208:211], a[60:63], a[172:175], a[208:211]\n"
"buffer_load_dwordx4 a[144:147], v2, s[20:23], s85 offen offset:3072 sc1\n"
"v_mfma_f32_16x16x32_bf16 a[208:211], a[80:83], a[176:179], a[208:211]\n"
"s_xor_b32 s85, s3, 0\n"
"s_mul_i32 s85, s85, 116736\n"
"v_mfma_f32_16x16x32_bf16 a[212:215], a[44:47], a[168:171], a[212:215]\n"
"s_add_u32 s85, s85, s45\n"
"v_mfma_f32_16x16x32_bf16 a[212:215], a[64:67], a[172:175], a[212:215]\n"
"v_mfma_f32_16x16x32_bf16 a[212:215], a[84:87], a[176:179], a[212:215]\n"
"buffer_load_dwordx4 a[148:151], v2, s[20:23], s85 offen offset:0 sc1\n"
"v_mfma_f32_16x16x32_bf16 a[216:219], a[48:51], a[168:171], a[216:219]\n"
"s_waitcnt lgkmcnt(0)\n"
"v_mfma_f32_16x16x32_bf16 a[216:219], a[68:71], a[172:175], a[216:219]\n"
"v_mfma_f32_16x16x32_bf16 a[216:219], a[88:91], a[176:179], a[216:219]\n"
"v_mfma_f32_16x16x32_bf16 a[220:223], a[52:55], a[168:171], a[220:223]\n"
"buffer_load_dwordx4 a[152:155], v2, s[20:23], s85 offen offset:1024 sc1\n"
"v_mfma_f32_16x16x32_bf16 a[220:223], a[72:75], a[172:175], a[220:223]\n"
"v_mfma_f32_16x16x32_bf16 a[220:223], a[92:95], a[176:179], a[220:223]\n"
"v_mfma_f32_16x16x32_bf16 a[224:227], a[56:59], a[168:171], a[224:227]\n"
"v_mfma_f32_16x16x32_bf16 a[224:227], a[76:79], a[172:175], a[224:227]\n"
"v_mfma_f32_16x16x32_bf16 a[224:227], a[96:99], a[176:179], a[224:227]\n"
"buffer_load_dwordx4 a[156:159], v2, s[20:23], s85 offen offset:2048 sc1\n"
"s_waitcnt vmcnt(11)\n"
"v_mfma_f32_16x16x32_bf16 a[208:211], a[100:103], a[180:183], a[208:211]\n"
"v_mfma_f32_16x16x32_bf16 a[208:211], v[236:239], a[184:187], a[208:211]\n"
"v_mfma_f32_16x16x32_bf16 a[208:211], v[216:219], v[20:23], a[208:211]\n"
"v_mfma_f32_16x16x32_bf16 a[212:215], v[252:255], a[180:183], a[212:215]\n"
"buffer_load_dwordx4 a[160:163], v2, s[20:23], s85 offen offset:3072 sc1\n"
"v_mfma_f32_16x16x32_bf16 a[212:215], v[232:235], a[184:187], a[212:215]\n"
"v_mfma_f32_16x16x32_bf16 a[212:215], v[212:215], v[20:23], a[212:215]\n"
"s_add_u32 s85, s85, 4096\n"
"v_mfma_f32_16x16x32_bf16 a[216:219], v[248:251], a[180:183], a[216:219]\n"
"v_mfma_f32_16x16x32_bf16 a[216:219], v[228:231], a[184:187], a[216:219]\n"
"buffer_load_dwordx4 a[164:167], v2, s[20:23], s85 offen offset:0 sc1\n"
"v_mfma_f32_16x16x32_bf16 a[216:219], v[208:211], v[20:23], a[216:219]\n"
"v_mfma_f32_16x16x32_bf16 a[220:223], v[244:247], a[180:183], a[220:223]\n"
"ds_write_b128 v15, a[208:211] offset:20480\n"
"v_mfma_f32_16x16x32_bf16 a[220:223], v[224:227], a[184:187], a[220:223]\n"
"v_mfma_f32_16x16x32_bf16 a[220:223], v[204:207], v[20:23], a[220:223]\n"
"v_mfma_f32_16x16x32_bf16 a[224:227], v[240:243], a[180:183], a[224:227]\n"
"ds_write_b128 v15, a[212:215] offset:24576\n"
"v_mfma_f32_16x16x32_bf16 a[224:227], v[220:223], a[184:187], a[224:227]\n"
"buffer_load_dwordx4 a[168:171], v2, s[20:23], s85 offen offset:1024 sc1\n"
"v_mfma_f32_16x16x32_bf16 a[224:227], v[200:203], v[20:23], a[224:227]\n"
"s_waitcnt vmcnt(12)\n"
"v_mfma_f32_16x16x32_bf16 a[188:191], a[0:3], v[24:27], 0\n"
"ds_write_b128 v15, a[216:219] offset:28672\n"
"v_mfma_f32_16x16x32_bf16 a[188:191], a[20:23], v[28:31], a[188:191]\n"
"buffer_load_dwordx4 a[172:175], v2, s[20:23], s85 offen offset:2048 sc1\n"
"v_mfma_f32_16x16x32_bf16 a[192:195], a[4:7], v[24:27], 0\n"
"v_mfma_f32_16x16x32_bf16 a[192:195], a[24:27], v[28:31], a[192:195]\n"
"ds_write_b128 v15, a[220:223] offset:32768\n"
"v_mfma_f32_16x16x32_bf16 a[196:199], a[8:11], v[24:27], 0\n"
"v_mfma_f32_16x16x32_bf16 a[196:199], a[28:31], v[28:31], a[196:199]\n"
"buffer_load_dwordx4 a[176:179], v2, s[20:23], s85 offen offset:3072 sc1\n"
"v_mfma_f32_16x16x32_bf16 a[200:203], a[12:15], v[24:27], 0\n"
"ds_write_b128 v15, a[224:227] offset:36864\n"
"v_mfma_f32_16x16x32_bf16 a[200:203], a[32:35], v[28:31], a[200:203]\n"
"s_xor_b32 s85, s3, 3\n"
"s_mul_i32 s85, s85, 116736\n"
"v_mfma_f32_16x16x32_bf16 a[204:207], a[16:19], v[24:27], 0\n"
"s_add_u32 s85, s85, s44\n"
"v_mfma_f32_16x16x32_bf16 a[204:207], a[36:39], v[28:31], a[204:207]\n"
"buffer_load_dwordx4 a[180:183], v2, s[20:23], s85 offen offset:0 sc1\n"
"s_waitcnt vmcnt(12)\n"
"v_mfma_f32_16x16x32_bf16 a[188:191], a[40:43], v[32:35], a[188:191]\n"
"v_mfma_f32_16x16x32_bf16 a[188:191], a[60:63], a[128:131], a[188:191]\n"
"v_mfma_f32_16x16x32_bf16 a[188:191], a[80:83], a[132:135], a[188:191]\n"
"v_mfma_f32_16x16x32_bf16 a[192:195], a[44:47], v[32:35], a[192:195]\n"
"v_mfma_f32_16x16x32_bf16 a[192:195], a[64:67], a[128:131], a[192:195]\n"
"buffer_load_dwordx4 a[184:187], v2, s[20:23], s85 offen offset:1024 sc1\n"
"v_mfma_f32_16x16x32_bf16 a[192:195], a[84:87], a[132:135], a[192:195]\n"
"v_mfma_f32_16x16x32_bf16 a[196:199], a[48:51], v[32:35], a[196:199]\n"
"s_waitcnt lgkmcnt(0)\n"
"s_barrier\n"
"v_mfma_f32_16x16x32_bf16 a[196:199], a[68:71], a[128:131], a[196:199]\n"
"ds_read_b128 v[116:119], v156 offset:0\n"
"ds_read_b128 v[120:123], v156 offset:4096\n"
"v_mfma_f32_16x16x32_bf16 a[196:199], a[88:91], a[132:135], a[196:199]\n"
"ds_read_b128 v[124:127], v156 offset:8192\n"
"ds_read_b128 v[128:131], v156 offset:12288\n"
"v_mfma_f32_16x16x32_bf16 a[200:203], a[52:55], v[32:35], a[200:203]\n"
"ds_read_b128 v[132:135], v156 offset:16384\n"
"buffer_load_dwordx4 v[20:23], v2, s[20:23], s85 offen offset:2048 sc1\n"
"v_mfma_f32_16x16x32_bf16 a[200:203], a[72:75], a[128:131], a[200:203]\n"
"ds_read_b128 v[136:139], v157 offset:20480\n"
"ds_read_b128 v[140:143], v157 offset:24576\n"
"v_mfma_f32_16x16x32_bf16 a[200:203], a[92:95], a[132:135], a[200:203]\n"
"ds_read_b128 v[144:147], v157 offset:28672\n"
"ds_read_b128 v[148:151], v157 offset:32768\n"
"v_mfma_f32_16x16x32_bf16 a[204:207], a[56:59], v[32:35], a[204:207]\n"
"ds_read_b128 v[152:155], v157 offset:36864\n"
"buffer_load_dwordx4 v[24:27], v2, s[20:23], s85 offen offset:3072 sc1\n"
"v_mfma_f32_16x16x32_bf16 a[204:207], a[76:79], a[128:131], a[204:207]\n"
"v_mfma_f32_16x16x32_bf16 a[204:207], a[96:99], a[132:135], a[204:207]\n"
"s_waitcnt vmcnt(12)\n"
"v_mfma_f32_16x16x32_bf16 a[188:191], a[100:103], a[136:139], a[188:191]\n"
"s_add_u32 s85, s85, 4096\n"
"v_mfma_f32_16x16x32_bf16 a[188:191], v[236:239], a[140:143], a[188:191]\n"
"v_mfma_f32_16x16x32_bf16 a[188:191], v[216:219], a[144:147], a[188:191]\n"
"buffer_load_dwordx4 v[28:31], v2, s[20:23], s85 offen offset:0 sc1\n"
"v_mfma_f32_16x16x32_bf16 a[192:195], v[252:255], a[136:139], a[192:195]\n"
"v_mfma_f32_16x16x32_bf16 a[192:195], v[232:235], a[140:143], a[192:195]\n"
"s_waitcnt lgkmcnt(4)\n"
"v_add_f32 v116, v116, v136\n"
"v_add_f32 v117, v117, v137\n"
"v_mfma_f32_16x16x32_bf16 a[192:195], v[212:215], a[144:147], a[192:195]\n"
"v_add_f32 v118, v118, v138\n"
"v_add_f32 v119, v119, v139\n"
"v_mfma_f32_16x16x32_bf16 a[196:199], v[248:251], a[136:139], a[196:199]\n"
"buffer_load_dwordx4 v[32:35], v2, s[20:23], s85 offen offset:1024 sc1\n"
"s_waitcnt lgkmcnt(3)\n"
"v_add_f32 v120, v120, v140\n"
"v_mfma_f32_16x16x32_bf16 a[196:199], v[228:231], a[140:143], a[196:199]\n"
"v_add_f32 v121, v121, v141\n"
"v_add_f32 v122, v122, v142\n"
"v_mfma_f32_16x16x32_bf16 a[196:199], v[208:211], a[144:147], a[196:199]\n"
"v_add_f32 v123, v123, v143\n"
"s_waitcnt lgkmcnt(2)\n"
"v_add_f32 v124, v124, v144\n"
"v_mfma_f32_16x16x32_bf16 a[200:203], v[244:247], a[136:139], a[200:203]\n"
"ds_write_b128 v15, a[188:191] offset:40960\n"
"v_mfma_f32_16x16x32_bf16 a[200:203], v[224:227], a[140:143], a[200:203]\n"
"buffer_load_dwordx4 a[208:211], v2, s[20:23], s85 offen offset:2048 sc1\n"
"v_add_f32 v125, v125, v145\n"
"v_mfma_f32_16x16x32_bf16 a[200:203], v[204:207], a[144:147], a[200:203]\n"
"v_add_f32 v126, v126, v146\n"
"v_add_f32 v127, v127, v147\n"
"v_mfma_f32_16x16x32_bf16 a[204:207], v[240:243], a[136:139], a[204:207]\n"
"ds_write_b128 v15, a[192:195] offset:45056\n"
"v_mfma_f32_16x16x32_bf16 a[204:207], v[220:223], a[140:143], a[204:207]\n"
"s_waitcnt lgkmcnt(3)\n"
"v_add_f32 v128, v128, v148\n"
"v_add_f32 v129, v129, v149\n"
"v_mfma_f32_16x16x32_bf16 a[204:207], v[200:203], a[144:147], a[204:207]\n"
"v_add_f32 v130, v130, v150\n"
"s_waitcnt vmcnt(13)\n"
"v_mfma_f32_16x16x32_bf16 v[76:79], a[0:3], a[148:151], 0\n"
"ds_write_b128 v15, a[196:199] offset:49152\n"
"v_mfma_f32_16x16x32_bf16 v[76:79], a[20:23], a[152:155], v[76:79]\n"
"buffer_load_dwordx4 a[212:215], v2, s[20:23], s85 offen offset:3072 sc1\n"
"s_xor_b32 s85, s3, 2\n"
"v_mfma_f32_16x16x32_bf16 v[80:83], a[4:7], a[148:151], 0\n"
"s_mul_i32 s85, s85, 116736\n"
"s_add_u32 s85, s85, s44\n"
"v_mfma_f32_16x16x32_bf16 v[80:83], a[24:27], a[152:155], v[80:83]\n"
"ds_write_b128 v15, a[200:203] offset:53248\n"
"v_mfma_f32_16x16x32_bf16 v[84:87], a[8:11], a[148:151], 0\n"
"buffer_load_dwordx4 a[216:219], v2, s[20:23], s85 offen offset:0 sc1\n"
"v_add_f32 v131, v131, v151\n"
"v_mfma_f32_16x16x32_bf16 v[84:87], a[28:31], a[152:155], v[84:87]\n"
"s_waitcnt lgkmcnt(4)\n"
"v_add_f32 v132, v132, v152\n"
"v_add_f32 v133, v133, v153\n"
"v_mfma_f32_16x16x32_bf16 v[88:91], a[12:15], a[148:151], 0\n"
"ds_write_b128 v15, a[204:207] offset:57344\n"
"v_mfma_f32_16x16x32_bf16 v[88:91], a[32:35], a[152:155], v[88:91]\n"
"v_add_f32 v134, v134, v154\n"
"v_add_f32 v135, v135, v155\n"
"v_mfma_f32_16x16x32_bf16 v[92:95], a[16:19], a[148:151], 0\n"
"buffer_load_dwordx4 a[220:223], v2, s[20:23], s85 offen offset:1024 sc1\n"
"v_mfma_f32_16x16x32_bf16 v[92:95], a[36:39], a[152:155], v[92:95]\n"
"s_waitcnt vmcnt(13)\n"
"v_mfma_f32_16x16x32_bf16 v[76:79], a[40:43], a[156:159], v[76:79]\n"
"v_mfma_f32_16x16x32_bf16 v[76:79], a[60:63], a[160:163], v[76:79]\n"
"v_mfma_f32_16x16x32_bf16 v[76:79], a[80:83], a[164:167], v[76:79]\n"
"buffer_load_dwordx4 a[224:227], v2, s[20:23], s85 offen offset:2048 sc1\n"
"v_mfma_f32_16x16x32_bf16 v[80:83], a[44:47], a[156:159], v[80:83]\n"
"v_mfma_f32_16x16x32_bf16 v[80:83], a[64:67], a[160:163], v[80:83]\n"
"s_waitcnt lgkmcnt(0)\n"
"s_barrier\n"
"v_mfma_f32_16x16x32_bf16 v[80:83], a[84:87], a[164:167], v[80:83]\n"
"ds_read_b128 v[56:59], v158 offset:40960\n"
"ds_read_b128 v[60:63], v158 offset:45056\n"
"v_mfma_f32_16x16x32_bf16 v[84:87], a[48:51], a[156:159], v[84:87]\n"
"ds_read_b128 v[64:67], v158 offset:49152\n"
"ds_read_b128 v[68:71], v158 offset:53248\n"
"v_mfma_f32_16x16x32_bf16 v[84:87], a[68:71], a[160:163], v[84:87]\n"
"ds_read_b128 v[72:75], v158 offset:57344\n"
"v_mfma_f32_16x16x32_bf16 v[84:87], a[88:91], a[164:167], v[84:87]\n"
"v_mfma_f32_16x16x32_bf16 v[88:91], a[52:55], a[156:159], v[88:91]\n"
"v_mfma_f32_16x16x32_bf16 v[88:91], a[72:75], a[160:163], v[88:91]\n"
"v_mfma_f32_16x16x32_bf16 v[88:91], a[92:95], a[164:167], v[88:91]\n"
"v_mfma_f32_16x16x32_bf16 v[92:95], a[56:59], a[156:159], v[92:95]\n"
"v_mfma_f32_16x16x32_bf16 v[92:95], a[76:79], a[160:163], v[92:95]\n"
"v_mfma_f32_16x16x32_bf16 v[92:95], a[96:99], a[164:167], v[92:95]\n"
"s_waitcnt vmcnt(11)\n"
"v_mfma_f32_16x16x32_bf16 v[76:79], a[100:103], a[168:171], v[76:79]\n"
"buffer_load_dwordx4 a[188:191], v2, s[20:23], s85 offen offset:3072 sc1\n"
"v_mfma_f32_16x16x32_bf16 v[76:79], v[236:239], a[172:175], v[76:79]\n"
"v_mfma_f32_16x16x32_bf16 v[76:79], v[216:219], a[176:179], v[76:79]\n"
"s_add_u32 s85, s85, 4096\n"
"v_mfma_f32_16x16x32_bf16 v[80:83], v[252:255], a[168:171], v[80:83]\n"
"v_mfma_f32_16x16x32_bf16 v[80:83], v[232:235], a[172:175], v[80:83]\n"
"buffer_load_dwordx4 a[192:195], v2, s[20:23], s85 offen offset:0 sc1\n"
"v_mfma_f32_16x16x32_bf16 v[80:83], v[212:215], a[176:179], v[80:83]\n"
"v_mfma_f32_16x16x32_bf16 v[84:87], v[248:251], a[168:171], v[84:87]\n"
"v_mfma_f32_16x16x32_bf16 v[84:87], v[228:231], a[172:175], v[84:87]\n"
"s_nop 0\n"
"v_add_f32 v116, v116, v76\n"
"v_add_f32 v117, v117, v77\n"
"v_mfma_f32_16x16x32_bf16 v[84:87], v[208:211], a[176:179], v[84:87]\n"
"buffer_load_dwordx4 a[196:199], v2, s[20:23], s85 offen offset:1024 sc1\n"
"v_add_f32 v118, v118, v78\n"
"v_mfma_f32_16x16x32_bf16 v[88:91], v[244:247], a[168:171], v[88:91]\n"
"v_add_f32 v119, v119, v79\n"
"s_waitcnt lgkmcnt(4)\n"
"v_add_f32 v116, v116, v56\n"
"v_mfma_f32_16x16x32_bf16 v[88:91], v[224:227], a[172:175], v[88:91]\n"
"v_add_f32 v117, v117, v57\n"
"v_add_f32 v118, v118, v58\n"
"v_mfma_f32_16x16x32_bf16 v[88:91], v[204:207], a[176:179], v[88:91]\n"
"v_add_f32 v119, v119, v59\n"
"v_cvt_pk_bf16_f32 v96, v116, v117\n"
"v_mfma_f32_16x16x32_bf16 v[92:95], v[240:243], a[168:171], v[92:95]\n"
"v_cvt_pk_bf16_f32 v97, v118, v16\n"
"v_add_f32 v120, v120, v80\n"
"v_mfma_f32_16x16x32_bf16 v[92:95], v[220:223], a[172:175], v[92:95]\n"
"v_add_f32 v121, v121, v81\n"
"v_add_f32 v122, v122, v82\n"
"v_mfma_f32_16x16x32_bf16 v[92:95], v[200:203], a[176:179], v[92:95]\n"
"v_add_f32 v123, v123, v83\n"
"s_waitcnt vmcnt(12)\n"
"v_mfma_f32_16x16x32_bf16 a[128:131], a[0:3], a[180:183], 0\n"
"s_waitcnt lgkmcnt(3)\n"
"v_add_f32 v120, v120, v60\n"
"v_add_f32 v121, v121, v61\n"
"v_mfma_f32_16x16x32_bf16 a[128:131], a[20:23], a[184:187], a[128:131]\n"
"v_add_f32 v122, v122, v62\n"
"v_add_f32 v123, v123, v63\n"
"v_mfma_f32_16x16x32_bf16 a[132:135], a[4:7], a[180:183], 0\n"
"v_cvt_pk_bf16_f32 v98, v119, v120\n"
"v_cvt_pk_bf16_f32 v99, v121, v16\n"
"v_mfma_f32_16x16x32_bf16 a[132:135], a[24:27], a[184:187], a[132:135]\n"
"buffer_store_dwordx4 v[96:99], v2, s[20:23], s60 offen offset:0 sc1\n"
"v_mfma_f32_16x16x32_bf16 a[136:139], a[8:11], a[180:183], 0\n"
"v_add_f32 v124, v124, v84\n"
"v_add_f32 v125, v125, v85\n"
"v_mfma_f32_16x16x32_bf16 a[136:139], a[28:31], a[184:187], a[136:139]\n"
"v_add_f32 v126, v126, v86\n"
"v_add_f32 v127, v127, v87\n"
"v_mfma_f32_16x16x32_bf16 a[140:143], a[12:15], a[180:183], 0\n"
"s_waitcnt lgkmcnt(2)\n"
"v_add_f32 v124, v124, v64\n"
"v_add_f32 v125, v125, v65\n"
"v_mfma_f32_16x16x32_bf16 a[140:143], a[32:35], a[184:187], a[140:143]\n"
"v_add_f32 v126, v126, v66\n"
"v_add_f32 v127, v127, v67\n"
"v_mfma_f32_16x16x32_bf16 a[144:147], a[16:19], a[180:183], 0\n"
"v_cvt_pk_bf16_f32 v100, v122, v123\n"
"v_cvt_pk_bf16_f32 v101, v124, v16\n"
"v_mfma_f32_16x16x32_bf16 a[144:147], a[36:39], a[184:187], a[144:147]\n"
"v_cvt_pk_bf16_f32 v102, v125, v126\n"
"s_waitcnt vmcnt(10)\n"
"v_mfma_f32_16x16x32_bf16 a[128:131], a[40:43], v[20:23], a[128:131]\n"
"buffer_load_dwordx4 a[200:203], v2, s[20:23], s85 offen offset:2048 sc1\n"
"v_cvt_pk_bf16_f32 v103, v127, v16\n"
"v_mfma_f32_16x16x32_bf16 a[128:131], a[60:63], v[24:27], a[128:131]\n"
"buffer_store_dwordx4 v[100:103], v2, s[20:23], s60 offen offset:1024 sc1\n"
"v_mfma_f32_16x16x32_bf16 a[128:131], a[80:83], v[28:31], a[128:131]\n"
"v_add_f32 v128, v128, v88\n"
"v_add_f32 v129, v129, v89\n"
"v_mfma_f32_16x16x32_bf16 a[132:135], a[44:47], v[20:23], a[132:135]\n"
"v_add_f32 v130, v130, v90\n"
"v_add_f32 v131, v131, v91\n"
"v_mfma_f32_16x16x32_bf16 a[132:135], a[64:67], v[24:27], a[132:135]\n"
"s_waitcnt lgkmcnt(1)\n"
"v_add_f32 v128, v128, v68\n"
"v_add_f32 v129, v129, v69\n"
"v_mfma_f32_16x16x32_bf16 a[132:135], a[84:87], v[28:31], a[132:135]\n"
"v_add_f32 v130, v130, v70\n"
"v_add_f32 v131, v131, v71\n"
"v_mfma_f32_16x16x32_bf16 a[136:139], a[48:51], v[20:23], a[136:139]\n"
"v_cvt_pk_bf16_f32 v104, v128, v129\n"
"v_cvt_pk_bf16_f32 v105, v130, v16\n"
"v_mfma_f32_16x16x32_bf16 a[136:139], a[68:71], v[24:27], a[136:139]\n"
"v_add_f32 v132, v132, v92\n"
"v_add_f32 v133, v133, v93\n"
"v_mfma_f32_16x16x32_bf16 a[136:139], a[88:91], v[28:31], a[136:139]\n"
"v_add_f32 v134, v134, v94\n"
"v_add_f32 v135, v135, v95\n"
"v_mfma_f32_16x16x32_bf16 a[140:143], a[52:55], v[20:23], a[140:143]\n"
"buffer_load_dwordx4 a[204:207], v2, s[20:23], s85 offen offset:3072 sc1\n"
"s_waitcnt lgkmcnt(0)\n"
"v_add_f32 v132, v132, v72\n"
"v_mfma_f32_16x16x32_bf16 a[140:143], a[72:75], v[24:27], a[140:143]\n"
"v_add_f32 v133, v133, v73\n"
"v_add_f32 v134, v134, v74\n"
"v_mfma_f32_16x16x32_bf16 a[140:143], a[92:95], v[28:31], a[140:143]\n"
"v_add_f32 v135, v135, v75\n"
"v_cvt_pk_bf16_f32 v106, v131, v132\n"
"v_mfma_f32_16x16x32_bf16 a[144:147], a[56:59], v[20:23], a[144:147]\n"
"v_cvt_pk_bf16_f32 v107, v133, v16\n"
"v_cvt_pk_bf16_f32 v108, v134, v135\n"
"v_mfma_f32_16x16x32_bf16 a[144:147], a[76:79], v[24:27], a[144:147]\n"
"buffer_store_dwordx4 v[104:107], v2, s[20:23], s60 offen offset:2048 sc1\n"
"v_mfma_f32_16x16x32_bf16 a[144:147], a[96:99], v[28:31], a[144:147]\n"
"s_waitcnt vmcnt(11)\n"
"s_xor_b32 s85, s3, 1\n"
"v_mfma_f32_16x16x32_bf16 a[128:131], a[100:103], v[32:35], a[128:131]\n"
"s_mul_i32 s85, s85, 116736\n"
"s_add_u32 s85, s85, s44\n"
"v_mfma_f32_16x16x32_bf16 a[128:131], v[236:239], a[208:211], a[128:131]\n"
"buffer_load_dwordx4 v[20:23], v2, s[20:23], s85 offen offset:0 sc1\n"
"v_cvt_pk_bf16_f32 v109, v135, v16\n"
"v_mfma_f32_16x16x32_bf16 a[128:131], v[216:219], a[212:215], a[128:131]\n"
"buffer_store_dwordx2 v[108:109], v9, s[20:23], s60 offen offset:3072 sc1\n"
"v_mfma_f32_16x16x32_bf16 a[132:135], v[252:255], v[32:35], a[132:135]\n"
"v_lshlrev_b32 v76, 16, v44\n"
"v_lshlrev_b32 v78, 16, v45\n"
"v_mfma_f32_16x16x32_bf16 a[132:135], v[232:235], a[208:211], a[132:135]\n"
"buffer_load_dwordx4 v[24:27], v2, s[20:23], s85 offen offset:1024 sc1\n"
"v_lshlrev_b32 v80, 16, v46\n"
"v_mfma_f32_16x16x32_bf16 a[132:135], v[212:215], a[212:215], a[132:135]\n"
"v_lshlrev_b32 v82, 16, v47\n"
"v_lshlrev_b32 v84, 16, v48\n"
"v_mfma_f32_16x16x32_bf16 a[136:139], v[248:251], v[32:35], a[136:139]\n"
"v_lshlrev_b32 v86, 16, v49\n"
"v_lshlrev_b32 v88, 16, v50\n"
"v_mfma_f32_16x16x32_bf16 a[136:139], v[228:231], a[208:211], a[136:139]\n"
"v_lshlrev_b32 v90, 16, v51\n"
"v_lshlrev_b32 v92, 16, v52\n"
"v_mfma_f32_16x16x32_bf16 a[136:139], v[208:211], a[212:215], a[136:139]\n"
"buffer_load_dwordx4 v[28:31], v2, s[20:23], s85 offen offset:2048 sc1\n"
"v_lshlrev_b32 v94, 16, v53\n"
"v_mfma_f32_16x16x32_bf16 a[140:143], v[244:247], v[32:35], a[140:143]\n"
"v_and_b32 v77, 0xffff0000, v44\n"
"v_and_b32 v79, 0xffff0000, v45\n"
"v_mfma_f32_16x16x32_bf16 a[140:143], v[224:227], a[208:211], a[140:143]\n"
"v_and_b32 v81, 0xffff0000, v46\n"
"v_and_b32 v83, 0xffff0000, v47\n"
"v_mfma_f32_16x16x32_bf16 a[140:143], v[204:207], a[212:215], a[140:143]\n"
"v_and_b32 v85, 0xffff0000, v48\n"
"v_and_b32 v87, 0xffff0000, v49\n"
"v_mfma_f32_16x16x32_bf16 a[144:147], v[240:243], v[32:35], a[144:147]\n"
"v_and_b32 v89, 0xffff0000, v50\n"
"v_and_b32 v91, 0xffff0000, v51\n"
"v_mfma_f32_16x16x32_bf16 a[144:147], v[220:223], a[208:211], a[144:147]\n"
"v_and_b32 v93, 0xffff0000, v52\n"
"v_and_b32 v95, 0xffff0000, v53\n"
"v_mfma_f32_16x16x32_bf16 a[144:147], v[200:203], a[212:215], a[144:147]\n"
"v_add_u32 v14, 0, v1\n"
"s_waitcnt vmcnt(13)\n"
"v_mfma_f32_16x16x32_bf16 a[148:151], a[0:3], a[216:219], 0\n"
"buffer_load_dwordx4 v[32:35], v2, s[20:23], s85 offen offset:3072 sc1\n"
"s_add_u32 s85, s85, 4096\n"
"v_mfma_f32_16x16x32_bf16 a[148:151], a[20:23], a[220:223], a[148:151]\n"
"v_mul_u32_u24 v17, 6554, v14\n"
"v_lshrrev_b32 v17, 16, v17\n"
"v_mfma_f32_16x16x32_bf16 a[152:155], a[4:7], a[216:219], 0\n"
"buffer_load_dwordx4 a[208:211], v2, s[20:23], s85 offen offset:0 sc1\n"
"v_add_u32 v14, v14, v17\n"
"v_mfma_f32_16x16x32_bf16 a[152:155], a[24:27], a[220:223], a[152:155]\n"
"v_lshl_add_u32 v17, v14, 4, s62\n"
"v_add_u32 v14, 64, v1\n"
"v_mfma_f32_16x16x32_bf16 a[156:159], a[8:11], a[216:219], 0\n"
"v_mul_u32_u24 v18, 6554, v14\n"
"v_lshrrev_b32 v18, 16, v18\n"
"v_mfma_f32_16x16x32_bf16 a[156:159], a[28:31], a[220:223], a[156:159]\n"
"v_add_u32 v14, v14, v18\n"
"v_lshl_add_u32 v18, v14, 4, s62\n"
"v_mfma_f32_16x16x32_bf16 a[160:163], a[12:15], a[216:219], 0\n"
"buffer_load_dwordx4 a[212:215], v2, s[20:23], s85 offen offset:1024 sc1\n"
"v_add_u32 v14, 128, v1\n"
"v_mfma_f32_16x16x32_bf16 a[160:163], a[32:35], a[220:223], a[160:163]\n"
"v_mul_u32_u24 v19, 6554, v14\n"
"v_lshrrev_b32 v19, 16, v19\n"
"v_mfma_f32_16x16x32_bf16 a[164:167], a[16:19], a[216:219], 0\n"
"v_add_u32 v14, v14, v19\n"
"v_lshl_add_u32 v19, v14, 4, s62\n"
"v_mfma_f32_16x16x32_bf16 a[164:167], a[36:39], a[220:223], a[164:167]\n"
"s_waitcnt vmcnt(13)\n"
"v_mfma_f32_16x16x32_bf16 a[148:151], a[40:43], a[224:227], a[148:151]\n"
"buffer_load_dwordx4 v[136:139], v2, s[20:23], s85 offen offset:2048 sc1\n"
"v_mfma_f32_16x16x32_bf16 a[148:151], a[60:63], a[188:191], a[148:151]\n"
"v_mfma_f32_16x16x32_bf16 a[148:151], a[80:83], a[192:195], a[148:151]\n"
"v_mfma_f32_16x16x32_bf16 a[152:155], a[44:47], a[224:227], a[152:155]\n"
"v_mfma_f32_16x16x32_bf16 a[152:155], a[64:67], a[188:191], a[152:155]\n"
"buffer_load_dwordx4 v[140:143], v2, s[20:23], s85 offen offset:3072 sc1\n"
"v_mfma_f32_16x16x32_bf16 a[152:155], a[84:87], a[192:195], a[152:155]\n"
"v_mfma_f32_16x16x32_bf16 a[156:159], a[48:51], a[224:227], a[156:159]\n"
"v_mfma_f32_16x16x32_bf16 a[156:159], a[68:71], a[188:191], a[156:159]\n"
"v_mfma_f32_16x16x32_bf16 a[156:159], a[88:91], a[192:195], a[156:159]\n"
"v_mfma_f32_16x16x32_bf16 a[160:163], a[52:55], a[224:227], a[160:163]\n"
"buffer_load_dword v12, v11, s[16:19], 0 offen sc1\n"
"s_xor_b32 s85, s3, 0\n"
"v_mfma_f32_16x16x32_bf16 a[160:163], a[72:75], a[188:191], a[160:163]\n"
"s_mul_i32 s85, s85, 116736\n"
"s_add_u32 s85, s85, s44\n"
"v_mfma_f32_16x16x32_bf16 a[160:163], a[92:95], a[192:195], a[160:163]\n"
"buffer_load_dwordx4 v[144:147], v2, s[20:23], s85 offen offset:0 sc1\n"
"v_mfma_f32_16x16x32_bf16 a[164:167], a[56:59], a[224:227], a[164:167]\n"
"v_mfma_f32_16x16x32_bf16 a[164:167], a[76:79], a[188:191], a[164:167]\n"
"v_mfma_f32_16x16x32_bf16 a[164:167], a[96:99], a[192:195], a[164:167]\n"
"s_waitcnt vmcnt(12)\n"
"v_mfma_f32_16x16x32_bf16 a[148:151], a[100:103], a[196:199], a[148:151]\n"
"v_mfma_f32_16x16x32_bf16 a[148:151], v[236:239], a[200:203], a[148:151]\n"
"buffer_load_dwordx4 v[148:151], v2, s[20:23], s85 offen offset:1024 sc1\n"
"v_mfma_f32_16x16x32_bf16 a[148:151], v[216:219], a[204:207], a[148:151]\n"
"v_mfma_f32_16x16x32_bf16 a[152:155], v[252:255], a[196:199], a[152:155]\n"
"v_mfma_f32_16x16x32_bf16 a[152:155], v[232:235], a[200:203], a[152:155]\n"
"v_mfma_f32_16x16x32_bf16 a[152:155], v[212:215], a[204:207], a[152:155]\n"
"buffer_load_dwordx4 v[152:155], v2, s[20:23], s85 offen offset:2048 sc1\n"
"v_mfma_f32_16x16x32_bf16 a[156:159], v[248:251], a[196:199], a[156:159]\n"
"v_mfma_f32_16x16x32_bf16 a[156:159], v[228:231], a[200:203], a[156:159]\n"
"v_mfma_f32_16x16x32_bf16 a[156:159], v[208:211], a[204:207], a[156:159]\n"
"v_mfma_f32_16x16x32_bf16 a[160:163], v[244:247], a[196:199], a[160:163]\n"
"buffer_load_dwordx4 a[216:219], v2, s[20:23], s85 offen offset:3072 sc1\n"
"v_mfma_f32_16x16x32_bf16 a[160:163], v[224:227], a[200:203], a[160:163]\n"
"v_mfma_f32_16x16x32_bf16 a[160:163], v[204:207], a[204:207], a[160:163]\n"
"s_add_u32 s85, s85, 4096\n"
"v_mfma_f32_16x16x32_bf16 a[164:167], v[240:243], a[196:199], a[164:167]\n"
"v_mfma_f32_16x16x32_bf16 a[164:167], v[220:223], a[200:203], a[164:167]\n"
"buffer_load_dwordx4 a[220:223], v2, s[20:23], s85 offen offset:0 sc1\n"
"v_mfma_f32_16x16x32_bf16 a[164:167], v[200:203], a[204:207], a[164:167]\n"
"s_waitcnt vmcnt(12)\n"
"v_mfma_f32_16x16x32_bf16 a[168:171], a[0:3], v[20:23], 0\n"
"v_mfma_f32_16x16x32_bf16 a[168:171], a[20:23], v[24:27], a[168:171]\n"
"v_mfma_f32_16x16x32_bf16 a[172:175], a[4:7], v[20:23], 0\n"
"v_mfma_f32_16x16x32_bf16 a[172:175], a[24:27], v[24:27], a[172:175]\n"
"buffer_load_dwordx4 a[188:191], v2, s[20:23], s85 offen offset:1024 sc1\n"
"v_mfma_f32_16x16x32_bf16 a[176:179], a[8:11], v[20:23], 0\n"
"v_mfma_f32_16x16x32_bf16 a[176:179], a[28:31], v[24:27], a[176:179]\n"
"v_mfma_f32_16x16x32_bf16 a[180:183], a[12:15], v[20:23], 0\n"
"v_mfma_f32_16x16x32_bf16 a[180:183], a[32:35], v[24:27], a[180:183]\n"
"buffer_load_dwordx4 a[192:195], v2, s[20:23], s85 offen offset:2048 sc1\n"
"v_mfma_f32_16x16x32_bf16 a[184:187], a[16:19], v[20:23], 0\n"
"v_mfma_f32_16x16x32_bf16 a[184:187], a[36:39], v[24:27], a[184:187]\n"
"s_waitcnt vmcnt(11)\n"
"v_mfma_f32_16x16x32_bf16 a[168:171], a[40:43], v[28:31], a[168:171]\n"
"v_mfma_f32_16x16x32_bf16 a[168:171], a[60:63], v[32:35], a[168:171]\n"
"buffer_load_dwordx4 a[224:227], v2, s[20:23], s85 offen offset:3072 sc1\n"
"v_mfma_f32_16x16x32_bf16 a[168:171], a[80:83], a[208:211], a[168:171]\n"
"v_mfma_f32_16x16x32_bf16 a[172:175], a[44:47], v[28:31], a[172:175]\n"
"v_mfma_f32_16x16x32_bf16 a[172:175], a[64:67], v[32:35], a[172:175]\n"
"v_mfma_f32_16x16x32_bf16 a[172:175], a[84:87], a[208:211], a[172:175]\n"
"v_mfma_f32_16x16x32_bf16 a[176:179], a[48:51], v[28:31], a[176:179]\n"
"v_mfma_f32_16x16x32_bf16 a[176:179], a[68:71], v[32:35], a[176:179]\n"
"v_mfma_f32_16x16x32_bf16 a[176:179], a[88:91], a[208:211], a[176:179]\n"
"v_mfma_f32_16x16x32_bf16 a[180:183], a[52:55], v[28:31], a[180:183]\n"
"v_mfma_f32_16x16x32_bf16 a[180:183], a[72:75], v[32:35], a[180:183]\n"
"v_mfma_f32_16x16x32_bf16 a[180:183], a[92:95], a[208:211], a[180:183]\n"
"v_mfma_f32_16x16x32_bf16 a[184:187], a[56:59], v[28:31], a[184:187]\n"
"v_mfma_f32_16x16x32_bf16 a[184:187], a[76:79], v[32:35], a[184:187]\n"
"v_mfma_f32_16x16x32_bf16 a[184:187], a[96:99], a[208:211], a[184:187]\n"
"s_waitcnt vmcnt(9)\n"
"v_mfma_f32_16x16x32_bf16 a[168:171], a[100:103], a[212:215], a[168:171]\n"
"v_mfma_f32_16x16x32_bf16 a[168:171], v[236:239], v[136:139], a[168:171]\n"
"v_mfma_f32_16x16x32_bf16 a[168:171], v[216:219], v[140:143], a[168:171]\n"
"v_mfma_f32_16x16x32_bf16 a[172:175], v[252:255], a[212:215], a[172:175]\n"
"s_waitcnt vmcnt(8)\n"
"v_mfma_f32_16x16x32_bf16 a[172:175], v[232:235], v[136:139], a[172:175]\n"
"v_cmp_ne_u32 vcc, s52, v12\n"
"v_mfma_f32_16x16x32_bf16 a[172:175], v[212:215], v[140:143], a[172:175]\n"
"s_cbranch_vccnz .Lfb_slow_22\n"
".Lfb_back_23:\n"
"v_mfma_f32_16x16x32_bf16 a[176:179], v[248:251], a[212:215], a[176:179]\n"
"s_xor_b32 s85, s3, 3\n"
"s_mul_i32 s85, s85, 116736\n"
"v_mfma_f32_16x16x32_bf16 a[176:179], v[228:231], v[136:139], a[176:179]\n"
"s_add_u32 s85, s85, s84\n"
"v_mfma_f32_16x16x32_bf16 a[176:179], v[208:211], v[140:143], a[176:179]\n"
"buffer_load_dwordx4 a[196:199], v2, s[20:23], s85 offen offset:0 sc1\n"
"v_mfma_f32_16x16x32_bf16 a[180:183], v[244:247], a[212:215], a[180:183]\n"
"v_mfma_f32_16x16x32_bf16 a[180:183], v[224:227], v[136:139], a[180:183]\n"
"buffer_load_dwordx4 a[200:203], v2, s[20:23], s85 offen offset:1024 sc1\n"
"v_mfma_f32_16x16x32_bf16 a[180:183], v[204:207], v[140:143], a[180:183]\n"
"v_mfma_f32_16x16x32_bf16 a[184:187], v[240:243], a[212:215], a[184:187]\n"
"v_mfma_f32_16x16x32_bf16 a[184:187], v[220:223], v[136:139], a[184:187]\n"
"v_mfma_f32_16x16x32_bf16 a[184:187], v[200:203], v[140:143], a[184:187]\n"
"buffer_load_dwordx4 a[204:207], v2, s[20:23], s85 offen offset:2048 sc1\n"
"s_waitcnt vmcnt(9)\n"
"v_mfma_f32_16x16x32_bf16 v[96:99], a[0:3], v[144:147], 0\n"
"v_mfma_f32_16x16x32_bf16 v[96:99], a[20:23], v[148:151], v[96:99]\n"
"buffer_load_dwordx4 v[20:23], v2, s[20:23], s85 offen offset:3072 sc1\n"
"s_add_u32 s85, s85, 4096\n"
"v_mfma_f32_16x16x32_bf16 v[100:103], a[4:7], v[144:147], 0\n"
"v_mfma_f32_16x16x32_bf16 v[100:103], a[24:27], v[148:151], v[100:103]\n"
"buffer_load_dwordx4 v[24:27], v2, s[20:23], s85 offen offset:0 sc1\n"
"v_mfma_f32_16x16x32_bf16 v[104:107], a[8:11], v[144:147], 0\n"
"s_xor_b32 s85, s3, 2\n"
"s_mul_i32 s85, s85, 116736\n"
"v_mfma_f32_16x16x32_bf16 v[104:107], a[28:31], v[148:151], v[104:107]\n"
"s_add_u32 s85, s85, s84\n"
"v_mfma_f32_16x16x32_bf16 v[108:111], a[12:15], v[144:147], 0\n"
"v_mfma_f32_16x16x32_bf16 v[108:111], a[32:35], v[148:151], v[108:111]\n"
"buffer_load_dwordx4 v[28:31], v2, s[20:23], s85 offen offset:0 sc1\n"
"v_mfma_f32_16x16x32_bf16 v[112:115], a[16:19], v[144:147], 0\n"
"v_mfma_f32_16x16x32_bf16 v[112:115], a[36:39], v[148:151], v[112:115]\n"
"s_waitcnt vmcnt(9)\n"
"v_mfma_f32_16x16x32_bf16 v[96:99], a[40:43], v[152:155], v[96:99]\n"
"v_mfma_f32_16x16x32_bf16 v[96:99], a[60:63], a[216:219], v[96:99]\n"
"buffer_load_dwordx4 v[32:35], v2, s[20:23], s85 offen offset:1024 sc1\n"
"v_mfma_f32_16x16x32_bf16 v[96:99], a[80:83], a[220:223], v[96:99]\n"
"v_mfma_f32_16x16x32_bf16 v[100:103], a[44:47], v[152:155], v[100:103]\n"
"v_mfma_f32_16x16x32_bf16 v[100:103], a[64:67], a[216:219], v[100:103]\n"
"v_mfma_f32_16x16x32_bf16 v[100:103], a[84:87], a[220:223], v[100:103]\n"
"buffer_load_dwordx4 a[208:211], v2, s[20:23], s85 offen offset:2048 sc1\n"
"v_mfma_f32_16x16x32_bf16 v[104:107], a[48:51], v[152:155], v[104:107]\n"
"v_mfma_f32_16x16x32_bf16 v[104:107], a[68:71], a[216:219], v[104:107]\n"
"v_mfma_f32_16x16x32_bf16 v[104:107], a[88:91], a[220:223], v[104:107]\n"
"v_mfma_f32_16x16x32_bf16 v[108:111], a[52:55], v[152:155], v[108:111]\n"
"v_mfma_f32_16x16x32_bf16 v[108:111], a[72:75], a[216:219], v[108:111]\n"
"buffer_load_dwordx4 a[212:215], v2, s[20:23], s85 offen offset:3072 sc1\n"
"v_mfma_f32_16x16x32_bf16 v[108:111], a[92:95], a[220:223], v[108:111]\n"
"v_mfma_f32_16x16x32_bf16 v[112:115], a[56:59], v[152:155], v[112:115]\n"
"v_mfma_f32_16x16x32_bf16 v[112:115], a[76:79], a[216:219], v[112:115]\n"
"v_mfma_f32_16x16x32_bf16 v[112:115], a[96:99], a[220:223], v[112:115]\n"
"s_waitcnt vmcnt(9)\n"
"s_add_u32 s85, s85, 4096\n"
"v_mfma_f32_16x16x32_bf16 v[96:99], a[100:103], a[188:191], v[96:99]\n"
"buffer_load_dwordx4 a[216:219], v2, s[20:23], s85 offen offset:0 sc1\n"
"v_mfma_f32_16x16x32_bf16 v[96:99], v[236:239], a[192:195], v[96:99]\n"
"s_xor_b32 s85, s3, 1\n"
"s_mul_i32 s85, s85, 116736\n"
"v_mfma_f32_16x16x32_bf16 v[96:99], v[216:219], a[224:227], v[96:99]\n"
"s_add_u32 s85, s85, s84\n"
"v_mfma_f32_16x16x32_bf16 v[100:103], v[252:255], a[188:191], v[100:103]\n"
"v_mfma_f32_16x16x32_bf16 v[100:103], v[232:235], a[192:195], v[100:103]\n"
"buffer_load_dwordx4 a[220:223], v2, s[20:23], s85 offen offset:0 sc1\n"
"v_mfma_f32_16x16x32_bf16 v[100:103], v[212:215], a[224:227], v[100:103]\n"
"v_mfma_f32_16x16x32_bf16 v[104:107], v[248:251], a[188:191], v[104:107]\n"
"v_mfma_f32_16x16x32_bf16 v[104:107], v[228:231], a[192:195], v[104:107]\n"
"v_mfma_f32_16x16x32_bf16 v[104:107], v[208:211], a[224:227], v[104:107]\n"
"v_mfma_f32_16x16x32_bf16 v[108:111], v[244:247], a[188:191], v[108:111]\n"
"v_mfma_f32_16x16x32_bf16 v[108:111], v[224:227], a[192:195], v[108:111]\n"
"v_mfma_f32_16x16x32_bf16 v[108:111], v[204:207], a[224:227], v[108:111]\n"
"v_mfma_f32_16x16x32_bf16 v[112:115], v[240:243], a[188:191], v[112:115]\n"
"v_mfma_f32_16x16x32_bf16 v[112:115], v[220:223], a[192:195], v[112:115]\n"
"v_mfma_f32_16x16x32_bf16 v[112:115], v[200:203], a[224:227], v[112:115]\n"
"s_waitcnt vmcnt(9)\n"
"v_mfma_f32_16x16x32_bf16 a[128:131], v[196:199], a[196:199], a[128:131]\n"
"buffer_load_dwordx4 a[188:191], v2, s[20:23], s85 offen offset:1024 sc1\n"
"v_mfma_f32_16x16x32_bf16 a[128:131], a[116:119], a[200:203], a[128:131]\n"
"v_mfma_f32_16x16x32_bf16 a[132:135], v[192:195], a[196:199], a[132:135]\n"
"v_mfma_f32_16x16x32_bf16 a[132:135], a[120:123], a[200:203], a[132:135]\n"
"v_mfma_f32_16x16x32_bf16 a[136:139], a[104:107], a[196:199], a[136:139]\n"
"buffer_load_dwordx4 a[192:195], v2, s[20:23], s85 offen offset:2048 sc1\n"
"v_mfma_f32_16x16x32_bf16 a[136:139], a[124:127], a[200:203], a[136:139]\n"
"v_mfma_f32_16x16x32_bf16 a[140:143], a[108:111], a[196:199], a[140:143]\n"
"v_mfma_f32_16x16x32_bf16 a[140:143], a[240:243], a[200:203], a[140:143]\n"
"v_mfma_f32_16x16x32_bf16 a[144:147], a[112:115], a[196:199], a[144:147]\n"
"buffer_load_dwordx4 a[224:227], v2, s[20:23], s85 offen offset:3072 sc1\n"
"v_mfma_f32_16x16x32_bf16 a[144:147], a[244:247], a[200:203], a[144:147]\n"
"s_waitcnt vmcnt(9)\n"
"s_add_u32 s85, s85, 4096\n"
"v_mfma_f32_16x16x32_bf16 a[128:131], a[248:251], a[204:207], a[128:131]\n"
"buffer_load_dwordx4 a[196:199], v2, s[20:23], s85 offen offset:0 sc1\n"
"v_mfma_f32_16x16x32_bf16 a[128:131], v[176:179], v[20:23], a[128:131]\n"
"s_xor_b32 s85, s3, 0\n"
"s_mul_i32 s85, s85, 116736\n"
"v_mfma_f32_16x16x32_bf16 a[128:131], a[228:231], v[24:27], a[128:131]\n"
"s_add_u32 s85, s85, s84\n"
"v_mfma_f32_16x16x32_bf16 a[132:135], a[252:255], a[204:207], a[132:135]\n"
"v_mfma_f32_16x16x32_bf16 a[132:135], v[172:175], v[20:23], a[132:135]\n"
"buffer_load_dwordx4 a[200:203], v2, s[20:23], s85 offen offset:0 sc1\n"
"v_mfma_f32_16x16x32_bf16 a[132:135], a[232:235], v[24:27], a[132:135]\n"
"v_mfma_f32_16x16x32_bf16 a[136:139], v[188:191], a[204:207], a[136:139]\n"
"v_mfma_f32_16x16x32_bf16 a[136:139], v[168:171], v[20:23], a[136:139]\n"
"v_mfma_f32_16x16x32_bf16 a[136:139], a[236:239], v[24:27], a[136:139]\n"
"buffer_load_dwordx4 v[116:119], v2, s[20:23], s40 offen offset:0 sc1\n"
"v_mfma_f32_16x16x32_bf16 a[140:143], v[184:187], a[204:207], a[140:143]\n"
"ds_write_b128 v15, a[128:131] offset:0\n"
"v_mfma_f32_16x16x32_bf16 a[140:143], v[164:167], v[20:23], a[140:143]\n"
"v_mfma_f32_16x16x32_bf16 a[140:143], v[36:39], v[24:27], a[140:143]\n"
"v_mfma_f32_16x16x32_bf16 a[144:147], v[180:183], a[204:207], a[144:147]\n"
"ds_write_b128 v15, a[132:135] offset:4096\n"
"v_mfma_f32_16x16x32_bf16 a[144:147], v[160:163], v[20:23], a[144:147]\n"
"buffer_load_dwordx4 v[120:123], v2, s[20:23], s40 offen offset:1024 sc1\n"
"v_mfma_f32_16x16x32_bf16 a[144:147], v[40:43], v[24:27], a[144:147]\n"
"s_waitcnt vmcnt(11)\n"
"v_mfma_f32_16x16x32_bf16 a[148:151], v[196:199], v[28:31], a[148:151]\n"
"ds_write_b128 v15, a[136:139] offset:8192\n"
"v_mfma_f32_16x16x32_bf16 a[148:151], a[116:119], v[32:35], a[148:151]\n"
"buffer_load_dwordx4 v[20:23], v2, s[20:23], s85 offen offset:1024 sc1\n"
"v_mfma_f32_16x16x32_bf16 a[152:155], v[192:195], v[28:31], a[152:155]\n"
"v_mfma_f32_16x16x32_bf16 a[152:155], a[120:123], v[32:35], a[152:155]\n"
"ds_write_b128 v15, a[140:143] offset:12288\n"
"v_mfma_f32_16x16x32_bf16 a[156:159], a[104:107], v[28:31], a[156:159]\n"
"buffer_load_dwordx4 v[124:127], v2, s[20:23], s40 offen offset:2048 sc1\n"
"v_mfma_f32_16x16x32_bf16 a[156:159], a[124:127], v[32:35], a[156:159]\n"
"v_mfma_f32_16x16x32_bf16 a[160:163], a[108:111], v[28:31], a[160:163]\n"
"ds_write_b128 v15, a[144:147] offset:16384\n"
"v_mfma_f32_16x16x32_bf16 a[160:163], a[240:243], v[32:35], a[160:163]\n"
"v_mfma_f32_16x16x32_bf16 a[164:167], a[112:115], v[28:31], a[164:167]\n"
"buffer_load_dwordx2 v[128:129], v9, s[20:23], s40 offen offset:3072 sc1\n"
"v_mfma_f32_16x16x32_bf16 a[164:167], a[244:247], v[32:35], a[164:167]\n"
"s_waitcnt vmcnt(11)\n"
"v_mfma_f32_16x16x32_bf16 a[148:151], a[248:251], a[208:211], a[148:151]\n"
"buffer_load_dwordx4 v[24:27], v2, s[20:23], s85 offen offset:2048 sc1\n"
"v_mfma_f32_16x16x32_bf16 a[148:151], v[176:179], a[212:215], a[148:151]\n"
"s_waitcnt lgkmcnt(0)\n"
"v_mfma_f32_16x16x32_bf16 a[148:151], a[228:231], a[216:219], a[148:151]\n"
"buffer_load_dwordx4 a[204:207], v2, s[20:23], s85 offen offset:3072 sc1\n"
"s_add_u32 s85, s85, 4096\n"
"v_mfma_f32_16x16x32_bf16 a[152:155], a[252:255], a[208:211], a[152:155]\n"
"v_mfma_f32_16x16x32_bf16 a[152:155], v[172:175], a[212:215], a[152:155]\n"
"buffer_load_dwordx4 v[28:31], v2, s[20:23], s85 offen offset:0 sc1\n"
"v_mfma_f32_16x16x32_bf16 a[152:155], a[232:235], a[216:219], a[152:155]\n"
"v_mfma_f32_16x16x32_bf16 a[156:159], v[188:191], a[208:211], a[156:159]\n"
"v_mfma_f32_16x16x32_bf16 a[156:159], v[168:171], a[212:215], a[156:159]\n"
"v_mfma_f32_16x16x32_bf16 a[156:159], a[236:239], a[216:219], a[156:159]\n"
"v_mfma_f32_16x16x32_bf16 a[160:163], v[184:187], a[208:211], a[160:163]\n"
"ds_write_b128 v15, a[148:151] offset:20480\n"
"v_mfma_f32_16x16x32_bf16 a[160:163], v[164:167], a[212:215], a[160:163]\n"
"v_mfma_f32_16x16x32_bf16 a[160:163], v[36:39], a[216:219], a[160:163]\n"
"v_mfma_f32_16x16x32_bf16 a[164:167], v[180:183], a[208:211], a[164:167]\n"
"ds_write_b128 v15, a[152:155] offset:24576\n"
"v_mfma_f32_16x16x32_bf16 a[164:167], v[160:163], a[212:215], a[164:167]\n"
"v_mfma_f32_16x16x32_bf16 a[164:167], v[40:43], a[216:219], a[164:167]\n"
"s_waitcnt vmcnt(12)\n"
"v_mfma_f32_16x16x32_bf16 a[168:171], v[196:199], a[220:223], a[168:171]\n"
"ds_write_b128 v15, a[156:159] offset:28672\n"
"v_mfma_f32_16x16x32_bf16 a[168:171], a[116:119], a[188:191], a[168:171]\n"
"v_mfma_f32_16x16x32_bf16 a[172:175], v[192:195], a[220:223], a[172:175]\n"
"v_mfma_f32_16x16x32_bf16 a[172:175], a[120:123], a[188:191], a[172:175]\n"
"ds_write_b128 v15, a[160:163] offset:32768\n"
"v_mfma_f32_16x16x32_bf16 a[176:179], a[104:107], a[220:223], a[176:179]\n"
"v_mfma_f32_16x16x32_bf16 a[176:179], a[124:127], a[188:191], a[176:179]\n"
"v_mfma_f32_16x16x32_bf16 a[180:183], a[108:111], a[220:223], a[180:183]\n"
"ds_write_b128 v15, a[164:167] offset:36864\n"
"v_mfma_f32_16x16x32_bf16 a[180:183], a[240:243], a[188:191], a[180:183]\n"
"v_mfma_f32_16x16x32_bf16 a[184:187], a[112:115], a[220:223], a[184:187]\n"
"v_mfma_f32_16x16x32_bf16 a[184:187], a[244:247], a[188:191], a[184:187]\n"
"s_waitcnt vmcnt(9)\n"
"v_mfma_f32_16x16x32_bf16 a[168:171], a[248:251], a[192:195], a[168:171]\n"
"v_mfma_f32_16x16x32_bf16 a[168:171], v[176:179], a[224:227], a[168:171]\n"
"s_waitcnt lgkmcnt(0)\n"
"s_barrier\n"
"v_mfma_f32_16x16x32_bf16 a[168:171], a[228:231], a[196:199], a[168:171]\n"
"ds_read_b128 v[136:139], v156 offset:0\n"
"ds_read_b128 v[140:143], v156 offset:4096\n"
"v_mfma_f32_16x16x32_bf16 a[172:175], a[252:255], a[192:195], a[172:175]\n"
"ds_read_b128 v[144:147], v156 offset:8192\n"
"ds_read_b128 v[148:151], v156 offset:12288\n"
"v_mfma_f32_16x16x32_bf16 a[172:175], v[172:175], a[224:227], a[172:175]\n"
"ds_read_b128 v[152:155], v156 offset:16384\n"
"v_mfma_f32_16x16x32_bf16 a[172:175], a[232:235], a[196:199], a[172:175]\n"
"ds_read_b128 v[220:223], v157 offset:20480\n"
"ds_read_b128 v[224:227], v157 offset:24576\n"
"v_mfma_f32_16x16x32_bf16 a[176:179], v[188:191], a[192:195], a[176:179]\n"
"ds_read_b128 v[228:231], v157 offset:28672\n"
"ds_read_b128 v[232:235], v157 offset:32768\n"
"v_mfma_f32_16x16x32_bf16 a[176:179], v[168:171], a[224:227], a[176:179]\n"
"ds_read_b128 v[236:239], v157 offset:36864\n"
"v_mfma_f32_16x16x32_bf16 a[176:179], a[236:239], a[196:199], a[176:179]\n"
"v_mfma_f32_16x16x32_bf16 a[180:183], v[184:187], a[192:195], a[180:183]\n"
"ds_write_b128 v15, a[168:171] offset:40960\n"
"v_mfma_f32_16x16x32_bf16 a[180:183], v[164:167], a[224:227], a[180:183]\n"
"v_mfma_f32_16x16x32_bf16 a[180:183], v[36:39], a[196:199], a[180:183]\n"
"s_waitcnt lgkmcnt(10)\n"
"v_add_f32 v76, v76, v136\n"
"v_add_f32 v77, v77, v137\n"
"v_mfma_f32_16x16x32_bf16 a[184:187], v[180:183], a[192:195], a[184:187]\n"
"ds_write_b128 v15, a[172:175] offset:45056\n"
"v_mfma_f32_16x16x32_bf16 a[184:187], v[160:163], a[224:227], a[184:187]\n"
"v_add_f32 v78, v78, v138\n"
"v_add_f32 v79, v79, v139\n"
"v_mfma_f32_16x16x32_bf16 a[184:187], v[40:43], a[196:199], a[184:187]\n"
"s_waitcnt lgkmcnt(10)\n"
"v_add_f32 v80, v80, v140\n"
"s_waitcnt vmcnt(5)\n"
"v_mfma_f32_16x16x32_bf16 v[96:99], v[196:199], a[200:203], v[96:99]\n"
"ds_write_b128 v15, a[176:179] offset:49152\n"
"v_mfma_f32_16x16x32_bf16 v[96:99], a[116:119], v[20:23], v[96:99]\n"
"v_add_f32 v81, v81, v141\n"
"v_add_f32 v82, v82, v142\n"
"v_mfma_f32_16x16x32_bf16 v[100:103], v[192:195], a[200:203], v[100:103]\n"
"v_add_f32 v83, v83, v143\n"
"v_mfma_f32_16x16x32_bf16 v[100:103], a[120:123], v[20:23], v[100:103]\n"
"ds_write_b128 v15, a[180:183] offset:53248\n"
"v_mfma_f32_16x16x32_bf16 v[104:107], a[104:107], a[200:203], v[104:107]\n"
"s_waitcnt vmcnt(3)\n"
"v_mfma_f32_16x16x32_bf16 v[104:107], a[124:127], v[20:23], v[104:107]\n"
"v_xor_b32 v14, s35, v117\n"
"v_bitop3_b32 v14, v119, s35, v14 bitop3:0xbe\n"
"v_mfma_f32_16x16x32_bf16 v[108:111], a[108:111], a[200:203], v[108:111]\n"
"ds_write_b128 v15, a[184:187] offset:57344\n"
"v_mfma_f32_16x16x32_bf16 v[108:111], a[240:243], v[20:23], v[108:111]\n"
"v_bitop3_b32 v14, v121, s35, v14 bitop3:0xbe\n"
"v_bitop3_b32 v14, v123, s35, v14 bitop3:0xbe\n"
"v_mfma_f32_16x16x32_bf16 v[112:115], a[112:115], a[200:203], v[112:115]\n"
"v_bitop3_b32 v14, v125, s35, v14 bitop3:0xbe\n"
"v_bitop3_b32 v14, v127, s35, v14 bitop3:0xbe\n"
"v_mfma_f32_16x16x32_bf16 v[112:115], a[244:247], v[20:23], v[112:115]\n"
"v_bitop3_b32 v14, v129, s35, v14 bitop3:0xbe\n"
"s_waitcnt vmcnt(0)\n"
"v_mfma_f32_16x16x32_bf16 v[96:99], a[248:251], v[24:27], v[96:99]\n"
"v_cmp_lt_u32 vcc, 0xffff, v14\n"
"v_mfma_f32_16x16x32_bf16 v[96:99], v[176:179], a[204:207], v[96:99]\n"
"s_waitcnt lgkmcnt(0)\n"
"s_barrier\n"
"v_mfma_f32_16x16x32_bf16 v[96:99], a[228:231], v[28:31], v[96:99]\n"
"ds_read_b128 v[56:59], v158 offset:40960\n"
"ds_read_b128 v[60:63], v158 offset:45056\n"
"v_mfma_f32_16x16x32_bf16 v[100:103], a[252:255], v[24:27], v[100:103]\n"
"ds_read_b128 v[64:67], v158 offset:49152\n"
"ds_read_b128 v[68:71], v158 offset:53248\n"
"v_mfma_f32_16x16x32_bf16 v[100:103], v[172:175], a[204:207], v[100:103]\n"
"ds_read_b128 v[72:75], v158 offset:57344\n"
"s_cbranch_vccnz .Lfb_slow_25\n"
".Lfb_back_26:\n"
"v_mfma_f32_16x16x32_bf16 v[100:103], a[232:235], v[28:31], v[100:103]\n"
"v_lshlrev_b32 v240, 16, v116\n"
"v_and_b32 v241, 0xffff0000, v116\n"
"v_mfma_f32_16x16x32_bf16 v[104:107], v[188:191], v[24:27], v[104:107]\n"
"v_lshlrev_b32 v242, 16, v117\n"
"v_lshlrev_b32 v243, 16, v118\n"
"v_mfma_f32_16x16x32_bf16 v[104:107], v[168:171], a[204:207], v[104:107]\n"
"v_and_b32 v244, 0xffff0000, v118\n"
"v_lshlrev_b32 v245, 16, v119\n"
"v_mfma_f32_16x16x32_bf16 v[104:107], a[236:239], v[28:31], v[104:107]\n"
"v_lshlrev_b32 v246, 16, v120\n"
"v_and_b32 v247, 0xffff0000, v120\n"
"v_mfma_f32_16x16x32_bf16 v[108:111], v[184:187], v[24:27], v[108:111]\n"
"v_lshlrev_b32 v248, 16, v121\n"
"v_lshlrev_b32 v249, 16, v122\n"
"v_mfma_f32_16x16x32_bf16 v[108:111], v[164:167], a[204:207], v[108:111]\n"
"v_and_b32 v250, 0xffff0000, v122\n"
"v_lshlrev_b32 v251, 16, v123\n"
"v_mfma_f32_16x16x32_bf16 v[108:111], v[36:39], v[28:31], v[108:111]\n"
"v_lshlrev_b32 v252, 16, v124\n"
"v_and_b32 v253, 0xffff0000, v124\n"
"v_mfma_f32_16x16x32_bf16 v[112:115], v[180:183], v[24:27], v[112:115]\n"
"v_lshlrev_b32 v254, 16, v125\n"
"v_lshlrev_b32 v255, 16, v126\n"
"v_mfma_f32_16x16x32_bf16 v[112:115], v[160:163], a[204:207], v[112:115]\n"
"v_and_b32 v44, 0xffff0000, v126\n"
"v_lshlrev_b32 v45, 16, v127\n"
"v_mfma_f32_16x16x32_bf16 v[112:115], v[40:43], v[28:31], v[112:115]\n"
"v_lshlrev_b32 v46, 16, v128\n"
"v_and_b32 v47, 0xffff0000, v128\n"
"v_pk_add_f32 v[76:77], v[76:77], v[220:221] op_sel:[0,0] op_sel_hi:[1,1]\n"
"v_pk_add_f32 v[78:79], v[78:79], v[222:223] op_sel:[0,0] op_sel_hi:[1,1]\n"
"v_pk_add_f32 v[76:77], v[76:77], v[240:241] op_sel:[0,0] op_sel_hi:[1,1]\n"
"v_pk_add_f32 v[78:79], v[78:79], v[242:243] op_sel:[0,0] op_sel_hi:[1,1]\n"
"v_pk_add_f32 v[76:77], v[76:77], v[96:97] op_sel:[0,0] op_sel_hi:[1,1]\n"
"v_pk_add_f32 v[78:79], v[78:79], v[98:99] op_sel:[0,0] op_sel_hi:[1,1]\n"
"s_waitcnt lgkmcnt(4)\n"
"v_pk_add_f32 v[76:77], v[76:77], v[56:57] op_sel:[0,0] op_sel_hi:[1,1]\n"
"v_pk_add_f32 v[78:79], v[78:79], v[58:59] op_sel:[0,0] op_sel_hi:[1,1]\n"
"v_cvt_pk_bf16_f32 v76, v76, v77\n"
"v_cvt_pk_bf16_f32 v77, v78, v79\n"
"ds_write_b64 v10, v[76:77] offset:0\n"
"v_pk_add_f32 v[80:81], v[80:81], v[224:225] op_sel:[0,0] op_sel_hi:[1,1]\n"
"v_pk_add_f32 v[82:83], v[82:83], v[226:227] op_sel:[0,0] op_sel_hi:[1,1]\n"
"v_pk_add_f32 v[80:81], v[80:81], v[244:245] op_sel:[0,0] op_sel_hi:[1,1]\n"
"v_pk_add_f32 v[82:83], v[82:83], v[246:247] op_sel:[0,0] op_sel_hi:[1,1]\n"
"v_pk_add_f32 v[80:81], v[80:81], v[100:101] op_sel:[0,0] op_sel_hi:[1,1]\n"
"v_pk_add_f32 v[82:83], v[82:83], v[102:103] op_sel:[0,0] op_sel_hi:[1,1]\n"
"s_waitcnt lgkmcnt(4)\n"
"v_pk_add_f32 v[80:81], v[80:81], v[60:61] op_sel:[0,0] op_sel_hi:[1,1]\n"
"v_pk_add_f32 v[82:83], v[82:83], v[62:63] op_sel:[0,0] op_sel_hi:[1,1]\n"
"v_cvt_pk_bf16_f32 v80, v80, v81\n"
"v_cvt_pk_bf16_f32 v81, v82, v83\n"
"ds_write_b64 v10, v[80:81] offset:32\n"
"v_pk_add_f32 v[84:85], v[84:85], v[144:145] op_sel:[0,0] op_sel_hi:[1,1]\n"
"v_pk_add_f32 v[86:87], v[86:87], v[146:147] op_sel:[0,0] op_sel_hi:[1,1]\n"
"v_pk_add_f32 v[84:85], v[84:85], v[228:229] op_sel:[0,0] op_sel_hi:[1,1]\n"
"v_pk_add_f32 v[86:87], v[86:87], v[230:231] op_sel:[0,0] op_sel_hi:[1,1]\n"
"v_pk_add_f32 v[84:85], v[84:85], v[248:249] op_sel:[0,0] op_sel_hi:[1,1]\n"
"v_pk_add_f32 v[86:87], v[86:87], v[250:251] op_sel:[0,0] op_sel_hi:[1,1]\n"
"s_waitcnt lgkmcnt(4)\n"
"v_pk_add_f32 v[84:85], v[84:85], v[64:65] op_sel:[0,0] op_sel_hi:[1,1]\n"
"v_pk_add_f32 v[86:87], v[86:87], v[66:67] op_sel:[0,0] op_sel_hi:[1,1]\n"
"v_pk_add_f32 v[84:85], v[84:85], v[104:105] op_sel:[0,0] op_sel_hi:[1,1]\n"
"v_pk_add_f32 v[86:87], v[86:87], v[106:107] op_sel:[0,0] op_sel_hi:[1,1]\n"
"v_cvt_pk_bf16_f32 v84, v84, v85\n"
"v_cvt_pk_bf16_f32 v85, v86, v87\n"
"ds_write_b64 v10, v[84:85] offset:64\n"
"v_pk_add_f32 v[88:89], v[88:89], v[148:149] op_sel:[0,0] op_sel_hi:[1,1]\n"
"v_pk_add_f32 v[90:91], v[90:91], v[150:151] op_sel:[0,0] op_sel_hi:[1,1]\n"
"v_pk_add_f32 v[88:89], v[88:89], v[232:233] op_sel:[0,0] op_sel_hi:[1,1]\n"
"v_pk_add_f32 v[90:91], v[90:91], v[234:235] op_sel:[0,0] op_sel_hi:[1,1]\n"
"v_pk_add_f32 v[88:89], v[88:89], v[252:253] op_sel:[0,0] op_sel_hi:[1,1]\n"
"v_pk_add_f32 v[90:91], v[90:91], v[254:255] op_sel:[0,0] op_sel_hi:[1,1]\n"
"s_waitcnt lgkmcnt(4)\n"
"v_pk_add_f32 v[88:89], v[88:89], v[68:69] op_sel:[0,0] op_sel_hi:[1,1]\n"
"v_pk_add_f32 v[90:91], v[90:91], v[70:71] op_sel:[0,0] op_sel_hi:[1,1]\n"
"v_pk_add_f32 v[88:89], v[88:89], v[108:109] op_sel:[0,0] op_sel_hi:[1,1]\n"
"v_pk_add_f32 v[90:91], v[90:91], v[110:111] op_sel:[0,0] op_sel_hi:[1,1]\n"
"v_cvt_pk_bf16_f32 v88, v88, v89\n"
"v_cvt_pk_bf16_f32 v89, v90, v91\n"
"ds_write_b64 v10, v[88:89] offset:96\n"
"v_pk_add_f32 v[92:93], v[92:93], v[152:153] op_sel:[0,0] op_sel_hi:[1,1]\n"
"v_pk_add_f32 v[94:95], v[94:95], v[154:155] op_sel:[0,0] op_sel_hi:[1,1]\n"
"v_pk_add_f32 v[92:93], v[92:93], v[236:237] op_sel:[0,0] op_sel_hi:[1,1]\n"
"v_pk_add_f32 v[94:95], v[94:95], v[238:239] op_sel:[0,0] op_sel_hi:[1,1]\n"
"v_pk_add_f32 v[92:93], v[92:93], v[44:45] op_sel:[0,0] op_sel_hi:[1,1]\n"
"v_pk_add_f32 v[94:95], v[94:95], v[46:47] op_sel:[0,0] op_sel_hi:[1,1]\n"
"s_waitcnt lgkmcnt(4)\n"
"v_pk_add_f32 v[92:93], v[92:93], v[72:73] op_sel:[0,0] op_sel_hi:[1,1]\n"
"v_pk_add_f32 v[94:95], v[94:95], v[74:75] op_sel:[0,0] op_sel_hi:[1,1]\n"
"v_pk_add_f32 v[92:93], v[92:93], v[112:113] op_sel:[0,0] op_sel_hi:[1,1]\n"
"v_pk_add_f32 v[94:95], v[94:95], v[114:115] op_sel:[0,0] op_sel_hi:[1,1]\n"
"v_cvt_pk_bf16_f32 v92, v92, v93\n"
"v_cvt_pk_bf16_f32 v93, v94, v95\n"
"ds_write_b64 v10, v[92:93] offset:128\n"
"ds_read_b128 v[20:23], v17\n"
"ds_read_b128 v[24:27], v18\n"
"ds_read_b128 v[28:31], v19\n"
"s_waitcnt lgkmcnt(2)\n"
"buffer_store_dwordx4 v[20:23], v5, s[48:51], s79 offen offset:0 \n"
"s_waitcnt lgkmcnt(1)\n"
"buffer_store_dwordx4 v[24:27], v6, s[48:51], s79 offen offset:1024 \n"
"s_waitcnt lgkmcnt(0)\n"
"buffer_store_dwordx4 v[28:31], v7, s[48:51], s79 offen offset:2048 \n"
"s_endpgm\n"
".Lfb_slow_1:\n"
"s_mov_b32 s99, 0\n"
".Lfb_loop_3:\n"
"s_sleep 4\n"
"buffer_load_dword v37, v36, s[16:19], 0 offen sc1\n"
"s_waitcnt vmcnt(0)\n"
"v_cmp_ne_u32 vcc, s52, v37\n"
"s_nop 1\n"
"s_cbranch_vccz .Lfb_back_2\n"
"s_add_u32 s99, s99, 1\n"
"s_cmp_lt_u32 s99, 32768\n"
"s_cbranch_scc1 .Lfb_loop_3\n"
"s_branch .Lfb_back_2\n"
".Lfb_slow_5:\n"
"s_mov_b32 s99, 0\n"
".Lfb_loop_7:\n"
"s_sleep 2\n"
"buffer_load_dword v12, v10, s[16:19], 0 offen sc1\n"
"s_waitcnt vmcnt(0)\n"
"v_cmp_ne_u32 vcc, s52, v12\n"
"s_nop 1\n"
"s_cbranch_vccz .Lfb_back_6\n"
"s_add_u32 s99, s99, 1\n"
"s_cmp_lt_u32 s99, 32768\n"
"s_cbranch_scc1 .Lfb_loop_7\n"
"s_branch .Lfb_back_6\n"
".Lfb_slow_8:\n"
"s_mov_b32 s99, 0\n"
".Lfb_loop_10:\n"
"s_sleep 2\n"
"buffer_load_dword v12, v11, s[16:19], 0 offen sc1\n"
"s_waitcnt vmcnt(0)\n"
"v_cmp_ne_u32 vcc, s52, v12\n"
"s_nop 1\n"
"s_cbranch_vccz .Lfb_back_9\n"
"s_add_u32 s99, s99, 1\n"
"s_cmp_lt_u32 s99, 32768\n"
"s_cbranch_scc1 .Lfb_loop_10\n"
"s_branch .Lfb_back_9\n"
".Lfb_slow_11:\n"
"s_mov_b32 s99, 0\n"
".Lfb_loop_13:\n"
"s_sleep 2\n"
"buffer_load_dwordx4 v[116:119], v2, s[20:23], s40 offen offset:0 sc1\n"
"buffer_load_dwordx4 v[120:123], v2, s[20:23], s40 offen offset:1024 sc1\n"
"buffer_load_dwordx4 v[124:127], v2, s[20:23], s40 offen offset:2048 sc1\n"
"buffer_load_dwordx2 v[128:129], v9, s[20:23], s40 offen offset:3072 sc1\n"
"s_waitcnt vmcnt(0)\n"
"v_xor_b32 v14, s35, v117\n"
"v_bitop3_b32 v14, v119, s35, v14 bitop3:0xbe\n"
"v_bitop3_b32 v14, v121, s35, v14 bitop3:0xbe\n"
"v_bitop3_b32 v14, v123, s35, v14 bitop3:0xbe\n"
"v_bitop3_b32 v14, v125, s35, v14 bitop3:0xbe\n"
"v_bitop3_b32 v14, v127, s35, v14 bitop3:0xbe\n"
"v_bitop3_b32 v14, v129, s35, v14 bitop3:0xbe\n"
"v_cmp_lt_u32 vcc, 0xffff, v14\n"
"s_nop 1\n"
"s_cbranch_vccz .Lfb_back_12\n"
"s_add_u32 s99, s99, 1\n"
"s_cmp_lt_u32 s99, 32768\n"
"s_cbranch_scc1 .Lfb_loop_13\n"
"s_branch .Lfb_back_12\n"
".Lfb_agg:\n"
"s_sleep 127\n"
"s_sleep 90\n"
"v_lshlrev_b32 v60, 7, v0\n"
"v_add_u32 v60, 212992, v60\n"
"s_mov_b32 s99, 0\n"
".Lfb_agl_14:\n"
"buffer_load_dword v61, v60, s[16:19], 0 offen sc1\n"
"s_waitcnt vmcnt(0)\n"
"v_cmp_ne_u32 vcc, s52, v61\n"
"s_nop 1\n"
"s_cbranch_vccz .Lfb_agd_15\n"
"s_sleep 8\n"
"s_add_u32 s99, s99, 1\n"
"s_cmp_lt_u32 s99, 32768\n"
"s_cbranch_scc1 .Lfb_agl_14\n"
".Lfb_agd_15:\n"
"s_barrier\n"
"v_mov_b32 v61, 1073741824\n"
"v_cmp_eq_u32 vcc, 0, v0\n"
"s_nop 1\n"
"v_cndmask_b32 v60, v61, v0, vcc\n"
"v_mov_b32 v61, s52\n"
"buffer_store_dword v61, v60, s[16:19], 0 offen offset:0 sc1\n"
"s_waitcnt vmcnt(0)\n"
"s_branch .Lfb_aggb\n"
".Lfb_slow_16:\n"
"s_mov_b32 s99, 0\n"
".Lfb_loop_18:\n"
"s_sleep 4\n"
"buffer_load_dword v60, v56, s[16:19], 0 offen sc1\n"
"s_waitcnt vmcnt(0)\n"
"v_cmp_ne_u32 vcc, s52, v60\n"
"s_nop 1\n"
"s_cbranch_vccz .Lfb_back_17\n"
"s_add_u32 s99, s99, 1\n"
"s_cmp_lt_u32 s99, 32768\n"
"s_cbranch_scc1 .Lfb_loop_18\n"
"s_branch .Lfb_back_17\n"
".Lfb_slow_19:\n"
"s_mov_b32 s99, 0\n"
".Lfb_loop_21:\n"
"s_sleep 2\n"
"buffer_load_dword v12, v10, s[16:19], 0 offen sc1\n"
"s_waitcnt vmcnt(0)\n"
"v_cmp_ne_u32 vcc, s52, v12\n"
"s_nop 1\n"
"s_cbranch_vccz .Lfb_back_20\n"
"s_add_u32 s99, s99, 1\n"
"s_cmp_lt_u32 s99, 32768\n"
"s_cbranch_scc1 .Lfb_loop_21\n"
"s_branch .Lfb_back_20\n"
".Lfb_slow_22:\n"
"s_mov_b32 s99, 0\n"
".Lfb_loop_24:\n"
"s_sleep 2\n"
"buffer_load_dword v12, v11, s[16:19], 0 offen sc1\n"
"s_waitcnt vmcnt(0)\n"
"v_cmp_ne_u32 vcc, s52, v12\n"
"s_nop 1\n"
"s_cbranch_vccz .Lfb_back_23\n"
"s_add_u32 s99, s99, 1\n"
"s_cmp_lt_u32 s99, 32768\n"
"s_cbranch_scc1 .Lfb_loop_24\n"
"s_branch .Lfb_back_23\n"
".Lfb_slow_25:\n"
"s_mov_b32 s99, 0\n"
".Lfb_loop_27:\n"
"s_sleep 2\n"
"buffer_load_dwordx4 v[116:119], v2, s[20:23], s40 offen offset:0 sc1\n"
"buffer_load_dwordx4 v[120:123], v2, s[20:23], s40 offen offset:1024 sc1\n"
"buffer_load_dwordx4 v[124:127], v2, s[20:23], s40 offen offset:2048 sc1\n"
"buffer_load_dwordx2 v[128:129], v9, s[20:23], s40 offen offset:3072 sc1\n"
"s_waitcnt vmcnt(0)\n"
"v_xor_b32 v14, s35, v117\n"
"v_bitop3_b32 v14, v119, s35, v14 bitop3:0xbe\n"
"v_bitop3_b32 v14, v121, s35, v14 bitop3:0xbe\n"
"v_bitop3_b32 v14, v123, s35, v14 bitop3:0xbe\n"
"v_bitop3_b32 v14, v125, s35, v14 bitop3:0xbe\n"
"v_bitop3_b32 v14, v127, s35, v14 bitop3:0xbe\n"
"v_bitop3_b32 v14, v129, s35, v14 bitop3:0xbe\n"
"v_cmp_lt_u32 vcc, 0xffff, v14\n"
"s_nop 1\n"
"s_cbranch_vccz .Lfb_back_26\n"
"s_add_u32 s99, s99, 1\n"
"s_cmp_lt_u32 s99, 32768\n"
"s_cbranch_scc1 .Lfb_loop_27\n"
"s_branch .Lfb_back_26\n"
".Lfb_slow_28:\n"
"s_mov_b32 s99, 0\n"
".Lfb_loop_30:\n"
"s_sleep 4\n"
"buffer_load_dword v49, v48, s[16:19], 0 offen sc1\n"
"s_waitcnt vmcnt(0)\n"
"v_cmp_ne_u32 vcc, s52, v49\n"
"s_nop 1\n"
"s_cbranch_vccz .Lfb_back_29\n"
"s_add_u32 s99, s99, 1\n"
"s_cmp_lt_u32 s99, 32768\n"
"s_cbranch_scc1 .Lfb_loop_30\n"
"s_branch .Lfb_back_29\n"
".Lfb_slow_31:\n"
"s_mov_b32 s99, 0\n"
".Lfb_loop_33:\n"
"s_sleep 2\n"
"buffer_load_dword v11, v10, s[16:19], 0 offen sc1\n"
"s_waitcnt vmcnt(0)\n"
"v_cmp_ne_u32 vcc, s52, v11\n"
"s_nop 1\n"
"s_cbranch_vccz .Lfb_back_32\n"
"s_add_u32 s99, s99, 1\n"
"s_cmp_lt_u32 s99, 32768\n"
"s_cbranch_scc1 .Lfb_loop_33\n"
"s_branch .Lfb_back_32\n"
".Lfb_slow_34:\n"
"s_mov_b32 s99, 0\n"
".Lfb_loop_36:\n"
"s_sleep 2\n"
"buffer_load_dword v13, v11, s[16:19], 0 offen sc1\n"
"s_waitcnt vmcnt(0)\n"
"v_cmp_ne_u32 vcc, s52, v13\n"
"s_nop 1\n"
"s_cbranch_vccz .Lfb_back_35\n"
"s_add_u32 s99, s99, 1\n"
"s_cmp_lt_u32 s99, 32768\n"
"s_cbranch_scc1 .Lfb_loop_36\n"
"s_branch .Lfb_back_35\n"
".Lfb_slow_37:\n"
"s_mov_b32 s99, 0\n"
".Lfb_loop_39:\n"
"s_sleep 2\n"
"buffer_load_dword v73, v72, s[16:19], 0 offen sc1\n"
"s_waitcnt vmcnt(0)\n"
"v_cmp_ne_u32 vcc, s52, v73\n"
"s_nop 1\n"
"s_cbranch_vccz .Lfb_back_38\n"
"s_add_u32 s99, s99, 1\n"
"s_cmp_lt_u32 s99, 32768\n"
"s_cbranch_scc1 .Lfb_loop_39\n"
"s_branch .Lfb_back_38\n"
".Lfb_slow_40:\n"
"s_mov_b32 s99, 0\n"
".Lfb_loop_42:\n"
"s_sleep 2\n"
"buffer_load_dword v12, v11, s[16:19], 0 offen sc1\n"
"s_waitcnt vmcnt(0)\n"
"v_cmp_ne_u32 vcc, s52, v12\n"
"s_nop 1\n"
"s_cbranch_vccz .Lfb_back_41\n"
"s_add_u32 s99, s99, 1\n"
"s_cmp_lt_u32 s99, 32768\n"
"s_cbranch_scc1 .Lfb_loop_42\n"
"s_branch .Lfb_back_41\n"
".Lfb_slow_43:\n"
"s_mov_b32 s99, 0\n"
".Lfb_loop_45:\n"
"s_sleep 2\n"
"buffer_load_dwordx4 v[116:119], v2, s[20:23], s40 offen offset:0 sc1\n"
"buffer_load_dwordx4 v[120:123], v2, s[20:23], s40 offen offset:1024 sc1\n"
"buffer_load_dwordx4 v[124:127], v2, s[20:23], s40 offen offset:2048 sc1\n"
"buffer_load_dwordx2 v[128:129], v9, s[20:23], s40 offen offset:3072 sc1\n"
"s_waitcnt vmcnt(0)\n"
"v_xor_b32 v14, s35, v117\n"
"v_bitop3_b32 v14, v119, s35, v14 bitop3:0xbe\n"
"v_bitop3_b32 v14, v121, s35, v14 bitop3:0xbe\n"
"v_bitop3_b32 v14, v123, s35, v14 bitop3:0xbe\n"
"v_bitop3_b32 v14, v125, s35, v14 bitop3:0xbe\n"
"v_bitop3_b32 v14, v127, s35, v14 bitop3:0xbe\n"
"v_bitop3_b32 v14, v129, s35, v14 bitop3:0xbe\n"
"v_cmp_lt_u32 vcc, 0xffff, v14\n"
"s_nop 1\n"
"s_cbranch_vccz .Lfb_back_44\n"
"s_add_u32 s99, s99, 1\n"
"s_cmp_lt_u32 s99, 32768\n"
"s_cbranch_scc1 .Lfb_loop_45\n"
"s_branch .Lfb_back_44\n"
".Lfb_end:\n"
        : "+v"(tid)
        : "s"(__builtin_amdgcn_kernarg_segment_ptr()), "s"(blockIdx.x)
        : "v1", "v2", "v3", "v4", "v5", "v6", "v7", "v8", "v9", "v10", "v11", "v12", "v13", "v14", "v15", "v16", "v17", "v18", "v19", "v20", "v21", "v22", "v23", "v24", "v25", "v26", "v27", "v28", "v29", "v30", "v31", "v32", "v33", "v34", "v35", "v36", "v37", "v38", "v39", "v40", "v41", "v42", "v43", "v44", "v45", "v46", "v47", "v48", "v49", "v50", "v51", "v52", "v53", "v54", "v55", "v56", "v57", "v58", "v59", "v60", "v61", "v62", "v63", "v64", "v65", "v66", "v67", "v68", "v69", "v70", "v71", "v72", "v73", "v74", "v75", "v76", "v77", "v78", "v79", "v80", "v81", "v82", "v83", "v84", "v85", "v86", "v87", "v88", "v89", "v90", "v91", "v92", "v93", "v94", "v95", "v96", "v97", "v98", "v99", "v100", "v101", "v102", "v103", "v104", "v105", "v106", "v107", "v108", "v109", "v110", "v111", "v112", "v113", "v114", "v115", "v116", "v117", "v118", "v119", "v120", "v121", "v122", "v123", "v124", "v125", "v126", "v127", "v128", "v129", "v130", "v131", "v132", "v133", "v134", "v135", "v136", "v137", "v138", "v139", "v140", "v141", "v142", "v143", "v144", "v145", "v146", "v147", "v148", "v149", "v150", "v151", "v152", "v153", "v154", "v155", "v156", "v157", "v158", "v159", "v160", "v161", "v162", "v163", "v164", "v165", "v166", "v167", "v168", "v169", "v170", "v171", "v172", "v173", "v174", "v175", "v176", "v177", "v178", "v179", "v180", "v181", "v182", "v183", "v184", "v185", "v186", "v187", "v188", "v189", "v190", "v191", "v192", "v193", "v194", "v195", "v196", "v197", "v198", "v199", "v200", "v201", "v202", "v203", "v204", "v205", "v206", "v207", "v208", "v209", "v210", "v211", "v212", "v213", "v214", "v215", "v216", "v217", "v218", "v219", "v220", "v221", "v222", "v223", "v224", "v225", "v226", "v227", "v228", "v229", "v230", "v231", "v232", "v233", "v234", "v235", "v236", "v237", "v238", "v239", "v240", "v241", "v242", "v243", "v244", "v245", "v246", "v247", "v248", "v249", "v250", "v251", "v252", "v253", "v254", "v255", "a0", "a1", "a2", "a3", "a4", "a5", "a6", "a7", "a8", "a9", "a10", "a11", "a12", "a13", "a14", "a15", "a16", "a17", "a18", "a19", "a20", "a21", "a22", "a23", "a24", "a25", "a26", "a27", "a28", "a29", "a30", "a31", "a32", "a33", "a34", "a35", "a36", "a37", "a38", "a39", "a40", "a41", "a42", "a43", "a44", "a45", "a46", "a47", "a48", "a49", "a50", "a51", "a52", "a53", "a54", "a55", "a56", "a57", "a58", "a59", "a60", "a61", "a62", "a63", "a64", "a65", "a66", "a67", "a68", "a69", "a70", "a71", "a72", "a73", "a74", "a75", "a76", "a77", "a78", "a79", "a80", "a81", "a82", "a83", "a84", "a85", "a86", "a87", "a88", "a89", "a90", "a91", "a92", "a93", "a94", "a95", "a96", "a97", "a98", "a99", "a100", "a101", "a102", "a103", "a104", "a105", "a106", "a107", "a108", "a109", "a110", "a111", "a112", "a113", "a114", "a115", "a116", "a117", "a118", "a119", "a120", "a121", "a122", "a123", "a124", "a125", "a126", "a127", "a128", "a129", "a130", "a131", "a132", "a133", "a134", "a135", "a136", "a137", "a138", "a139", "a140", "a141", "a142", "a143", "a144", "a145", "a146", "a147", "a148", "a149", "a150", "a151", "a152", "a153", "a154", "a155", "a156", "a157", "a158", "a159", "a160", "a161", "a162", "a163", "a164", "a165", "a166", "a167", "a168", "a169", "a170", "a171", "a172", "a173", "a174", "a175", "a176", "a177", "a178", "a179", "a180", "a181", "a182", "a183", "a184", "a185", "a186", "a187", "a188", "a189", "a190", "a191", "a192", "a193", "a194", "a195", "a196", "a197", "a198", "a199", "a200", "a201", "a202", "a203", "a204", "a205", "a206", "a207", "a208", "a209", "a210", "a211", "a212", "a213", "a214", "a215", "a216", "a217", "a218", "a219", "a220", "a221", "a222", "a223", "a224", "a225", "a226", "a227", "a228", "a229", "a230", "a231", "a232", "a233", "a234", "a235", "a236", "a237", "a238", "a239", "a240", "a241", "a242", "a243", "a244", "a245", "a246", "a247", "a248", "a249", "a250", "a251", "a252", "a253", "a254", "a255", "s3", "s4", "s5", "s6", "s7", "s8", "s9", "s10", "s11", "s12", "s13", "s14", "s15", "s16", "s17", "s18", "s19", "s20", "s21", "s22", "s23", "s24", "s25", "s26", "s27", "s28", "s29", "s30", "s31", "s34", "s35", "s36", "s37", "s38", "s39", "s40", "s41", "s42", "s43", "s44", "s45", "s46", "s47", "s48", "s49", "s50", "s51", "s52", "s53", "s54", "s55", "s56", "s57", "s58", "s59", "s60", "s61", "s62", "s63", "s64", "s65", "s66", "s67", "s68", "s69", "s70", "s71", "s72", "s73", "s74", "s75", "s76", "s77", "s78", "s79", "s80", "s81", "s82", "s83", "s84", "s85", "s86", "s87", "s88", "s89", "s90", "s91", "s92", "s93", "s94", "s95", "s96", "s97", "s98", "s99", "vcc", "scc", "memory");
}
static const BlockTab kTab = {{1280u,0u,138659232u,128u,277317184u,256u,415975136u,384u,689096576u,640u,827754528u,768u,966412480u,896u,1105070432u,1024u,1280u,4440064u,138659232u,4440192u,277317184u,4440320u,415975136u,4440448u,689096576u,4440704u,827754528u,4440832u,966412480u,4440960u,1105070432u,4441088u,134464768u,8880256u,273122720u,8880384u,411780672u,8880512u,550438624u,8880640u,823560064u,8880896u,962218016u,8881024u,1100875968u,8881152u,1239533920u,8881280u,268928256u,13320448u,407586208u,13320576u,546244160u,13320704u,684902112u,13320832u,958023552u,13321088u,1096681504u,13321216u,1235339456u,13321344u,29362528u,13320192u,403391744u,17760640u,542049696u,17760768u,680707648u,17760896u,819365600u,17761024u,1092487040u,17761280u,1231144992u,17761408u,25168064u,17760256u,163826016u,17760384u,537855232u,22200832u,676513184u,22200960u,815171136u,22201088u,953829088u,22201216u,1226950528u,22201472u,20973600u,22200320u,159631552u,22200448u,298289504u,22200576u,672318720u,26641024u,810976672u,26641152u,949634624u,26641280u,1088292576u,26641408u,16779136u,26640384u,155437088u,26640512u,294095040u,26640640u,432752992u,26640768u,806782208u,31081216u,945440160u,31081344u,1084098112u,31081472u,1222756064u,31081600u,151242624u,31080576u,289900576u,31080704u,428558528u,31080832u,567216480u,31080960u,941245696u,35521408u,1079903648u,35521536u,1218561600u,35521664u,12584672u,35520512u,285706112u,35520768u,424364064u,35520896u,563022016u,35521024u,701679968u,35521152u,1075709184u,39961600u,1214367136u,39961728u,8390208u,39960576u,147048160u,39960704u,420169600u,39960960u,558827552u,39961088u,697485504u,39961216u,836143456u,39961344u,1210172672u,44401792u,4195744u,44400640u,142853696u,44400768u,281511648u,44400896u,554633088u,44401152u,693291040u,44401280u,831948992u,44401408u,970606944u,44401536u,0u,0u,147022208u,128u,294044416u,256u,441066624u,384u,722527744u,640u,869549952u,768u,1016572160u,896u,1163594368u,1024u,134438912u,4644992u,281461120u,4645120u,428483328u,4645248u,575505536u,4645376u,856966656u,4645632u,1003988864u,4645760u,1151011072u,4645888u,1298033280u,4646016u,403316736u,9290112u,550338944u,9290240u,697361152u,9290368u,844383360u,9290496u,1125844480u,9290752u,1272866688u,9290880u,75499776u,9289728u,222521984u,9289856u,672194560u,13935232u,819216768u,13935360u,966238976u,13935488u,1113261184u,13935616u,50333184u,13934592u,197355392u,13934720u,344377600u,13934848u,491399808u,13934976u,941072384u,18580352u,1088094592u,18580480u,1235116800u,18580608u,37749888u,18579456u,319211008u,18579712u,466233216u,18579840u,613255424u,18579968u,760277632u,18580096u,1075511296u,23225344u,1222533504u,23225472u,25166592u,23224320u,172188800u,23224448u,453649920u,23224704u,600672128u,23224832u,747694336u,23224960u,894716544u,23225088u,4194432u,0u,151216640u,128u,298238848u,256u,445261056u,384u,726722176u,640u,873744384u,768u,1020766592u,896u,1167788800u,1024u,138633344u,4644992u,285655552u,4645120u,432677760u,4645248u,579699968u,4645376u,861161088u,4645632u,1008183296u,4645760u,1155205504u,4645888u,1302227712u,4646016u,407511168u,9290112u,554533376u,9290240u,701555584u,9290368u,848577792u,9290496u,1130038912u,9290752u,1277061120u,9290880u,79694208u,9289728u,226716416u,9289856u,676388992u,13935232u,823411200u,13935360u,970433408u,13935488u,1117455616u,13935616u,54527616u,13934592u,201549824u,13934720u,348572032u,13934848u,495594240u,13934976u,945266816u,18580352u,1092289024u,18580480u,1239311232u,18580608u,41944320u,18579456u,323405440u,18579712u,470427648u,18579840u,617449856u,18579968u,764472064u,18580096u,1079705728u,23225344u,1226727936u,23225472u,29361024u,23224320u,176383232u,23224448u,457844352u,23224704u,604866560u,23224832u,751888768u,23224960u,898910976u,23225088u,8388864u,0u,155411072u,128u,302433280u,256u,449455488u,384u,730916608u,640u,877938816u,768u,1024961024u,896u,1171983232u,1024u,142827776u,4644992u,289849984u,4645120u,436872192u,4645248u,583894400u,4645376u,865355520u,4645632u,1012377728u,4645760u,1159399936u,4645888u,1306422144u,4646016u,411705600u,9290112u,558727808u,9290240u,705750016u,9290368u,852772224u,9290496u,1134233344u,9290752u,1281255552u,9290880u,83888640u,9289728u,230910848u,9289856u,680583424u,13935232u,827605632u,13935360u,974627840u,13935488u,1121650048u,13935616u,58722048u,13934592u,205744256u,13934720u,352766464u,13934848u,499788672u,13934976u,949461248u,18580352u,1096483456u,18580480u,1243505664u,18580608u,46138752u,18579456u,327599872u,18579712u,474622080u,18579840u,621644288u,18579968u,768666496u,18580096u,1083900160u,23225344u,1230922368u,23225472u,33555456u,23224320u,180577664u,23224448u,462038784u,23224704u,609060992u,23224832u,756083200u,23224960u,903105408u,23225088u,100666368u,0u,772860928u,13935232u,373738624u,256u,1045933184u,13935488u,781249792u,640u,109055232u,13934592u,1054322048u,896u,382127488u,13934848u,235105280u,4644992u,1041738752u,18580352u,508177536u,4645248u,1314811008u,18580608u,915688704u,4645632u,377933056u,18579712u,1188760960u,4645888u,651005312u,18579968u,503983104u,9290112u,1176177664u,23225344u,777055360u,9290368u,104860800u,23224320u,1184566528u,9290752u,512371968u,23224704u,113249664u,9289728u,785444224u,23224960u}};

extern "C" void kernel_launch(void* const* d_in, const int* in_sizes, int n_in, void* d_out, int out_size, void* d_ws,
                              size_t ws_size, hipStream_t stream) {
    fused_block_kernel<<<256, 256, 0, stream>>>(d_in[0], d_in[1], d_in[2], d_in[4], d_in[5], d_in[6], d_out, d_ws, kTab);
}
```
